```python
import torch

def _softmax(x, axis=-1):
    return torch.softmax(x, dim=axis)
def _gelu(x):
    return torch.nn.functional.gelu(x, approximate="tanh")
def _silu(x):
    return torch.nn.functional.silu(x)
def _sigmoid(x):
    return torch.sigmoid(x)
def _layernorm(x, axis=-1, eps=1e-5):
    m = x.mean(dim=axis, keepdim=True)
    v = x.var(dim=axis, keepdim=True, unbiased=False)
    return (x - m) / torch.sqrt(v + eps)
def _scatter_add(tgt, idx, src, axis=0):
    return tgt.clone().index_add_(axis, idx, src)
def _segment_sum(src, seg, n):
    out = torch.zeros((n,) + src.shape[1:], dtype=src.dtype)
    return out.index_add_(0, seg, src)
def _pad(x, pw):
    flat = tuple(v for pair in reversed(pw) for v in pair)
    return torch.nn.functional.pad(x, flat)

kernel_name = 'sequential_preLN__compressed_kv_cla__dense_activation_silu__prompt'

def setup_inputs():

    _w = (lambda a: (lambda t: t.to(torch.bfloat16) if t.is_floating_point() else t)(torch.as_tensor(a)))
    def _A_setup():
        B, H, Sq, Sk, D, M = 1, 16, 2048, 2048, 256, 448
        Dc = 4 * D
        return {
            "x": _w((lambda *s: torch.randn(*s, dtype=torch.bfloat16) * 0.05)(B, Sq, M)),
            "c_kv": _w((lambda *s: torch.randn(*s, dtype=torch.bfloat16) * 0.4)(B, Sk, Dc)),
            "w_q": _w((lambda *s: torch.randn(*s, dtype=torch.bfloat16) * 1.6)(M, H, D) * (M ** -0.5)),
            "w_uk": _w((lambda *s: torch.randn(*s, dtype=torch.bfloat16) * 1.6)(Dc, H, D) * (Dc ** -0.5)),
            "w_uv": _w((lambda *s: torch.randn(*s, dtype=torch.bfloat16) * 0.4)(Dc, H, D) * (Dc ** -0.5)),
            "w_o": _w((lambda *s: torch.randn(*s, dtype=torch.bfloat16) * 0.4)(H, D, M) * ((H * D) ** -0.5)),
        }
    _A_in = _A_setup()

    def _B_setup():
        B, S, D, F = 1, 2048, 448, 1792
        return {
            "x": _w((lambda *s: torch.randn(*s, dtype=torch.bfloat16) * 0.05)(B, S, D)),
            "w1": _w((lambda *s: torch.randn(*s, dtype=torch.bfloat16) * 0.8)(D, F) * (D ** -0.5)),
            "w2": _w((lambda *s: torch.randn(*s, dtype=torch.bfloat16) * 0.1)(F, D) * (F ** -0.5)),
        }
    _B_in = _B_setup()

    return {
        "A_x": _A_in["x"],
        "A_c_kv": _A_in["c_kv"],
        "A_w_q": _A_in["w_q"],
        "A_w_uk": _A_in["w_uk"],
        "A_w_uv": _A_in["w_uv"],
        "A_w_o": _A_in["w_o"],
        "B_x": _B_in["x"],
        "B_w1": _B_in["w1"],
        "B_w2": _B_in["w2"],
    }

def reference(A_x, A_c_kv, A_w_q, A_w_uk, A_w_uv, A_w_o, B_x, B_w1, B_w2):
    def _A_ref(x, c_kv, w_q, w_uk, w_uv, w_o):
        B, Sq, M = x.shape[0], x.shape[1], x.shape[2]
        Sk = c_kv.shape[1]
        H, D = w_q.shape[1], w_q.shape[2]
        q = torch.einsum("bqm,mhd->bhqd", x, w_q)
        k = torch.einsum("bkc,chd->bhkd", c_kv, w_uk)
        v = torch.einsum("bkc,chd->bhkd", c_kv, w_uv)
        scores = torch.einsum("bhqd,bhkd->bhqk", q, k) * (D ** -0.5)
        off = Sk - Sq
        qi = torch.arange(Sq)[:, None]
        kj = torch.arange(Sk)[None, :]
        cmask = kj <= (qi + off)
        scores = torch.where(cmask[None, None], scores, -1e9)
        p = _softmax(scores, axis=-1)
        ctx = torch.einsum("bhqk,bhkd->bhqd", p, v)
        return torch.einsum("bhqd,hdm->bqm", ctx, w_o)

    def _B_ref(x, w1, w2):
        h = torch.einsum("bsd,df->bsf", x, w1)
        h = _silu(h)
        out = torch.einsum("bsf,fd->bsd", h, w2)
        return out

    x = A_x
    h = x + _A_ref(_layernorm(x, axis=-1), A_c_kv, A_w_q, A_w_uk, A_w_uv, A_w_o)
    h2 = h + _B_ref(_layernorm(h, axis=-1), B_w1, B_w2)
    return h2
```

```cpp
#include <hip/hip_runtime.h>
#include <hip/hip_bf16.h>
typedef unsigned short u16;
__device__ inline float bf2f(u16 b){ return __uint_as_float(((unsigned)b)<<16); }
__device__ inline u16 f2bf(float f){ unsigned u=__float_as_uint(f); u += 0x7FFFu + ((u>>16)&1u); return (u16)(u>>16); }

__global__ void n_ln(const u16* x, u16* y, int M){
  int row = blockIdx.x; int t = threadIdx.x;
  float v[7]; float s=0;
  for(int i=0;i<7;i++){ v[i]=bf2f(x[row*448+t+64*i]); s+=v[i]; }
  for(int o=32;o>=1;o>>=1) s+=__shfl_xor(s,o);
  float m=s/448.f; float q=0;
  for(int i=0;i<7;i++){ float d=v[i]-m; q+=d*d; }
  for(int o=32;o>=1;o>>=1) q+=__shfl_xor(q,o);
  float r=1.f/sqrtf(q/448.f+1e-5f);
  for(int i=0;i<7;i++) y[row*448+t+64*i]=f2bf((v[i]-m)*r);
}
__global__ void n_gemm(const u16* A,const u16* B,u16* C,int M,int N,int K,int mode,const u16* res){
  __shared__ float As[64][17]; __shared__ float Bs[16][65];
  int tx=threadIdx.x&15, ty=threadIdx.x>>4; int m0=blockIdx.y*64, n0=blockIdx.x*64;
  float acc[4][4]={};
  for(int k0=0;k0<K;k0+=16){
    for(int i=threadIdx.x;i<64*16;i+=256){ int r=i>>4,c=i&15; As[r][c]=bf2f(A[(size_t)(m0+r)*K+k0+c]); }
    for(int i=threadIdx.x;i<16*64;i+=256){ int r=i>>6,c=i&63; Bs[r][c]=bf2f(B[(size_t)(k0+r)*N+n0+c]); }
    __syncthreads();
    for(int k=0;k<16;k++){ float a[4],b[4]; for(int i=0;i<4;i++){a[i]=As[ty*4+i][k]; b[i]=Bs[k][tx*4+i];}
      for(int i=0;i<4;i++)for(int j=0;j<4;j++) acc[i][j]+=a[i]*b[j]; }
    __syncthreads();
  }
  for(int i=0;i<4;i++)for(int j=0;j<4;j++){ float v=acc[i][j]; size_t o=(size_t)(m0+ty*4+i)*N+n0+tx*4+j;
    if(mode==1){ v=bf2f(f2bf(v)); v=v/(1.f+__expf(-v)); }
    if(mode==2){ v=bf2f(f2bf(v))+bf2f(res[o]); }
    C[o]=f2bf(v); }
}
__global__ void n_attn(const u16* Q,const u16* K,const u16* V,u16* O){
  int row=blockIdx.x, h=blockIdx.y, t=threadIdx.x;
  __shared__ float q[256]; __shared__ float p[2048]; __shared__ float red[256];
  q[t]=bf2f(Q[(size_t)row*4096+h*256+t]); __syncthreads();
  float mx=-1e30f;
  for(int j=t;j<=row;j+=256){ const u16* k=K+(size_t)j*4096+h*256; float s=0; for(int d=0;d<256;d++) s+=q[d]*bf2f(k[d]); s*=0.0625f; p[j]=s; mx=fmaxf(mx,s);}
  red[t]=mx; __syncthreads(); for(int o=128;o>=1;o>>=1){ if(t<o) red[t]=fmaxf(red[t],red[t+o]); __syncthreads(); } mx=red[0]; __syncthreads();
  float sm=0; for(int j=t;j<=row;j+=256){ float e=__expf(p[j]-mx); p[j]=e; sm+=e; }
  red[t]=sm; __syncthreads(); for(int o=128;o>=1;o>>=1){ if(t<o) red[t]+=red[t+o]; __syncthreads(); } sm=red[0];
  float a=0; for(int j=0;j<=row;j++) a+=p[j]*bf2f(V[(size_t)j*4096+h*256+t]);
  O[(size_t)row*4096+h*256+t]=f2bf(a/sm);
}
extern "C" void kernel_launch(void* const* d_in, const int* in_sizes, int n_in, void* d_out, int out_size, void* d_ws, size_t ws_size, hipStream_t stream){
  const u16* x=(const u16*)d_in[0]; const u16* c=(const u16*)d_in[1]; const u16* wq=(const u16*)d_in[2]; const u16* wuk=(const u16*)d_in[3];
  const u16* wuv=(const u16*)d_in[4]; const u16* wo=(const u16*)d_in[5]; const u16* w1=(const u16*)d_in[7]; const u16* w2=(const u16*)d_in[8];
  char* ws=(char*)d_ws; size_t MB=1<<20;
  u16* xn=(u16*)(ws); u16* Q=(u16*)(ws+4*MB); u16* K=(u16*)(ws+24*MB); u16* V=(u16*)(ws+44*MB); u16* O=(u16*)(ws+64*MB); u16* h=(u16*)(ws+84*MB); u16* hn=(u16*)(ws+88*MB); u16* g=(u16*)(ws+92*MB);
  n_ln<<<2048,64,0,stream>>>(x,xn,2048);
  n_gemm<<<dim3(64,32),256,0,stream>>>(xn,wq,Q,2048,4096,448,0,nullptr);
  n_gemm<<<dim3(64,32),256,0,stream>>>(c,wuk,K,2048,4096,1024,0,nullptr);
  n_gemm<<<dim3(64,32),256,0,stream>>>(c,wuv,V,2048,4096,1024,0,nullptr);
  n_attn<<<dim3(2048,16),256,0,stream>>>(Q,K,V,O);
  n_gemm<<<dim3(7,32),256,0,stream>>>(O,wo,h,2048,448,4096,2,x);
  n_ln<<<2048,64,0,stream>>>(h,hn,2048);
  n_gemm<<<dim3(28,32),256,0,stream>>>(hn,w1,g,2048,1792,448,1,nullptr);
  n_gemm<<<dim3(7,32),256,0,stream>>>(g,w2,(u16*)d_out,2048,448,1792,2,h);
}
```

```cpp
#include <hip/hip_runtime.h>
typedef unsigned short u16;
#define DI __device__ __forceinline__
#define RSRC __amdgpu_buffer_rsrc_t
DI unsigned rfl(unsigned x) { return __builtin_amdgcn_readfirstlane(x); }
DI RSRC mk_rsrc(const void* p, unsigned bytes) { return __builtin_amdgcn_make_buffer_rsrc((void*)p, (short)0, (int)bytes, 0x00020000); }
#define M1_ASM \
"Lbeg%=:\n" \
"s_load_dwordx2 s[24:25], %[ka], 0\n" \
"s_load_dwordx2 s[20:21], %[ka], 16\n" \
"s_load_dwordx2 s[28:29], %[ka], 40\n" \
"s_lshr_b32 s36, %[bw], 8\n" \
"s_and_b32 s95, %[bw], 0xff\n" \
"v_mov_b32 v225, %[vc0]\n" \
"v_xor_b32 v226, 64, %[vc0]\n" \
"v_mov_b32 v227, %[vw0]\n" \
"v_xor_b32 v228, 64, %[vw0]\n" \
"s_lshl_b32 s37, s36, 13\n" \
"s_lshl_b32 s38, s36, 12\n" \
"s_lshl_b32 s39, s36, 17\n" \
"s_lshl_b32 s40, s36, 16\n" \
"s_add_u32 m0, s37, 0\n" \
"s_add_u32 s52, s39, 0\n" \
"buffer_load_dwordx4 v225, %[rc], s52 offen lds\n" \
"v_mov_b32 v224, %[vl16]\n" \
"s_lshr_b32 s19, s95, 3\n" \
"s_lshr_b32 s17, s95, 4\n" \
"s_and_b32 s80, s95, 7\n" \
"s_lshl_b32 s80, s80, 1\n" \
"s_and_b32 s81, s19, 1\n" \
"s_add_u32 m0, s37, 1024\n" \
"s_add_u32 s52, s39, 16384\n" \
"buffer_load_dwordx4 v226, %[rc], s52 offen lds\n" \
"s_add_u32 s16, s80, s81\n" \
"s_and_b32 s54, s17, 1\n" \
"s_lshr_b32 s80, s17, 1\n" \
"s_sub_u32 s81, 15, s80\n" \
"s_cmp_eq_u32 s54, 0\n" \
"s_cselect_b32 s18, s81, s80\n" \
"v_cmp_gt_u32 vcc, 0x380, %[vl16]\n" \
"s_add_u32 m0, s37, 2048\n" \
"s_add_u32 s52, s39, 32768\n" \
"buffer_load_dwordx4 v225, %[rc], s52 offen lds\n" \
"v_bfrev_b32 v175, 1\n" \
"v_lshlrev_b32 v176, 4, %[vl16]\n" \
"v_cndmask_b32 v174, v175, %[vl16], vcc\n" \
"v_cndmask_b32 v175, v175, v176, vcc\n" \
"s_lshl_b32 s80, s95, 2\n" \
"s_add_u32 s80, s80, s36\n" \
"s_add_u32 m0, s37, 3072\n" \
"s_add_u32 s52, s39, 49152\n" \
"buffer_load_dwordx4 v226, %[rc], s52 offen lds\n" \
"s_mul_i32 s81, s80, 1792\n" \
"s_waitcnt lgkmcnt(0)\n" \
"s_add_u32 s24, s24, s81\n" \
"s_addc_u32 s25, s25, 0\n" \
"s_and_b32 s25, s25, 0xffff\n" \
"s_mov_b32 s26, 1792\n" \
"s_mov_b32 s27, 0x20000\n" \
"s_add_u32 m0, s38, 98304\n" \
"s_add_u32 s52, s40, 0\n" \
"buffer_load_dwordx4 v227, %[rw], s52 offen lds\n" \
"buffer_load_dwordx4 v[144:147], v174, s[24:27], 0 offen\n" \
"buffer_load_dwordx4 v[148:151], v174, s[24:27], 0 offen offset:896\n" \
"s_lshr_b32 s81, s80, 3\n" \
"s_mul_i32 s81, s81, 14336\n" \
"s_and_b32 s80, s80, 7\n" \
"s_lshl_b32 s80, s80, 5\n" \
"s_add_u32 s80, s80, s81\n" \
"s_add_u32 m0, s38, 99328\n" \
"s_add_u32 s52, s40, 16384\n" \
"buffer_load_dwordx4 v228, %[rw], s52 offen lds\n" \
"s_add_u32 s68, s80, 34603008\n" \
"s_lshl_b32 s80, s16, 9\n" \
"s_sub_u32 s22, 3670016, s80\n" \
"s_add_u32 s20, s20, s80\n" \
"s_addc_u32 s21, s21, 0\n" \
"s_and_b32 s21, s21, 0xffff\n" \
"s_nop 0\n" \
"s_add_u32 m0, s38, 100352\n" \
"s_add_u32 s52, s40, 32768\n" \
"buffer_load_dwordx4 v227, %[rw], s52 offen lds\n" \
"s_mov_b32 s23, 0x20000\n" \
"s_and_b32 s29, s29, 0xffff\n" \
"s_mov_b32 s30, 3670016\n" \
"s_mov_b32 s31, 0x20000\n" \
"v_bfe_u32 v180, v224, 4, 4\n" \
"v_lshrrev_b32 v181, 8, v224\n" \
"s_add_u32 m0, s38, 101376\n" \
"s_add_u32 s52, s40, 49152\n" \
"buffer_load_dwordx4 v228, %[rw], s52 offen lds\n" \
"v_lshrrev_b32 v182, 1, v180\n" \
"v_xor_b32 v182, v182, v181\n" \
"v_lshlrev_b32 v182, 4, v182\n" \
"v_lshl_add_u32 v229, v180, 7, v182\n" \
"s_lshr_b32 s80, s36, 1\n" \
"s_lshl_b32 s80, s80, 14\n" \
"s_add_u32 m0, s37, 4096\n" \
"s_add_u32 s52, s39, 65536\n" \
"buffer_load_dwordx4 v225, %[rc], s52 offen lds\n" \
"v_add_u32 v229, s80, v229\n" \
"v_xor_b32 v230, 64, v229\n" \
"v_lshrrev_b32 v182, 2, v180\n" \
"v_lshl_add_u32 v183, v181, 3, v182\n" \
"v_lshlrev_b32 v183, 9, v183\n" \
"v_and_b32 v184, 3, v180\n" \
"s_add_u32 m0, s37, 5120\n" \
"s_add_u32 s52, s39, 81920\n" \
"buffer_load_dwordx4 v226, %[rc], s52 offen lds\n" \
"v_lshl_add_u32 v183, v184, 3, v183\n" \
"s_and_b32 s80, s36, 1\n" \
"s_lshl_b32 s80, s80, 8\n" \
"s_add_u32 s80, s80, 98304\n" \
"v_add_u32 v183, s80, v183\n" \
"v_and_b32 v184, 1, v181\n" \
"s_add_u32 m0, s37, 6144\n" \
"s_add_u32 s52, s39, 98304\n" \
"buffer_load_dwordx4 v225, %[rc], s52 offen lds\n" \
"v_lshl_or_b32 v184, v184, 2, v182\n" \
"v_lshlrev_b32 v184, 5, v184\n" \
"v_xor_b32 v231, 0, v184\n" \
"v_xor_b32 v232, 32, v184\n" \
"v_xor_b32 v233, 64, v184\n" \
"v_xor_b32 v234, 96, v184\n" \
"s_add_u32 m0, s37, 7168\n" \
"s_add_u32 s52, s39, 114688\n" \
"buffer_load_dwordx4 v226, %[rc], s52 offen lds\n" \
"v_xor_b32 v235, 128, v184\n" \
"v_xor_b32 v236, 160, v184\n" \
"v_xor_b32 v237, 192, v184\n" \
"v_xor_b32 v238, 224, v184\n" \
"v_add_u32 v231, v183, v231\n" \
"v_add_u32 v232, v183, v232\n" \
"s_add_u32 m0, s38, 114688\n" \
"s_add_u32 s52, s40, 262144\n" \
"buffer_load_dwordx4 v227, %[rw], s52 offen lds\n" \
"v_add_u32 v233, v183, v233\n" \
"v_add_u32 v234, v183, v234\n" \
"v_add_u32 v235, v183, v235\n" \
"v_add_u32 v236, v183, v236\n" \
"v_add_u32 v237, v183, v237\n" \
"v_add_u32 v238, v183, v238\n" \
"s_add_u32 m0, s38, 115712\n" \
"s_add_u32 s52, s40, 278528\n" \
"buffer_load_dwordx4 v228, %[rw], s52 offen lds\n" \
"v_mul_u32_u24 v239, 0xe00, v181\n" \
"v_lshl_add_u32 v239, v180, 1, v239\n" \
"s_add_u32 s41, s37, 69632\n" \
"s_add_u32 s43, s39, 256\n" \
"s_add_u32 s45, s38, 98304\n" \
"s_add_u32 s47, s40, 1048576\n" \
"s_add_u32 m0, s38, 116736\n" \
"s_add_u32 s52, s40, 294912\n" \
"buffer_load_dwordx4 v227, %[rw], s52 offen lds\n" \
"s_add_u32 s42, s37, 65536\n" \
"s_add_u32 s44, s39, 256\n" \
"s_add_u32 s46, s38, 147456\n" \
"s_add_u32 s48, s40, 786432\n" \
"s_mov_b32 s49, 0xffff0000\n" \
"s_mov_b32 s50, 0x8000\n" \
"s_add_u32 m0, s38, 117760\n" \
"s_add_u32 s52, s40, 311296\n" \
"buffer_load_dwordx4 v228, %[rw], s52 offen lds\n" \
"s_mov_b32 s53, 0\n" \
"v_lshrrev_b32 v185, 6, v224\n" \
"v_lshlrev_b32 v185, 13, v185\n" \
"v_bfe_u32 v186, v224, 4, 2\n" \
"v_lshl_add_u32 v185, v186, 7, v185\n" \
"v_lshlrev_b32 v186, 3, v224\n" \
"s_add_u32 m0, s37, 32768\n" \
"s_add_u32 s52, s39, 128\n" \
"buffer_load_dwordx4 v225, %[rc], s52 offen lds\n" \
"v_lshrrev_b32 v187, 4, v224\n" \
"v_cmp_gt_u32 s[84:85], 28, v187\n" \
"v_bfrev_b32 v187, 1\n" \
"s_lshl_b32 s80, s17, 2\n" \
"s_add_u32 s80, s80, s36\n" \
"s_mul_i32 s83, s80, 57344\n" \
"s_add_u32 m0, s37, 33792\n" \
"s_add_u32 s52, s39, 16512\n" \
"buffer_load_dwordx4 v226, %[rc], s52 offen lds\n" \
"s_mul_i32 s94, s80, 3584\n" \
"v_cndmask_b32 v185, v187, v185, s[84:85]\n" \
"v_cndmask_b32 v186, v187, v186, s[84:85]\n" \
"s_mul_i32 s81, s16, 229376\n" \
"s_add_u32 s94, s94, s81\n" \
"s_add_u32 m0, s37, 34816\n" \
"s_add_u32 s52, s39, 32896\n" \
"buffer_load_dwordx4 v225, %[rc], s52 offen lds\n" \
"s_add_u32 m0, s37, 35840\n" \
"s_add_u32 s52, s39, 49280\n" \
"buffer_load_dwordx4 v226, %[rc], s52 offen lds\n" \
"s_add_u32 m0, s37, 36864\n" \
"s_add_u32 s52, s39, 65664\n" \
"buffer_load_dwordx4 v225, %[rc], s52 offen lds\n" \
"s_add_u32 m0, s37, 37888\n" \
"s_add_u32 s52, s39, 82048\n" \
"buffer_load_dwordx4 v226, %[rc], s52 offen lds\n" \
"s_add_u32 m0, s37, 38912\n" \
"s_add_u32 s52, s39, 98432\n" \
"buffer_load_dwordx4 v225, %[rc], s52 offen lds\n" \
"s_add_u32 m0, s37, 39936\n" \
"s_add_u32 s52, s39, 114816\n" \
"buffer_load_dwordx4 v226, %[rc], s52 offen lds\n" \
"s_add_u32 m0, s38, 131072\n" \
"s_add_u32 s52, s40, 524288\n" \
"buffer_load_dwordx4 v227, %[rw], s52 offen lds\n" \
"s_add_u32 m0, s38, 132096\n" \
"s_add_u32 s52, s40, 540672\n" \
"buffer_load_dwordx4 v228, %[rw], s52 offen lds\n" \
"s_add_u32 m0, s38, 133120\n" \
"s_add_u32 s52, s40, 557056\n" \
"buffer_load_dwordx4 v227, %[rw], s52 offen lds\n" \
"s_add_u32 m0, s38, 134144\n" \
"s_add_u32 s52, s40, 573440\n" \
"buffer_load_dwordx4 v228, %[rw], s52 offen lds\n" \
"s_mov_b32 s64, s83\n" \
"s_mov_b32 s65, s94\n" \
"s_load_dwordx2 s[88:89], %[ka], 48\n" \
"s_load_dwordx2 s[32:33], %[ka], 56\n" \
"s_lshl_b32 s80, s95, 2\n" \
"s_add_u32 s80, s80, s36\n" \
"s_mul_i32 s80, s80, 1568\n" \
"v_lshlrev_b32 v190, 3, v224\n" \
"v_lshrrev_b32 v191, 4, v224\n" \
"v_cmp_gt_u32 s[84:85], 13, v191\n" \
"s_waitcnt lgkmcnt(0)\n" \
"s_nop 0\n" \
"v_cndmask_b32 v190, v187, v190, s[84:85]\n" \
"s_and_b32 s89, s89, 0xffff\n" \
"s_mov_b32 s90, 1605632\n" \
"s_mov_b32 s91, 0x20000\n" \
"s_and_b32 s33, s33, 0xffff\n" \
"s_mov_b32 s34, 1605632\n" \
"s_mov_b32 s35, 0x20000\n" \
"s_mov_b32 s66, s80\n" \
"s_waitcnt vmcnt(16)\n" \
"s_barrier\n" \
"ds_read_b128 v[0:3], v229 offset:0\n" \
"ds_read_b64_tr_b16 v[32:33], v231\n" \
"ds_read_b64_tr_b16 v[34:35], v231 offset:2048\n" \
"ds_read_b128 v[4:7], v229 offset:2048\n" \
"ds_read_b64_tr_b16 v[36:37], v232\n" \
"ds_read_b64_tr_b16 v[38:39], v232 offset:2048\n" \
"ds_read_b128 v[8:11], v229 offset:4096\n" \
"ds_read_b64_tr_b16 v[40:41], v233\n" \
"ds_read_b64_tr_b16 v[42:43], v233 offset:2048\n" \
"ds_read_b128 v[12:15], v229 offset:6144\n" \
"ds_read_b64_tr_b16 v[44:45], v234\n" \
"ds_read_b64_tr_b16 v[46:47], v234 offset:2048\n" \
"ds_read_b128 v[16:19], v229 offset:8192\n" \
"ds_read_b64_tr_b16 v[48:49], v235\n" \
"ds_read_b64_tr_b16 v[50:51], v235 offset:2048\n" \
"ds_read_b128 v[20:23], v229 offset:10240\n" \
"ds_read_b64_tr_b16 v[52:53], v236\n" \
"ds_read_b64_tr_b16 v[54:55], v236 offset:2048\n" \
"ds_read_b128 v[24:27], v229 offset:12288\n" \
"ds_read_b64_tr_b16 v[56:57], v237\n" \
"ds_read_b64_tr_b16 v[58:59], v237 offset:2048\n" \
"ds_read_b128 v[28:31], v229 offset:14336\n" \
"ds_read_b64_tr_b16 v[60:61], v238\n" \
"ds_read_b64_tr_b16 v[62:63], v238 offset:2048\n" \
"v_add_u32 v229, 0x8000, v229\n" \
"s_cmp_lg_u32 s54, 0\n" \
"s_cbranch_scc1 LV%=\n" \
"s_waitcnt vmcnt(12) lgkmcnt(0)\n" \
"s_barrier\n" \
"v_mfma_f32_16x16x32_bf16 a[0:3], v[32:35], v[0:3], 0\n" \
"ds_read_b128 v[64:67], v230\n" \
"v_lshlrev_b32 v152, 16, v144\n" \
"v_mfma_f32_16x16x32_bf16 a[4:7], v[36:39], v[0:3], 0\n" \
"s_add_u32 s52, s39, 256\n" \
"s_add_u32 m0, s37, 65536\n" \
"v_mfma_f32_16x16x32_bf16 a[8:11], v[40:43], v[0:3], 0\n" \
"ds_read_b64_tr_b16 v[96:97], v231 offset:16384\n" \
"v_lshlrev_b32 v160, 16, v148\n" \
"v_mfma_f32_16x16x32_bf16 a[12:15], v[44:47], v[0:3], 0\n" \
"buffer_load_dwordx4 v225, %[rc], s52 offen lds\n" \
"v_and_b32 v153, 0xffff0000, v144\n" \
"v_mfma_f32_16x16x32_bf16 a[16:19], v[48:51], v[0:3], 0\n" \
"ds_read_b64_tr_b16 v[98:99], v231 offset:18432\n" \
"v_and_b32 v161, 0xffff0000, v148\n" \
"v_mfma_f32_16x16x32_bf16 a[20:23], v[52:55], v[0:3], 0\n" \
"s_add_u32 s52, s40, 786432\n" \
"s_add_u32 m0, s38, 147456\n" \
"v_mfma_f32_16x16x32_bf16 a[24:27], v[56:59], v[0:3], 0\n" \
"ds_read_b64_tr_b16 v[100:101], v232 offset:16384\n" \
"v_lshlrev_b32 v154, 16, v145\n" \
"v_mfma_f32_16x16x32_bf16 a[28:31], v[60:63], v[0:3], 0\n" \
"buffer_load_dwordx4 v227, %[rw], s52 offen lds\n" \
"v_lshlrev_b32 v162, 16, v149\n" \
"v_mfma_f32_16x16x32_bf16 a[32:35], v[32:35], v[4:7], 0\n" \
"ds_read_b64_tr_b16 v[102:103], v232 offset:18432\n" \
"v_and_b32 v155, 0xffff0000, v145\n" \
"v_mfma_f32_16x16x32_bf16 a[36:39], v[36:39], v[4:7], 0\n" \
"s_add_u32 s52, s39, 16640\n" \
"s_add_u32 m0, s37, 66560\n" \
"v_mfma_f32_16x16x32_bf16 a[40:43], v[40:43], v[4:7], 0\n" \
"ds_read_b64_tr_b16 v[104:105], v233 offset:16384\n" \
"v_and_b32 v163, 0xffff0000, v149\n" \
"v_mfma_f32_16x16x32_bf16 a[44:47], v[44:47], v[4:7], 0\n" \
"buffer_load_dwordx4 v226, %[rc], s52 offen lds\n" \
"v_lshlrev_b32 v156, 16, v146\n" \
"v_mfma_f32_16x16x32_bf16 a[48:51], v[48:51], v[4:7], 0\n" \
"ds_read_b64_tr_b16 v[106:107], v233 offset:18432\n" \
"v_lshlrev_b32 v164, 16, v150\n" \
"v_mfma_f32_16x16x32_bf16 a[52:55], v[52:55], v[4:7], 0\n" \
"s_add_u32 s52, s40, 802816\n" \
"s_add_u32 m0, s38, 148480\n" \
"v_mfma_f32_16x16x32_bf16 a[56:59], v[56:59], v[4:7], 0\n" \
"ds_read_b64_tr_b16 v[108:109], v234 offset:16384\n" \
"v_and_b32 v157, 0xffff0000, v146\n" \
"v_mfma_f32_16x16x32_bf16 a[60:63], v[60:63], v[4:7], 0\n" \
"buffer_load_dwordx4 v228, %[rw], s52 offen lds\n" \
"v_and_b32 v165, 0xffff0000, v150\n" \
"v_mfma_f32_16x16x32_bf16 a[64:67], v[32:35], v[8:11], 0\n" \
"ds_read_b64_tr_b16 v[110:111], v234 offset:18432\n" \
"v_lshlrev_b32 v158, 16, v147\n" \
"v_mfma_f32_16x16x32_bf16 a[68:71], v[36:39], v[8:11], 0\n" \
"s_add_u32 s52, s39, 33024\n" \
"s_add_u32 m0, s37, 67584\n" \
"v_mfma_f32_16x16x32_bf16 a[72:75], v[40:43], v[8:11], 0\n" \
"ds_read_b64_tr_b16 v[112:113], v235 offset:16384\n" \
"v_lshlrev_b32 v166, 16, v151\n" \
"v_mfma_f32_16x16x32_bf16 a[76:79], v[44:47], v[8:11], 0\n" \
"buffer_load_dwordx4 v225, %[rc], s52 offen lds\n" \
"v_and_b32 v159, 0xffff0000, v147\n" \
"v_mfma_f32_16x16x32_bf16 a[80:83], v[48:51], v[8:11], 0\n" \
"ds_read_b64_tr_b16 v[114:115], v235 offset:18432\n" \
"v_and_b32 v167, 0xffff0000, v151\n" \
"v_mfma_f32_16x16x32_bf16 a[84:87], v[52:55], v[8:11], 0\n" \
"s_add_u32 s52, s40, 819200\n" \
"s_add_u32 m0, s38, 149504\n" \
"v_mfma_f32_16x16x32_bf16 a[88:91], v[56:59], v[8:11], 0\n" \
"ds_read_b64_tr_b16 v[116:117], v236 offset:16384\n" \
"v_add_f32 v168, v152, v153\n" \
"v_mfma_f32_16x16x32_bf16 a[92:95], v[60:63], v[8:11], 0\n" \
"buffer_load_dwordx4 v227, %[rw], s52 offen lds\n" \
"v_add_f32 v169, v160, v161\n" \
"v_mfma_f32_16x16x32_bf16 a[96:99], v[32:35], v[12:15], 0\n" \
"ds_read_b64_tr_b16 v[118:119], v236 offset:18432\n" \
"v_add_f32 v168, v168, v154\n" \
"v_mfma_f32_16x16x32_bf16 a[100:103], v[36:39], v[12:15], 0\n" \
"s_add_u32 s52, s39, 49408\n" \
"s_add_u32 m0, s37, 68608\n" \
"v_mfma_f32_16x16x32_bf16 a[104:107], v[40:43], v[12:15], 0\n" \
"ds_read_b64_tr_b16 v[120:121], v237 offset:16384\n" \
"v_add_f32 v169, v169, v162\n" \
"v_mfma_f32_16x16x32_bf16 a[108:111], v[44:47], v[12:15], 0\n" \
"buffer_load_dwordx4 v226, %[rc], s52 offen lds\n" \
"v_add_f32 v168, v168, v155\n" \
"v_mfma_f32_16x16x32_bf16 a[112:115], v[48:51], v[12:15], 0\n" \
"ds_read_b64_tr_b16 v[122:123], v237 offset:18432\n" \
"v_add_f32 v169, v169, v163\n" \
"v_mfma_f32_16x16x32_bf16 a[116:119], v[52:55], v[12:15], 0\n" \
"s_add_u32 s52, s40, 835584\n" \
"s_add_u32 m0, s38, 150528\n" \
"v_mfma_f32_16x16x32_bf16 a[120:123], v[56:59], v[12:15], 0\n" \
"ds_read_b64_tr_b16 v[124:125], v238 offset:16384\n" \
"v_add_f32 v168, v168, v156\n" \
"v_mfma_f32_16x16x32_bf16 a[124:127], v[60:63], v[12:15], 0\n" \
"buffer_load_dwordx4 v228, %[rw], s52 offen lds\n" \
"v_add_f32 v169, v169, v164\n" \
"v_mfma_f32_16x16x32_bf16 a[128:131], v[32:35], v[16:19], 0\n" \
"ds_read_b64_tr_b16 v[126:127], v238 offset:18432\n" \
"v_add_f32 v168, v168, v157\n" \
"v_mfma_f32_16x16x32_bf16 a[132:135], v[36:39], v[16:19], 0\n" \
"s_add_u32 s52, s43, 65536\n" \
"s_add_u32 m0, s41, 0\n" \
"v_mfma_f32_16x16x32_bf16 a[136:139], v[40:43], v[16:19], 0\n" \
"ds_read_b128 v[68:71], v230 offset:2048\n" \
"v_add_f32 v169, v169, v165\n" \
"v_mfma_f32_16x16x32_bf16 a[140:143], v[44:47], v[16:19], 0\n" \
"buffer_load_dwordx4 v225, %[rc], s52 offen lds\n" \
"v_add_f32 v168, v168, v158\n" \
"v_mfma_f32_16x16x32_bf16 a[144:147], v[48:51], v[16:19], 0\n" \
"ds_read_b128 v[72:75], v230 offset:4096\n" \
"v_add_f32 v169, v169, v166\n" \
"v_mfma_f32_16x16x32_bf16 a[148:151], v[52:55], v[16:19], 0\n" \
"s_add_u32 s52, s47, 0\n" \
"s_add_u32 m0, s45, 0\n" \
"v_mfma_f32_16x16x32_bf16 a[152:155], v[56:59], v[16:19], 0\n" \
"ds_read_b128 v[76:79], v230 offset:6144\n" \
"v_add_f32 v168, v168, v159\n" \
"v_mfma_f32_16x16x32_bf16 a[156:159], v[60:63], v[16:19], 0\n" \
"buffer_load_dwordx4 v227, %[rw], s52 offen lds\n" \
"v_add_f32 v169, v169, v167\n" \
"v_mfma_f32_16x16x32_bf16 a[160:163], v[32:35], v[20:23], 0\n" \
"ds_read_b128 v[80:83], v230 offset:8192\n" \
"v_add_f32_dpp v168, v168, v168 quad_perm:[1,0,3,2] row_mask:0xf bank_mask:0xf\n" \
"v_mfma_f32_16x16x32_bf16 a[164:167], v[36:39], v[20:23], 0\n" \
"s_add_u32 s52, s43, 81920\n" \
"s_add_u32 m0, s41, 1024\n" \
"v_mfma_f32_16x16x32_bf16 a[168:171], v[40:43], v[20:23], 0\n" \
"ds_read_b128 v[84:87], v230 offset:10240\n" \
"v_add_f32_dpp v169, v169, v169 quad_perm:[1,0,3,2] row_mask:0xf bank_mask:0xf\n" \
"v_mfma_f32_16x16x32_bf16 a[172:175], v[44:47], v[20:23], 0\n" \
"buffer_load_dwordx4 v226, %[rc], s52 offen lds\n" \
"v_add_f32_dpp v168, v168, v168 quad_perm:[2,3,0,1] row_mask:0xf bank_mask:0xf\n" \
"v_mfma_f32_16x16x32_bf16 a[176:179], v[48:51], v[20:23], 0\n" \
"ds_read_b128 v[88:91], v230 offset:12288\n" \
"v_add_f32_dpp v169, v169, v169 quad_perm:[2,3,0,1] row_mask:0xf bank_mask:0xf\n" \
"v_mfma_f32_16x16x32_bf16 a[180:183], v[52:55], v[20:23], 0\n" \
"s_add_u32 s52, s47, 16384\n" \
"s_add_u32 m0, s45, 1024\n" \
"v_mfma_f32_16x16x32_bf16 a[184:187], v[56:59], v[20:23], 0\n" \
"ds_read_b128 v[92:95], v230 offset:14336\n" \
"v_add_f32_dpp v168, v168, v168 row_half_mirror row_mask:0xf bank_mask:0xf\n" \
"v_mfma_f32_16x16x32_bf16 a[188:191], v[60:63], v[20:23], 0\n" \
"buffer_load_dwordx4 v228, %[rw], s52 offen lds\n" \
"v_add_f32_dpp v169, v169, v169 row_half_mirror row_mask:0xf bank_mask:0xf\n" \
"v_mfma_f32_16x16x32_bf16 a[192:195], v[32:35], v[24:27], 0\n" \
"s_add_u32 s42, s42, s49\n" \
"v_add_f32_dpp v168, v168, v168 row_mirror row_mask:0xf bank_mask:0xf\n" \
"v_mfma_f32_16x16x32_bf16 a[196:199], v[36:39], v[24:27], 0\n" \
"s_add_u32 s52, s43, 98304\n" \
"s_add_u32 m0, s41, 2048\n" \
"v_mfma_f32_16x16x32_bf16 a[200:203], v[40:43], v[24:27], 0\n" \
"s_add_u32 s44, s44, 128\n" \
"v_add_u32 v230, s50, v230\n" \
"v_mfma_f32_16x16x32_bf16 a[204:207], v[44:47], v[24:27], 0\n" \
"buffer_load_dwordx4 v225, %[rc], s52 offen lds\n" \
"v_add_f32_dpp v169, v169, v169 row_mirror row_mask:0xf bank_mask:0xf\n" \
"v_mfma_f32_16x16x32_bf16 a[208:211], v[48:51], v[24:27], 0\n" \
"s_xor_b32 s46, s46, 0x38000\n" \
"v_mov_b32 v170, v168\n" \
"v_mfma_f32_16x16x32_bf16 a[212:215], v[52:55], v[24:27], 0\n" \
"s_add_u32 s52, s47, 32768\n" \
"s_add_u32 m0, s45, 2048\n" \
"v_mfma_f32_16x16x32_bf16 a[216:219], v[56:59], v[24:27], 0\n" \
"s_add_u32 s48, s48, 524288\n" \
"v_mov_b32 v171, v169\n" \
"v_mfma_f32_16x16x32_bf16 a[220:223], v[60:63], v[24:27], 0\n" \
"buffer_load_dwordx4 v227, %[rw], s52 offen lds\n" \
"v_permlane16_swap_b32 v170, v168\n" \
"v_mfma_f32_16x16x32_bf16 a[224:227], v[32:35], v[28:31], 0\n" \
"s_add_u32 s51, s49, s50\n" \
"v_permlane16_swap_b32 v171, v169\n" \
"v_mfma_f32_16x16x32_bf16 a[228:231], v[36:39], v[28:31], 0\n" \
"s_add_u32 s52, s43, 114688\n" \
"s_add_u32 m0, s41, 3072\n" \
"v_mfma_f32_16x16x32_bf16 a[232:235], v[40:43], v[28:31], 0\n" \
"s_sub_u32 s51, 0, s51\n" \
"v_add_f32 v168, v168, v170\n" \
"v_mfma_f32_16x16x32_bf16 a[236:239], v[44:47], v[28:31], 0\n" \
"buffer_load_dwordx4 v226, %[rc], s52 offen lds\n" \
"v_add_f32 v169, v169, v171\n" \
"v_mfma_f32_16x16x32_bf16 a[240:243], v[48:51], v[28:31], 0\n" \
"v_mov_b32 v170, v168\n" \
"v_mov_b32 v171, v169\n" \
"v_mfma_f32_16x16x32_bf16 a[244:247], v[52:55], v[28:31], 0\n" \
"s_add_u32 s52, s47, 49152\n" \
"s_add_u32 m0, s45, 3072\n" \
"v_mfma_f32_16x16x32_bf16 a[248:251], v[56:59], v[28:31], 0\n" \
"v_permlane32_swap_b32 v170, v168\n" \
"v_permlane32_swap_b32 v171, v169\n" \
"v_mfma_f32_16x16x32_bf16 a[252:255], v[60:63], v[28:31], 0\n" \
"buffer_load_dwordx4 v228, %[rw], s52 offen lds\n" \
"v_add_f32 v168, v168, v170\n" \
"s_waitcnt vmcnt(16) lgkmcnt(0)\n" \
"s_barrier\n" \
"v_mfma_f32_16x16x32_bf16 a[0:3], v[96:99], v[64:67], a[0:3]\n" \
"ds_read_b128 v[0:3], v229\n" \
"v_xor_b32 v231, 0x38000, v231\n" \
"v_mfma_f32_16x16x32_bf16 a[4:7], v[100:103], v[64:67], a[4:7]\n" \
"s_add_u32 s52, s44, 0\n" \
"v_add_f32 v169, v169, v171\n" \
"v_mfma_f32_16x16x32_bf16 a[8:11], v[104:107], v[64:67], a[8:11]\n" \
"ds_read_b64_tr_b16 v[32:33], v231 offset:0\n" \
"v_mul_f32 v168, 0x3b124925, v168\n" \
"v_mfma_f32_16x16x32_bf16 a[12:15], v[108:111], v[64:67], a[12:15]\n" \
"s_add_u32 m0, s42, 0\n" \
"v_mul_f32 v169, 0x3b124925, v169\n" \
"v_mfma_f32_16x16x32_bf16 a[16:19], v[112:115], v[64:67], a[16:19]\n" \
"ds_read_b64_tr_b16 v[34:35], v231 offset:2048\n" \
"v_xor_b32 v232, 0x38000, v232\n" \
"v_mfma_f32_16x16x32_bf16 a[20:23], v[116:119], v[64:67], a[20:23]\n" \
"buffer_load_dwordx4 v225, %[rc], s52 offen lds\n" \
"v_sub_f32 v152, v152, v168\n" \
"v_mfma_f32_16x16x32_bf16 a[24:27], v[120:123], v[64:67], a[24:27]\n" \
"ds_read_b64_tr_b16 v[36:37], v232 offset:0\n" \
"v_sub_f32 v160, v160, v169\n" \
"v_mfma_f32_16x16x32_bf16 a[28:31], v[124:127], v[64:67], a[28:31]\n" \
"s_add_u32 s41, s42, 4096\n" \
"v_sub_f32 v153, v153, v168\n" \
"v_mfma_f32_16x16x32_bf16 a[32:35], v[96:99], v[68:71], a[32:35]\n" \
"ds_read_b64_tr_b16 v[38:39], v232 offset:2048\n" \
"v_xor_b32 v233, 0x38000, v233\n" \
"v_mfma_f32_16x16x32_bf16 a[36:39], v[100:103], v[68:71], a[36:39]\n" \
"s_add_u32 s52, s48, 0\n" \
"buffer_load_dword v188, v185, s[20:23], s64 offen\n" \
"v_mfma_f32_16x16x32_bf16 a[40:43], v[104:107], v[68:71], a[40:43]\n" \
"ds_read_b64_tr_b16 v[40:41], v233 offset:0\n" \
"v_sub_f32 v161, v161, v169\n" \
"v_mfma_f32_16x16x32_bf16 a[44:47], v[108:111], v[68:71], a[44:47]\n" \
"s_add_u32 m0, s46, 0\n" \
"v_sub_f32 v154, v154, v168\n" \
"v_mfma_f32_16x16x32_bf16 a[48:51], v[112:115], v[68:71], a[48:51]\n" \
"ds_read_b64_tr_b16 v[42:43], v233 offset:2048\n" \
"v_xor_b32 v234, 0x38000, v234\n" \
"v_mfma_f32_16x16x32_bf16 a[52:55], v[116:119], v[68:71], a[52:55]\n" \
"buffer_load_dwordx4 v227, %[rw], s52 offen lds\n" \
"v_sub_f32 v162, v162, v169\n" \
"v_mfma_f32_16x16x32_bf16 a[56:59], v[120:123], v[68:71], a[56:59]\n" \
"ds_read_b64_tr_b16 v[44:45], v234 offset:0\n" \
"v_sub_f32 v155, v155, v168\n" \
"v_mfma_f32_16x16x32_bf16 a[60:63], v[124:127], v[68:71], a[60:63]\n" \
"s_add_u32 s43, s43, 128\n" \
"v_sub_f32 v163, v163, v169\n" \
"v_mfma_f32_16x16x32_bf16 a[64:67], v[96:99], v[72:75], a[64:67]\n" \
"ds_read_b64_tr_b16 v[46:47], v234 offset:2048\n" \
"v_xor_b32 v235, 0x38000, v235\n" \
"v_mfma_f32_16x16x32_bf16 a[68:71], v[100:103], v[72:75], a[68:71]\n" \
"s_add_u32 s52, s44, 16384\n" \
"v_sub_f32 v156, v156, v168\n" \
"v_mfma_f32_16x16x32_bf16 a[72:75], v[104:107], v[72:75], a[72:75]\n" \
"ds_read_b64_tr_b16 v[48:49], v235 offset:0\n" \
"v_sub_f32 v164, v164, v169\n" \
"v_mfma_f32_16x16x32_bf16 a[76:79], v[108:111], v[72:75], a[76:79]\n" \
"s_add_u32 m0, s42, 1024\n" \
"v_sub_f32 v157, v157, v168\n" \
"v_mfma_f32_16x16x32_bf16 a[80:83], v[112:115], v[72:75], a[80:83]\n" \
"ds_read_b64_tr_b16 v[50:51], v235 offset:2048\n" \
"v_xor_b32 v236, 0x38000, v236\n" \
"v_mfma_f32_16x16x32_bf16 a[84:87], v[116:119], v[72:75], a[84:87]\n" \
"buffer_load_dwordx4 v226, %[rc], s52 offen lds\n" \
"v_sub_f32 v165, v165, v169\n" \
"v_mfma_f32_16x16x32_bf16 a[88:91], v[120:123], v[72:75], a[88:91]\n" \
"ds_read_b64_tr_b16 v[52:53], v236 offset:0\n" \
"v_sub_f32 v158, v158, v168\n" \
"v_mfma_f32_16x16x32_bf16 a[92:95], v[124:127], v[72:75], a[92:95]\n" \
"s_xor_b32 s45, s45, 0x38000\n" \
"v_sub_f32 v166, v166, v169\n" \
"v_mfma_f32_16x16x32_bf16 a[96:99], v[96:99], v[76:79], a[96:99]\n" \
"ds_read_b64_tr_b16 v[54:55], v236 offset:2048\n" \
"v_xor_b32 v237, 0x38000, v237\n" \
"v_mfma_f32_16x16x32_bf16 a[100:103], v[100:103], v[76:79], a[100:103]\n" \
"s_add_u32 s52, s48, 16384\n" \
"buffer_load_dword v189, v186, s[28:31], s65 offen\n" \
"v_mfma_f32_16x16x32_bf16 a[104:107], v[104:107], v[76:79], a[104:107]\n" \
"ds_read_b64_tr_b16 v[56:57], v237 offset:0\n" \
"v_sub_f32 v159, v159, v168\n" \
"v_mfma_f32_16x16x32_bf16 a[108:111], v[108:111], v[76:79], a[108:111]\n" \
"s_add_u32 m0, s46, 1024\n" \
"v_sub_f32 v167, v167, v169\n" \
"v_mfma_f32_16x16x32_bf16 a[112:115], v[112:115], v[76:79], a[112:115]\n" \
"ds_read_b64_tr_b16 v[58:59], v237 offset:2048\n" \
"v_xor_b32 v238, 0x38000, v238\n" \
"v_mfma_f32_16x16x32_bf16 a[116:119], v[116:119], v[76:79], a[116:119]\n" \
"buffer_load_dwordx4 v228, %[rw], s52 offen lds\n" \
"v_mul_f32 v168, v152, v152\n" \
"v_mfma_f32_16x16x32_bf16 a[120:123], v[120:123], v[76:79], a[120:123]\n" \
"ds_read_b64_tr_b16 v[60:61], v238 offset:0\n" \
"v_mul_f32 v169, v160, v160\n" \
"v_mfma_f32_16x16x32_bf16 a[124:127], v[124:127], v[76:79], a[124:127]\n" \
"s_add_u32 s47, s47, 524288\n" \
"v_fmac_f32 v168, v153, v153\n" \
"v_mfma_f32_16x16x32_bf16 a[128:131], v[96:99], v[80:83], a[128:131]\n" \
"ds_read_b64_tr_b16 v[62:63], v238 offset:2048\n" \
"v_fmac_f32 v169, v161, v161\n" \
"v_mfma_f32_16x16x32_bf16 a[132:135], v[100:103], v[80:83], a[132:135]\n" \
"s_add_u32 s52, s44, 32768\n" \
"v_fmac_f32 v168, v154, v154\n" \
"v_mfma_f32_16x16x32_bf16 a[136:139], v[104:107], v[80:83], a[136:139]\n" \
"ds_read_b128 v[4:7], v229 offset:2048\n" \
"v_fmac_f32 v169, v162, v162\n" \
"v_mfma_f32_16x16x32_bf16 a[140:143], v[108:111], v[80:83], a[140:143]\n" \
"s_add_u32 m0, s42, 2048\n" \
"v_fmac_f32 v168, v155, v155\n" \
"v_mfma_f32_16x16x32_bf16 a[144:147], v[112:115], v[80:83], a[144:147]\n" \
"ds_read_b128 v[8:11], v229 offset:4096\n" \
"v_fmac_f32 v169, v163, v163\n" \
"v_mfma_f32_16x16x32_bf16 a[148:151], v[116:119], v[80:83], a[148:151]\n" \
"buffer_load_dwordx4 v225, %[rc], s52 offen lds\n" \
"v_fmac_f32 v168, v156, v156\n" \
"v_mfma_f32_16x16x32_bf16 a[152:155], v[120:123], v[80:83], a[152:155]\n" \
"ds_read_b128 v[12:15], v229 offset:6144\n" \
"v_fmac_f32 v169, v164, v164\n" \
"v_mfma_f32_16x16x32_bf16 a[156:159], v[124:127], v[80:83], a[156:159]\n" \
"s_mov_b32 s49, s50\n" \
"v_fmac_f32 v168, v157, v157\n" \
"v_mfma_f32_16x16x32_bf16 a[160:163], v[96:99], v[84:87], a[160:163]\n" \
"ds_read_b128 v[16:19], v229 offset:8192\n" \
"v_fmac_f32 v169, v165, v165\n" \
"v_mfma_f32_16x16x32_bf16 a[164:167], v[100:103], v[84:87], a[164:167]\n" \
"s_add_u32 s52, s48, 32768\n" \
"buffer_load_dword v191, v190, s[88:91], s66 offen\n" \
"v_mfma_f32_16x16x32_bf16 a[168:171], v[104:107], v[84:87], a[168:171]\n" \
"ds_read_b128 v[20:23], v229 offset:10240\n" \
"v_fmac_f32 v168, v158, v158\n" \
"v_mfma_f32_16x16x32_bf16 a[172:175], v[108:111], v[84:87], a[172:175]\n" \
"s_add_u32 m0, s46, 2048\n" \
"v_fmac_f32 v169, v166, v166\n" \
"v_mfma_f32_16x16x32_bf16 a[176:179], v[112:115], v[84:87], a[176:179]\n" \
"ds_read_b128 v[24:27], v229 offset:12288\n" \
"v_fmac_f32 v168, v159, v159\n" \
"v_mfma_f32_16x16x32_bf16 a[180:183], v[116:119], v[84:87], a[180:183]\n" \
"buffer_load_dwordx4 v227, %[rw], s52 offen lds\n" \
"v_fmac_f32 v169, v167, v167\n" \
"v_mfma_f32_16x16x32_bf16 a[184:187], v[120:123], v[84:87], a[184:187]\n" \
"ds_read_b128 v[28:31], v229 offset:14336\n" \
"v_cndmask_b32 v168, 0, v168, vcc\n" \
"v_mfma_f32_16x16x32_bf16 a[188:191], v[124:127], v[84:87], a[188:191]\n" \
"s_mov_b32 s50, s51\n" \
"v_cndmask_b32 v169, 0, v169, vcc\n" \
"v_mfma_f32_16x16x32_bf16 a[192:195], v[96:99], v[88:91], a[192:195]\n" \
"s_add_u32 s53, s53, 1\n" \
"v_add_f32_dpp v168, v168, v168 quad_perm:[1,0,3,2] row_mask:0xf bank_mask:0xf\n" \
"v_mfma_f32_16x16x32_bf16 a[196:199], v[100:103], v[88:91], a[196:199]\n" \
"s_add_u32 s52, s44, 49152\n" \
"v_add_f32_dpp v169, v169, v169 quad_perm:[1,0,3,2] row_mask:0xf bank_mask:0xf\n" \
"v_mfma_f32_16x16x32_bf16 a[200:203], v[104:107], v[88:91], a[200:203]\n" \
"v_add_u32 v229, s51, v229\n" \
"v_add_f32_dpp v168, v168, v168 quad_perm:[2,3,0,1] row_mask:0xf bank_mask:0xf\n" \
"v_mfma_f32_16x16x32_bf16 a[204:207], v[108:111], v[88:91], a[204:207]\n" \
"s_add_u32 m0, s42, 3072\n" \
"v_add_f32_dpp v169, v169, v169 quad_perm:[2,3,0,1] row_mask:0xf bank_mask:0xf\n" \
"v_mfma_f32_16x16x32_bf16 a[208:211], v[112:115], v[88:91], a[208:211]\n" \
"v_add_f32_dpp v168, v168, v168 row_half_mirror row_mask:0xf bank_mask:0xf\n" \
"v_add_f32_dpp v169, v169, v169 row_half_mirror row_mask:0xf bank_mask:0xf\n" \
"v_mfma_f32_16x16x32_bf16 a[212:215], v[116:119], v[88:91], a[212:215]\n" \
"buffer_load_dwordx4 v226, %[rc], s52 offen lds\n" \
"v_add_f32_dpp v168, v168, v168 row_mirror row_mask:0xf bank_mask:0xf\n" \
"v_mfma_f32_16x16x32_bf16 a[216:219], v[120:123], v[88:91], a[216:219]\n" \
"v_add_f32_dpp v169, v169, v169 row_mirror row_mask:0xf bank_mask:0xf\n" \
"v_mov_b32 v170, v168\n" \
"v_mfma_f32_16x16x32_bf16 a[220:223], v[124:127], v[88:91], a[220:223]\n" \
"v_mov_b32 v171, v169\n" \
"v_permlane16_swap_b32 v170, v168\n" \
"v_mfma_f32_16x16x32_bf16 a[224:227], v[96:99], v[92:95], a[224:227]\n" \
"v_permlane16_swap_b32 v171, v169\n" \
"v_add_f32 v168, v168, v170\n" \
"v_mfma_f32_16x16x32_bf16 a[228:231], v[100:103], v[92:95], a[228:231]\n" \
"s_add_u32 s52, s48, 49152\n" \
"buffer_load_dword v192, v190, s[32:35], s66 offen\n" \
"v_mfma_f32_16x16x32_bf16 a[232:235], v[104:107], v[92:95], a[232:235]\n" \
"v_add_f32 v169, v169, v171\n" \
"v_mov_b32 v170, v168\n" \
"v_mfma_f32_16x16x32_bf16 a[236:239], v[108:111], v[92:95], a[236:239]\n" \
"s_add_u32 m0, s46, 3072\n" \
"v_mov_b32 v171, v169\n" \
"v_mfma_f32_16x16x32_bf16 a[240:243], v[112:115], v[92:95], a[240:243]\n" \
"v_permlane32_swap_b32 v170, v168\n" \
"v_permlane32_swap_b32 v171, v169\n" \
"v_mfma_f32_16x16x32_bf16 a[244:247], v[116:119], v[92:95], a[244:247]\n" \
"buffer_load_dwordx4 v228, %[rw], s52 offen lds\n" \
"v_add_f32 v168, v168, v170\n" \
"v_mfma_f32_16x16x32_bf16 a[248:251], v[120:123], v[92:95], a[248:251]\n" \
"v_add_f32 v169, v169, v171\n" \
"v_mov_b32 v172, 0x3727c5ac\n" \
"v_mfma_f32_16x16x32_bf16 a[252:255], v[124:127], v[92:95], a[252:255]\n" \
"v_mov_b32 v173, 0x3727c5ac\n" \
"v_fmac_f32 v172, 0x3b124925, v168\n" \
"v_fmac_f32 v173, 0x3b124925, v169\n" \
"v_rsq_f32 v172, v172\n" \
"v_rsq_f32 v173, v173\n" \
"v_mul_f32 v172, 0x3db8aa3b, v172\n" \
"v_mul_f32 v173, 0x3db8aa3b, v173\n" \
"v_mul_f32 v152, v152, v172\n" \
"v_mul_f32 v160, v160, v173\n" \
"v_mul_f32 v153, v153, v172\n" \
"v_mul_f32 v161, v161, v173\n" \
"v_mul_f32 v154, v154, v172\n" \
"v_mul_f32 v162, v162, v173\n" \
"v_mul_f32 v155, v155, v172\n" \
"v_mul_f32 v163, v163, v173\n" \
"v_mul_f32 v156, v156, v172\n" \
"v_mul_f32 v164, v164, v173\n" \
"v_mul_f32 v157, v157, v172\n" \
"v_mul_f32 v165, v165, v173\n" \
"v_mul_f32 v158, v158, v172\n" \
"v_mul_f32 v166, v166, v173\n" \
"v_mul_f32 v159, v159, v172\n" \
"v_mul_f32 v167, v167, v173\n" \
"v_cvt_pk_bf16_f32 v144, v152, v153\n" \
"v_cvt_pk_bf16_f32 v148, v160, v161\n" \
"v_cvt_pk_bf16_f32 v145, v154, v155\n" \
"v_cvt_pk_bf16_f32 v149, v162, v163\n" \
"v_cvt_pk_bf16_f32 v146, v156, v157\n" \
"v_cvt_pk_bf16_f32 v150, v164, v165\n" \
"v_cvt_pk_bf16_f32 v147, v158, v159\n" \
"v_cvt_pk_bf16_f32 v151, v166, v167\n" \
"buffer_store_dwordx4 v[144:147], v175, %[rs], s68 offen sc1\n" \
"s_nop 0\n" \
"buffer_store_dwordx4 v[148:151], v175, %[rs], s68 offen offset:16 sc1\n" \
"s_lshr_b32 s80, s17, 1\n" \
"s_lshl_b32 s81, s16, 3\n" \
"s_add_u32 s81, s81, s80\n" \
"s_mul_i32 s83, s81, 28672\n" \
"s_lshl_b32 s80, s80, 12\n" \
"s_mul_i32 s94, s16, 229376\n" \
"s_add_u32 s94, s94, s80\n" \
"s_add_u32 s94, s94, 54525952\n" \
"s_lshr_b32 s80, s17, 1\n" \
"s_lshl_b32 s80, s80, 17\n" \
"s_lshr_b32 s81, s36, 1\n" \
"s_lshl_b32 s81, s81, 16\n" \
"s_add_u32 s80, s80, s81\n" \
"s_lshl_b32 s81, s16, 20\n" \
"s_add_u32 s80, s80, s81\n" \
"s_and_b32 s81, s36, 1\n" \
"s_lshl_b32 s81, s81, 12\n" \
"s_lshl_b32 s81, s81, 1\n" \
"s_add_u32 s80, s80, s81\n" \
"s_lshl_b32 s81, s54, 24\n" \
"s_add_u32 s80, s80, s81\n" \
"s_add_u32 s55, s80, 1048576\n" \
"s_lshr_b32 s80, s95, 4\n" \
"s_lshl_b32 s56, s80, 8\n" \
"s_and_b32 s80, s95, 15\n" \
"s_lshl_b32 s80, s80, 2\n" \
"s_add_u32 s56, s56, s80\n" \
"s_lshl_b32 s57, s18, 8\n" \
"v_bfe_u32 v240, v224, 4, 4\n" \
"v_min_u32 v241, 13, v240\n" \
"v_lshlrev_b32 v240, 2, v240\n" \
"v_lshlrev_b32 v241, 2, v241\n" \
"s_lshl_b32 s80, s18, 3\n" \
"s_add_u32 s81, s80, s36\n" \
"s_mul_i32 s58, s81, 14336\n" \
"s_add_u32 s58, s58, 34603008\n" \
"s_sub_u32 s81, s80, s36\n" \
"s_mul_i32 s71, s81, 14336\n" \
"s_add_u32 s71, s71, 34703360\n" \
"s_lshl_b32 s80, s36, 2\n" \
"s_add_u32 s80, s80, 0\n" \
"s_min_u32 s80, s80, 13\n" \
"s_mul_i32 s81, s54, 14\n" \
"s_add_u32 s80, s80, s81\n" \
"s_lshl_b32 s72, s80, 5\n" \
"s_add_u32 s72, s72, s83\n" \
"s_lshr_b32 s81, s80, 2\n" \
"s_lshl_b32 s81, s81, 15\n" \
"s_and_b32 s80, s80, 3\n" \
"s_lshl_b32 s80, s80, 10\n" \
"s_add_u32 s80, s80, s81\n" \
"s_add_u32 s76, s80, s94\n" \
"s_lshl_b32 s80, s36, 2\n" \
"s_add_u32 s80, s80, 1\n" \
"s_min_u32 s80, s80, 13\n" \
"s_mul_i32 s81, s54, 14\n" \
"s_add_u32 s80, s80, s81\n" \
"s_lshl_b32 s73, s80, 5\n" \
"s_add_u32 s73, s73, s83\n" \
"s_lshr_b32 s81, s80, 2\n" \
"s_lshl_b32 s81, s81, 15\n" \
"s_and_b32 s80, s80, 3\n" \
"s_lshl_b32 s80, s80, 10\n" \
"s_add_u32 s80, s80, s81\n" \
"s_add_u32 s77, s80, s94\n" \
"s_lshl_b32 s80, s36, 2\n" \
"s_add_u32 s80, s80, 2\n" \
"s_min_u32 s80, s80, 13\n" \
"s_mul_i32 s81, s54, 14\n" \
"s_add_u32 s80, s80, s81\n" \
"s_lshl_b32 s74, s80, 5\n" \
"s_add_u32 s74, s74, s83\n" \
"s_lshr_b32 s81, s80, 2\n" \
"s_lshl_b32 s81, s81, 15\n" \
"s_and_b32 s80, s80, 3\n" \
"s_lshl_b32 s80, s80, 10\n" \
"s_add_u32 s80, s80, s81\n" \
"s_add_u32 s78, s80, s94\n" \
"s_lshl_b32 s80, s36, 2\n" \
"s_add_u32 s80, s80, 3\n" \
"s_min_u32 s80, s80, 13\n" \
"s_mul_i32 s81, s54, 14\n" \
"s_add_u32 s80, s80, s81\n" \
"s_lshl_b32 s75, s80, 5\n" \
"s_add_u32 s75, s75, s83\n" \
"s_lshr_b32 s81, s80, 2\n" \
"s_lshl_b32 s81, s81, 15\n" \
"s_and_b32 s80, s80, 3\n" \
"s_lshl_b32 s80, s80, 10\n" \
"s_add_u32 s80, s80, s81\n" \
"s_add_u32 s79, s80, s94\n" \
"s_and_b32 s82, s36, 1\n" \
"s_lshl_b32 s82, s82, 8\n" \
"s_sub_u32 s82, 0xfffe8000, s82\n" \
".p2align 6\n" \
"LkvK%=:\n" \
"s_waitcnt vmcnt(16) lgkmcnt(0)\n" \
"s_barrier\n" \
"v_mfma_f32_16x16x32_bf16 a[0:3], v[32:35], v[0:3], a[0:3]\n" \
"ds_read_b128 v[64:67], v230\n" \
"v_mfma_f32_16x16x32_bf16 a[4:7], v[36:39], v[0:3], a[4:7]\n" \
"s_add_u32 s52, s43, 65536\n" \
"v_mfma_f32_16x16x32_bf16 a[8:11], v[40:43], v[0:3], a[8:11]\n" \
"ds_read_b64_tr_b16 v[96:97], v231 offset:16384\n" \
"v_mfma_f32_16x16x32_bf16 a[12:15], v[44:47], v[0:3], a[12:15]\n" \
"s_add_u32 m0, s41, 0\n" \
"v_mfma_f32_16x16x32_bf16 a[16:19], v[48:51], v[0:3], a[16:19]\n" \
"ds_read_b64_tr_b16 v[98:99], v231 offset:18432\n" \
"v_mfma_f32_16x16x32_bf16 a[20:23], v[52:55], v[0:3], a[20:23]\n" \
"buffer_load_dwordx4 v225, %[rc], s52 offen lds\n" \
"v_mfma_f32_16x16x32_bf16 a[24:27], v[56:59], v[0:3], a[24:27]\n" \
"ds_read_b64_tr_b16 v[100:101], v232 offset:16384\n" \
"v_mfma_f32_16x16x32_bf16 a[28:31], v[60:63], v[0:3], a[28:31]\n" \
"s_add_u32 s42, s42, s49\n" \
"v_mfma_f32_16x16x32_bf16 a[32:35], v[32:35], v[4:7], a[32:35]\n" \
"ds_read_b64_tr_b16 v[102:103], v232 offset:18432\n" \
"v_mfma_f32_16x16x32_bf16 a[36:39], v[36:39], v[4:7], a[36:39]\n" \
"s_add_u32 s52, s47, 0\n" \
"v_mfma_f32_16x16x32_bf16 a[40:43], v[40:43], v[4:7], a[40:43]\n" \
"ds_read_b64_tr_b16 v[104:105], v233 offset:16384\n" \
"v_mfma_f32_16x16x32_bf16 a[44:47], v[44:47], v[4:7], a[44:47]\n" \
"s_add_u32 m0, s45, 0\n" \
"v_mfma_f32_16x16x32_bf16 a[48:51], v[48:51], v[4:7], a[48:51]\n" \
"ds_read_b64_tr_b16 v[106:107], v233 offset:18432\n" \
"v_mfma_f32_16x16x32_bf16 a[52:55], v[52:55], v[4:7], a[52:55]\n" \
"buffer_load_dwordx4 v227, %[rw], s52 offen lds\n" \
"v_mfma_f32_16x16x32_bf16 a[56:59], v[56:59], v[4:7], a[56:59]\n" \
"ds_read_b64_tr_b16 v[108:109], v234 offset:16384\n" \
"v_mfma_f32_16x16x32_bf16 a[60:63], v[60:63], v[4:7], a[60:63]\n" \
"s_add_u32 s44, s44, 128\n" \
"v_mfma_f32_16x16x32_bf16 a[64:67], v[32:35], v[8:11], a[64:67]\n" \
"ds_read_b64_tr_b16 v[110:111], v234 offset:18432\n" \
"v_mfma_f32_16x16x32_bf16 a[68:71], v[36:39], v[8:11], a[68:71]\n" \
"s_add_u32 s52, s43, 81920\n" \
"v_mfma_f32_16x16x32_bf16 a[72:75], v[40:43], v[8:11], a[72:75]\n" \
"ds_read_b64_tr_b16 v[112:113], v235 offset:16384\n" \
"v_mfma_f32_16x16x32_bf16 a[76:79], v[44:47], v[8:11], a[76:79]\n" \
"s_add_u32 m0, s41, 1024\n" \
"v_mfma_f32_16x16x32_bf16 a[80:83], v[48:51], v[8:11], a[80:83]\n" \
"ds_read_b64_tr_b16 v[114:115], v235 offset:18432\n" \
"v_mfma_f32_16x16x32_bf16 a[84:87], v[52:55], v[8:11], a[84:87]\n" \
"buffer_load_dwordx4 v226, %[rc], s52 offen lds\n" \
"v_mfma_f32_16x16x32_bf16 a[88:91], v[56:59], v[8:11], a[88:91]\n" \
"ds_read_b64_tr_b16 v[116:117], v236 offset:16384\n" \
"v_mfma_f32_16x16x32_bf16 a[92:95], v[60:63], v[8:11], a[92:95]\n" \
"s_xor_b32 s46, s46, 0x38000\n" \
"v_mfma_f32_16x16x32_bf16 a[96:99], v[32:35], v[12:15], a[96:99]\n" \
"ds_read_b64_tr_b16 v[118:119], v236 offset:18432\n" \
"v_mfma_f32_16x16x32_bf16 a[100:103], v[36:39], v[12:15], a[100:103]\n" \
"s_add_u32 s52, s47, 16384\n" \
"v_mfma_f32_16x16x32_bf16 a[104:107], v[40:43], v[12:15], a[104:107]\n" \
"ds_read_b64_tr_b16 v[120:121], v237 offset:16384\n" \
"v_mfma_f32_16x16x32_bf16 a[108:111], v[44:47], v[12:15], a[108:111]\n" \
"s_add_u32 m0, s45, 1024\n" \
"v_mfma_f32_16x16x32_bf16 a[112:115], v[48:51], v[12:15], a[112:115]\n" \
"ds_read_b64_tr_b16 v[122:123], v237 offset:18432\n" \
"v_mfma_f32_16x16x32_bf16 a[116:119], v[52:55], v[12:15], a[116:119]\n" \
"buffer_load_dwordx4 v228, %[rw], s52 offen lds\n" \
"v_mfma_f32_16x16x32_bf16 a[120:123], v[56:59], v[12:15], a[120:123]\n" \
"ds_read_b64_tr_b16 v[124:125], v238 offset:16384\n" \
"v_mfma_f32_16x16x32_bf16 a[124:127], v[60:63], v[12:15], a[124:127]\n" \
"s_add_u32 s48, s48, 524288\n" \
"v_mfma_f32_16x16x32_bf16 a[128:131], v[32:35], v[16:19], a[128:131]\n" \
"ds_read_b64_tr_b16 v[126:127], v238 offset:18432\n" \
"v_mfma_f32_16x16x32_bf16 a[132:135], v[36:39], v[16:19], a[132:135]\n" \
"s_add_u32 s52, s43, 98304\n" \
"v_mfma_f32_16x16x32_bf16 a[136:139], v[40:43], v[16:19], a[136:139]\n" \
"ds_read_b128 v[68:71], v230 offset:2048\n" \
"v_mfma_f32_16x16x32_bf16 a[140:143], v[44:47], v[16:19], a[140:143]\n" \
"s_add_u32 m0, s41, 2048\n" \
"v_mfma_f32_16x16x32_bf16 a[144:147], v[48:51], v[16:19], a[144:147]\n" \
"ds_read_b128 v[72:75], v230 offset:4096\n" \
"v_mfma_f32_16x16x32_bf16 a[148:151], v[52:55], v[16:19], a[148:151]\n" \
"buffer_load_dwordx4 v225, %[rc], s52 offen lds\n" \
"v_mfma_f32_16x16x32_bf16 a[152:155], v[56:59], v[16:19], a[152:155]\n" \
"ds_read_b128 v[76:79], v230 offset:6144\n" \
"v_mfma_f32_16x16x32_bf16 a[156:159], v[60:63], v[16:19], a[156:159]\n" \
"s_add_u32 s51, s49, s50\n" \
"v_mfma_f32_16x16x32_bf16 a[160:163], v[32:35], v[20:23], a[160:163]\n" \
"ds_read_b128 v[80:83], v230 offset:8192\n" \
"v_mfma_f32_16x16x32_bf16 a[164:167], v[36:39], v[20:23], a[164:167]\n" \
"s_add_u32 s52, s47, 32768\n" \
"v_mfma_f32_16x16x32_bf16 a[168:171], v[40:43], v[20:23], a[168:171]\n" \
"ds_read_b128 v[84:87], v230 offset:10240\n" \
"v_mfma_f32_16x16x32_bf16 a[172:175], v[44:47], v[20:23], a[172:175]\n" \
"s_add_u32 m0, s45, 2048\n" \
"v_mfma_f32_16x16x32_bf16 a[176:179], v[48:51], v[20:23], a[176:179]\n" \
"ds_read_b128 v[88:91], v230 offset:12288\n" \
"v_mfma_f32_16x16x32_bf16 a[180:183], v[52:55], v[20:23], a[180:183]\n" \
"buffer_load_dwordx4 v227, %[rw], s52 offen lds\n" \
"v_mfma_f32_16x16x32_bf16 a[184:187], v[56:59], v[20:23], a[184:187]\n" \
"ds_read_b128 v[92:95], v230 offset:14336\n" \
"v_mfma_f32_16x16x32_bf16 a[188:191], v[60:63], v[20:23], a[188:191]\n" \
"s_sub_u32 s51, 0, s51\n" \
"v_mfma_f32_16x16x32_bf16 a[192:195], v[32:35], v[24:27], a[192:195]\n" \
"v_mfma_f32_16x16x32_bf16 a[196:199], v[36:39], v[24:27], a[196:199]\n" \
"s_add_u32 s52, s43, 114688\n" \
"v_mfma_f32_16x16x32_bf16 a[200:203], v[40:43], v[24:27], a[200:203]\n" \
"v_add_u32 v230, s50, v230\n" \
"v_mfma_f32_16x16x32_bf16 a[204:207], v[44:47], v[24:27], a[204:207]\n" \
"s_add_u32 m0, s41, 3072\n" \
"v_mfma_f32_16x16x32_bf16 a[208:211], v[48:51], v[24:27], a[208:211]\n" \
"v_mfma_f32_16x16x32_bf16 a[212:215], v[52:55], v[24:27], a[212:215]\n" \
"buffer_load_dwordx4 v226, %[rc], s52 offen lds\n" \
"v_mfma_f32_16x16x32_bf16 a[216:219], v[56:59], v[24:27], a[216:219]\n" \
"v_mfma_f32_16x16x32_bf16 a[220:223], v[60:63], v[24:27], a[220:223]\n" \
"v_mfma_f32_16x16x32_bf16 a[224:227], v[32:35], v[28:31], a[224:227]\n" \
"v_mfma_f32_16x16x32_bf16 a[228:231], v[36:39], v[28:31], a[228:231]\n" \
"s_add_u32 s52, s47, 49152\n" \
"v_mfma_f32_16x16x32_bf16 a[232:235], v[40:43], v[28:31], a[232:235]\n" \
"v_mfma_f32_16x16x32_bf16 a[236:239], v[44:47], v[28:31], a[236:239]\n" \
"s_add_u32 m0, s45, 3072\n" \
"v_mfma_f32_16x16x32_bf16 a[240:243], v[48:51], v[28:31], a[240:243]\n" \
"v_mfma_f32_16x16x32_bf16 a[244:247], v[52:55], v[28:31], a[244:247]\n" \
"buffer_load_dwordx4 v228, %[rw], s52 offen lds\n" \
"v_mfma_f32_16x16x32_bf16 a[248:251], v[56:59], v[28:31], a[248:251]\n" \
"v_mfma_f32_16x16x32_bf16 a[252:255], v[60:63], v[28:31], a[252:255]\n" \
"s_waitcnt vmcnt(16) lgkmcnt(0)\n" \
"s_barrier\n" \
"v_mfma_f32_16x16x32_bf16 a[0:3], v[96:99], v[64:67], a[0:3]\n" \
"ds_read_b128 v[0:3], v229\n" \
"v_xor_b32 v231, 0x38000, v231\n" \
"v_mfma_f32_16x16x32_bf16 a[4:7], v[100:103], v[64:67], a[4:7]\n" \
"s_add_u32 s52, s44, 0\n" \
"v_mfma_f32_16x16x32_bf16 a[8:11], v[104:107], v[64:67], a[8:11]\n" \
"ds_read_b64_tr_b16 v[32:33], v231 offset:0\n" \
"v_mfma_f32_16x16x32_bf16 a[12:15], v[108:111], v[64:67], a[12:15]\n" \
"s_add_u32 m0, s42, 0\n" \
"v_mfma_f32_16x16x32_bf16 a[16:19], v[112:115], v[64:67], a[16:19]\n" \
"ds_read_b64_tr_b16 v[34:35], v231 offset:2048\n" \
"v_xor_b32 v232, 0x38000, v232\n" \
"v_mfma_f32_16x16x32_bf16 a[20:23], v[116:119], v[64:67], a[20:23]\n" \
"buffer_load_dwordx4 v225, %[rc], s52 offen lds\n" \
"v_mfma_f32_16x16x32_bf16 a[24:27], v[120:123], v[64:67], a[24:27]\n" \
"ds_read_b64_tr_b16 v[36:37], v232 offset:0\n" \
"v_mfma_f32_16x16x32_bf16 a[28:31], v[124:127], v[64:67], a[28:31]\n" \
"s_add_u32 s41, s42, 4096\n" \
"v_mfma_f32_16x16x32_bf16 a[32:35], v[96:99], v[68:71], a[32:35]\n" \
"ds_read_b64_tr_b16 v[38:39], v232 offset:2048\n" \
"v_xor_b32 v233, 0x38000, v233\n" \
"v_mfma_f32_16x16x32_bf16 a[36:39], v[100:103], v[68:71], a[36:39]\n" \
"s_add_u32 s52, s48, 0\n" \
"v_mfma_f32_16x16x32_bf16 a[40:43], v[104:107], v[68:71], a[40:43]\n" \
"ds_read_b64_tr_b16 v[40:41], v233 offset:0\n" \
"v_mfma_f32_16x16x32_bf16 a[44:47], v[108:111], v[68:71], a[44:47]\n" \
"s_add_u32 m0, s46, 0\n" \
"v_mfma_f32_16x16x32_bf16 a[48:51], v[112:115], v[68:71], a[48:51]\n" \
"ds_read_b64_tr_b16 v[42:43], v233 offset:2048\n" \
"v_xor_b32 v234, 0x38000, v234\n" \
"v_mfma_f32_16x16x32_bf16 a[52:55], v[116:119], v[68:71], a[52:55]\n" \
"buffer_load_dwordx4 v227, %[rw], s52 offen lds\n" \
"v_mfma_f32_16x16x32_bf16 a[56:59], v[120:123], v[68:71], a[56:59]\n" \
"ds_read_b64_tr_b16 v[44:45], v234 offset:0\n" \
"v_mfma_f32_16x16x32_bf16 a[60:63], v[124:127], v[68:71], a[60:63]\n" \
"s_add_u32 s43, s43, 128\n" \
"v_mfma_f32_16x16x32_bf16 a[64:67], v[96:99], v[72:75], a[64:67]\n" \
"ds_read_b64_tr_b16 v[46:47], v234 offset:2048\n" \
"v_xor_b32 v235, 0x38000, v235\n" \
"v_mfma_f32_16x16x32_bf16 a[68:71], v[100:103], v[72:75], a[68:71]\n" \
"s_add_u32 s52, s44, 16384\n" \
"v_mfma_f32_16x16x32_bf16 a[72:75], v[104:107], v[72:75], a[72:75]\n" \
"ds_read_b64_tr_b16 v[48:49], v235 offset:0\n" \
"v_mfma_f32_16x16x32_bf16 a[76:79], v[108:111], v[72:75], a[76:79]\n" \
"s_add_u32 m0, s42, 1024\n" \
"v_mfma_f32_16x16x32_bf16 a[80:83], v[112:115], v[72:75], a[80:83]\n" \
"ds_read_b64_tr_b16 v[50:51], v235 offset:2048\n" \
"v_xor_b32 v236, 0x38000, v236\n" \
"v_mfma_f32_16x16x32_bf16 a[84:87], v[116:119], v[72:75], a[84:87]\n" \
"buffer_load_dwordx4 v226, %[rc], s52 offen lds\n" \
"v_mfma_f32_16x16x32_bf16 a[88:91], v[120:123], v[72:75], a[88:91]\n" \
"ds_read_b64_tr_b16 v[52:53], v236 offset:0\n" \
"v_mfma_f32_16x16x32_bf16 a[92:95], v[124:127], v[72:75], a[92:95]\n" \
"s_xor_b32 s45, s45, 0x38000\n" \
"v_mfma_f32_16x16x32_bf16 a[96:99], v[96:99], v[76:79], a[96:99]\n" \
"ds_read_b64_tr_b16 v[54:55], v236 offset:2048\n" \
"v_xor_b32 v237, 0x38000, v237\n" \
"v_mfma_f32_16x16x32_bf16 a[100:103], v[100:103], v[76:79], a[100:103]\n" \
"s_add_u32 s52, s48, 16384\n" \
"v_mfma_f32_16x16x32_bf16 a[104:107], v[104:107], v[76:79], a[104:107]\n" \
"ds_read_b64_tr_b16 v[56:57], v237 offset:0\n" \
"v_mfma_f32_16x16x32_bf16 a[108:111], v[108:111], v[76:79], a[108:111]\n" \
"s_add_u32 m0, s46, 1024\n" \
"v_mfma_f32_16x16x32_bf16 a[112:115], v[112:115], v[76:79], a[112:115]\n" \
"ds_read_b64_tr_b16 v[58:59], v237 offset:2048\n" \
"v_xor_b32 v238, 0x38000, v238\n" \
"v_mfma_f32_16x16x32_bf16 a[116:119], v[116:119], v[76:79], a[116:119]\n" \
"buffer_load_dwordx4 v228, %[rw], s52 offen lds\n" \
"v_mfma_f32_16x16x32_bf16 a[120:123], v[120:123], v[76:79], a[120:123]\n" \
"ds_read_b64_tr_b16 v[60:61], v238 offset:0\n" \
"v_mfma_f32_16x16x32_bf16 a[124:127], v[124:127], v[76:79], a[124:127]\n" \
"s_add_u32 s47, s47, 524288\n" \
"v_mfma_f32_16x16x32_bf16 a[128:131], v[96:99], v[80:83], a[128:131]\n" \
"ds_read_b64_tr_b16 v[62:63], v238 offset:2048\n" \
"v_mfma_f32_16x16x32_bf16 a[132:135], v[100:103], v[80:83], a[132:135]\n" \
"s_add_u32 s52, s44, 32768\n" \
"v_mfma_f32_16x16x32_bf16 a[136:139], v[104:107], v[80:83], a[136:139]\n" \
"ds_read_b128 v[4:7], v229 offset:2048\n" \
"v_mfma_f32_16x16x32_bf16 a[140:143], v[108:111], v[80:83], a[140:143]\n" \
"s_add_u32 m0, s42, 2048\n" \
"v_mfma_f32_16x16x32_bf16 a[144:147], v[112:115], v[80:83], a[144:147]\n" \
"ds_read_b128 v[8:11], v229 offset:4096\n" \
"v_mfma_f32_16x16x32_bf16 a[148:151], v[116:119], v[80:83], a[148:151]\n" \
"buffer_load_dwordx4 v225, %[rc], s52 offen lds\n" \
"v_mfma_f32_16x16x32_bf16 a[152:155], v[120:123], v[80:83], a[152:155]\n" \
"ds_read_b128 v[12:15], v229 offset:6144\n" \
"v_mfma_f32_16x16x32_bf16 a[156:159], v[124:127], v[80:83], a[156:159]\n" \
"s_mov_b32 s49, s50\n" \
"v_mfma_f32_16x16x32_bf16 a[160:163], v[96:99], v[84:87], a[160:163]\n" \
"ds_read_b128 v[16:19], v229 offset:8192\n" \
"v_mfma_f32_16x16x32_bf16 a[164:167], v[100:103], v[84:87], a[164:167]\n" \
"s_add_u32 s52, s48, 32768\n" \
"v_mfma_f32_16x16x32_bf16 a[168:171], v[104:107], v[84:87], a[168:171]\n" \
"ds_read_b128 v[20:23], v229 offset:10240\n" \
"v_mfma_f32_16x16x32_bf16 a[172:175], v[108:111], v[84:87], a[172:175]\n" \
"s_add_u32 m0, s46, 2048\n" \
"v_mfma_f32_16x16x32_bf16 a[176:179], v[112:115], v[84:87], a[176:179]\n" \
"ds_read_b128 v[24:27], v229 offset:12288\n" \
"v_mfma_f32_16x16x32_bf16 a[180:183], v[116:119], v[84:87], a[180:183]\n" \
"buffer_load_dwordx4 v227, %[rw], s52 offen lds\n" \
"v_mfma_f32_16x16x32_bf16 a[184:187], v[120:123], v[84:87], a[184:187]\n" \
"ds_read_b128 v[28:31], v229 offset:14336\n" \
"v_mfma_f32_16x16x32_bf16 a[188:191], v[124:127], v[84:87], a[188:191]\n" \
"s_mov_b32 s50, s51\n" \
"v_mfma_f32_16x16x32_bf16 a[192:195], v[96:99], v[88:91], a[192:195]\n" \
"s_add_u32 s53, s53, 1\n" \
"v_mfma_f32_16x16x32_bf16 a[196:199], v[100:103], v[88:91], a[196:199]\n" \
"s_add_u32 s52, s44, 49152\n" \
"v_mfma_f32_16x16x32_bf16 a[200:203], v[104:107], v[88:91], a[200:203]\n" \
"v_add_u32 v229, s51, v229\n" \
"v_mfma_f32_16x16x32_bf16 a[204:207], v[108:111], v[88:91], a[204:207]\n" \
"s_add_u32 m0, s42, 3072\n" \
"v_mfma_f32_16x16x32_bf16 a[208:211], v[112:115], v[88:91], a[208:211]\n" \
"v_mfma_f32_16x16x32_bf16 a[212:215], v[116:119], v[88:91], a[212:215]\n" \
"buffer_load_dwordx4 v226, %[rc], s52 offen lds\n" \
"v_mfma_f32_16x16x32_bf16 a[216:219], v[120:123], v[88:91], a[216:219]\n" \
"v_mfma_f32_16x16x32_bf16 a[220:223], v[124:127], v[88:91], a[220:223]\n" \
"v_mfma_f32_16x16x32_bf16 a[224:227], v[96:99], v[92:95], a[224:227]\n" \
"v_mfma_f32_16x16x32_bf16 a[228:231], v[100:103], v[92:95], a[228:231]\n" \
"s_add_u32 s52, s48, 49152\n" \
"v_mfma_f32_16x16x32_bf16 a[232:235], v[104:107], v[92:95], a[232:235]\n" \
"v_mfma_f32_16x16x32_bf16 a[236:239], v[108:111], v[92:95], a[236:239]\n" \
"s_add_u32 m0, s46, 3072\n" \
"v_mfma_f32_16x16x32_bf16 a[240:243], v[112:115], v[92:95], a[240:243]\n" \
"v_mfma_f32_16x16x32_bf16 a[244:247], v[116:119], v[92:95], a[244:247]\n" \
"buffer_load_dwordx4 v228, %[rw], s52 offen lds\n" \
"v_mfma_f32_16x16x32_bf16 a[248:251], v[120:123], v[92:95], a[248:251]\n" \
"s_cmp_lg_u32 s53, 3\n" \
"v_mfma_f32_16x16x32_bf16 a[252:255], v[124:127], v[92:95], a[252:255]\n" \
"s_cbranch_scc1 LnsK%=\n" \
"s_cmp_lg_u32 s36, 0\n" \
"s_cbranch_scc1 LsxK%=\n" \
"s_mov_b64 s[96:97], exec\n" \
"s_mov_b64 exec, 1\n" \
"v_mov_b32 v223, %[tag]\n" \
"buffer_store_dword v223, off, %[rs], s56 sc1\n" \
"s_mov_b64 exec, s[96:97]\n" \
"LsxK%=:\n" \
"LnsK%=:\n" \
"s_cmp_lt_u32 s53, 13\n" \
"s_cbranch_scc1 LkvK%=\n" \
"s_waitcnt vmcnt(16) lgkmcnt(0)\n" \
"s_barrier\n" \
"v_mfma_f32_16x16x32_bf16 a[0:3], v[32:35], v[0:3], a[0:3]\n" \
"ds_read_b128 v[64:67], v230\n" \
"buffer_load_dword v222, v240, %[rs], s57 offen sc1\n" \
"v_mfma_f32_16x16x32_bf16 a[4:7], v[36:39], v[0:3], a[4:7]\n" \
"s_add_u32 s52, s39, 67456\n" \
"v_mfma_f32_16x16x32_bf16 a[8:11], v[40:43], v[0:3], a[8:11]\n" \
"ds_read_b64_tr_b16 v[96:97], v231 offset:16384\n" \
"v_mfma_f32_16x16x32_bf16 a[12:15], v[44:47], v[0:3], a[12:15]\n" \
"s_add_u32 m0, s37, 4096\n" \
"v_mfma_f32_16x16x32_bf16 a[16:19], v[48:51], v[0:3], a[16:19]\n" \
"ds_read_b64_tr_b16 v[98:99], v231 offset:18432\n" \
"v_mfma_f32_16x16x32_bf16 a[20:23], v[52:55], v[0:3], a[20:23]\n" \
"buffer_load_dwordx4 v225, %[rc], s52 offen lds\n" \
"v_mfma_f32_16x16x32_bf16 a[24:27], v[56:59], v[0:3], a[24:27]\n" \
"ds_read_b64_tr_b16 v[100:101], v232 offset:16384\n" \
"v_mfma_f32_16x16x32_bf16 a[28:31], v[60:63], v[0:3], a[28:31]\n" \
"s_add_u32 s94, s72, 0\n" \
"v_mfma_f32_16x16x32_bf16 a[32:35], v[32:35], v[4:7], a[32:35]\n" \
"ds_read_b64_tr_b16 v[102:103], v232 offset:18432\n" \
"v_mfma_f32_16x16x32_bf16 a[36:39], v[36:39], v[4:7], a[36:39]\n" \
"s_add_u32 s52, s40, 7864320\n" \
"v_mfma_f32_16x16x32_bf16 a[40:43], v[40:43], v[4:7], a[40:43]\n" \
"ds_read_b64_tr_b16 v[104:105], v233 offset:16384\n" \
"v_mfma_f32_16x16x32_bf16 a[44:47], v[44:47], v[4:7], a[44:47]\n" \
"s_add_u32 m0, s38, 131072\n" \
"v_mfma_f32_16x16x32_bf16 a[48:51], v[48:51], v[4:7], a[48:51]\n" \
"ds_read_b64_tr_b16 v[106:107], v233 offset:18432\n" \
"v_mfma_f32_16x16x32_bf16 a[52:55], v[52:55], v[4:7], a[52:55]\n" \
"buffer_load_dwordx4 v227, %[rw], s52 offen lds\n" \
"v_mfma_f32_16x16x32_bf16 a[56:59], v[56:59], v[4:7], a[56:59]\n" \
"ds_read_b64_tr_b16 v[108:109], v234 offset:16384\n" \
"v_mfma_f32_16x16x32_bf16 a[60:63], v[60:63], v[4:7], a[60:63]\n" \
"buffer_load_ushort v128, v239, s[28:31], s94 offen offset:0\n" \
"v_mfma_f32_16x16x32_bf16 a[64:67], v[32:35], v[8:11], a[64:67]\n" \
"ds_read_b64_tr_b16 v[110:111], v234 offset:18432\n" \
"v_mfma_f32_16x16x32_bf16 a[68:71], v[36:39], v[8:11], a[68:71]\n" \
"s_add_u32 s52, s39, 83840\n" \
"v_mfma_f32_16x16x32_bf16 a[72:75], v[40:43], v[8:11], a[72:75]\n" \
"ds_read_b64_tr_b16 v[112:113], v235 offset:16384\n" \
"v_mfma_f32_16x16x32_bf16 a[76:79], v[44:47], v[8:11], a[76:79]\n" \
"s_add_u32 m0, s37, 5120\n" \
"v_mfma_f32_16x16x32_bf16 a[80:83], v[48:51], v[8:11], a[80:83]\n" \
"ds_read_b64_tr_b16 v[114:115], v235 offset:18432\n" \
"v_mfma_f32_16x16x32_bf16 a[84:87], v[52:55], v[8:11], a[84:87]\n" \
"buffer_load_dwordx4 v226, %[rc], s52 offen lds\n" \
"v_mfma_f32_16x16x32_bf16 a[88:91], v[56:59], v[8:11], a[88:91]\n" \
"ds_read_b64_tr_b16 v[116:117], v236 offset:16384\n" \
"v_mfma_f32_16x16x32_bf16 a[92:95], v[60:63], v[8:11], a[92:95]\n" \
"buffer_load_ushort v129, v239, s[28:31], s94 offen offset:896\n" \
"v_mfma_f32_16x16x32_bf16 a[96:99], v[32:35], v[12:15], a[96:99]\n" \
"ds_read_b64_tr_b16 v[118:119], v236 offset:18432\n" \
"v_mfma_f32_16x16x32_bf16 a[100:103], v[36:39], v[12:15], a[100:103]\n" \
"s_add_u32 s52, s40, 7880704\n" \
"v_mfma_f32_16x16x32_bf16 a[104:107], v[40:43], v[12:15], a[104:107]\n" \
"ds_read_b64_tr_b16 v[120:121], v237 offset:16384\n" \
"v_mfma_f32_16x16x32_bf16 a[108:111], v[44:47], v[12:15], a[108:111]\n" \
"s_add_u32 m0, s38, 132096\n" \
"v_mfma_f32_16x16x32_bf16 a[112:115], v[48:51], v[12:15], a[112:115]\n" \
"ds_read_b64_tr_b16 v[122:123], v237 offset:18432\n" \
"v_mfma_f32_16x16x32_bf16 a[116:119], v[52:55], v[12:15], a[116:119]\n" \
"buffer_load_dwordx4 v228, %[rw], s52 offen lds\n" \
"v_mfma_f32_16x16x32_bf16 a[120:123], v[56:59], v[12:15], a[120:123]\n" \
"ds_read_b64_tr_b16 v[124:125], v238 offset:16384\n" \
"v_mfma_f32_16x16x32_bf16 a[124:127], v[60:63], v[12:15], a[124:127]\n" \
"buffer_load_ushort v130, v239, s[28:31], s94 offen offset:1792\n" \
"v_mfma_f32_16x16x32_bf16 a[128:131], v[32:35], v[16:19], a[128:131]\n" \
"ds_read_b64_tr_b16 v[126:127], v238 offset:18432\n" \
"v_mfma_f32_16x16x32_bf16 a[132:135], v[36:39], v[16:19], a[132:135]\n" \
"s_add_u32 s52, s39, 100224\n" \
"v_mfma_f32_16x16x32_bf16 a[136:139], v[40:43], v[16:19], a[136:139]\n" \
"ds_read_b128 v[68:71], v230 offset:2048\n" \
"v_mfma_f32_16x16x32_bf16 a[140:143], v[44:47], v[16:19], a[140:143]\n" \
"s_add_u32 m0, s37, 6144\n" \
"v_mfma_f32_16x16x32_bf16 a[144:147], v[48:51], v[16:19], a[144:147]\n" \
"ds_read_b128 v[72:75], v230 offset:4096\n" \
"v_mfma_f32_16x16x32_bf16 a[148:151], v[52:55], v[16:19], a[148:151]\n" \
"buffer_load_dwordx4 v225, %[rc], s52 offen lds\n" \
"v_mfma_f32_16x16x32_bf16 a[152:155], v[56:59], v[16:19], a[152:155]\n" \
"ds_read_b128 v[76:79], v230 offset:6144\n" \
"v_mfma_f32_16x16x32_bf16 a[156:159], v[60:63], v[16:19], a[156:159]\n" \
"buffer_load_ushort v131, v239, s[28:31], s94 offen offset:2688\n" \
"v_mfma_f32_16x16x32_bf16 a[160:163], v[32:35], v[20:23], a[160:163]\n" \
"ds_read_b128 v[80:83], v230 offset:8192\n" \
"v_mfma_f32_16x16x32_bf16 a[164:167], v[36:39], v[20:23], a[164:167]\n" \
"s_add_u32 s52, s40, 7897088\n" \
"v_mfma_f32_16x16x32_bf16 a[168:171], v[40:43], v[20:23], a[168:171]\n" \
"ds_read_b128 v[84:87], v230 offset:10240\n" \
"v_mfma_f32_16x16x32_bf16 a[172:175], v[44:47], v[20:23], a[172:175]\n" \
"s_add_u32 m0, s38, 133120\n" \
"v_mfma_f32_16x16x32_bf16 a[176:179], v[48:51], v[20:23], a[176:179]\n" \
"ds_read_b128 v[88:91], v230 offset:12288\n" \
"v_mfma_f32_16x16x32_bf16 a[180:183], v[52:55], v[20:23], a[180:183]\n" \
"buffer_load_dwordx4 v227, %[rw], s52 offen lds\n" \
"v_mfma_f32_16x16x32_bf16 a[184:187], v[56:59], v[20:23], a[184:187]\n" \
"ds_read_b128 v[92:95], v230 offset:14336\n" \
"v_mfma_f32_16x16x32_bf16 a[188:191], v[60:63], v[20:23], a[188:191]\n" \
"s_add_u32 s94, s72, 14336\n" \
"v_mfma_f32_16x16x32_bf16 a[192:195], v[32:35], v[24:27], a[192:195]\n" \
"buffer_load_ushort v132, v239, s[28:31], s94 offen offset:0\n" \
"v_mfma_f32_16x16x32_bf16 a[196:199], v[36:39], v[24:27], a[196:199]\n" \
"s_add_u32 s52, s39, 116608\n" \
"buffer_load_ushort v133, v239, s[28:31], s94 offen offset:896\n" \
"v_mfma_f32_16x16x32_bf16 a[200:203], v[40:43], v[24:27], a[200:203]\n" \
"v_add_u32 v230, 0x8000, v230\n" \
"buffer_load_ushort v134, v239, s[28:31], s94 offen offset:1792\n" \
"v_mfma_f32_16x16x32_bf16 a[204:207], v[44:47], v[24:27], a[204:207]\n" \
"s_add_u32 m0, s37, 7168\n" \
"buffer_load_ushort v135, v239, s[28:31], s94 offen offset:2688\n" \
"v_mfma_f32_16x16x32_bf16 a[208:211], v[48:51], v[24:27], a[208:211]\n" \
"s_add_u32 s94, s73, 0\n" \
"v_mfma_f32_16x16x32_bf16 a[212:215], v[52:55], v[24:27], a[212:215]\n" \
"buffer_load_dwordx4 v226, %[rc], s52 offen lds\n" \
"v_mfma_f32_16x16x32_bf16 a[216:219], v[56:59], v[24:27], a[216:219]\n" \
"buffer_load_ushort v136, v239, s[28:31], s94 offen offset:0\n" \
"v_mfma_f32_16x16x32_bf16 a[220:223], v[60:63], v[24:27], a[220:223]\n" \
"buffer_load_ushort v137, v239, s[28:31], s94 offen offset:896\n" \
"v_mfma_f32_16x16x32_bf16 a[224:227], v[32:35], v[28:31], a[224:227]\n" \
"buffer_load_ushort v138, v239, s[28:31], s94 offen offset:1792\n" \
"v_mfma_f32_16x16x32_bf16 a[228:231], v[36:39], v[28:31], a[228:231]\n" \
"s_add_u32 s52, s40, 7913472\n" \
"buffer_load_ushort v139, v239, s[28:31], s94 offen offset:2688\n" \
"v_mfma_f32_16x16x32_bf16 a[232:235], v[40:43], v[28:31], a[232:235]\n" \
"s_add_u32 s94, s73, 14336\n" \
"v_mfma_f32_16x16x32_bf16 a[236:239], v[44:47], v[28:31], a[236:239]\n" \
"s_add_u32 m0, s38, 134144\n" \
"buffer_load_ushort v140, v239, s[28:31], s94 offen offset:0\n" \
"v_mfma_f32_16x16x32_bf16 a[240:243], v[48:51], v[28:31], a[240:243]\n" \
"buffer_load_ushort v141, v239, s[28:31], s94 offen offset:896\n" \
"v_mfma_f32_16x16x32_bf16 a[244:247], v[52:55], v[28:31], a[244:247]\n" \
"buffer_load_dwordx4 v228, %[rw], s52 offen lds\n" \
"v_mfma_f32_16x16x32_bf16 a[248:251], v[56:59], v[28:31], a[248:251]\n" \
"buffer_load_ushort v142, v239, s[28:31], s94 offen offset:1792\n" \
"v_mfma_f32_16x16x32_bf16 a[252:255], v[60:63], v[28:31], a[252:255]\n" \
"buffer_load_ushort v143, v239, s[28:31], s94 offen offset:2688\n" \
"s_waitcnt vmcnt(33) lgkmcnt(0)\n" \
"s_barrier\n" \
"v_mfma_f32_16x16x32_bf16 a[0:3], v[96:99], v[64:67], a[0:3]\n" \
"ds_read_b128 v[0:3], v229\n" \
"v_xor_b32 v231, 0x38000, v231\n" \
"v_mfma_f32_16x16x32_bf16 a[4:7], v[100:103], v[64:67], a[4:7]\n" \
"s_add_u32 s52, s40, 0\n" \
"v_mfma_f32_16x16x32_bf16 a[8:11], v[104:107], v[64:67], a[8:11]\n" \
"ds_read_b64_tr_b16 v[32:33], v231 offset:0\n" \
"v_mfma_f32_16x16x32_bf16 a[12:15], v[108:111], v[64:67], a[12:15]\n" \
"s_add_u32 m0, s38, 32768\n" \
"v_mfma_f32_16x16x32_bf16 a[16:19], v[112:115], v[64:67], a[16:19]\n" \
"ds_read_b64_tr_b16 v[34:35], v231 offset:2048\n" \
"v_xor_b32 v232, 0x38000, v232\n" \
"v_mfma_f32_16x16x32_bf16 a[20:23], v[116:119], v[64:67], a[20:23]\n" \
"buffer_load_dwordx4 v227, s[20:23], s52 offen lds\n" \
"v_mfma_f32_16x16x32_bf16 a[24:27], v[120:123], v[64:67], a[24:27]\n" \
"ds_read_b64_tr_b16 v[36:37], v232 offset:0\n" \
"v_mfma_f32_16x16x32_bf16 a[28:31], v[124:127], v[64:67], a[28:31]\n" \
"s_add_u32 s94, s74, 0\n" \
"v_mfma_f32_16x16x32_bf16 a[32:35], v[96:99], v[68:71], a[32:35]\n" \
"ds_read_b64_tr_b16 v[38:39], v232 offset:2048\n" \
"v_xor_b32 v233, 0x38000, v233\n" \
"v_mfma_f32_16x16x32_bf16 a[36:39], v[100:103], v[68:71], a[36:39]\n" \
"s_add_u32 s52, s40, 8126464\n" \
"v_mfma_f32_16x16x32_bf16 a[40:43], v[104:107], v[68:71], a[40:43]\n" \
"ds_read_b64_tr_b16 v[40:41], v233 offset:0\n" \
"v_mfma_f32_16x16x32_bf16 a[44:47], v[108:111], v[68:71], a[44:47]\n" \
"s_add_u32 m0, s38, 147456\n" \
"v_mfma_f32_16x16x32_bf16 a[48:51], v[112:115], v[68:71], a[48:51]\n" \
"ds_read_b64_tr_b16 v[42:43], v233 offset:2048\n" \
"v_xor_b32 v234, 0x38000, v234\n" \
"v_mfma_f32_16x16x32_bf16 a[52:55], v[116:119], v[68:71], a[52:55]\n" \
"buffer_load_dwordx4 v227, %[rw], s52 offen lds\n" \
"v_mfma_f32_16x16x32_bf16 a[56:59], v[120:123], v[68:71], a[56:59]\n" \
"ds_read_b64_tr_b16 v[44:45], v234 offset:0\n" \
"v_mfma_f32_16x16x32_bf16 a[60:63], v[124:127], v[68:71], a[60:63]\n" \
"buffer_load_ushort v144, v239, s[28:31], s94 offen offset:0\n" \
"v_mfma_f32_16x16x32_bf16 a[64:67], v[96:99], v[72:75], a[64:67]\n" \
"ds_read_b64_tr_b16 v[46:47], v234 offset:2048\n" \
"v_xor_b32 v235, 0x38000, v235\n" \
"v_mfma_f32_16x16x32_bf16 a[68:71], v[100:103], v[72:75], a[68:71]\n" \
"s_add_u32 s52, s40, 16384\n" \
"v_mfma_f32_16x16x32_bf16 a[72:75], v[104:107], v[72:75], a[72:75]\n" \
"ds_read_b64_tr_b16 v[48:49], v235 offset:0\n" \
"v_mfma_f32_16x16x32_bf16 a[76:79], v[108:111], v[72:75], a[76:79]\n" \
"s_add_u32 m0, s38, 33792\n" \
"v_mfma_f32_16x16x32_bf16 a[80:83], v[112:115], v[72:75], a[80:83]\n" \
"ds_read_b64_tr_b16 v[50:51], v235 offset:2048\n" \
"v_xor_b32 v236, 0x38000, v236\n" \
"v_mfma_f32_16x16x32_bf16 a[84:87], v[116:119], v[72:75], a[84:87]\n" \
"buffer_load_dwordx4 v228, s[20:23], s52 offen lds\n" \
"v_mfma_f32_16x16x32_bf16 a[88:91], v[120:123], v[72:75], a[88:91]\n" \
"ds_read_b64_tr_b16 v[52:53], v236 offset:0\n" \
"v_mfma_f32_16x16x32_bf16 a[92:95], v[124:127], v[72:75], a[92:95]\n" \
"buffer_load_ushort v145, v239, s[28:31], s94 offen offset:896\n" \
"v_mfma_f32_16x16x32_bf16 a[96:99], v[96:99], v[76:79], a[96:99]\n" \
"ds_read_b64_tr_b16 v[54:55], v236 offset:2048\n" \
"v_xor_b32 v237, 0x38000, v237\n" \
"v_mfma_f32_16x16x32_bf16 a[100:103], v[100:103], v[76:79], a[100:103]\n" \
"s_add_u32 s52, s40, 8142848\n" \
"v_mfma_f32_16x16x32_bf16 a[104:107], v[104:107], v[76:79], a[104:107]\n" \
"ds_read_b64_tr_b16 v[56:57], v237 offset:0\n" \
"v_mfma_f32_16x16x32_bf16 a[108:111], v[108:111], v[76:79], a[108:111]\n" \
"s_add_u32 m0, s38, 148480\n" \
"v_mfma_f32_16x16x32_bf16 a[112:115], v[112:115], v[76:79], a[112:115]\n" \
"ds_read_b64_tr_b16 v[58:59], v237 offset:2048\n" \
"v_xor_b32 v238, 0x38000, v238\n" \
"v_mfma_f32_16x16x32_bf16 a[116:119], v[116:119], v[76:79], a[116:119]\n" \
"buffer_load_dwordx4 v228, %[rw], s52 offen lds\n" \
"v_mfma_f32_16x16x32_bf16 a[120:123], v[120:123], v[76:79], a[120:123]\n" \
"ds_read_b64_tr_b16 v[60:61], v238 offset:0\n" \
"v_mfma_f32_16x16x32_bf16 a[124:127], v[124:127], v[76:79], a[124:127]\n" \
"buffer_load_ushort v146, v239, s[28:31], s94 offen offset:1792\n" \
"v_mfma_f32_16x16x32_bf16 a[128:131], v[96:99], v[80:83], a[128:131]\n" \
"ds_read_b64_tr_b16 v[62:63], v238 offset:2048\n" \
"v_mfma_f32_16x16x32_bf16 a[132:135], v[100:103], v[80:83], a[132:135]\n" \
"s_add_u32 s52, s40, 32768\n" \
"v_mfma_f32_16x16x32_bf16 a[136:139], v[104:107], v[80:83], a[136:139]\n" \
"ds_read_b128 v[4:7], v229 offset:2048\n" \
"v_mfma_f32_16x16x32_bf16 a[140:143], v[108:111], v[80:83], a[140:143]\n" \
"s_add_u32 m0, s38, 34816\n" \
"v_mfma_f32_16x16x32_bf16 a[144:147], v[112:115], v[80:83], a[144:147]\n" \
"ds_read_b128 v[8:11], v229 offset:4096\n" \
"v_mfma_f32_16x16x32_bf16 a[148:151], v[116:119], v[80:83], a[148:151]\n" \
"buffer_load_dwordx4 v227, s[20:23], s52 offen lds\n" \
"v_mfma_f32_16x16x32_bf16 a[152:155], v[120:123], v[80:83], a[152:155]\n" \
"ds_read_b128 v[12:15], v229 offset:6144\n" \
"v_mfma_f32_16x16x32_bf16 a[156:159], v[124:127], v[80:83], a[156:159]\n" \
"buffer_load_ushort v147, v239, s[28:31], s94 offen offset:2688\n" \
"v_mfma_f32_16x16x32_bf16 a[160:163], v[96:99], v[84:87], a[160:163]\n" \
"ds_read_b128 v[16:19], v229 offset:8192\n" \
"v_mfma_f32_16x16x32_bf16 a[164:167], v[100:103], v[84:87], a[164:167]\n" \
"s_add_u32 s52, s40, 8159232\n" \
"v_mfma_f32_16x16x32_bf16 a[168:171], v[104:107], v[84:87], a[168:171]\n" \
"ds_read_b128 v[20:23], v229 offset:10240\n" \
"v_mfma_f32_16x16x32_bf16 a[172:175], v[108:111], v[84:87], a[172:175]\n" \
"s_add_u32 m0, s38, 149504\n" \
"v_mfma_f32_16x16x32_bf16 a[176:179], v[112:115], v[84:87], a[176:179]\n" \
"ds_read_b128 v[24:27], v229 offset:12288\n" \
"v_mfma_f32_16x16x32_bf16 a[180:183], v[116:119], v[84:87], a[180:183]\n" \
"buffer_load_dwordx4 v227, %[rw], s52 offen lds\n" \
"v_mfma_f32_16x16x32_bf16 a[184:187], v[120:123], v[84:87], a[184:187]\n" \
"ds_read_b128 v[28:31], v229 offset:14336\n" \
"v_mfma_f32_16x16x32_bf16 a[188:191], v[124:127], v[84:87], a[188:191]\n" \
"s_add_u32 s94, s74, 14336\n" \
"v_mfma_f32_16x16x32_bf16 a[192:195], v[96:99], v[88:91], a[192:195]\n" \
"buffer_load_ushort v148, v239, s[28:31], s94 offen offset:0\n" \
"v_mfma_f32_16x16x32_bf16 a[196:199], v[100:103], v[88:91], a[196:199]\n" \
"s_add_u32 s52, s40, 49152\n" \
"buffer_load_ushort v149, v239, s[28:31], s94 offen offset:896\n" \
"v_mfma_f32_16x16x32_bf16 a[200:203], v[104:107], v[88:91], a[200:203]\n" \
"v_add_u32 v229, 0xffff0000, v229\n" \
"buffer_load_ushort v150, v239, s[28:31], s94 offen offset:1792\n" \
"v_mfma_f32_16x16x32_bf16 a[204:207], v[108:111], v[88:91], a[204:207]\n" \
"s_add_u32 m0, s38, 35840\n" \
"buffer_load_ushort v151, v239, s[28:31], s94 offen offset:2688\n" \
"v_mfma_f32_16x16x32_bf16 a[208:211], v[112:115], v[88:91], a[208:211]\n" \
"s_add_u32 s94, s75, 0\n" \
"v_mfma_f32_16x16x32_bf16 a[212:215], v[116:119], v[88:91], a[212:215]\n" \
"buffer_load_dwordx4 v228, s[20:23], s52 offen lds\n" \
"v_mfma_f32_16x16x32_bf16 a[216:219], v[120:123], v[88:91], a[216:219]\n" \
"buffer_load_ushort v152, v239, s[28:31], s94 offen offset:0\n" \
"v_mfma_f32_16x16x32_bf16 a[220:223], v[124:127], v[88:91], a[220:223]\n" \
"buffer_load_ushort v153, v239, s[28:31], s94 offen offset:896\n" \
"v_mfma_f32_16x16x32_bf16 a[224:227], v[96:99], v[92:95], a[224:227]\n" \
"buffer_load_ushort v154, v239, s[28:31], s94 offen offset:1792\n" \
"v_mfma_f32_16x16x32_bf16 a[228:231], v[100:103], v[92:95], a[228:231]\n" \
"s_add_u32 s52, s40, 8175616\n" \
"buffer_load_ushort v155, v239, s[28:31], s94 offen offset:2688\n" \
"v_mfma_f32_16x16x32_bf16 a[232:235], v[104:107], v[92:95], a[232:235]\n" \
"s_add_u32 s94, s75, 14336\n" \
"v_mfma_f32_16x16x32_bf16 a[236:239], v[108:111], v[92:95], a[236:239]\n" \
"s_add_u32 m0, s38, 150528\n" \
"buffer_load_ushort v156, v239, s[28:31], s94 offen offset:0\n" \
"v_mfma_f32_16x16x32_bf16 a[240:243], v[112:115], v[92:95], a[240:243]\n" \
"buffer_load_ushort v157, v239, s[28:31], s94 offen offset:896\n" \
"v_mfma_f32_16x16x32_bf16 a[244:247], v[116:119], v[92:95], a[244:247]\n" \
"buffer_load_dwordx4 v228, %[rw], s52 offen lds\n" \
"v_mfma_f32_16x16x32_bf16 a[248:251], v[120:123], v[92:95], a[248:251]\n" \
"buffer_load_ushort v158, v239, s[28:31], s94 offen offset:1792\n" \
"v_mfma_f32_16x16x32_bf16 a[252:255], v[124:127], v[92:95], a[252:255]\n" \
"buffer_load_ushort v159, v239, s[28:31], s94 offen offset:2688\n" \
"s_waitcnt vmcnt(48)\n" \
"v_cmp_ne_u32 s[96:97], %[tag], v222\n" \
"s_nop 1\n" \
"s_cmp_eq_u64 s[96:97], 0\n" \
"s_cbranch_scc1 LxokK%=\n" \
"s_mov_b32 s81, 0\n" \
"LxwK_p%=:\n" \
"buffer_load_dword v208, v240, %[rs], s57 offen sc1\n" \
"s_waitcnt vmcnt(0)\n" \
"v_cmp_ne_u32 s[96:97], %[tag], v208\n" \
"s_nop 1\n" \
"s_cmp_eq_u64 s[96:97], 0\n" \
"s_cbranch_scc1 LxwK_d%=\n" \
"s_sleep 4\n" \
"s_add_u32 s81, s81, 1\n" \
"s_cmp_lt_u32 s81, 0x20000\n" \
"s_cbranch_scc1 LxwK_p%=\n" \
"LxwK_d%=:\n" \
"LxokK%=:\n" \
"s_waitcnt vmcnt(48) lgkmcnt(0)\n" \
"s_barrier\n" \
"v_mfma_f32_16x16x32_bf16 a[0:3], v[32:35], v[0:3], a[0:3]\n" \
"ds_read_b128 v[64:67], v230\n" \
"v_mfma_f32_16x16x32_bf16 a[4:7], v[36:39], v[0:3], a[4:7]\n" \
"s_add_u32 s52, s40, 262144\n" \
"v_mfma_f32_16x16x32_bf16 a[8:11], v[40:43], v[0:3], a[8:11]\n" \
"ds_read_b64_tr_b16 v[96:97], v231 offset:16384\n" \
"v_mfma_f32_16x16x32_bf16 a[12:15], v[44:47], v[0:3], a[12:15]\n" \
"s_add_u32 m0, s38, 49152\n" \
"v_mfma_f32_16x16x32_bf16 a[16:19], v[48:51], v[0:3], a[16:19]\n" \
"ds_read_b64_tr_b16 v[98:99], v231 offset:18432\n" \
"v_mfma_f32_16x16x32_bf16 a[20:23], v[52:55], v[0:3], a[20:23]\n" \
"buffer_load_dwordx4 v227, s[20:23], s52 offen lds\n" \
"v_mfma_f32_16x16x32_bf16 a[24:27], v[56:59], v[0:3], a[24:27]\n" \
"ds_read_b64_tr_b16 v[100:101], v232 offset:16384\n" \
"v_mfma_f32_16x16x32_bf16 a[28:31], v[60:63], v[0:3], a[28:31]\n" \
"v_mfma_f32_16x16x32_bf16 a[32:35], v[32:35], v[4:7], a[32:35]\n" \
"ds_read_b64_tr_b16 v[102:103], v232 offset:18432\n" \
"v_mfma_f32_16x16x32_bf16 a[36:39], v[36:39], v[4:7], a[36:39]\n" \
"s_add_u32 s52, s40, 1048576\n" \
"v_mfma_f32_16x16x32_bf16 a[40:43], v[40:43], v[4:7], a[40:43]\n" \
"ds_read_b64_tr_b16 v[104:105], v233 offset:16384\n" \
"v_mfma_f32_16x16x32_bf16 a[44:47], v[44:47], v[4:7], a[44:47]\n" \
"s_add_u32 m0, s38, 98304\n" \
"v_mfma_f32_16x16x32_bf16 a[48:51], v[48:51], v[4:7], a[48:51]\n" \
"ds_read_b64_tr_b16 v[106:107], v233 offset:18432\n" \
"v_mfma_f32_16x16x32_bf16 a[52:55], v[52:55], v[4:7], a[52:55]\n" \
"buffer_load_dwordx4 v227, s[20:23], s52 offen lds\n" \
"v_mfma_f32_16x16x32_bf16 a[56:59], v[56:59], v[4:7], a[56:59]\n" \
"ds_read_b64_tr_b16 v[108:109], v234 offset:16384\n" \
"v_mfma_f32_16x16x32_bf16 a[60:63], v[60:63], v[4:7], a[60:63]\n" \
"s_add_u32 s80, s58, 0\n" \
"v_mfma_f32_16x16x32_bf16 a[64:67], v[32:35], v[8:11], a[64:67]\n" \
"ds_read_b64_tr_b16 v[110:111], v234 offset:18432\n" \
"v_mfma_f32_16x16x32_bf16 a[68:71], v[36:39], v[8:11], a[68:71]\n" \
"s_add_u32 s52, s40, 278528\n" \
"buffer_load_dwordx4 v[160:163], v224, %[rs], s80 offen sc1\n" \
"v_mfma_f32_16x16x32_bf16 a[72:75], v[40:43], v[8:11], a[72:75]\n" \
"ds_read_b64_tr_b16 v[112:113], v235 offset:16384\n" \
"v_mfma_f32_16x16x32_bf16 a[76:79], v[44:47], v[8:11], a[76:79]\n" \
"s_add_u32 m0, s38, 50176\n" \
"v_mfma_f32_16x16x32_bf16 a[80:83], v[48:51], v[8:11], a[80:83]\n" \
"ds_read_b64_tr_b16 v[114:115], v235 offset:18432\n" \
"v_mfma_f32_16x16x32_bf16 a[84:87], v[52:55], v[8:11], a[84:87]\n" \
"buffer_load_dwordx4 v228, s[20:23], s52 offen lds\n" \
"v_mfma_f32_16x16x32_bf16 a[88:91], v[56:59], v[8:11], a[88:91]\n" \
"ds_read_b64_tr_b16 v[116:117], v236 offset:16384\n" \
"v_mfma_f32_16x16x32_bf16 a[92:95], v[60:63], v[8:11], a[92:95]\n" \
"v_mfma_f32_16x16x32_bf16 a[96:99], v[32:35], v[12:15], a[96:99]\n" \
"ds_read_b64_tr_b16 v[118:119], v236 offset:18432\n" \
"v_mfma_f32_16x16x32_bf16 a[100:103], v[36:39], v[12:15], a[100:103]\n" \
"s_add_u32 s52, s40, 1064960\n" \
"v_mfma_f32_16x16x32_bf16 a[104:107], v[40:43], v[12:15], a[104:107]\n" \
"ds_read_b64_tr_b16 v[120:121], v237 offset:16384\n" \
"v_mfma_f32_16x16x32_bf16 a[108:111], v[44:47], v[12:15], a[108:111]\n" \
"s_add_u32 m0, s38, 99328\n" \
"v_mfma_f32_16x16x32_bf16 a[112:115], v[48:51], v[12:15], a[112:115]\n" \
"ds_read_b64_tr_b16 v[122:123], v237 offset:18432\n" \
"v_mfma_f32_16x16x32_bf16 a[116:119], v[52:55], v[12:15], a[116:119]\n" \
"buffer_load_dwordx4 v228, s[20:23], s52 offen lds\n" \
"v_mfma_f32_16x16x32_bf16 a[120:123], v[56:59], v[12:15], a[120:123]\n" \
"ds_read_b64_tr_b16 v[124:125], v238 offset:16384\n" \
"v_mfma_f32_16x16x32_bf16 a[124:127], v[60:63], v[12:15], a[124:127]\n" \
"s_add_u32 s80, s71, 0\n" \
"v_mfma_f32_16x16x32_bf16 a[128:131], v[32:35], v[16:19], a[128:131]\n" \
"ds_read_b64_tr_b16 v[126:127], v238 offset:18432\n" \
"v_mfma_f32_16x16x32_bf16 a[132:135], v[36:39], v[16:19], a[132:135]\n" \
"s_add_u32 s52, s40, 294912\n" \
"buffer_load_dwordx4 v[164:167], v224, %[rs], s80 offen sc1\n" \
"v_mfma_f32_16x16x32_bf16 a[136:139], v[40:43], v[16:19], a[136:139]\n" \
"ds_read_b128 v[68:71], v230 offset:2048\n" \
"v_mfma_f32_16x16x32_bf16 a[140:143], v[44:47], v[16:19], a[140:143]\n" \
"s_add_u32 m0, s38, 51200\n" \
"v_mfma_f32_16x16x32_bf16 a[144:147], v[48:51], v[16:19], a[144:147]\n" \
"ds_read_b128 v[72:75], v230 offset:4096\n" \
"v_mfma_f32_16x16x32_bf16 a[148:151], v[52:55], v[16:19], a[148:151]\n" \
"buffer_load_dwordx4 v227, s[20:23], s52 offen lds\n" \
"v_mfma_f32_16x16x32_bf16 a[152:155], v[56:59], v[16:19], a[152:155]\n" \
"ds_read_b128 v[76:79], v230 offset:6144\n" \
"v_mfma_f32_16x16x32_bf16 a[156:159], v[60:63], v[16:19], a[156:159]\n" \
"v_mfma_f32_16x16x32_bf16 a[160:163], v[32:35], v[20:23], a[160:163]\n" \
"ds_read_b128 v[80:83], v230 offset:8192\n" \
"v_mfma_f32_16x16x32_bf16 a[164:167], v[36:39], v[20:23], a[164:167]\n" \
"s_add_u32 s52, s40, 1081344\n" \
"v_mfma_f32_16x16x32_bf16 a[168:171], v[40:43], v[20:23], a[168:171]\n" \
"ds_read_b128 v[84:87], v230 offset:10240\n" \
"v_mfma_f32_16x16x32_bf16 a[172:175], v[44:47], v[20:23], a[172:175]\n" \
"s_add_u32 m0, s38, 100352\n" \
"v_mfma_f32_16x16x32_bf16 a[176:179], v[48:51], v[20:23], a[176:179]\n" \
"ds_read_b128 v[88:91], v230 offset:12288\n" \
"v_mfma_f32_16x16x32_bf16 a[180:183], v[52:55], v[20:23], a[180:183]\n" \
"buffer_load_dwordx4 v227, s[20:23], s52 offen lds\n" \
"v_mfma_f32_16x16x32_bf16 a[184:187], v[56:59], v[20:23], a[184:187]\n" \
"ds_read_b128 v[92:95], v230 offset:14336\n" \
"v_mfma_f32_16x16x32_bf16 a[188:191], v[60:63], v[20:23], a[188:191]\n" \
"s_add_u32 s80, s58, 1024\n" \
"v_mfma_f32_16x16x32_bf16 a[192:195], v[32:35], v[24:27], a[192:195]\n" \
"v_mfma_f32_16x16x32_bf16 a[196:199], v[36:39], v[24:27], a[196:199]\n" \
"s_add_u32 s52, s40, 311296\n" \
"buffer_load_dwordx4 v[168:171], v224, %[rs], s80 offen sc1\n" \
"v_mfma_f32_16x16x32_bf16 a[200:203], v[40:43], v[24:27], a[200:203]\n" \
"v_add_u32 v230, 0xffff0000, v230\n" \
"v_mfma_f32_16x16x32_bf16 a[204:207], v[44:47], v[24:27], a[204:207]\n" \
"s_add_u32 m0, s38, 52224\n" \
"v_mfma_f32_16x16x32_bf16 a[208:211], v[48:51], v[24:27], a[208:211]\n" \
"v_mfma_f32_16x16x32_bf16 a[212:215], v[52:55], v[24:27], a[212:215]\n" \
"buffer_load_dwordx4 v228, s[20:23], s52 offen lds\n" \
"v_mfma_f32_16x16x32_bf16 a[216:219], v[56:59], v[24:27], a[216:219]\n" \
"v_mfma_f32_16x16x32_bf16 a[220:223], v[60:63], v[24:27], a[220:223]\n" \
"v_mfma_f32_16x16x32_bf16 a[224:227], v[32:35], v[28:31], a[224:227]\n" \
"v_mfma_f32_16x16x32_bf16 a[228:231], v[36:39], v[28:31], a[228:231]\n" \
"s_add_u32 s52, s40, 1097728\n" \
"v_mfma_f32_16x16x32_bf16 a[232:235], v[40:43], v[28:31], a[232:235]\n" \
"v_mfma_f32_16x16x32_bf16 a[236:239], v[44:47], v[28:31], a[236:239]\n" \
"s_add_u32 m0, s38, 101376\n" \
"v_mfma_f32_16x16x32_bf16 a[240:243], v[48:51], v[28:31], a[240:243]\n" \
"v_mfma_f32_16x16x32_bf16 a[244:247], v[52:55], v[28:31], a[244:247]\n" \
"buffer_load_dwordx4 v228, s[20:23], s52 offen lds\n" \
"v_mfma_f32_16x16x32_bf16 a[248:251], v[56:59], v[28:31], a[248:251]\n" \
"v_mfma_f32_16x16x32_bf16 a[252:255], v[60:63], v[28:31], a[252:255]\n" \
"s_waitcnt vmcnt(37) lgkmcnt(0)\n" \
"s_barrier\n" \
"v_mfma_f32_16x16x32_bf16 a[0:3], v[96:99], v[64:67], a[0:3]\n" \
"ds_read_b128 v[0:3], v229\n" \
"v_xor_b32 v231, 0x38000, v231\n" \
"v_mfma_f32_16x16x32_bf16 a[4:7], v[100:103], v[64:67], a[4:7]\n" \
"s_add_u32 s52, s40, 524288\n" \
"v_mfma_f32_16x16x32_bf16 a[8:11], v[104:107], v[64:67], a[8:11]\n" \
"ds_read_b64_tr_b16 v[32:33], v231 offset:0\n" \
"v_mfma_f32_16x16x32_bf16 a[12:15], v[108:111], v[64:67], a[12:15]\n" \
"s_add_u32 m0, s38, 65536\n" \
"v_mfma_f32_16x16x32_bf16 a[16:19], v[112:115], v[64:67], a[16:19]\n" \
"ds_read_b64_tr_b16 v[34:35], v231 offset:2048\n" \
"v_xor_b32 v232, 0x38000, v232\n" \
"v_mfma_f32_16x16x32_bf16 a[20:23], v[116:119], v[64:67], a[20:23]\n" \
"buffer_load_dwordx4 v227, s[20:23], s52 offen lds\n" \
"v_mfma_f32_16x16x32_bf16 a[24:27], v[120:123], v[64:67], a[24:27]\n" \
"ds_read_b64_tr_b16 v[36:37], v232 offset:0\n" \
"v_mfma_f32_16x16x32_bf16 a[28:31], v[124:127], v[64:67], a[28:31]\n" \
"v_mfma_f32_16x16x32_bf16 a[32:35], v[96:99], v[68:71], a[32:35]\n" \
"ds_read_b64_tr_b16 v[38:39], v232 offset:2048\n" \
"v_xor_b32 v233, 0x38000, v233\n" \
"v_mfma_f32_16x16x32_bf16 a[36:39], v[100:103], v[68:71], a[36:39]\n" \
"s_add_u32 s52, s40, 1310720\n" \
"v_mfma_f32_16x16x32_bf16 a[40:43], v[104:107], v[68:71], a[40:43]\n" \
"ds_read_b64_tr_b16 v[40:41], v233 offset:0\n" \
"v_mfma_f32_16x16x32_bf16 a[44:47], v[108:111], v[68:71], a[44:47]\n" \
"s_add_u32 m0, s38, 114688\n" \
"v_mfma_f32_16x16x32_bf16 a[48:51], v[112:115], v[68:71], a[48:51]\n" \
"ds_read_b64_tr_b16 v[42:43], v233 offset:2048\n" \
"v_xor_b32 v234, 0x38000, v234\n" \
"v_mfma_f32_16x16x32_bf16 a[52:55], v[116:119], v[68:71], a[52:55]\n" \
"buffer_load_dwordx4 v227, s[20:23], s52 offen lds\n" \
"v_mfma_f32_16x16x32_bf16 a[56:59], v[120:123], v[68:71], a[56:59]\n" \
"ds_read_b64_tr_b16 v[44:45], v234 offset:0\n" \
"v_mfma_f32_16x16x32_bf16 a[60:63], v[124:127], v[68:71], a[60:63]\n" \
"s_add_u32 s80, s71, 1024\n" \
"v_mfma_f32_16x16x32_bf16 a[64:67], v[96:99], v[72:75], a[64:67]\n" \
"ds_read_b64_tr_b16 v[46:47], v234 offset:2048\n" \
"v_xor_b32 v235, 0x38000, v235\n" \
"v_mfma_f32_16x16x32_bf16 a[68:71], v[100:103], v[72:75], a[68:71]\n" \
"s_add_u32 s52, s40, 540672\n" \
"buffer_load_dwordx4 v[172:175], v224, %[rs], s80 offen sc1\n" \
"v_mfma_f32_16x16x32_bf16 a[72:75], v[104:107], v[72:75], a[72:75]\n" \
"ds_read_b64_tr_b16 v[48:49], v235 offset:0\n" \
"v_mfma_f32_16x16x32_bf16 a[76:79], v[108:111], v[72:75], a[76:79]\n" \
"s_add_u32 m0, s38, 66560\n" \
"v_mfma_f32_16x16x32_bf16 a[80:83], v[112:115], v[72:75], a[80:83]\n" \
"ds_read_b64_tr_b16 v[50:51], v235 offset:2048\n" \
"v_xor_b32 v236, 0x38000, v236\n" \
"v_mfma_f32_16x16x32_bf16 a[84:87], v[116:119], v[72:75], a[84:87]\n" \
"buffer_load_dwordx4 v228, s[20:23], s52 offen lds\n" \
"v_mfma_f32_16x16x32_bf16 a[88:91], v[120:123], v[72:75], a[88:91]\n" \
"ds_read_b64_tr_b16 v[52:53], v236 offset:0\n" \
"v_mfma_f32_16x16x32_bf16 a[92:95], v[124:127], v[72:75], a[92:95]\n" \
"v_mfma_f32_16x16x32_bf16 a[96:99], v[96:99], v[76:79], a[96:99]\n" \
"ds_read_b64_tr_b16 v[54:55], v236 offset:2048\n" \
"v_xor_b32 v237, 0x38000, v237\n" \
"v_mfma_f32_16x16x32_bf16 a[100:103], v[100:103], v[76:79], a[100:103]\n" \
"s_add_u32 s52, s40, 1327104\n" \
"v_mfma_f32_16x16x32_bf16 a[104:107], v[104:107], v[76:79], a[104:107]\n" \
"ds_read_b64_tr_b16 v[56:57], v237 offset:0\n" \
"v_mfma_f32_16x16x32_bf16 a[108:111], v[108:111], v[76:79], a[108:111]\n" \
"s_add_u32 m0, s38, 115712\n" \
"v_mfma_f32_16x16x32_bf16 a[112:115], v[112:115], v[76:79], a[112:115]\n" \
"ds_read_b64_tr_b16 v[58:59], v237 offset:2048\n" \
"v_xor_b32 v238, 0x38000, v238\n" \
"v_mfma_f32_16x16x32_bf16 a[116:119], v[116:119], v[76:79], a[116:119]\n" \
"buffer_load_dwordx4 v228, s[20:23], s52 offen lds\n" \
"v_mfma_f32_16x16x32_bf16 a[120:123], v[120:123], v[76:79], a[120:123]\n" \
"ds_read_b64_tr_b16 v[60:61], v238 offset:0\n" \
"v_mfma_f32_16x16x32_bf16 a[124:127], v[124:127], v[76:79], a[124:127]\n" \
"s_add_u32 s80, s58, 2048\n" \
"v_mfma_f32_16x16x32_bf16 a[128:131], v[96:99], v[80:83], a[128:131]\n" \
"ds_read_b64_tr_b16 v[62:63], v238 offset:2048\n" \
"v_mfma_f32_16x16x32_bf16 a[132:135], v[100:103], v[80:83], a[132:135]\n" \
"s_add_u32 s52, s40, 557056\n" \
"buffer_load_dwordx4 v[176:179], v224, %[rs], s80 offen sc1\n" \
"v_mfma_f32_16x16x32_bf16 a[136:139], v[104:107], v[80:83], a[136:139]\n" \
"ds_read_b128 v[4:7], v229 offset:2048\n" \
"v_mfma_f32_16x16x32_bf16 a[140:143], v[108:111], v[80:83], a[140:143]\n" \
"s_add_u32 m0, s38, 67584\n" \
"v_mfma_f32_16x16x32_bf16 a[144:147], v[112:115], v[80:83], a[144:147]\n" \
"ds_read_b128 v[8:11], v229 offset:4096\n" \
"v_mfma_f32_16x16x32_bf16 a[148:151], v[116:119], v[80:83], a[148:151]\n" \
"buffer_load_dwordx4 v227, s[20:23], s52 offen lds\n" \
"v_mfma_f32_16x16x32_bf16 a[152:155], v[120:123], v[80:83], a[152:155]\n" \
"ds_read_b128 v[12:15], v229 offset:6144\n" \
"v_mfma_f32_16x16x32_bf16 a[156:159], v[124:127], v[80:83], a[156:159]\n" \
"v_mfma_f32_16x16x32_bf16 a[160:163], v[96:99], v[84:87], a[160:163]\n" \
"ds_read_b128 v[16:19], v229 offset:8192\n" \
"v_mfma_f32_16x16x32_bf16 a[164:167], v[100:103], v[84:87], a[164:167]\n" \
"s_add_u32 s52, s40, 1343488\n" \
"v_mfma_f32_16x16x32_bf16 a[168:171], v[104:107], v[84:87], a[168:171]\n" \
"ds_read_b128 v[20:23], v229 offset:10240\n" \
"v_mfma_f32_16x16x32_bf16 a[172:175], v[108:111], v[84:87], a[172:175]\n" \
"s_add_u32 m0, s38, 116736\n" \
"v_mfma_f32_16x16x32_bf16 a[176:179], v[112:115], v[84:87], a[176:179]\n" \
"ds_read_b128 v[24:27], v229 offset:12288\n" \
"v_mfma_f32_16x16x32_bf16 a[180:183], v[116:119], v[84:87], a[180:183]\n" \
"buffer_load_dwordx4 v227, s[20:23], s52 offen lds\n" \
"v_mfma_f32_16x16x32_bf16 a[184:187], v[120:123], v[84:87], a[184:187]\n" \
"ds_read_b128 v[28:31], v229 offset:14336\n" \
"v_mfma_f32_16x16x32_bf16 a[188:191], v[124:127], v[84:87], a[188:191]\n" \
"s_add_u32 s80, s71, 2048\n" \
"v_mfma_f32_16x16x32_bf16 a[192:195], v[96:99], v[88:91], a[192:195]\n" \
"v_mfma_f32_16x16x32_bf16 a[196:199], v[100:103], v[88:91], a[196:199]\n" \
"s_add_u32 s52, s40, 573440\n" \
"buffer_load_dwordx4 v[180:183], v224, %[rs], s80 offen sc1\n" \
"v_mfma_f32_16x16x32_bf16 a[200:203], v[104:107], v[88:91], a[200:203]\n" \
"v_mfma_f32_16x16x32_bf16 a[204:207], v[108:111], v[88:91], a[204:207]\n" \
"s_add_u32 m0, s38, 68608\n" \
"v_mfma_f32_16x16x32_bf16 a[208:211], v[112:115], v[88:91], a[208:211]\n" \
"v_mfma_f32_16x16x32_bf16 a[212:215], v[116:119], v[88:91], a[212:215]\n" \
"buffer_load_dwordx4 v228, s[20:23], s52 offen lds\n" \
"v_mfma_f32_16x16x32_bf16 a[216:219], v[120:123], v[88:91], a[216:219]\n" \
"v_mfma_f32_16x16x32_bf16 a[220:223], v[124:127], v[88:91], a[220:223]\n" \
"v_mfma_f32_16x16x32_bf16 a[224:227], v[96:99], v[92:95], a[224:227]\n" \
"v_mfma_f32_16x16x32_bf16 a[228:231], v[100:103], v[92:95], a[228:231]\n" \
"s_add_u32 s52, s40, 1359872\n" \
"v_mfma_f32_16x16x32_bf16 a[232:235], v[104:107], v[92:95], a[232:235]\n" \
"v_mfma_f32_16x16x32_bf16 a[236:239], v[108:111], v[92:95], a[236:239]\n" \
"s_add_u32 m0, s38, 117760\n" \
"v_mfma_f32_16x16x32_bf16 a[240:243], v[112:115], v[92:95], a[240:243]\n" \
"v_mfma_f32_16x16x32_bf16 a[244:247], v[116:119], v[92:95], a[244:247]\n" \
"buffer_load_dwordx4 v228, s[20:23], s52 offen lds\n" \
"v_mfma_f32_16x16x32_bf16 a[248:251], v[120:123], v[92:95], a[248:251]\n" \
"v_mfma_f32_16x16x32_bf16 a[252:255], v[124:127], v[92:95], a[252:255]\n" \
"s_waitcnt vmcnt(22) lgkmcnt(0)\n" \
"s_barrier\n" \
"v_mfma_f32_16x16x32_bf16 a[0:3], v[32:35], v[0:3], a[0:3]\n" \
"ds_read_b128 v[64:67], v230\n" \
"v_mfma_f32_16x16x32_bf16 a[4:7], v[36:39], v[0:3], a[4:7]\n" \
"s_add_u32 s52, s40, 786432\n" \
"v_mfma_f32_16x16x32_bf16 a[8:11], v[40:43], v[0:3], a[8:11]\n" \
"ds_read_b64_tr_b16 v[96:97], v231 offset:16384\n" \
"v_mfma_f32_16x16x32_bf16 a[12:15], v[44:47], v[0:3], a[12:15]\n" \
"s_add_u32 m0, s38, 81920\n" \
"v_mfma_f32_16x16x32_bf16 a[16:19], v[48:51], v[0:3], a[16:19]\n" \
"ds_read_b64_tr_b16 v[98:99], v231 offset:18432\n" \
"v_mfma_f32_16x16x32_bf16 a[20:23], v[52:55], v[0:3], a[20:23]\n" \
"buffer_load_dwordx4 v227, s[20:23], s52 offen lds\n" \
"v_lshl_or_b32 v210, v133, 16, v132\n" \
"v_lshl_or_b32 v211, v135, 16, v134\n" \
"v_mfma_f32_16x16x32_bf16 a[24:27], v[56:59], v[0:3], a[24:27]\n" \
"ds_read_b64_tr_b16 v[100:101], v232 offset:16384\n" \
"v_mfma_f32_16x16x32_bf16 a[28:31], v[60:63], v[0:3], a[28:31]\n" \
"v_lshl_or_b32 v208, v129, 16, v128\n" \
"v_lshl_or_b32 v209, v131, 16, v130\n" \
"v_mfma_f32_16x16x32_bf16 a[32:35], v[32:35], v[4:7], a[32:35]\n" \
"ds_read_b64_tr_b16 v[102:103], v232 offset:18432\n" \
"v_mfma_f32_16x16x32_bf16 a[36:39], v[36:39], v[4:7], a[36:39]\n" \
"s_add_u32 s52, s40, 1572864\n" \
"buffer_store_dwordx4 v[208:211], v224, %[rs], s76 offen sc1\n" \
"v_mfma_f32_16x16x32_bf16 a[40:43], v[40:43], v[4:7], a[40:43]\n" \
"ds_read_b64_tr_b16 v[104:105], v233 offset:16384\n" \
"v_mfma_f32_16x16x32_bf16 a[44:47], v[44:47], v[4:7], a[44:47]\n" \
"s_add_u32 m0, s38, 131072\n" \
"v_mfma_f32_16x16x32_bf16 a[48:51], v[48:51], v[4:7], a[48:51]\n" \
"ds_read_b64_tr_b16 v[106:107], v233 offset:18432\n" \
"v_mfma_f32_16x16x32_bf16 a[52:55], v[52:55], v[4:7], a[52:55]\n" \
"buffer_load_dwordx4 v227, s[20:23], s52 offen lds\n" \
"v_mfma_f32_16x16x32_bf16 a[56:59], v[56:59], v[4:7], a[56:59]\n" \
"ds_read_b64_tr_b16 v[108:109], v234 offset:16384\n" \
"v_mfma_f32_16x16x32_bf16 a[60:63], v[60:63], v[4:7], a[60:63]\n" \
"s_add_u32 s80, s58, 3072\n" \
"v_mfma_f32_16x16x32_bf16 a[64:67], v[32:35], v[8:11], a[64:67]\n" \
"ds_read_b64_tr_b16 v[110:111], v234 offset:18432\n" \
"v_mfma_f32_16x16x32_bf16 a[68:71], v[36:39], v[8:11], a[68:71]\n" \
"s_add_u32 s52, s40, 802816\n" \
"buffer_load_dwordx4 v[184:187], v224, %[rs], s80 offen sc1\n" \
"v_mfma_f32_16x16x32_bf16 a[72:75], v[40:43], v[8:11], a[72:75]\n" \
"ds_read_b64_tr_b16 v[112:113], v235 offset:16384\n" \
"v_mfma_f32_16x16x32_bf16 a[76:79], v[44:47], v[8:11], a[76:79]\n" \
"s_add_u32 m0, s38, 82944\n" \
"v_mfma_f32_16x16x32_bf16 a[80:83], v[48:51], v[8:11], a[80:83]\n" \
"ds_read_b64_tr_b16 v[114:115], v235 offset:18432\n" \
"v_mfma_f32_16x16x32_bf16 a[84:87], v[52:55], v[8:11], a[84:87]\n" \
"buffer_load_dwordx4 v228, s[20:23], s52 offen lds\n" \
"v_lshl_or_b32 v214, v141, 16, v140\n" \
"v_lshl_or_b32 v215, v143, 16, v142\n" \
"v_mfma_f32_16x16x32_bf16 a[88:91], v[56:59], v[8:11], a[88:91]\n" \
"ds_read_b64_tr_b16 v[116:117], v236 offset:16384\n" \
"v_mfma_f32_16x16x32_bf16 a[92:95], v[60:63], v[8:11], a[92:95]\n" \
"v_lshl_or_b32 v212, v137, 16, v136\n" \
"v_lshl_or_b32 v213, v139, 16, v138\n" \
"v_mfma_f32_16x16x32_bf16 a[96:99], v[32:35], v[12:15], a[96:99]\n" \
"ds_read_b64_tr_b16 v[118:119], v236 offset:18432\n" \
"v_mfma_f32_16x16x32_bf16 a[100:103], v[36:39], v[12:15], a[100:103]\n" \
"s_add_u32 s52, s40, 1589248\n" \
"buffer_store_dwordx4 v[212:215], v224, %[rs], s77 offen sc1\n" \
"v_mfma_f32_16x16x32_bf16 a[104:107], v[40:43], v[12:15], a[104:107]\n" \
"ds_read_b64_tr_b16 v[120:121], v237 offset:16384\n" \
"v_mfma_f32_16x16x32_bf16 a[108:111], v[44:47], v[12:15], a[108:111]\n" \
"s_add_u32 m0, s38, 132096\n" \
"v_mfma_f32_16x16x32_bf16 a[112:115], v[48:51], v[12:15], a[112:115]\n" \
"ds_read_b64_tr_b16 v[122:123], v237 offset:18432\n" \
"v_mfma_f32_16x16x32_bf16 a[116:119], v[52:55], v[12:15], a[116:119]\n" \
"buffer_load_dwordx4 v228, s[20:23], s52 offen lds\n" \
"v_mfma_f32_16x16x32_bf16 a[120:123], v[56:59], v[12:15], a[120:123]\n" \
"ds_read_b64_tr_b16 v[124:125], v238 offset:16384\n" \
"v_mfma_f32_16x16x32_bf16 a[124:127], v[60:63], v[12:15], a[124:127]\n" \
"s_add_u32 s80, s71, 3072\n" \
"v_mfma_f32_16x16x32_bf16 a[128:131], v[32:35], v[16:19], a[128:131]\n" \
"ds_read_b64_tr_b16 v[126:127], v238 offset:18432\n" \
"v_mfma_f32_16x16x32_bf16 a[132:135], v[36:39], v[16:19], a[132:135]\n" \
"s_add_u32 s52, s40, 819200\n" \
"buffer_load_dwordx4 v[188:191], v224, %[rs], s80 offen sc1\n" \
"v_mfma_f32_16x16x32_bf16 a[136:139], v[40:43], v[16:19], a[136:139]\n" \
"ds_read_b128 v[68:71], v230 offset:2048\n" \
"v_mfma_f32_16x16x32_bf16 a[140:143], v[44:47], v[16:19], a[140:143]\n" \
"s_add_u32 m0, s38, 83968\n" \
"v_mfma_f32_16x16x32_bf16 a[144:147], v[48:51], v[16:19], a[144:147]\n" \
"ds_read_b128 v[72:75], v230 offset:4096\n" \
"v_mfma_f32_16x16x32_bf16 a[148:151], v[52:55], v[16:19], a[148:151]\n" \
"buffer_load_dwordx4 v227, s[20:23], s52 offen lds\n" \
"v_lshl_or_b32 v218, v149, 16, v148\n" \
"v_lshl_or_b32 v219, v151, 16, v150\n" \
"v_mfma_f32_16x16x32_bf16 a[152:155], v[56:59], v[16:19], a[152:155]\n" \
"ds_read_b128 v[76:79], v230 offset:6144\n" \
"v_mfma_f32_16x16x32_bf16 a[156:159], v[60:63], v[16:19], a[156:159]\n" \
"v_lshl_or_b32 v216, v145, 16, v144\n" \
"v_lshl_or_b32 v217, v147, 16, v146\n" \
"v_mfma_f32_16x16x32_bf16 a[160:163], v[32:35], v[20:23], a[160:163]\n" \
"ds_read_b128 v[80:83], v230 offset:8192\n" \
"v_mfma_f32_16x16x32_bf16 a[164:167], v[36:39], v[20:23], a[164:167]\n" \
"s_add_u32 s52, s40, 1605632\n" \
"buffer_store_dwordx4 v[216:219], v224, %[rs], s78 offen sc1\n" \
"v_mfma_f32_16x16x32_bf16 a[168:171], v[40:43], v[20:23], a[168:171]\n" \
"ds_read_b128 v[84:87], v230 offset:10240\n" \
"v_mfma_f32_16x16x32_bf16 a[172:175], v[44:47], v[20:23], a[172:175]\n" \
"s_add_u32 m0, s38, 133120\n" \
"v_mfma_f32_16x16x32_bf16 a[176:179], v[48:51], v[20:23], a[176:179]\n" \
"ds_read_b128 v[88:91], v230 offset:12288\n" \
"v_mfma_f32_16x16x32_bf16 a[180:183], v[52:55], v[20:23], a[180:183]\n" \
"buffer_load_dwordx4 v227, s[20:23], s52 offen lds\n" \
"v_mfma_f32_16x16x32_bf16 a[184:187], v[56:59], v[20:23], a[184:187]\n" \
"ds_read_b128 v[92:95], v230 offset:14336\n" \
"v_mfma_f32_16x16x32_bf16 a[188:191], v[60:63], v[20:23], a[188:191]\n" \
"s_add_u32 s80, s58, 4096\n" \
"v_mfma_f32_16x16x32_bf16 a[192:195], v[32:35], v[24:27], a[192:195]\n" \
"v_mfma_f32_16x16x32_bf16 a[196:199], v[36:39], v[24:27], a[196:199]\n" \
"s_add_u32 s52, s40, 835584\n" \
"buffer_load_dwordx4 v[192:195], v224, %[rs], s80 offen sc1\n" \
"v_mfma_f32_16x16x32_bf16 a[200:203], v[40:43], v[24:27], a[200:203]\n" \
"v_lshl_or_b32 v220, v153, 16, v152\n" \
"v_lshl_or_b32 v221, v155, 16, v154\n" \
"v_mfma_f32_16x16x32_bf16 a[204:207], v[44:47], v[24:27], a[204:207]\n" \
"s_add_u32 m0, s38, 84992\n" \
"v_mfma_f32_16x16x32_bf16 a[208:211], v[48:51], v[24:27], a[208:211]\n" \
"v_lshl_or_b32 v222, v157, 16, v156\n" \
"v_lshl_or_b32 v223, v159, 16, v158\n" \
"v_mfma_f32_16x16x32_bf16 a[212:215], v[52:55], v[24:27], a[212:215]\n" \
"buffer_load_dwordx4 v228, s[20:23], s52 offen lds\n" \
"v_mfma_f32_16x16x32_bf16 a[216:219], v[56:59], v[24:27], a[216:219]\n" \
"v_mfma_f32_16x16x32_bf16 a[220:223], v[60:63], v[24:27], a[220:223]\n" \
"v_mfma_f32_16x16x32_bf16 a[224:227], v[32:35], v[28:31], a[224:227]\n" \
"v_mfma_f32_16x16x32_bf16 a[228:231], v[36:39], v[28:31], a[228:231]\n" \
"s_add_u32 s52, s40, 1622016\n" \
"buffer_store_dwordx4 v[220:223], v224, %[rs], s79 offen sc1\n" \
"v_mfma_f32_16x16x32_bf16 a[232:235], v[40:43], v[28:31], a[232:235]\n" \
"v_mfma_f32_16x16x32_bf16 a[236:239], v[44:47], v[28:31], a[236:239]\n" \
"s_add_u32 m0, s38, 134144\n" \
"v_mfma_f32_16x16x32_bf16 a[240:243], v[48:51], v[28:31], a[240:243]\n" \
"v_mfma_f32_16x16x32_bf16 a[244:247], v[52:55], v[28:31], a[244:247]\n" \
"buffer_load_dwordx4 v228, s[20:23], s52 offen lds\n" \
"v_mfma_f32_16x16x32_bf16 a[248:251], v[56:59], v[28:31], a[248:251]\n" \
"v_mfma_f32_16x16x32_bf16 a[252:255], v[60:63], v[28:31], a[252:255]\n" \
"s_waitcnt vmcnt(63) lgkmcnt(0)\n" \
"s_barrier\n" \
"v_mfma_f32_16x16x32_bf16 a[0:3], v[96:99], v[64:67], a[0:3]\n" \
"v_mfma_f32_16x16x32_bf16 a[4:7], v[100:103], v[64:67], a[4:7]\n" \
"s_add_u32 s52, s40, 1835008\n" \
"v_mfma_f32_16x16x32_bf16 a[8:11], v[104:107], v[64:67], a[8:11]\n" \
"v_add_u32 v231, s82, v231\n" \
"v_mfma_f32_16x16x32_bf16 a[12:15], v[108:111], v[64:67], a[12:15]\n" \
"s_add_u32 m0, s38, 147456\n" \
"v_mfma_f32_16x16x32_bf16 a[16:19], v[112:115], v[64:67], a[16:19]\n" \
"v_add_u32 v232, s82, v232\n" \
"v_mfma_f32_16x16x32_bf16 a[20:23], v[116:119], v[64:67], a[20:23]\n" \
"buffer_load_dwordx4 v227, s[20:23], s52 offen lds\n" \
"v_mfma_f32_16x16x32_bf16 a[24:27], v[120:123], v[64:67], a[24:27]\n" \
"v_add_u32 v233, s82, v233\n" \
"v_accvgpr_read_b32 v208, a0\n" \
"v_accvgpr_read_b32 v209, a1\n" \
"v_mfma_f32_16x16x32_bf16 a[28:31], v[124:127], v[64:67], a[28:31]\n" \
"v_accvgpr_read_b32 v210, a2\n" \
"v_accvgpr_read_b32 v211, a3\n" \
"v_cvt_pk_bf16_f32 v212, v208, v209\n" \
"v_mfma_f32_16x16x32_bf16 a[32:35], v[96:99], v[68:71], a[32:35]\n" \
"v_add_u32 v234, s82, v234\n" \
"v_cvt_pk_bf16_f32 v213, v210, v211\n" \
"v_accvgpr_read_b32 v208, a4\n" \
"v_mfma_f32_16x16x32_bf16 a[36:39], v[100:103], v[68:71], a[36:39]\n" \
"s_add_u32 s52, s40, 2097152\n" \
"v_accvgpr_read_b32 v209, a5\n" \
"v_accvgpr_read_b32 v210, a6\n" \
"v_mfma_f32_16x16x32_bf16 a[40:43], v[104:107], v[68:71], a[40:43]\n" \
"v_add_u32 v235, s82, v235\n" \
"v_accvgpr_read_b32 v211, a7\n" \
"v_cvt_pk_bf16_f32 v214, v208, v209\n" \
"v_mfma_f32_16x16x32_bf16 a[44:47], v[108:111], v[68:71], a[44:47]\n" \
"s_add_u32 m0, s38, 0\n" \
"v_cvt_pk_bf16_f32 v215, v210, v211\n" \
"s_add_u32 s83, s55, 0\n" \
"buffer_store_dwordx4 v[212:215], v224, %[rs], s83 offen offset:0 sc1\n" \
"v_mfma_f32_16x16x32_bf16 a[48:51], v[112:115], v[68:71], a[48:51]\n" \
"v_add_u32 v236, s82, v236\n" \
"v_accvgpr_read_b32 v208, a8\n" \
"v_accvgpr_read_b32 v209, a9\n" \
"v_mfma_f32_16x16x32_bf16 a[52:55], v[116:119], v[68:71], a[52:55]\n" \
"buffer_load_dwordx4 v227, s[20:23], s52 offen lds\n" \
"v_accvgpr_read_b32 v210, a10\n" \
"v_accvgpr_read_b32 v211, a11\n" \
"v_mfma_f32_16x16x32_bf16 a[56:59], v[120:123], v[68:71], a[56:59]\n" \
"v_add_u32 v237, s82, v237\n" \
"v_cvt_pk_bf16_f32 v216, v208, v209\n" \
"v_cvt_pk_bf16_f32 v217, v210, v211\n" \
"v_mfma_f32_16x16x32_bf16 a[60:63], v[124:127], v[68:71], a[60:63]\n" \
"s_add_u32 s80, s71, 4096\n" \
"v_accvgpr_read_b32 v208, a12\n" \
"v_accvgpr_read_b32 v209, a13\n" \
"v_mfma_f32_16x16x32_bf16 a[64:67], v[96:99], v[72:75], a[64:67]\n" \
"v_add_u32 v238, s82, v238\n" \
"v_accvgpr_read_b32 v210, a14\n" \
"v_accvgpr_read_b32 v211, a15\n" \
"v_mfma_f32_16x16x32_bf16 a[68:71], v[100:103], v[72:75], a[68:71]\n" \
"s_add_u32 s52, s40, 1851392\n" \
"buffer_load_dwordx4 v[196:199], v224, %[rs], s80 offen sc1\n" \
"v_cvt_pk_bf16_f32 v218, v208, v209\n" \
"v_mfma_f32_16x16x32_bf16 a[72:75], v[104:107], v[72:75], a[72:75]\n" \
"v_cvt_pk_bf16_f32 v219, v210, v211\n" \
"v_accvgpr_read_b32 v208, a16\n" \
"v_accvgpr_read_b32 v209, a17\n" \
"v_mfma_f32_16x16x32_bf16 a[76:79], v[108:111], v[72:75], a[76:79]\n" \
"s_add_u32 m0, s38, 148480\n" \
"v_accvgpr_read_b32 v210, a18\n" \
"v_accvgpr_read_b32 v211, a19\n" \
"v_mfma_f32_16x16x32_bf16 a[80:83], v[112:115], v[72:75], a[80:83]\n" \
"v_cvt_pk_bf16_f32 v220, v208, v209\n" \
"v_cvt_pk_bf16_f32 v221, v210, v211\n" \
"v_accvgpr_read_b32 v208, a20\n" \
"v_mfma_f32_16x16x32_bf16 a[84:87], v[116:119], v[72:75], a[84:87]\n" \
"buffer_load_dwordx4 v228, s[20:23], s52 offen lds\n" \
"v_accvgpr_read_b32 v209, a21\n" \
"v_accvgpr_read_b32 v210, a22\n" \
"v_mfma_f32_16x16x32_bf16 a[88:91], v[120:123], v[72:75], a[88:91]\n" \
"v_accvgpr_read_b32 v211, a23\n" \
"v_cvt_pk_bf16_f32 v222, v208, v209\n" \
"v_cvt_pk_bf16_f32 v223, v210, v211\n" \
"v_mfma_f32_16x16x32_bf16 a[92:95], v[124:127], v[72:75], a[92:95]\n" \
"v_accvgpr_read_b32 v208, a24\n" \
"v_accvgpr_read_b32 v209, a25\n" \
"v_accvgpr_read_b32 v210, a26\n" \
"v_mfma_f32_16x16x32_bf16 a[96:99], v[96:99], v[76:79], a[96:99]\n" \
"v_accvgpr_read_b32 v211, a27\n" \
"v_cvt_pk_bf16_f32 v212, v208, v209\n" \
"v_cvt_pk_bf16_f32 v213, v210, v211\n" \
"v_mfma_f32_16x16x32_bf16 a[100:103], v[100:103], v[76:79], a[100:103]\n" \
"s_add_u32 s52, s40, 2113536\n" \
"buffer_store_dwordx4 v[216:219], v224, %[rs], s83 offen offset:2048 sc1\n" \
"v_accvgpr_read_b32 v208, a28\n" \
"v_mfma_f32_16x16x32_bf16 a[104:107], v[104:107], v[76:79], a[104:107]\n" \
"v_accvgpr_read_b32 v209, a29\n" \
"v_accvgpr_read_b32 v210, a30\n" \
"v_accvgpr_read_b32 v211, a31\n" \
"v_mfma_f32_16x16x32_bf16 a[108:111], v[108:111], v[76:79], a[108:111]\n" \
"s_add_u32 m0, s38, 1024\n" \
"v_cvt_pk_bf16_f32 v214, v208, v209\n" \
"v_cvt_pk_bf16_f32 v215, v210, v211\n" \
"v_mfma_f32_16x16x32_bf16 a[112:115], v[112:115], v[76:79], a[112:115]\n" \
"v_accvgpr_read_b32 v208, a32\n" \
"v_accvgpr_read_b32 v209, a33\n" \
"v_accvgpr_read_b32 v210, a34\n" \
"v_mfma_f32_16x16x32_bf16 a[116:119], v[116:119], v[76:79], a[116:119]\n" \
"buffer_load_dwordx4 v228, s[20:23], s52 offen lds\n" \
"v_accvgpr_read_b32 v211, a35\n" \
"v_cvt_pk_bf16_f32 v216, v208, v209\n" \
"v_mfma_f32_16x16x32_bf16 a[120:123], v[120:123], v[76:79], a[120:123]\n" \
"v_cvt_pk_bf16_f32 v217, v210, v211\n" \
"v_accvgpr_read_b32 v208, a36\n" \
"v_accvgpr_read_b32 v209, a37\n" \
"v_mfma_f32_16x16x32_bf16 a[124:127], v[124:127], v[76:79], a[124:127]\n" \
"s_add_u32 s80, s58, 5120\n" \
"v_accvgpr_read_b32 v210, a38\n" \
"v_accvgpr_read_b32 v211, a39\n" \
"v_mfma_f32_16x16x32_bf16 a[128:131], v[96:99], v[80:83], a[128:131]\n" \
"v_cvt_pk_bf16_f32 v218, v208, v209\n" \
"v_cvt_pk_bf16_f32 v219, v210, v211\n" \
"v_mfma_f32_16x16x32_bf16 a[132:135], v[100:103], v[80:83], a[132:135]\n" \
"s_add_u32 s52, s40, 1867776\n" \
"buffer_load_dwordx4 v[200:203], v224, %[rs], s80 offen sc1\n" \
"v_mfma_f32_16x16x32_bf16 a[136:139], v[104:107], v[80:83], a[136:139]\n" \
"v_mfma_f32_16x16x32_bf16 a[140:143], v[108:111], v[80:83], a[140:143]\n" \
"s_add_u32 m0, s38, 149504\n" \
"v_mfma_f32_16x16x32_bf16 a[144:147], v[112:115], v[80:83], a[144:147]\n" \
"v_mfma_f32_16x16x32_bf16 a[148:151], v[116:119], v[80:83], a[148:151]\n" \
"buffer_load_dwordx4 v227, s[20:23], s52 offen lds\n" \
"v_mfma_f32_16x16x32_bf16 a[152:155], v[120:123], v[80:83], a[152:155]\n" \
"v_mfma_f32_16x16x32_bf16 a[156:159], v[124:127], v[80:83], a[156:159]\n" \
"v_mfma_f32_16x16x32_bf16 a[160:163], v[96:99], v[84:87], a[160:163]\n" \
"v_mfma_f32_16x16x32_bf16 a[164:167], v[100:103], v[84:87], a[164:167]\n" \
"s_add_u32 s52, s40, 2129920\n" \
"s_add_u32 s83, s55, 4096\n" \
"buffer_store_dwordx4 v[220:223], v224, %[rs], s83 offen offset:0 sc1\n" \
"v_mfma_f32_16x16x32_bf16 a[168:171], v[104:107], v[84:87], a[168:171]\n" \
"v_accvgpr_read_b32 v208, a40\n" \
"v_accvgpr_read_b32 v209, a41\n" \
"v_accvgpr_read_b32 v210, a42\n" \
"v_mfma_f32_16x16x32_bf16 a[172:175], v[108:111], v[84:87], a[172:175]\n" \
"s_add_u32 m0, s38, 2048\n" \
"v_accvgpr_read_b32 v211, a43\n" \
"v_cvt_pk_bf16_f32 v220, v208, v209\n" \
"v_mfma_f32_16x16x32_bf16 a[176:179], v[112:115], v[84:87], a[176:179]\n" \
"v_cvt_pk_bf16_f32 v221, v210, v211\n" \
"v_accvgpr_read_b32 v208, a44\n" \
"v_accvgpr_read_b32 v209, a45\n" \
"v_mfma_f32_16x16x32_bf16 a[180:183], v[116:119], v[84:87], a[180:183]\n" \
"buffer_load_dwordx4 v227, s[20:23], s52 offen lds\n" \
"v_accvgpr_read_b32 v210, a46\n" \
"v_accvgpr_read_b32 v211, a47\n" \
"v_mfma_f32_16x16x32_bf16 a[184:187], v[120:123], v[84:87], a[184:187]\n" \
"v_cvt_pk_bf16_f32 v222, v208, v209\n" \
"v_cvt_pk_bf16_f32 v223, v210, v211\n" \
"v_mfma_f32_16x16x32_bf16 a[188:191], v[124:127], v[84:87], a[188:191]\n" \
"s_add_u32 s80, s71, 5120\n" \
"v_mfma_f32_16x16x32_bf16 a[192:195], v[96:99], v[88:91], a[192:195]\n" \
"v_mfma_f32_16x16x32_bf16 a[196:199], v[100:103], v[88:91], a[196:199]\n" \
"s_add_u32 s52, s40, 1884160\n" \
"buffer_load_dwordx4 v[204:207], v224, %[rs], s80 offen sc1\n" \
"v_mfma_f32_16x16x32_bf16 a[200:203], v[104:107], v[88:91], a[200:203]\n" \
"ds_read_b64_tr_b16 v[128:129], v231 offset:0\n" \
"v_mfma_f32_16x16x32_bf16 a[204:207], v[108:111], v[88:91], a[204:207]\n" \
"s_add_u32 m0, s38, 150528\n" \
"ds_read_b64_tr_b16 v[130:131], v231 offset:2048\n" \
"v_mfma_f32_16x16x32_bf16 a[208:211], v[112:115], v[88:91], a[208:211]\n" \
"ds_read_b64_tr_b16 v[132:133], v232 offset:0\n" \
"v_mfma_f32_16x16x32_bf16 a[212:215], v[116:119], v[88:91], a[212:215]\n" \
"buffer_load_dwordx4 v228, s[20:23], s52 offen lds\n" \
"ds_read_b64_tr_b16 v[134:135], v232 offset:2048\n" \
"v_mfma_f32_16x16x32_bf16 a[216:219], v[120:123], v[88:91], a[216:219]\n" \
"ds_read_b64_tr_b16 v[136:137], v233 offset:0\n" \
"v_mfma_f32_16x16x32_bf16 a[220:223], v[124:127], v[88:91], a[220:223]\n" \
"ds_read_b64_tr_b16 v[138:139], v233 offset:2048\n" \
"v_mfma_f32_16x16x32_bf16 a[224:227], v[96:99], v[92:95], a[224:227]\n" \
"ds_read_b64_tr_b16 v[140:141], v234 offset:0\n" \
"v_mfma_f32_16x16x32_bf16 a[228:231], v[100:103], v[92:95], a[228:231]\n" \
"s_add_u32 s52, s40, 2146304\n" \
"ds_read_b64_tr_b16 v[142:143], v234 offset:2048\n" \
"buffer_store_dwordx4 v[212:215], v224, %[rs], s83 offen offset:2048 sc1\n" \
"v_mfma_f32_16x16x32_bf16 a[232:235], v[104:107], v[92:95], a[232:235]\n" \
"ds_read_b64_tr_b16 v[144:145], v235 offset:0\n" \
"v_accvgpr_read_b32 v208, a48\n" \
"v_accvgpr_read_b32 v209, a49\n" \
"v_mfma_f32_16x16x32_bf16 a[236:239], v[108:111], v[92:95], a[236:239]\n" \
"s_add_u32 m0, s38, 3072\n" \
"ds_read_b64_tr_b16 v[146:147], v235 offset:2048\n" \
"v_accvgpr_read_b32 v210, a50\n" \
"v_mfma_f32_16x16x32_bf16 a[240:243], v[112:115], v[92:95], a[240:243]\n" \
"ds_read_b64_tr_b16 v[148:149], v236 offset:0\n" \
"v_accvgpr_read_b32 v211, a51\n" \
"v_cvt_pk_bf16_f32 v212, v208, v209\n" \
"v_mfma_f32_16x16x32_bf16 a[244:247], v[116:119], v[92:95], a[244:247]\n" \
"buffer_load_dwordx4 v228, s[20:23], s52 offen lds\n" \
"ds_read_b64_tr_b16 v[150:151], v236 offset:2048\n" \
"v_cvt_pk_bf16_f32 v213, v210, v211\n" \
"v_mfma_f32_16x16x32_bf16 a[248:251], v[120:123], v[92:95], a[248:251]\n" \
"v_accvgpr_read_b32 v208, a52\n" \
"v_accvgpr_read_b32 v209, a53\n" \
"v_accvgpr_read_b32 v210, a54\n" \
"v_mfma_f32_16x16x32_bf16 a[252:255], v[124:127], v[92:95], a[252:255]\n" \
"v_accvgpr_read_b32 v211, a55\n" \
"v_cvt_pk_bf16_f32 v214, v208, v209\n" \
"v_cvt_pk_bf16_f32 v215, v210, v211\n" \
"s_waitcnt vmcnt(46)\n" \
"s_waitcnt lgkmcnt(8)\n" \
"v_mfma_f32_16x16x32_bf16 v[0:3], v[128:131], v[160:163], 0\n" \
"ds_read_b64_tr_b16 v[152:153], v237 offset:0\n" \
"s_add_u32 s52, s40, 2359296\n" \
"v_mfma_f32_16x16x32_bf16 v[4:7], v[128:131], v[164:167], 0\n" \
"ds_read_b64_tr_b16 v[154:155], v237 offset:2048\n" \
"s_add_u32 m0, s38, 16384\n" \
"v_mfma_f32_16x16x32_bf16 v[8:11], v[132:135], v[160:163], 0\n" \
"ds_read_b64_tr_b16 v[156:157], v238 offset:0\n" \
"v_mfma_f32_16x16x32_bf16 v[12:15], v[132:135], v[164:167], 0\n" \
"ds_read_b64_tr_b16 v[158:159], v238 offset:2048\n" \
"s_waitcnt lgkmcnt(8)\n" \
"buffer_load_dwordx4 v227, s[20:23], s52 offen lds\n" \
"v_mfma_f32_16x16x32_bf16 v[16:19], v[136:139], v[160:163], 0\n" \
"ds_read_b64_tr_b16 v[128:129], v231 offset:256\n" \
"v_mfma_f32_16x16x32_bf16 v[20:23], v[136:139], v[164:167], 0\n" \
"ds_read_b64_tr_b16 v[130:131], v231 offset:2304\n" \
"s_add_u32 s52, s40, 2375680\n" \
"v_mfma_f32_16x16x32_bf16 v[24:27], v[140:143], v[160:163], 0\n" \
"ds_read_b64_tr_b16 v[132:133], v232 offset:256\n" \
"s_add_u32 m0, s38, 17408\n" \
"v_mfma_f32_16x16x32_bf16 v[28:31], v[140:143], v[164:167], 0\n" \
"ds_read_b64_tr_b16 v[134:135], v232 offset:2304\n" \
"s_waitcnt lgkmcnt(8)\n" \
"s_add_u32 s83, s55, 0\n" \
"buffer_store_dwordx4 v[216:219], v224, %[rs], s83 offen offset:1024 sc1\n" \
"v_mfma_f32_16x16x32_bf16 v[32:35], v[144:147], v[160:163], 0\n" \
"ds_read_b64_tr_b16 v[136:137], v233 offset:256\n" \
"buffer_load_dwordx4 v228, s[20:23], s52 offen lds\n" \
"v_accvgpr_read_b32 v208, a56\n" \
"v_mfma_f32_16x16x32_bf16 v[36:39], v[144:147], v[164:167], 0\n" \
"ds_read_b64_tr_b16 v[138:139], v233 offset:2304\n" \
"v_accvgpr_read_b32 v209, a57\n" \
"v_accvgpr_read_b32 v210, a58\n" \
"v_mfma_f32_16x16x32_bf16 v[40:43], v[148:151], v[160:163], 0\n" \
"ds_read_b64_tr_b16 v[140:141], v234 offset:256\n" \
"s_add_u32 s52, s40, 2392064\n" \
"v_accvgpr_read_b32 v211, a59\n" \
"v_mfma_f32_16x16x32_bf16 v[44:47], v[148:151], v[164:167], 0\n" \
"ds_read_b64_tr_b16 v[142:143], v234 offset:2304\n" \
"s_waitcnt lgkmcnt(8)\n" \
"s_add_u32 m0, s38, 18432\n" \
"v_mfma_f32_16x16x32_bf16 v[48:51], v[152:155], v[160:163], 0\n" \
"ds_read_b64_tr_b16 v[144:145], v235 offset:256\n" \
"v_cvt_pk_bf16_f32 v216, v208, v209\n" \
"v_cvt_pk_bf16_f32 v217, v210, v211\n" \
"v_mfma_f32_16x16x32_bf16 v[52:55], v[152:155], v[164:167], 0\n" \
"ds_read_b64_tr_b16 v[146:147], v235 offset:2304\n" \
"buffer_load_dwordx4 v227, s[20:23], s52 offen lds\n" \
"v_accvgpr_read_b32 v208, a60\n" \
"v_mfma_f32_16x16x32_bf16 v[56:59], v[156:159], v[160:163], 0\n" \
"ds_read_b64_tr_b16 v[148:149], v236 offset:256\n" \
"v_accvgpr_read_b32 v209, a61\n" \
"v_accvgpr_read_b32 v210, a62\n" \
"v_mfma_f32_16x16x32_bf16 v[60:63], v[156:159], v[164:167], 0\n" \
"ds_read_b64_tr_b16 v[150:151], v236 offset:2304\n" \
"s_waitcnt lgkmcnt(8)\n" \
"s_add_u32 s52, s40, 2408448\n" \
"v_mfma_f32_16x16x32_bf16 v[64:67], v[128:131], v[160:163], 0\n" \
"ds_read_b64_tr_b16 v[152:153], v237 offset:256\n" \
"s_add_u32 m0, s38, 19456\n" \
"v_accvgpr_read_b32 v211, a63\n" \
"v_mfma_f32_16x16x32_bf16 v[68:71], v[128:131], v[164:167], 0\n" \
"ds_read_b64_tr_b16 v[154:155], v237 offset:2304\n" \
"v_cvt_pk_bf16_f32 v218, v208, v209\n" \
"v_cvt_pk_bf16_f32 v219, v210, v211\n" \
"v_mfma_f32_16x16x32_bf16 v[72:75], v[132:135], v[160:163], 0\n" \
"ds_read_b64_tr_b16 v[156:157], v238 offset:256\n" \
"buffer_load_dwordx4 v228, s[20:23], s52 offen lds\n" \
"v_mfma_f32_16x16x32_bf16 v[76:79], v[132:135], v[164:167], 0\n" \
"ds_read_b64_tr_b16 v[158:159], v238 offset:2304\n" \
"s_waitcnt lgkmcnt(8)\n" \
"s_waitcnt vmcnt(43)\n" \
"s_barrier\n" \
"v_mfma_f32_16x16x32_bf16 v[80:83], v[136:139], v[160:163], 0\n" \
"ds_read_b64_tr_b16 v[128:129], v231 offset:16384\n" \
"v_mfma_f32_16x16x32_bf16 v[84:87], v[136:139], v[164:167], 0\n" \
"ds_read_b64_tr_b16 v[130:131], v231 offset:18432\n" \
"v_mfma_f32_16x16x32_bf16 v[88:91], v[140:143], v[160:163], 0\n" \
"ds_read_b64_tr_b16 v[132:133], v232 offset:16384\n" \
"buffer_store_dwordx4 v[220:223], v224, %[rs], s83 offen offset:3072 sc1\n" \
"v_accvgpr_read_b32 v208, a64\n" \
"v_mfma_f32_16x16x32_bf16 v[92:95], v[140:143], v[164:167], 0\n" \
"ds_read_b64_tr_b16 v[134:135], v232 offset:18432\n" \
"s_waitcnt lgkmcnt(8)\n" \
"v_accvgpr_read_b32 v209, a65\n" \
"v_mfma_f32_16x16x32_bf16 v[96:99], v[144:147], v[160:163], 0\n" \
"ds_read_b64_tr_b16 v[136:137], v233 offset:16384\n" \
"v_accvgpr_read_b32 v210, a66\n" \
"v_accvgpr_read_b32 v211, a67\n" \
"v_mfma_f32_16x16x32_bf16 v[100:103], v[144:147], v[164:167], 0\n" \
"ds_read_b64_tr_b16 v[138:139], v233 offset:18432\n" \
"v_cvt_pk_bf16_f32 v220, v208, v209\n" \
"v_cvt_pk_bf16_f32 v221, v210, v211\n" \
"v_mfma_f32_16x16x32_bf16 v[104:107], v[148:151], v[160:163], 0\n" \
"ds_read_b64_tr_b16 v[140:141], v234 offset:16384\n" \
"v_accvgpr_read_b32 v208, a68\n" \
"v_accvgpr_read_b32 v209, a69\n" \
"v_mfma_f32_16x16x32_bf16 v[108:111], v[148:151], v[164:167], 0\n" \
"ds_read_b64_tr_b16 v[142:143], v234 offset:18432\n" \
"s_waitcnt lgkmcnt(8)\n" \
"v_accvgpr_read_b32 v210, a70\n" \
"v_mfma_f32_16x16x32_bf16 v[112:115], v[152:155], v[160:163], 0\n" \
"ds_read_b64_tr_b16 v[144:145], v235 offset:16384\n" \
"v_accvgpr_read_b32 v211, a71\n" \
"v_cvt_pk_bf16_f32 v222, v208, v209\n" \
"v_mfma_f32_16x16x32_bf16 v[116:119], v[152:155], v[164:167], 0\n" \
"ds_read_b64_tr_b16 v[146:147], v235 offset:18432\n" \
"v_cvt_pk_bf16_f32 v223, v210, v211\n" \
"v_mfma_f32_16x16x32_bf16 v[120:123], v[156:159], v[160:163], 0\n" \
"ds_read_b64_tr_b16 v[148:149], v236 offset:16384\n" \
"v_mfma_f32_16x16x32_bf16 v[124:127], v[156:159], v[164:167], 0\n" \
"ds_read_b64_tr_b16 v[150:151], v236 offset:18432\n" \
"s_waitcnt lgkmcnt(8)\n" \
"v_mfma_f32_16x16x32_bf16 v[0:3], v[128:131], v[168:171], v[0:3]\n" \
"ds_read_b64_tr_b16 v[152:153], v237 offset:16384\n" \
"v_mfma_f32_16x16x32_bf16 v[4:7], v[128:131], v[172:175], v[4:7]\n" \
"ds_read_b64_tr_b16 v[154:155], v237 offset:18432\n" \
"v_mfma_f32_16x16x32_bf16 v[8:11], v[132:135], v[168:171], v[8:11]\n" \
"ds_read_b64_tr_b16 v[156:157], v238 offset:16384\n" \
"v_mfma_f32_16x16x32_bf16 v[12:15], v[132:135], v[172:175], v[12:15]\n" \
"ds_read_b64_tr_b16 v[158:159], v238 offset:18432\n" \
"s_waitcnt lgkmcnt(8)\n" \
"v_mfma_f32_16x16x32_bf16 v[16:19], v[136:139], v[168:171], v[16:19]\n" \
"ds_read_b64_tr_b16 v[128:129], v231 offset:16640\n" \
"s_add_u32 s83, s55, 4096\n" \
"buffer_store_dwordx4 v[212:215], v224, %[rs], s83 offen offset:1024 sc1\n" \
"v_mfma_f32_16x16x32_bf16 v[20:23], v[136:139], v[172:175], v[20:23]\n" \
"ds_read_b64_tr_b16 v[130:131], v231 offset:18688\n" \
"v_accvgpr_read_b32 v208, a72\n" \
"v_accvgpr_read_b32 v209, a73\n" \
"v_mfma_f32_16x16x32_bf16 v[24:27], v[140:143], v[168:171], v[24:27]\n" \
"ds_read_b64_tr_b16 v[132:133], v232 offset:16640\n" \
"v_accvgpr_read_b32 v210, a74\n" \
"v_accvgpr_read_b32 v211, a75\n" \
"v_mfma_f32_16x16x32_bf16 v[28:31], v[140:143], v[172:175], v[28:31]\n" \
"ds_read_b64_tr_b16 v[134:135], v232 offset:18688\n" \
"s_waitcnt lgkmcnt(8)\n" \
"v_cvt_pk_bf16_f32 v212, v208, v209\n" \
"v_mfma_f32_16x16x32_bf16 v[32:35], v[144:147], v[168:171], v[32:35]\n" \
"ds_read_b64_tr_b16 v[136:137], v233 offset:16640\n" \
"v_cvt_pk_bf16_f32 v213, v210, v211\n" \
"v_accvgpr_read_b32 v208, a76\n" \
"v_mfma_f32_16x16x32_bf16 v[36:39], v[144:147], v[172:175], v[36:39]\n" \
"ds_read_b64_tr_b16 v[138:139], v233 offset:18688\n" \
"s_add_u32 s80, s58, 6144\n" \
"v_accvgpr_read_b32 v209, a77\n" \
"v_mfma_f32_16x16x32_bf16 v[40:43], v[148:151], v[168:171], v[40:43]\n" \
"ds_read_b64_tr_b16 v[140:141], v234 offset:16640\n" \
"v_accvgpr_read_b32 v210, a78\n" \
"v_accvgpr_read_b32 v211, a79\n" \
"v_mfma_f32_16x16x32_bf16 v[44:47], v[148:151], v[172:175], v[44:47]\n" \
"ds_read_b64_tr_b16 v[142:143], v234 offset:18688\n" \
"s_waitcnt lgkmcnt(8)\n" \
"buffer_load_dwordx4 v[160:163], v224, %[rs], s80 offen sc1\n" \
"v_mfma_f32_16x16x32_bf16 v[48:51], v[152:155], v[168:171], v[48:51]\n" \
"ds_read_b64_tr_b16 v[144:145], v235 offset:16640\n" \
"v_cvt_pk_bf16_f32 v214, v208, v209\n" \
"v_cvt_pk_bf16_f32 v215, v210, v211\n" \
"v_mfma_f32_16x16x32_bf16 v[52:55], v[152:155], v[172:175], v[52:55]\n" \
"ds_read_b64_tr_b16 v[146:147], v235 offset:18688\n" \
"v_mfma_f32_16x16x32_bf16 v[56:59], v[156:159], v[168:171], v[56:59]\n" \
"ds_read_b64_tr_b16 v[148:149], v236 offset:16640\n" \
"v_mfma_f32_16x16x32_bf16 v[60:63], v[156:159], v[172:175], v[60:63]\n" \
"ds_read_b64_tr_b16 v[150:151], v236 offset:18688\n" \
"s_waitcnt lgkmcnt(8)\n" \
"s_add_u32 s80, s71, 6144\n" \
"v_mfma_f32_16x16x32_bf16 v[64:67], v[128:131], v[168:171], v[64:67]\n" \
"ds_read_b64_tr_b16 v[152:153], v237 offset:16640\n" \
"v_mfma_f32_16x16x32_bf16 v[68:71], v[128:131], v[172:175], v[68:71]\n" \
"ds_read_b64_tr_b16 v[154:155], v237 offset:18688\n" \
"buffer_load_dwordx4 v[164:167], v224, %[rs], s80 offen sc1\n" \
"v_mfma_f32_16x16x32_bf16 v[72:75], v[132:135], v[168:171], v[72:75]\n" \
"ds_read_b64_tr_b16 v[156:157], v238 offset:16640\n" \
"v_mfma_f32_16x16x32_bf16 v[76:79], v[132:135], v[172:175], v[76:79]\n" \
"ds_read_b64_tr_b16 v[158:159], v238 offset:18688\n" \
"s_waitcnt lgkmcnt(8)\n" \
"s_waitcnt vmcnt(40)\n" \
"s_barrier\n" \
"v_mfma_f32_16x16x32_bf16 v[80:83], v[136:139], v[168:171], v[80:83]\n" \
"ds_read_b64_tr_b16 v[128:129], v231 offset:32768\n" \
"s_add_u32 s52, s40, 2621440\n" \
"v_mfma_f32_16x16x32_bf16 v[84:87], v[136:139], v[172:175], v[84:87]\n" \
"ds_read_b64_tr_b16 v[130:131], v231 offset:34816\n" \
"s_add_u32 m0, s38, 32768\n" \
"buffer_store_dwordx4 v[216:219], v224, %[rs], s83 offen offset:3072 sc1\n" \
"v_mfma_f32_16x16x32_bf16 v[88:91], v[140:143], v[168:171], v[88:91]\n" \
"ds_read_b64_tr_b16 v[132:133], v232 offset:32768\n" \
"v_accvgpr_read_b32 v208, a80\n" \
"v_accvgpr_read_b32 v209, a81\n" \
"v_mfma_f32_16x16x32_bf16 v[92:95], v[140:143], v[172:175], v[92:95]\n" \
"ds_read_b64_tr_b16 v[134:135], v232 offset:34816\n" \
"s_waitcnt lgkmcnt(8)\n" \
"buffer_load_dwordx4 v227, s[20:23], s52 offen lds\n" \
"v_mfma_f32_16x16x32_bf16 v[96:99], v[144:147], v[168:171], v[96:99]\n" \
"ds_read_b64_tr_b16 v[136:137], v233 offset:32768\n" \
"v_accvgpr_read_b32 v210, a82\n" \
"v_accvgpr_read_b32 v211, a83\n" \
"v_mfma_f32_16x16x32_bf16 v[100:103], v[144:147], v[172:175], v[100:103]\n" \
"ds_read_b64_tr_b16 v[138:139], v233 offset:34816\n" \
"s_add_u32 s52, s40, 2637824\n" \
"v_cvt_pk_bf16_f32 v216, v208, v209\n" \
"v_mfma_f32_16x16x32_bf16 v[104:107], v[148:151], v[168:171], v[104:107]\n" \
"ds_read_b64_tr_b16 v[140:141], v234 offset:32768\n" \
"s_add_u32 m0, s38, 33792\n" \
"v_cvt_pk_bf16_f32 v217, v210, v211\n" \
"v_mfma_f32_16x16x32_bf16 v[108:111], v[148:151], v[172:175], v[108:111]\n" \
"ds_read_b64_tr_b16 v[142:143], v234 offset:34816\n" \
"s_waitcnt lgkmcnt(8)\n" \
"v_accvgpr_read_b32 v208, a84\n" \
"v_mfma_f32_16x16x32_bf16 v[112:115], v[152:155], v[168:171], v[112:115]\n" \
"ds_read_b64_tr_b16 v[144:145], v235 offset:32768\n" \
"buffer_load_dwordx4 v228, s[20:23], s52 offen lds\n" \
"v_accvgpr_read_b32 v209, a85\n" \
"v_mfma_f32_16x16x32_bf16 v[116:119], v[152:155], v[172:175], v[116:119]\n" \
"ds_read_b64_tr_b16 v[146:147], v235 offset:34816\n" \
"v_accvgpr_read_b32 v210, a86\n" \
"v_accvgpr_read_b32 v211, a87\n" \
"v_mfma_f32_16x16x32_bf16 v[120:123], v[156:159], v[168:171], v[120:123]\n" \
"ds_read_b64_tr_b16 v[148:149], v236 offset:32768\n" \
"s_add_u32 s52, s40, 2654208\n" \
"v_cvt_pk_bf16_f32 v218, v208, v209\n" \
"v_mfma_f32_16x16x32_bf16 v[124:127], v[156:159], v[172:175], v[124:127]\n" \
"ds_read_b64_tr_b16 v[150:151], v236 offset:34816\n" \
"s_waitcnt lgkmcnt(8)\n" \
"s_add_u32 m0, s38, 34816\n" \
"v_mfma_f32_16x16x32_bf16 v[0:3], v[128:131], v[176:179], v[0:3]\n" \
"ds_read_b64_tr_b16 v[152:153], v237 offset:32768\n" \
"v_cvt_pk_bf16_f32 v219, v210, v211\n" \
"v_mfma_f32_16x16x32_bf16 v[4:7], v[128:131], v[180:183], v[4:7]\n" \
"ds_read_b64_tr_b16 v[154:155], v237 offset:34816\n" \
"buffer_load_dwordx4 v227, s[20:23], s52 offen lds\n" \
"v_mfma_f32_16x16x32_bf16 v[8:11], v[132:135], v[176:179], v[8:11]\n" \
"ds_read_b64_tr_b16 v[156:157], v238 offset:32768\n" \
"v_mfma_f32_16x16x32_bf16 v[12:15], v[132:135], v[180:183], v[12:15]\n" \
"ds_read_b64_tr_b16 v[158:159], v238 offset:34816\n" \
"s_waitcnt lgkmcnt(8)\n" \
"s_add_u32 s52, s40, 2670592\n" \
"v_mfma_f32_16x16x32_bf16 v[16:19], v[136:139], v[176:179], v[16:19]\n" \
"ds_read_b64_tr_b16 v[128:129], v231 offset:33024\n" \
"s_add_u32 m0, s38, 35840\n" \
"v_mfma_f32_16x16x32_bf16 v[20:23], v[136:139], v[180:183], v[20:23]\n" \
"ds_read_b64_tr_b16 v[130:131], v231 offset:35072\n" \
"s_add_u32 s83, s55, 16384\n" \
"buffer_store_dwordx4 v[220:223], v224, %[rs], s83 offen offset:0 sc1\n" \
"v_mfma_f32_16x16x32_bf16 v[24:27], v[140:143], v[176:179], v[24:27]\n" \
"ds_read_b64_tr_b16 v[132:133], v232 offset:33024\n" \
"buffer_load_dwordx4 v228, s[20:23], s52 offen lds\n" \
"v_accvgpr_read_b32 v208, a88\n" \
"v_mfma_f32_16x16x32_bf16 v[28:31], v[140:143], v[180:183], v[28:31]\n" \
"ds_read_b64_tr_b16 v[134:135], v232 offset:35072\n" \
"s_waitcnt lgkmcnt(8)\n" \
"v_accvgpr_read_b32 v209, a89\n" \
"v_mfma_f32_16x16x32_bf16 v[32:35], v[144:147], v[176:179], v[32:35]\n" \
"ds_read_b64_tr_b16 v[136:137], v233 offset:33024\n" \
"v_accvgpr_read_b32 v210, a90\n" \
"v_accvgpr_read_b32 v211, a91\n" \
"v_mfma_f32_16x16x32_bf16 v[36:39], v[144:147], v[180:183], v[36:39]\n" \
"ds_read_b64_tr_b16 v[138:139], v233 offset:35072\n" \
"s_add_u32 s80, s58, 7168\n" \
"v_cvt_pk_bf16_f32 v220, v208, v209\n" \
"v_mfma_f32_16x16x32_bf16 v[40:43], v[148:151], v[176:179], v[40:43]\n" \
"ds_read_b64_tr_b16 v[140:141], v234 offset:33024\n" \
"v_cvt_pk_bf16_f32 v221, v210, v211\n" \
"v_accvgpr_read_b32 v208, a92\n" \
"v_mfma_f32_16x16x32_bf16 v[44:47], v[148:151], v[180:183], v[44:47]\n" \
"ds_read_b64_tr_b16 v[142:143], v234 offset:35072\n" \
"s_waitcnt lgkmcnt(8)\n" \
"buffer_load_dwordx4 v[168:171], v224, %[rs], s80 offen sc1\n" \
"v_mfma_f32_16x16x32_bf16 v[48:51], v[152:155], v[176:179], v[48:51]\n" \
"ds_read_b64_tr_b16 v[144:145], v235 offset:33024\n" \
"v_accvgpr_read_b32 v209, a93\n" \
"v_accvgpr_read_b32 v210, a94\n" \
"v_mfma_f32_16x16x32_bf16 v[52:55], v[152:155], v[180:183], v[52:55]\n" \
"ds_read_b64_tr_b16 v[146:147], v235 offset:35072\n" \
"v_accvgpr_read_b32 v211, a95\n" \
"v_cvt_pk_bf16_f32 v222, v208, v209\n" \
"v_mfma_f32_16x16x32_bf16 v[56:59], v[156:159], v[176:179], v[56:59]\n" \
"ds_read_b64_tr_b16 v[148:149], v236 offset:33024\n" \
"v_cvt_pk_bf16_f32 v223, v210, v211\n" \
"v_mfma_f32_16x16x32_bf16 v[60:63], v[156:159], v[180:183], v[60:63]\n" \
"ds_read_b64_tr_b16 v[150:151], v236 offset:35072\n" \
"s_waitcnt lgkmcnt(8)\n" \
"s_add_u32 s80, s71, 7168\n" \
"v_mfma_f32_16x16x32_bf16 v[64:67], v[128:131], v[176:179], v[64:67]\n" \
"ds_read_b64_tr_b16 v[152:153], v237 offset:33024\n" \
"v_mfma_f32_16x16x32_bf16 v[68:71], v[128:131], v[180:183], v[68:71]\n" \
"ds_read_b64_tr_b16 v[154:155], v237 offset:35072\n" \
"buffer_load_dwordx4 v[172:175], v224, %[rs], s80 offen sc1\n" \
"v_mfma_f32_16x16x32_bf16 v[72:75], v[132:135], v[176:179], v[72:75]\n" \
"ds_read_b64_tr_b16 v[156:157], v238 offset:33024\n" \
"v_mfma_f32_16x16x32_bf16 v[76:79], v[132:135], v[180:183], v[76:79]\n" \
"ds_read_b64_tr_b16 v[158:159], v238 offset:35072\n" \
"s_waitcnt lgkmcnt(8)\n" \
"s_waitcnt vmcnt(34)\n" \
"s_barrier\n" \
"v_mfma_f32_16x16x32_bf16 v[80:83], v[136:139], v[176:179], v[80:83]\n" \
"ds_read_b64_tr_b16 v[128:129], v231 offset:49152\n" \
"s_add_u32 s52, s40, 2883584\n" \
"v_mfma_f32_16x16x32_bf16 v[84:87], v[136:139], v[180:183], v[84:87]\n" \
"ds_read_b64_tr_b16 v[130:131], v231 offset:51200\n" \
"s_add_u32 m0, s38, 49152\n" \
"buffer_store_dwordx4 v[212:215], v224, %[rs], s83 offen offset:2048 sc1\n" \
"v_mfma_f32_16x16x32_bf16 v[88:91], v[140:143], v[176:179], v[88:91]\n" \
"ds_read_b64_tr_b16 v[132:133], v232 offset:49152\n" \
"v_accvgpr_read_b32 v208, a96\n" \
"v_accvgpr_read_b32 v209, a97\n" \
"v_mfma_f32_16x16x32_bf16 v[92:95], v[140:143], v[180:183], v[92:95]\n" \
"ds_read_b64_tr_b16 v[134:135], v232 offset:51200\n" \
"s_waitcnt lgkmcnt(8)\n" \
"buffer_load_dwordx4 v227, s[20:23], s52 offen lds\n" \
"v_mfma_f32_16x16x32_bf16 v[96:99], v[144:147], v[176:179], v[96:99]\n" \
"ds_read_b64_tr_b16 v[136:137], v233 offset:49152\n" \
"v_accvgpr_read_b32 v210, a98\n" \
"v_accvgpr_read_b32 v211, a99\n" \
"v_mfma_f32_16x16x32_bf16 v[100:103], v[144:147], v[180:183], v[100:103]\n" \
"ds_read_b64_tr_b16 v[138:139], v233 offset:51200\n" \
"s_add_u32 s52, s40, 2899968\n" \
"v_cvt_pk_bf16_f32 v212, v208, v209\n" \
"v_mfma_f32_16x16x32_bf16 v[104:107], v[148:151], v[176:179], v[104:107]\n" \
"ds_read_b64_tr_b16 v[140:141], v234 offset:49152\n" \
"s_add_u32 m0, s38, 50176\n" \
"v_cvt_pk_bf16_f32 v213, v210, v211\n" \
"v_mfma_f32_16x16x32_bf16 v[108:111], v[148:151], v[180:183], v[108:111]\n" \
"ds_read_b64_tr_b16 v[142:143], v234 offset:51200\n" \
"s_waitcnt lgkmcnt(8)\n" \
"v_accvgpr_read_b32 v208, a100\n" \
"v_mfma_f32_16x16x32_bf16 v[112:115], v[152:155], v[176:179], v[112:115]\n" \
"ds_read_b64_tr_b16 v[144:145], v235 offset:49152\n" \
"buffer_load_dwordx4 v228, s[20:23], s52 offen lds\n" \
"v_accvgpr_read_b32 v209, a101\n" \
"v_mfma_f32_16x16x32_bf16 v[116:119], v[152:155], v[180:183], v[116:119]\n" \
"ds_read_b64_tr_b16 v[146:147], v235 offset:51200\n" \
"v_accvgpr_read_b32 v210, a102\n" \
"v_accvgpr_read_b32 v211, a103\n" \
"v_mfma_f32_16x16x32_bf16 v[120:123], v[156:159], v[176:179], v[120:123]\n" \
"ds_read_b64_tr_b16 v[148:149], v236 offset:49152\n" \
"s_add_u32 s52, s40, 2916352\n" \
"v_cvt_pk_bf16_f32 v214, v208, v209\n" \
"v_mfma_f32_16x16x32_bf16 v[124:127], v[156:159], v[180:183], v[124:127]\n" \
"ds_read_b64_tr_b16 v[150:151], v236 offset:51200\n" \
"s_waitcnt lgkmcnt(8)\n" \
"s_add_u32 m0, s38, 51200\n" \
"v_mfma_f32_16x16x32_bf16 v[0:3], v[128:131], v[184:187], v[0:3]\n" \
"ds_read_b64_tr_b16 v[152:153], v237 offset:49152\n" \
"v_cvt_pk_bf16_f32 v215, v210, v211\n" \
"v_mfma_f32_16x16x32_bf16 v[4:7], v[128:131], v[188:191], v[4:7]\n" \
"ds_read_b64_tr_b16 v[154:155], v237 offset:51200\n" \
"buffer_load_dwordx4 v227, s[20:23], s52 offen lds\n" \
"v_mfma_f32_16x16x32_bf16 v[8:11], v[132:135], v[184:187], v[8:11]\n" \
"ds_read_b64_tr_b16 v[156:157], v238 offset:49152\n" \
"v_mfma_f32_16x16x32_bf16 v[12:15], v[132:135], v[188:191], v[12:15]\n" \
"ds_read_b64_tr_b16 v[158:159], v238 offset:51200\n" \
"s_waitcnt lgkmcnt(8)\n" \
"s_add_u32 s52, s40, 2932736\n" \
"v_mfma_f32_16x16x32_bf16 v[16:19], v[136:139], v[184:187], v[16:19]\n" \
"ds_read_b64_tr_b16 v[128:129], v231 offset:49408\n" \
"s_add_u32 m0, s38, 52224\n" \
"v_mfma_f32_16x16x32_bf16 v[20:23], v[136:139], v[188:191], v[20:23]\n" \
"ds_read_b64_tr_b16 v[130:131], v231 offset:51456\n" \
"s_add_u32 s83, s55, 20480\n" \
"buffer_store_dwordx4 v[216:219], v224, %[rs], s83 offen offset:0 sc1\n" \
"v_mfma_f32_16x16x32_bf16 v[24:27], v[140:143], v[184:187], v[24:27]\n" \
"ds_read_b64_tr_b16 v[132:133], v232 offset:49408\n" \
"v_add_u32 v231, 0x10000, v231\n" \
"buffer_load_dwordx4 v228, s[20:23], s52 offen lds\n" \
"v_mfma_f32_16x16x32_bf16 v[28:31], v[140:143], v[188:191], v[28:31]\n" \
"ds_read_b64_tr_b16 v[134:135], v232 offset:51456\n" \
"s_waitcnt lgkmcnt(8)\n" \
"v_accvgpr_read_b32 v208, a104\n" \
"v_mfma_f32_16x16x32_bf16 v[32:35], v[144:147], v[184:187], v[32:35]\n" \
"ds_read_b64_tr_b16 v[136:137], v233 offset:49408\n" \
"v_add_u32 v232, 0x10000, v232\n" \
"v_accvgpr_read_b32 v209, a105\n" \
"v_mfma_f32_16x16x32_bf16 v[36:39], v[144:147], v[188:191], v[36:39]\n" \
"ds_read_b64_tr_b16 v[138:139], v233 offset:51456\n" \
"s_add_u32 s80, s58, 8192\n" \
"v_accvgpr_read_b32 v210, a106\n" \
"v_mfma_f32_16x16x32_bf16 v[40:43], v[148:151], v[184:187], v[40:43]\n" \
"ds_read_b64_tr_b16 v[140:141], v234 offset:49408\n" \
"v_add_u32 v233, 0x10000, v233\n" \
"v_accvgpr_read_b32 v211, a107\n" \
"v_mfma_f32_16x16x32_bf16 v[44:47], v[148:151], v[188:191], v[44:47]\n" \
"ds_read_b64_tr_b16 v[142:143], v234 offset:51456\n" \
"s_waitcnt lgkmcnt(8)\n" \
"buffer_load_dwordx4 v[176:179], v224, %[rs], s80 offen sc1\n" \
"v_mfma_f32_16x16x32_bf16 v[48:51], v[152:155], v[184:187], v[48:51]\n" \
"ds_read_b64_tr_b16 v[144:145], v235 offset:49408\n" \
"v_add_u32 v234, 0x10000, v234\n" \
"v_cvt_pk_bf16_f32 v216, v208, v209\n" \
"v_mfma_f32_16x16x32_bf16 v[52:55], v[152:155], v[188:191], v[52:55]\n" \
"ds_read_b64_tr_b16 v[146:147], v235 offset:51456\n" \
"v_cvt_pk_bf16_f32 v217, v210, v211\n" \
"v_accvgpr_read_b32 v208, a108\n" \
"v_mfma_f32_16x16x32_bf16 v[56:59], v[156:159], v[184:187], v[56:59]\n" \
"ds_read_b64_tr_b16 v[148:149], v236 offset:49408\n" \
"v_add_u32 v235, 0x10000, v235\n" \
"v_accvgpr_read_b32 v209, a109\n" \
"v_mfma_f32_16x16x32_bf16 v[60:63], v[156:159], v[188:191], v[60:63]\n" \
"ds_read_b64_tr_b16 v[150:151], v236 offset:51456\n" \
"s_waitcnt lgkmcnt(8)\n" \
"s_add_u32 s80, s71, 8192\n" \
"v_mfma_f32_16x16x32_bf16 v[64:67], v[128:131], v[184:187], v[64:67]\n" \
"ds_read_b64_tr_b16 v[152:153], v237 offset:49408\n" \
"v_add_u32 v236, 0x10000, v236\n" \
"v_accvgpr_read_b32 v210, a110\n" \
"v_mfma_f32_16x16x32_bf16 v[68:71], v[128:131], v[188:191], v[68:71]\n" \
"ds_read_b64_tr_b16 v[154:155], v237 offset:51456\n" \
"buffer_load_dwordx4 v[180:183], v224, %[rs], s80 offen sc1\n" \
"v_accvgpr_read_b32 v211, a111\n" \
"v_mfma_f32_16x16x32_bf16 v[72:75], v[132:135], v[184:187], v[72:75]\n" \
"ds_read_b64_tr_b16 v[156:157], v238 offset:49408\n" \
"v_add_u32 v237, 0x10000, v237\n" \
"v_cvt_pk_bf16_f32 v218, v208, v209\n" \
"v_mfma_f32_16x16x32_bf16 v[76:79], v[132:135], v[188:191], v[76:79]\n" \
"ds_read_b64_tr_b16 v[158:159], v238 offset:51456\n" \
"s_waitcnt lgkmcnt(8)\n" \
"v_cvt_pk_bf16_f32 v219, v210, v211\n" \
"s_waitcnt vmcnt(36)\n" \
"s_barrier\n" \
"v_mfma_f32_16x16x32_bf16 v[80:83], v[136:139], v[184:187], v[80:83]\n" \
"ds_read_b64_tr_b16 v[128:129], v231 offset:0\n" \
"v_add_u32 v238, 0x10000, v238\n" \
"s_add_u32 s52, s40, 3145728\n" \
"v_mfma_f32_16x16x32_bf16 v[84:87], v[136:139], v[188:191], v[84:87]\n" \
"ds_read_b64_tr_b16 v[130:131], v231 offset:2048\n" \
"s_add_u32 m0, s38, 65536\n" \
"buffer_store_dwordx4 v[220:223], v224, %[rs], s83 offen offset:2048 sc1\n" \
"v_mfma_f32_16x16x32_bf16 v[88:91], v[140:143], v[184:187], v[88:91]\n" \
"ds_read_b64_tr_b16 v[132:133], v232 offset:0\n" \
"v_accvgpr_read_b32 v208, a112\n" \
"v_accvgpr_read_b32 v209, a113\n" \
"v_mfma_f32_16x16x32_bf16 v[92:95], v[140:143], v[188:191], v[92:95]\n" \
"ds_read_b64_tr_b16 v[134:135], v232 offset:2048\n" \
"s_waitcnt lgkmcnt(8)\n" \
"buffer_load_dwordx4 v227, s[20:23], s52 offen lds\n" \
"v_mfma_f32_16x16x32_bf16 v[96:99], v[144:147], v[184:187], v[96:99]\n" \
"ds_read_b64_tr_b16 v[136:137], v233 offset:0\n" \
"v_accvgpr_read_b32 v210, a114\n" \
"v_accvgpr_read_b32 v211, a115\n" \
"v_mfma_f32_16x16x32_bf16 v[100:103], v[144:147], v[188:191], v[100:103]\n" \
"ds_read_b64_tr_b16 v[138:139], v233 offset:2048\n" \
"s_add_u32 s52, s40, 3162112\n" \
"v_cvt_pk_bf16_f32 v220, v208, v209\n" \
"v_mfma_f32_16x16x32_bf16 v[104:107], v[148:151], v[184:187], v[104:107]\n" \
"ds_read_b64_tr_b16 v[140:141], v234 offset:0\n" \
"s_add_u32 m0, s38, 66560\n" \
"v_cvt_pk_bf16_f32 v221, v210, v211\n" \
"v_mfma_f32_16x16x32_bf16 v[108:111], v[148:151], v[188:191], v[108:111]\n" \
"ds_read_b64_tr_b16 v[142:143], v234 offset:2048\n" \
"s_waitcnt lgkmcnt(8)\n" \
"v_accvgpr_read_b32 v208, a116\n" \
"v_mfma_f32_16x16x32_bf16 v[112:115], v[152:155], v[184:187], v[112:115]\n" \
"ds_read_b64_tr_b16 v[144:145], v235 offset:0\n" \
"buffer_load_dwordx4 v228, s[20:23], s52 offen lds\n" \
"v_accvgpr_read_b32 v209, a117\n" \
"v_mfma_f32_16x16x32_bf16 v[116:119], v[152:155], v[188:191], v[116:119]\n" \
"ds_read_b64_tr_b16 v[146:147], v235 offset:2048\n" \
"v_accvgpr_read_b32 v210, a118\n" \
"v_accvgpr_read_b32 v211, a119\n" \
"v_mfma_f32_16x16x32_bf16 v[120:123], v[156:159], v[184:187], v[120:123]\n" \
"ds_read_b64_tr_b16 v[148:149], v236 offset:0\n" \
"s_add_u32 s52, s40, 3178496\n" \
"v_cvt_pk_bf16_f32 v222, v208, v209\n" \
"v_mfma_f32_16x16x32_bf16 v[124:127], v[156:159], v[188:191], v[124:127]\n" \
"ds_read_b64_tr_b16 v[150:151], v236 offset:2048\n" \
"s_waitcnt lgkmcnt(8)\n" \
"s_add_u32 m0, s38, 67584\n" \
"v_mfma_f32_16x16x32_bf16 v[0:3], v[128:131], v[192:195], v[0:3]\n" \
"ds_read_b64_tr_b16 v[152:153], v237 offset:0\n" \
"v_cvt_pk_bf16_f32 v223, v210, v211\n" \
"v_mfma_f32_16x16x32_bf16 v[4:7], v[128:131], v[196:199], v[4:7]\n" \
"ds_read_b64_tr_b16 v[154:155], v237 offset:2048\n" \
"buffer_load_dwordx4 v227, s[20:23], s52 offen lds\n" \
"v_mfma_f32_16x16x32_bf16 v[8:11], v[132:135], v[192:195], v[8:11]\n" \
"ds_read_b64_tr_b16 v[156:157], v238 offset:0\n" \
"v_mfma_f32_16x16x32_bf16 v[12:15], v[132:135], v[196:199], v[12:15]\n" \
"ds_read_b64_tr_b16 v[158:159], v238 offset:2048\n" \
"s_waitcnt lgkmcnt(8)\n" \
"s_add_u32 s52, s40, 3194880\n" \
"v_mfma_f32_16x16x32_bf16 v[16:19], v[136:139], v[192:195], v[16:19]\n" \
"ds_read_b64_tr_b16 v[128:129], v231 offset:256\n" \
"s_add_u32 m0, s38, 68608\n" \
"v_mfma_f32_16x16x32_bf16 v[20:23], v[136:139], v[196:199], v[20:23]\n" \
"ds_read_b64_tr_b16 v[130:131], v231 offset:2304\n" \
"s_add_u32 s83, s55, 16384\n" \
"buffer_store_dwordx4 v[212:215], v224, %[rs], s83 offen offset:1024 sc1\n" \
"v_mfma_f32_16x16x32_bf16 v[24:27], v[140:143], v[192:195], v[24:27]\n" \
"ds_read_b64_tr_b16 v[132:133], v232 offset:256\n" \
"buffer_load_dwordx4 v228, s[20:23], s52 offen lds\n" \
"v_accvgpr_read_b32 v208, a120\n" \
"v_mfma_f32_16x16x32_bf16 v[28:31], v[140:143], v[196:199], v[28:31]\n" \
"ds_read_b64_tr_b16 v[134:135], v232 offset:2304\n" \
"s_waitcnt lgkmcnt(8)\n" \
"v_accvgpr_read_b32 v209, a121\n" \
"v_mfma_f32_16x16x32_bf16 v[32:35], v[144:147], v[192:195], v[32:35]\n" \
"ds_read_b64_tr_b16 v[136:137], v233 offset:256\n" \
"v_accvgpr_read_b32 v210, a122\n" \
"v_accvgpr_read_b32 v211, a123\n" \
"v_mfma_f32_16x16x32_bf16 v[36:39], v[144:147], v[196:199], v[36:39]\n" \
"ds_read_b64_tr_b16 v[138:139], v233 offset:2304\n" \
"s_add_u32 s80, s58, 9216\n" \
"v_cvt_pk_bf16_f32 v212, v208, v209\n" \
"v_mfma_f32_16x16x32_bf16 v[40:43], v[148:151], v[192:195], v[40:43]\n" \
"ds_read_b64_tr_b16 v[140:141], v234 offset:256\n" \
"v_cvt_pk_bf16_f32 v213, v210, v211\n" \
"v_accvgpr_read_b32 v208, a124\n" \
"v_mfma_f32_16x16x32_bf16 v[44:47], v[148:151], v[196:199], v[44:47]\n" \
"ds_read_b64_tr_b16 v[142:143], v234 offset:2304\n" \
"s_waitcnt lgkmcnt(8)\n" \
"buffer_load_dwordx4 v[184:187], v224, %[rs], s80 offen sc1\n" \
"v_mfma_f32_16x16x32_bf16 v[48:51], v[152:155], v[192:195], v[48:51]\n" \
"ds_read_b64_tr_b16 v[144:145], v235 offset:256\n" \
"v_accvgpr_read_b32 v209, a125\n" \
"v_accvgpr_read_b32 v210, a126\n" \
"v_mfma_f32_16x16x32_bf16 v[52:55], v[152:155], v[196:199], v[52:55]\n" \
"ds_read_b64_tr_b16 v[146:147], v235 offset:2304\n" \
"v_accvgpr_read_b32 v211, a127\n" \
"v_cvt_pk_bf16_f32 v214, v208, v209\n" \
"v_mfma_f32_16x16x32_bf16 v[56:59], v[156:159], v[192:195], v[56:59]\n" \
"ds_read_b64_tr_b16 v[148:149], v236 offset:256\n" \
"v_cvt_pk_bf16_f32 v215, v210, v211\n" \
"v_mfma_f32_16x16x32_bf16 v[60:63], v[156:159], v[196:199], v[60:63]\n" \
"ds_read_b64_tr_b16 v[150:151], v236 offset:2304\n" \
"s_waitcnt lgkmcnt(8)\n" \
"s_add_u32 s80, s71, 9216\n" \
"v_mfma_f32_16x16x32_bf16 v[64:67], v[128:131], v[192:195], v[64:67]\n" \
"ds_read_b64_tr_b16 v[152:153], v237 offset:256\n" \
"v_mfma_f32_16x16x32_bf16 v[68:71], v[128:131], v[196:199], v[68:71]\n" \
"ds_read_b64_tr_b16 v[154:155], v237 offset:2304\n" \
"buffer_load_dwordx4 v[188:191], v224, %[rs], s80 offen sc1\n" \
"v_mfma_f32_16x16x32_bf16 v[72:75], v[132:135], v[192:195], v[72:75]\n" \
"ds_read_b64_tr_b16 v[156:157], v238 offset:256\n" \
"v_mfma_f32_16x16x32_bf16 v[76:79], v[132:135], v[196:199], v[76:79]\n" \
"ds_read_b64_tr_b16 v[158:159], v238 offset:2304\n" \
"s_waitcnt lgkmcnt(8)\n" \
"s_waitcnt vmcnt(36)\n" \
"s_barrier\n" \
"v_mfma_f32_16x16x32_bf16 v[80:83], v[136:139], v[192:195], v[80:83]\n" \
"ds_read_b64_tr_b16 v[128:129], v231 offset:16384\n" \
"s_add_u32 s52, s40, 3407872\n" \
"v_mfma_f32_16x16x32_bf16 v[84:87], v[136:139], v[196:199], v[84:87]\n" \
"ds_read_b64_tr_b16 v[130:131], v231 offset:18432\n" \
"s_add_u32 m0, s38, 81920\n" \
"buffer_store_dwordx4 v[216:219], v224, %[rs], s83 offen offset:3072 sc1\n" \
"v_mfma_f32_16x16x32_bf16 v[88:91], v[140:143], v[192:195], v[88:91]\n" \
"ds_read_b64_tr_b16 v[132:133], v232 offset:16384\n" \
"v_accvgpr_read_b32 v208, a128\n" \
"v_accvgpr_read_b32 v209, a129\n" \
"v_mfma_f32_16x16x32_bf16 v[92:95], v[140:143], v[196:199], v[92:95]\n" \
"ds_read_b64_tr_b16 v[134:135], v232 offset:18432\n" \
"s_waitcnt lgkmcnt(8)\n" \
"buffer_load_dwordx4 v227, s[20:23], s52 offen lds\n" \
"v_mfma_f32_16x16x32_bf16 v[96:99], v[144:147], v[192:195], v[96:99]\n" \
"ds_read_b64_tr_b16 v[136:137], v233 offset:16384\n" \
"v_accvgpr_read_b32 v210, a130\n" \
"v_accvgpr_read_b32 v211, a131\n" \
"v_mfma_f32_16x16x32_bf16 v[100:103], v[144:147], v[196:199], v[100:103]\n" \
"ds_read_b64_tr_b16 v[138:139], v233 offset:18432\n" \
"s_add_u32 s52, s40, 3424256\n" \
"v_cvt_pk_bf16_f32 v216, v208, v209\n" \
"v_mfma_f32_16x16x32_bf16 v[104:107], v[148:151], v[192:195], v[104:107]\n" \
"ds_read_b64_tr_b16 v[140:141], v234 offset:16384\n" \
"s_add_u32 m0, s38, 82944\n" \
"v_cvt_pk_bf16_f32 v217, v210, v211\n" \
"v_mfma_f32_16x16x32_bf16 v[108:111], v[148:151], v[196:199], v[108:111]\n" \
"ds_read_b64_tr_b16 v[142:143], v234 offset:18432\n" \
"s_waitcnt lgkmcnt(8)\n" \
"v_accvgpr_read_b32 v208, a132\n" \
"v_mfma_f32_16x16x32_bf16 v[112:115], v[152:155], v[192:195], v[112:115]\n" \
"ds_read_b64_tr_b16 v[144:145], v235 offset:16384\n" \
"buffer_load_dwordx4 v228, s[20:23], s52 offen lds\n" \
"v_accvgpr_read_b32 v209, a133\n" \
"v_mfma_f32_16x16x32_bf16 v[116:119], v[152:155], v[196:199], v[116:119]\n" \
"ds_read_b64_tr_b16 v[146:147], v235 offset:18432\n" \
"v_accvgpr_read_b32 v210, a134\n" \
"v_accvgpr_read_b32 v211, a135\n" \
"v_mfma_f32_16x16x32_bf16 v[120:123], v[156:159], v[192:195], v[120:123]\n" \
"ds_read_b64_tr_b16 v[148:149], v236 offset:16384\n" \
"s_add_u32 s52, s40, 3440640\n" \
"v_cvt_pk_bf16_f32 v218, v208, v209\n" \
"v_mfma_f32_16x16x32_bf16 v[124:127], v[156:159], v[196:199], v[124:127]\n" \
"ds_read_b64_tr_b16 v[150:151], v236 offset:18432\n" \
"s_waitcnt lgkmcnt(8)\n" \
"s_add_u32 m0, s38, 83968\n" \
"v_mfma_f32_16x16x32_bf16 v[0:3], v[128:131], v[200:203], v[0:3]\n" \
"ds_read_b64_tr_b16 v[152:153], v237 offset:16384\n" \
"v_cvt_pk_bf16_f32 v219, v210, v211\n" \
"v_mfma_f32_16x16x32_bf16 v[4:7], v[128:131], v[204:207], v[4:7]\n" \
"ds_read_b64_tr_b16 v[154:155], v237 offset:18432\n" \
"buffer_load_dwordx4 v227, s[20:23], s52 offen lds\n" \
"v_mfma_f32_16x16x32_bf16 v[8:11], v[132:135], v[200:203], v[8:11]\n" \
"ds_read_b64_tr_b16 v[156:157], v238 offset:16384\n" \
"v_mfma_f32_16x16x32_bf16 v[12:15], v[132:135], v[204:207], v[12:15]\n" \
"ds_read_b64_tr_b16 v[158:159], v238 offset:18432\n" \
"s_waitcnt lgkmcnt(8)\n" \
"s_add_u32 s52, s40, 3457024\n" \
"v_mfma_f32_16x16x32_bf16 v[16:19], v[136:139], v[200:203], v[16:19]\n" \
"ds_read_b64_tr_b16 v[128:129], v231 offset:16640\n" \
"s_add_u32 m0, s38, 84992\n" \
"v_mfma_f32_16x16x32_bf16 v[20:23], v[136:139], v[204:207], v[20:23]\n" \
"ds_read_b64_tr_b16 v[130:131], v231 offset:18688\n" \
"s_add_u32 s83, s55, 20480\n" \
"buffer_store_dwordx4 v[220:223], v224, %[rs], s83 offen offset:1024 sc1\n" \
"v_mfma_f32_16x16x32_bf16 v[24:27], v[140:143], v[200:203], v[24:27]\n" \
"ds_read_b64_tr_b16 v[132:133], v232 offset:16640\n" \
"buffer_load_dwordx4 v228, s[20:23], s52 offen lds\n" \
"v_accvgpr_read_b32 v208, a136\n" \
"v_mfma_f32_16x16x32_bf16 v[28:31], v[140:143], v[204:207], v[28:31]\n" \
"ds_read_b64_tr_b16 v[134:135], v232 offset:18688\n" \
"s_waitcnt lgkmcnt(8)\n" \
"v_accvgpr_read_b32 v209, a137\n" \
"v_mfma_f32_16x16x32_bf16 v[32:35], v[144:147], v[200:203], v[32:35]\n" \
"ds_read_b64_tr_b16 v[136:137], v233 offset:16640\n" \
"v_accvgpr_read_b32 v210, a138\n" \
"v_accvgpr_read_b32 v211, a139\n" \
"v_mfma_f32_16x16x32_bf16 v[36:39], v[144:147], v[204:207], v[36:39]\n" \
"ds_read_b64_tr_b16 v[138:139], v233 offset:18688\n" \
"s_add_u32 s80, s58, 10240\n" \
"v_cvt_pk_bf16_f32 v220, v208, v209\n" \
"v_mfma_f32_16x16x32_bf16 v[40:43], v[148:151], v[200:203], v[40:43]\n" \
"ds_read_b64_tr_b16 v[140:141], v234 offset:16640\n" \
"v_cvt_pk_bf16_f32 v221, v210, v211\n" \
"v_accvgpr_read_b32 v208, a140\n" \
"v_mfma_f32_16x16x32_bf16 v[44:47], v[148:151], v[204:207], v[44:47]\n" \
"ds_read_b64_tr_b16 v[142:143], v234 offset:18688\n" \
"s_waitcnt lgkmcnt(8)\n" \
"buffer_load_dwordx4 v[192:195], v224, %[rs], s80 offen sc1\n" \
"v_mfma_f32_16x16x32_bf16 v[48:51], v[152:155], v[200:203], v[48:51]\n" \
"ds_read_b64_tr_b16 v[144:145], v235 offset:16640\n" \
"v_accvgpr_read_b32 v209, a141\n" \
"v_accvgpr_read_b32 v210, a142\n" \
"v_mfma_f32_16x16x32_bf16 v[52:55], v[152:155], v[204:207], v[52:55]\n" \
"ds_read_b64_tr_b16 v[146:147], v235 offset:18688\n" \
"v_accvgpr_read_b32 v211, a143\n" \
"v_cvt_pk_bf16_f32 v222, v208, v209\n" \
"v_mfma_f32_16x16x32_bf16 v[56:59], v[156:159], v[200:203], v[56:59]\n" \
"ds_read_b64_tr_b16 v[148:149], v236 offset:16640\n" \
"v_cvt_pk_bf16_f32 v223, v210, v211\n" \
"v_mfma_f32_16x16x32_bf16 v[60:63], v[156:159], v[204:207], v[60:63]\n" \
"ds_read_b64_tr_b16 v[150:151], v236 offset:18688\n" \
"s_waitcnt lgkmcnt(8)\n" \
"s_add_u32 s80, s71, 10240\n" \
"v_mfma_f32_16x16x32_bf16 v[64:67], v[128:131], v[200:203], v[64:67]\n" \
"ds_read_b64_tr_b16 v[152:153], v237 offset:16640\n" \
"v_mfma_f32_16x16x32_bf16 v[68:71], v[128:131], v[204:207], v[68:71]\n" \
"ds_read_b64_tr_b16 v[154:155], v237 offset:18688\n" \
"buffer_load_dwordx4 v[196:199], v224, %[rs], s80 offen sc1\n" \
"v_mfma_f32_16x16x32_bf16 v[72:75], v[132:135], v[200:203], v[72:75]\n" \
"ds_read_b64_tr_b16 v[156:157], v238 offset:16640\n" \
"v_mfma_f32_16x16x32_bf16 v[76:79], v[132:135], v[204:207], v[76:79]\n" \
"ds_read_b64_tr_b16 v[158:159], v238 offset:18688\n" \
"s_waitcnt lgkmcnt(8)\n" \
"s_waitcnt vmcnt(32)\n" \
"s_barrier\n" \
"v_mfma_f32_16x16x32_bf16 v[80:83], v[136:139], v[200:203], v[80:83]\n" \
"ds_read_b64_tr_b16 v[128:129], v231 offset:32768\n" \
"v_mfma_f32_16x16x32_bf16 v[84:87], v[136:139], v[204:207], v[84:87]\n" \
"ds_read_b64_tr_b16 v[130:131], v231 offset:34816\n" \
"buffer_store_dwordx4 v[212:215], v224, %[rs], s83 offen offset:3072 sc1\n" \
"v_accvgpr_read_b32 v208, a144\n" \
"v_mfma_f32_16x16x32_bf16 v[88:91], v[140:143], v[200:203], v[88:91]\n" \
"ds_read_b64_tr_b16 v[132:133], v232 offset:32768\n" \
"v_accvgpr_read_b32 v209, a145\n" \
"v_accvgpr_read_b32 v210, a146\n" \
"v_mfma_f32_16x16x32_bf16 v[92:95], v[140:143], v[204:207], v[92:95]\n" \
"ds_read_b64_tr_b16 v[134:135], v232 offset:34816\n" \
"s_waitcnt lgkmcnt(8)\n" \
"v_accvgpr_read_b32 v211, a147\n" \
"v_mfma_f32_16x16x32_bf16 v[96:99], v[144:147], v[200:203], v[96:99]\n" \
"ds_read_b64_tr_b16 v[136:137], v233 offset:32768\n" \
"v_cvt_pk_bf16_f32 v212, v208, v209\n" \
"v_cvt_pk_bf16_f32 v213, v210, v211\n" \
"v_mfma_f32_16x16x32_bf16 v[100:103], v[144:147], v[204:207], v[100:103]\n" \
"ds_read_b64_tr_b16 v[138:139], v233 offset:34816\n" \
"v_accvgpr_read_b32 v208, a148\n" \
"v_accvgpr_read_b32 v209, a149\n" \
"v_mfma_f32_16x16x32_bf16 v[104:107], v[148:151], v[200:203], v[104:107]\n" \
"ds_read_b64_tr_b16 v[140:141], v234 offset:32768\n" \
"v_accvgpr_read_b32 v210, a150\n" \
"v_accvgpr_read_b32 v211, a151\n" \
"v_mfma_f32_16x16x32_bf16 v[108:111], v[148:151], v[204:207], v[108:111]\n" \
"ds_read_b64_tr_b16 v[142:143], v234 offset:34816\n" \
"s_waitcnt lgkmcnt(8)\n" \
"v_cvt_pk_bf16_f32 v214, v208, v209\n" \
"v_mfma_f32_16x16x32_bf16 v[112:115], v[152:155], v[200:203], v[112:115]\n" \
"ds_read_b64_tr_b16 v[144:145], v235 offset:32768\n" \
"v_cvt_pk_bf16_f32 v215, v210, v211\n" \
"v_mfma_f32_16x16x32_bf16 v[116:119], v[152:155], v[204:207], v[116:119]\n" \
"ds_read_b64_tr_b16 v[146:147], v235 offset:34816\n" \
"v_mfma_f32_16x16x32_bf16 v[120:123], v[156:159], v[200:203], v[120:123]\n" \
"ds_read_b64_tr_b16 v[148:149], v236 offset:32768\n" \
"v_mfma_f32_16x16x32_bf16 v[124:127], v[156:159], v[204:207], v[124:127]\n" \
"ds_read_b64_tr_b16 v[150:151], v236 offset:34816\n" \
"s_waitcnt lgkmcnt(8)\n" \
"v_mfma_f32_16x16x32_bf16 v[0:3], v[128:131], v[160:163], v[0:3]\n" \
"ds_read_b64_tr_b16 v[152:153], v237 offset:32768\n" \
"v_mfma_f32_16x16x32_bf16 v[4:7], v[128:131], v[164:167], v[4:7]\n" \
"ds_read_b64_tr_b16 v[154:155], v237 offset:34816\n" \
"v_mfma_f32_16x16x32_bf16 v[8:11], v[132:135], v[160:163], v[8:11]\n" \
"ds_read_b64_tr_b16 v[156:157], v238 offset:32768\n" \
"v_mfma_f32_16x16x32_bf16 v[12:15], v[132:135], v[164:167], v[12:15]\n" \
"ds_read_b64_tr_b16 v[158:159], v238 offset:34816\n" \
"s_waitcnt lgkmcnt(8)\n" \
"s_add_u32 s83, s55, 32768\n" \
"buffer_store_dwordx4 v[216:219], v224, %[rs], s83 offen offset:0 sc1\n" \
"v_mfma_f32_16x16x32_bf16 v[16:19], v[136:139], v[160:163], v[16:19]\n" \
"ds_read_b64_tr_b16 v[128:129], v231 offset:33024\n" \
"v_accvgpr_read_b32 v208, a152\n" \
"v_accvgpr_read_b32 v209, a153\n" \
"v_mfma_f32_16x16x32_bf16 v[20:23], v[136:139], v[164:167], v[20:23]\n" \
"ds_read_b64_tr_b16 v[130:131], v231 offset:35072\n" \
"v_accvgpr_read_b32 v210, a154\n" \
"v_accvgpr_read_b32 v211, a155\n" \
"v_mfma_f32_16x16x32_bf16 v[24:27], v[140:143], v[160:163], v[24:27]\n" \
"ds_read_b64_tr_b16 v[132:133], v232 offset:33024\n" \
"v_cvt_pk_bf16_f32 v216, v208, v209\n" \
"v_cvt_pk_bf16_f32 v217, v210, v211\n" \
"v_mfma_f32_16x16x32_bf16 v[28:31], v[140:143], v[164:167], v[28:31]\n" \
"ds_read_b64_tr_b16 v[134:135], v232 offset:35072\n" \
"s_waitcnt lgkmcnt(8)\n" \
"v_accvgpr_read_b32 v208, a156\n" \
"v_mfma_f32_16x16x32_bf16 v[32:35], v[144:147], v[160:163], v[32:35]\n" \
"ds_read_b64_tr_b16 v[136:137], v233 offset:33024\n" \
"v_accvgpr_read_b32 v209, a157\n" \
"v_accvgpr_read_b32 v210, a158\n" \
"v_mfma_f32_16x16x32_bf16 v[36:39], v[144:147], v[164:167], v[36:39]\n" \
"ds_read_b64_tr_b16 v[138:139], v233 offset:35072\n" \
"s_add_u32 s80, s58, 11264\n" \
"v_accvgpr_read_b32 v211, a159\n" \
"v_mfma_f32_16x16x32_bf16 v[40:43], v[148:151], v[160:163], v[40:43]\n" \
"ds_read_b64_tr_b16 v[140:141], v234 offset:33024\n" \
"v_cvt_pk_bf16_f32 v218, v208, v209\n" \
"v_cvt_pk_bf16_f32 v219, v210, v211\n" \
"v_mfma_f32_16x16x32_bf16 v[44:47], v[148:151], v[164:167], v[44:47]\n" \
"ds_read_b64_tr_b16 v[142:143], v234 offset:35072\n" \
"s_waitcnt lgkmcnt(8)\n" \
"buffer_load_dwordx4 v[200:203], v224, %[rs], s80 offen sc1\n" \
"v_mfma_f32_16x16x32_bf16 v[48:51], v[152:155], v[160:163], v[48:51]\n" \
"ds_read_b64_tr_b16 v[144:145], v235 offset:33024\n" \
"v_mfma_f32_16x16x32_bf16 v[52:55], v[152:155], v[164:167], v[52:55]\n" \
"ds_read_b64_tr_b16 v[146:147], v235 offset:35072\n" \
"v_mfma_f32_16x16x32_bf16 v[56:59], v[156:159], v[160:163], v[56:59]\n" \
"ds_read_b64_tr_b16 v[148:149], v236 offset:33024\n" \
"v_mfma_f32_16x16x32_bf16 v[60:63], v[156:159], v[164:167], v[60:63]\n" \
"ds_read_b64_tr_b16 v[150:151], v236 offset:35072\n" \
"s_waitcnt lgkmcnt(8)\n" \
"s_add_u32 s80, s71, 11264\n" \
"v_mfma_f32_16x16x32_bf16 v[64:67], v[128:131], v[160:163], v[64:67]\n" \
"ds_read_b64_tr_b16 v[152:153], v237 offset:33024\n" \
"v_mfma_f32_16x16x32_bf16 v[68:71], v[128:131], v[164:167], v[68:71]\n" \
"ds_read_b64_tr_b16 v[154:155], v237 offset:35072\n" \
"buffer_load_dwordx4 v[204:207], v224, %[rs], s80 offen sc1\n" \
"v_mfma_f32_16x16x32_bf16 v[72:75], v[132:135], v[160:163], v[72:75]\n" \
"ds_read_b64_tr_b16 v[156:157], v238 offset:33024\n" \
"v_mfma_f32_16x16x32_bf16 v[76:79], v[132:135], v[164:167], v[76:79]\n" \
"ds_read_b64_tr_b16 v[158:159], v238 offset:35072\n" \
"s_waitcnt lgkmcnt(8)\n" \
"s_waitcnt vmcnt(28)\n" \
"s_barrier\n" \
"v_mfma_f32_16x16x32_bf16 v[80:83], v[136:139], v[160:163], v[80:83]\n" \
"ds_read_b64_tr_b16 v[128:129], v231 offset:49152\n" \
"v_mfma_f32_16x16x32_bf16 v[84:87], v[136:139], v[164:167], v[84:87]\n" \
"ds_read_b64_tr_b16 v[130:131], v231 offset:51200\n" \
"buffer_store_dwordx4 v[220:223], v224, %[rs], s83 offen offset:2048 sc1\n" \
"v_accvgpr_read_b32 v208, a160\n" \
"v_mfma_f32_16x16x32_bf16 v[88:91], v[140:143], v[160:163], v[88:91]\n" \
"ds_read_b64_tr_b16 v[132:133], v232 offset:49152\n" \
"v_accvgpr_read_b32 v209, a161\n" \
"v_accvgpr_read_b32 v210, a162\n" \
"v_mfma_f32_16x16x32_bf16 v[92:95], v[140:143], v[164:167], v[92:95]\n" \
"ds_read_b64_tr_b16 v[134:135], v232 offset:51200\n" \
"s_waitcnt lgkmcnt(8)\n" \
"v_accvgpr_read_b32 v211, a163\n" \
"v_mfma_f32_16x16x32_bf16 v[96:99], v[144:147], v[160:163], v[96:99]\n" \
"ds_read_b64_tr_b16 v[136:137], v233 offset:49152\n" \
"v_cvt_pk_bf16_f32 v220, v208, v209\n" \
"v_cvt_pk_bf16_f32 v221, v210, v211\n" \
"v_mfma_f32_16x16x32_bf16 v[100:103], v[144:147], v[164:167], v[100:103]\n" \
"ds_read_b64_tr_b16 v[138:139], v233 offset:51200\n" \
"v_accvgpr_read_b32 v208, a164\n" \
"v_accvgpr_read_b32 v209, a165\n" \
"v_mfma_f32_16x16x32_bf16 v[104:107], v[148:151], v[160:163], v[104:107]\n" \
"ds_read_b64_tr_b16 v[140:141], v234 offset:49152\n" \
"v_accvgpr_read_b32 v210, a166\n" \
"v_accvgpr_read_b32 v211, a167\n" \
"v_mfma_f32_16x16x32_bf16 v[108:111], v[148:151], v[164:167], v[108:111]\n" \
"ds_read_b64_tr_b16 v[142:143], v234 offset:51200\n" \
"s_waitcnt lgkmcnt(8)\n" \
"v_cvt_pk_bf16_f32 v222, v208, v209\n" \
"v_mfma_f32_16x16x32_bf16 v[112:115], v[152:155], v[160:163], v[112:115]\n" \
"ds_read_b64_tr_b16 v[144:145], v235 offset:49152\n" \
"v_cvt_pk_bf16_f32 v223, v210, v211\n" \
"v_mfma_f32_16x16x32_bf16 v[116:119], v[152:155], v[164:167], v[116:119]\n" \
"ds_read_b64_tr_b16 v[146:147], v235 offset:51200\n" \
"v_mfma_f32_16x16x32_bf16 v[120:123], v[156:159], v[160:163], v[120:123]\n" \
"ds_read_b64_tr_b16 v[148:149], v236 offset:49152\n" \
"v_mfma_f32_16x16x32_bf16 v[124:127], v[156:159], v[164:167], v[124:127]\n" \
"ds_read_b64_tr_b16 v[150:151], v236 offset:51200\n" \
"s_waitcnt lgkmcnt(8)\n" \
"v_mfma_f32_16x16x32_bf16 v[0:3], v[128:131], v[168:171], v[0:3]\n" \
"ds_read_b64_tr_b16 v[152:153], v237 offset:49152\n" \
"v_mfma_f32_16x16x32_bf16 v[4:7], v[128:131], v[172:175], v[4:7]\n" \
"ds_read_b64_tr_b16 v[154:155], v237 offset:51200\n" \
"v_mfma_f32_16x16x32_bf16 v[8:11], v[132:135], v[168:171], v[8:11]\n" \
"ds_read_b64_tr_b16 v[156:157], v238 offset:49152\n" \
"v_mfma_f32_16x16x32_bf16 v[12:15], v[132:135], v[172:175], v[12:15]\n" \
"ds_read_b64_tr_b16 v[158:159], v238 offset:51200\n" \
"s_waitcnt lgkmcnt(8)\n" \
"s_add_u32 s83, s55, 36864\n" \
"buffer_store_dwordx4 v[212:215], v224, %[rs], s83 offen offset:0 sc1\n" \
"v_mfma_f32_16x16x32_bf16 v[16:19], v[136:139], v[168:171], v[16:19]\n" \
"ds_read_b64_tr_b16 v[128:129], v231 offset:49408\n" \
"v_accvgpr_read_b32 v208, a168\n" \
"v_accvgpr_read_b32 v209, a169\n" \
"v_mfma_f32_16x16x32_bf16 v[20:23], v[136:139], v[172:175], v[20:23]\n" \
"ds_read_b64_tr_b16 v[130:131], v231 offset:51456\n" \
"v_accvgpr_read_b32 v210, a170\n" \
"v_accvgpr_read_b32 v211, a171\n" \
"v_mfma_f32_16x16x32_bf16 v[24:27], v[140:143], v[168:171], v[24:27]\n" \
"ds_read_b64_tr_b16 v[132:133], v232 offset:49408\n" \
"v_add_u32 v231, 0xfffe8000, v231\n" \
"v_cvt_pk_bf16_f32 v212, v208, v209\n" \
"v_mfma_f32_16x16x32_bf16 v[28:31], v[140:143], v[172:175], v[28:31]\n" \
"ds_read_b64_tr_b16 v[134:135], v232 offset:51456\n" \
"s_waitcnt lgkmcnt(8)\n" \
"v_cvt_pk_bf16_f32 v213, v210, v211\n" \
"v_mfma_f32_16x16x32_bf16 v[32:35], v[144:147], v[168:171], v[32:35]\n" \
"ds_read_b64_tr_b16 v[136:137], v233 offset:49408\n" \
"v_add_u32 v232, 0xfffe8000, v232\n" \
"v_accvgpr_read_b32 v208, a172\n" \
"v_mfma_f32_16x16x32_bf16 v[36:39], v[144:147], v[172:175], v[36:39]\n" \
"ds_read_b64_tr_b16 v[138:139], v233 offset:51456\n" \
"s_add_u32 s80, s58, 12288\n" \
"v_accvgpr_read_b32 v209, a173\n" \
"v_mfma_f32_16x16x32_bf16 v[40:43], v[148:151], v[168:171], v[40:43]\n" \
"ds_read_b64_tr_b16 v[140:141], v234 offset:49408\n" \
"v_add_u32 v233, 0xfffe8000, v233\n" \
"v_accvgpr_read_b32 v210, a174\n" \
"v_mfma_f32_16x16x32_bf16 v[44:47], v[148:151], v[172:175], v[44:47]\n" \
"ds_read_b64_tr_b16 v[142:143], v234 offset:51456\n" \
"s_waitcnt lgkmcnt(8)\n" \
"buffer_load_dwordx4 v[160:163], v224, %[rs], s80 offen sc1\n" \
"v_mfma_f32_16x16x32_bf16 v[48:51], v[152:155], v[168:171], v[48:51]\n" \
"ds_read_b64_tr_b16 v[144:145], v235 offset:49408\n" \
"v_add_u32 v234, 0xfffe8000, v234\n" \
"v_accvgpr_read_b32 v211, a175\n" \
"v_mfma_f32_16x16x32_bf16 v[52:55], v[152:155], v[172:175], v[52:55]\n" \
"ds_read_b64_tr_b16 v[146:147], v235 offset:51456\n" \
"v_cvt_pk_bf16_f32 v214, v208, v209\n" \
"v_cvt_pk_bf16_f32 v215, v210, v211\n" \
"v_mfma_f32_16x16x32_bf16 v[56:59], v[156:159], v[168:171], v[56:59]\n" \
"ds_read_b64_tr_b16 v[148:149], v236 offset:49408\n" \
"v_add_u32 v235, 0xfffe8000, v235\n" \
"v_mfma_f32_16x16x32_bf16 v[60:63], v[156:159], v[172:175], v[60:63]\n" \
"ds_read_b64_tr_b16 v[150:151], v236 offset:51456\n" \
"s_waitcnt lgkmcnt(8)\n" \
"s_add_u32 s80, s71, 12288\n" \
"v_mfma_f32_16x16x32_bf16 v[64:67], v[128:131], v[168:171], v[64:67]\n" \
"ds_read_b64_tr_b16 v[152:153], v237 offset:49408\n" \
"v_add_u32 v236, 0xfffe8000, v236\n" \
"v_mfma_f32_16x16x32_bf16 v[68:71], v[128:131], v[172:175], v[68:71]\n" \
"ds_read_b64_tr_b16 v[154:155], v237 offset:51456\n" \
"buffer_load_dwordx4 v[164:167], v224, %[rs], s80 offen sc1\n" \
"v_mfma_f32_16x16x32_bf16 v[72:75], v[132:135], v[168:171], v[72:75]\n" \
"ds_read_b64_tr_b16 v[156:157], v238 offset:49408\n" \
"v_add_u32 v237, 0xfffe8000, v237\n" \
"v_mfma_f32_16x16x32_bf16 v[76:79], v[132:135], v[172:175], v[76:79]\n" \
"ds_read_b64_tr_b16 v[158:159], v238 offset:51456\n" \
"s_waitcnt lgkmcnt(8)\n" \
"s_waitcnt vmcnt(24)\n" \
"s_barrier\n" \
"v_mfma_f32_16x16x32_bf16 v[80:83], v[136:139], v[168:171], v[80:83]\n" \
"ds_read_b64_tr_b16 v[128:129], v231 offset:0\n" \
"v_add_u32 v238, 0xfffe8000, v238\n" \
"v_mfma_f32_16x16x32_bf16 v[84:87], v[136:139], v[172:175], v[84:87]\n" \
"ds_read_b64_tr_b16 v[130:131], v231 offset:2048\n" \
"buffer_store_dwordx4 v[216:219], v224, %[rs], s83 offen offset:2048 sc1\n" \
"v_accvgpr_read_b32 v208, a176\n" \
"v_mfma_f32_16x16x32_bf16 v[88:91], v[140:143], v[168:171], v[88:91]\n" \
"ds_read_b64_tr_b16 v[132:133], v232 offset:0\n" \
"v_accvgpr_read_b32 v209, a177\n" \
"v_accvgpr_read_b32 v210, a178\n" \
"v_mfma_f32_16x16x32_bf16 v[92:95], v[140:143], v[172:175], v[92:95]\n" \
"ds_read_b64_tr_b16 v[134:135], v232 offset:2048\n" \
"s_waitcnt lgkmcnt(8)\n" \
"v_accvgpr_read_b32 v211, a179\n" \
"v_mfma_f32_16x16x32_bf16 v[96:99], v[144:147], v[168:171], v[96:99]\n" \
"ds_read_b64_tr_b16 v[136:137], v233 offset:0\n" \
"v_cvt_pk_bf16_f32 v216, v208, v209\n" \
"v_cvt_pk_bf16_f32 v217, v210, v211\n" \
"v_mfma_f32_16x16x32_bf16 v[100:103], v[144:147], v[172:175], v[100:103]\n" \
"ds_read_b64_tr_b16 v[138:139], v233 offset:2048\n" \
"v_accvgpr_read_b32 v208, a180\n" \
"v_accvgpr_read_b32 v209, a181\n" \
"v_mfma_f32_16x16x32_bf16 v[104:107], v[148:151], v[168:171], v[104:107]\n" \
"ds_read_b64_tr_b16 v[140:141], v234 offset:0\n" \
"v_accvgpr_read_b32 v210, a182\n" \
"v_accvgpr_read_b32 v211, a183\n" \
"v_mfma_f32_16x16x32_bf16 v[108:111], v[148:151], v[172:175], v[108:111]\n" \
"ds_read_b64_tr_b16 v[142:143], v234 offset:2048\n" \
"s_waitcnt lgkmcnt(8)\n" \
"v_cvt_pk_bf16_f32 v218, v208, v209\n" \
"v_mfma_f32_16x16x32_bf16 v[112:115], v[152:155], v[168:171], v[112:115]\n" \
"ds_read_b64_tr_b16 v[144:145], v235 offset:0\n" \
"v_cvt_pk_bf16_f32 v219, v210, v211\n" \
"v_mfma_f32_16x16x32_bf16 v[116:119], v[152:155], v[172:175], v[116:119]\n" \
"ds_read_b64_tr_b16 v[146:147], v235 offset:2048\n" \
"v_mfma_f32_16x16x32_bf16 v[120:123], v[156:159], v[168:171], v[120:123]\n" \
"ds_read_b64_tr_b16 v[148:149], v236 offset:0\n" \
"v_mfma_f32_16x16x32_bf16 v[124:127], v[156:159], v[172:175], v[124:127]\n" \
"ds_read_b64_tr_b16 v[150:151], v236 offset:2048\n" \
"s_waitcnt lgkmcnt(8)\n" \
"v_mfma_f32_16x16x32_bf16 v[0:3], v[128:131], v[176:179], v[0:3]\n" \
"ds_read_b64_tr_b16 v[152:153], v237 offset:0\n" \
"v_mfma_f32_16x16x32_bf16 v[4:7], v[128:131], v[180:183], v[4:7]\n" \
"ds_read_b64_tr_b16 v[154:155], v237 offset:2048\n" \
"v_mfma_f32_16x16x32_bf16 v[8:11], v[132:135], v[176:179], v[8:11]\n" \
"ds_read_b64_tr_b16 v[156:157], v238 offset:0\n" \
"v_mfma_f32_16x16x32_bf16 v[12:15], v[132:135], v[180:183], v[12:15]\n" \
"ds_read_b64_tr_b16 v[158:159], v238 offset:2048\n" \
"s_waitcnt lgkmcnt(8)\n" \
"s_add_u32 s83, s55, 32768\n" \
"buffer_store_dwordx4 v[220:223], v224, %[rs], s83 offen offset:1024 sc1\n" \
"v_mfma_f32_16x16x32_bf16 v[16:19], v[136:139], v[176:179], v[16:19]\n" \
"ds_read_b64_tr_b16 v[128:129], v231 offset:256\n" \
"v_accvgpr_read_b32 v208, a184\n" \
"v_accvgpr_read_b32 v209, a185\n" \
"v_mfma_f32_16x16x32_bf16 v[20:23], v[136:139], v[180:183], v[20:23]\n" \
"ds_read_b64_tr_b16 v[130:131], v231 offset:2304\n" \
"v_accvgpr_read_b32 v210, a186\n" \
"v_accvgpr_read_b32 v211, a187\n" \
"v_mfma_f32_16x16x32_bf16 v[24:27], v[140:143], v[176:179], v[24:27]\n" \
"ds_read_b64_tr_b16 v[132:133], v232 offset:256\n" \
"v_cvt_pk_bf16_f32 v220, v208, v209\n" \
"v_cvt_pk_bf16_f32 v221, v210, v211\n" \
"v_mfma_f32_16x16x32_bf16 v[28:31], v[140:143], v[180:183], v[28:31]\n" \
"ds_read_b64_tr_b16 v[134:135], v232 offset:2304\n" \
"s_waitcnt lgkmcnt(8)\n" \
"v_accvgpr_read_b32 v208, a188\n" \
"v_mfma_f32_16x16x32_bf16 v[32:35], v[144:147], v[176:179], v[32:35]\n" \
"ds_read_b64_tr_b16 v[136:137], v233 offset:256\n" \
"v_accvgpr_read_b32 v209, a189\n" \
"v_accvgpr_read_b32 v210, a190\n" \
"v_mfma_f32_16x16x32_bf16 v[36:39], v[144:147], v[180:183], v[36:39]\n" \
"ds_read_b64_tr_b16 v[138:139], v233 offset:2304\n" \
"s_add_u32 s80, s58, 13312\n" \
"v_accvgpr_read_b32 v211, a191\n" \
"v_mfma_f32_16x16x32_bf16 v[40:43], v[148:151], v[176:179], v[40:43]\n" \
"ds_read_b64_tr_b16 v[140:141], v234 offset:256\n" \
"v_cvt_pk_bf16_f32 v222, v208, v209\n" \
"v_cvt_pk_bf16_f32 v223, v210, v211\n" \
"v_mfma_f32_16x16x32_bf16 v[44:47], v[148:151], v[180:183], v[44:47]\n" \
"ds_read_b64_tr_b16 v[142:143], v234 offset:2304\n" \
"s_waitcnt lgkmcnt(8)\n" \
"buffer_load_dwordx4 v[168:171], v224, %[rs], s80 offen sc1\n" \
"v_mfma_f32_16x16x32_bf16 v[48:51], v[152:155], v[176:179], v[48:51]\n" \
"ds_read_b64_tr_b16 v[144:145], v235 offset:256\n" \
"v_mfma_f32_16x16x32_bf16 v[52:55], v[152:155], v[180:183], v[52:55]\n" \
"ds_read_b64_tr_b16 v[146:147], v235 offset:2304\n" \
"v_mfma_f32_16x16x32_bf16 v[56:59], v[156:159], v[176:179], v[56:59]\n" \
"ds_read_b64_tr_b16 v[148:149], v236 offset:256\n" \
"v_mfma_f32_16x16x32_bf16 v[60:63], v[156:159], v[180:183], v[60:63]\n" \
"ds_read_b64_tr_b16 v[150:151], v236 offset:2304\n" \
"s_waitcnt lgkmcnt(8)\n" \
"s_add_u32 s80, s71, 13312\n" \
"v_mfma_f32_16x16x32_bf16 v[64:67], v[128:131], v[176:179], v[64:67]\n" \
"ds_read_b64_tr_b16 v[152:153], v237 offset:256\n" \
"v_mfma_f32_16x16x32_bf16 v[68:71], v[128:131], v[180:183], v[68:71]\n" \
"ds_read_b64_tr_b16 v[154:155], v237 offset:2304\n" \
"buffer_load_dwordx4 v[172:175], v224, %[rs], s80 offen sc1\n" \
"v_mfma_f32_16x16x32_bf16 v[72:75], v[132:135], v[176:179], v[72:75]\n" \
"ds_read_b64_tr_b16 v[156:157], v238 offset:256\n" \
"v_mfma_f32_16x16x32_bf16 v[76:79], v[132:135], v[180:183], v[76:79]\n" \
"ds_read_b64_tr_b16 v[158:159], v238 offset:2304\n" \
"s_waitcnt lgkmcnt(8)\n" \
"s_waitcnt vmcnt(20)\n" \
"s_barrier\n" \
"v_mfma_f32_16x16x32_bf16 v[80:83], v[136:139], v[176:179], v[80:83]\n" \
"ds_read_b64_tr_b16 v[128:129], v231 offset:16384\n" \
"v_mfma_f32_16x16x32_bf16 v[84:87], v[136:139], v[180:183], v[84:87]\n" \
"ds_read_b64_tr_b16 v[130:131], v231 offset:18432\n" \
"buffer_store_dwordx4 v[212:215], v224, %[rs], s83 offen offset:3072 sc1\n" \
"v_accvgpr_read_b32 v208, a192\n" \
"v_mfma_f32_16x16x32_bf16 v[88:91], v[140:143], v[176:179], v[88:91]\n" \
"ds_read_b64_tr_b16 v[132:133], v232 offset:16384\n" \
"v_accvgpr_read_b32 v209, a193\n" \
"v_accvgpr_read_b32 v210, a194\n" \
"v_mfma_f32_16x16x32_bf16 v[92:95], v[140:143], v[180:183], v[92:95]\n" \
"ds_read_b64_tr_b16 v[134:135], v232 offset:18432\n" \
"s_waitcnt lgkmcnt(8)\n" \
"v_accvgpr_read_b32 v211, a195\n" \
"v_mfma_f32_16x16x32_bf16 v[96:99], v[144:147], v[176:179], v[96:99]\n" \
"ds_read_b64_tr_b16 v[136:137], v233 offset:16384\n" \
"v_cvt_pk_bf16_f32 v212, v208, v209\n" \
"v_cvt_pk_bf16_f32 v213, v210, v211\n" \
"v_mfma_f32_16x16x32_bf16 v[100:103], v[144:147], v[180:183], v[100:103]\n" \
"ds_read_b64_tr_b16 v[138:139], v233 offset:18432\n" \
"v_accvgpr_read_b32 v208, a196\n" \
"v_accvgpr_read_b32 v209, a197\n" \
"v_mfma_f32_16x16x32_bf16 v[104:107], v[148:151], v[176:179], v[104:107]\n" \
"ds_read_b64_tr_b16 v[140:141], v234 offset:16384\n" \
"v_accvgpr_read_b32 v210, a198\n" \
"v_accvgpr_read_b32 v211, a199\n" \
"v_mfma_f32_16x16x32_bf16 v[108:111], v[148:151], v[180:183], v[108:111]\n" \
"ds_read_b64_tr_b16 v[142:143], v234 offset:18432\n" \
"s_waitcnt lgkmcnt(8)\n" \
"v_cvt_pk_bf16_f32 v214, v208, v209\n" \
"v_mfma_f32_16x16x32_bf16 v[112:115], v[152:155], v[176:179], v[112:115]\n" \
"ds_read_b64_tr_b16 v[144:145], v235 offset:16384\n" \
"v_cvt_pk_bf16_f32 v215, v210, v211\n" \
"v_mfma_f32_16x16x32_bf16 v[116:119], v[152:155], v[180:183], v[116:119]\n" \
"ds_read_b64_tr_b16 v[146:147], v235 offset:18432\n" \
"v_mfma_f32_16x16x32_bf16 v[120:123], v[156:159], v[176:179], v[120:123]\n" \
"ds_read_b64_tr_b16 v[148:149], v236 offset:16384\n" \
"v_mfma_f32_16x16x32_bf16 v[124:127], v[156:159], v[180:183], v[124:127]\n" \
"ds_read_b64_tr_b16 v[150:151], v236 offset:18432\n" \
"s_waitcnt lgkmcnt(8)\n" \
"v_mfma_f32_16x16x32_bf16 v[0:3], v[128:131], v[184:187], v[0:3]\n" \
"ds_read_b64_tr_b16 v[152:153], v237 offset:16384\n" \
"v_mfma_f32_16x16x32_bf16 v[4:7], v[128:131], v[188:191], v[4:7]\n" \
"ds_read_b64_tr_b16 v[154:155], v237 offset:18432\n" \
"v_mfma_f32_16x16x32_bf16 v[8:11], v[132:135], v[184:187], v[8:11]\n" \
"ds_read_b64_tr_b16 v[156:157], v238 offset:16384\n" \
"v_mfma_f32_16x16x32_bf16 v[12:15], v[132:135], v[188:191], v[12:15]\n" \
"ds_read_b64_tr_b16 v[158:159], v238 offset:18432\n" \
"s_waitcnt lgkmcnt(8)\n" \
"s_add_u32 s83, s55, 36864\n" \
"buffer_store_dwordx4 v[216:219], v224, %[rs], s83 offen offset:1024 sc1\n" \
"v_mfma_f32_16x16x32_bf16 v[16:19], v[136:139], v[184:187], v[16:19]\n" \
"ds_read_b64_tr_b16 v[128:129], v231 offset:16640\n" \
"v_accvgpr_read_b32 v208, a200\n" \
"v_accvgpr_read_b32 v209, a201\n" \
"v_mfma_f32_16x16x32_bf16 v[20:23], v[136:139], v[188:191], v[20:23]\n" \
"ds_read_b64_tr_b16 v[130:131], v231 offset:18688\n" \
"v_accvgpr_read_b32 v210, a202\n" \
"v_accvgpr_read_b32 v211, a203\n" \
"v_mfma_f32_16x16x32_bf16 v[24:27], v[140:143], v[184:187], v[24:27]\n" \
"ds_read_b64_tr_b16 v[132:133], v232 offset:16640\n" \
"v_cvt_pk_bf16_f32 v216, v208, v209\n" \
"v_cvt_pk_bf16_f32 v217, v210, v211\n" \
"v_mfma_f32_16x16x32_bf16 v[28:31], v[140:143], v[188:191], v[28:31]\n" \
"ds_read_b64_tr_b16 v[134:135], v232 offset:18688\n" \
"s_waitcnt lgkmcnt(8)\n" \
"v_accvgpr_read_b32 v208, a204\n" \
"v_mfma_f32_16x16x32_bf16 v[32:35], v[144:147], v[184:187], v[32:35]\n" \
"ds_read_b64_tr_b16 v[136:137], v233 offset:16640\n" \
"v_accvgpr_read_b32 v209, a205\n" \
"v_accvgpr_read_b32 v210, a206\n" \
"v_mfma_f32_16x16x32_bf16 v[36:39], v[144:147], v[188:191], v[36:39]\n" \
"ds_read_b64_tr_b16 v[138:139], v233 offset:18688\n" \
"v_accvgpr_read_b32 v211, a207\n" \
"v_cvt_pk_bf16_f32 v218, v208, v209\n" \
"v_mfma_f32_16x16x32_bf16 v[40:43], v[148:151], v[184:187], v[40:43]\n" \
"ds_read_b64_tr_b16 v[140:141], v234 offset:16640\n" \
"v_cvt_pk_bf16_f32 v219, v210, v211\n" \
"v_mfma_f32_16x16x32_bf16 v[44:47], v[148:151], v[188:191], v[44:47]\n" \
"ds_read_b64_tr_b16 v[142:143], v234 offset:18688\n" \
"s_waitcnt lgkmcnt(8)\n" \
"v_mfma_f32_16x16x32_bf16 v[48:51], v[152:155], v[184:187], v[48:51]\n" \
"ds_read_b64_tr_b16 v[144:145], v235 offset:16640\n" \
"v_mfma_f32_16x16x32_bf16 v[52:55], v[152:155], v[188:191], v[52:55]\n" \
"ds_read_b64_tr_b16 v[146:147], v235 offset:18688\n" \
"v_mfma_f32_16x16x32_bf16 v[56:59], v[156:159], v[184:187], v[56:59]\n" \
"ds_read_b64_tr_b16 v[148:149], v236 offset:16640\n" \
"v_mfma_f32_16x16x32_bf16 v[60:63], v[156:159], v[188:191], v[60:63]\n" \
"ds_read_b64_tr_b16 v[150:151], v236 offset:18688\n" \
"s_waitcnt lgkmcnt(8)\n" \
"v_mfma_f32_16x16x32_bf16 v[64:67], v[128:131], v[184:187], v[64:67]\n" \
"ds_read_b64_tr_b16 v[152:153], v237 offset:16640\n" \
"v_mfma_f32_16x16x32_bf16 v[68:71], v[128:131], v[188:191], v[68:71]\n" \
"ds_read_b64_tr_b16 v[154:155], v237 offset:18688\n" \
"buffer_store_dwordx4 v[220:223], v224, %[rs], s83 offen offset:3072 sc1\n" \
"v_accvgpr_read_b32 v208, a208\n" \
"v_mfma_f32_16x16x32_bf16 v[72:75], v[132:135], v[184:187], v[72:75]\n" \
"ds_read_b64_tr_b16 v[156:157], v238 offset:16640\n" \
"v_accvgpr_read_b32 v209, a209\n" \
"v_accvgpr_read_b32 v210, a210\n" \
"v_mfma_f32_16x16x32_bf16 v[76:79], v[132:135], v[188:191], v[76:79]\n" \
"ds_read_b64_tr_b16 v[158:159], v238 offset:18688\n" \
"s_waitcnt lgkmcnt(8)\n" \
"v_accvgpr_read_b32 v211, a211\n" \
"s_waitcnt vmcnt(15)\n" \
"s_barrier\n" \
"v_mfma_f32_16x16x32_bf16 v[80:83], v[136:139], v[184:187], v[80:83]\n" \
"ds_read_b64_tr_b16 v[128:129], v231 offset:32768\n" \
"v_cvt_pk_bf16_f32 v220, v208, v209\n" \
"v_cvt_pk_bf16_f32 v221, v210, v211\n" \
"v_mfma_f32_16x16x32_bf16 v[84:87], v[136:139], v[188:191], v[84:87]\n" \
"ds_read_b64_tr_b16 v[130:131], v231 offset:34816\n" \
"v_accvgpr_read_b32 v208, a212\n" \
"v_accvgpr_read_b32 v209, a213\n" \
"v_mfma_f32_16x16x32_bf16 v[88:91], v[140:143], v[184:187], v[88:91]\n" \
"ds_read_b64_tr_b16 v[132:133], v232 offset:32768\n" \
"v_accvgpr_read_b32 v210, a214\n" \
"v_accvgpr_read_b32 v211, a215\n" \
"v_mfma_f32_16x16x32_bf16 v[92:95], v[140:143], v[188:191], v[92:95]\n" \
"ds_read_b64_tr_b16 v[134:135], v232 offset:34816\n" \
"s_waitcnt lgkmcnt(8)\n" \
"v_cvt_pk_bf16_f32 v222, v208, v209\n" \
"v_mfma_f32_16x16x32_bf16 v[96:99], v[144:147], v[184:187], v[96:99]\n" \
"ds_read_b64_tr_b16 v[136:137], v233 offset:32768\n" \
"v_cvt_pk_bf16_f32 v223, v210, v211\n" \
"v_mfma_f32_16x16x32_bf16 v[100:103], v[144:147], v[188:191], v[100:103]\n" \
"ds_read_b64_tr_b16 v[138:139], v233 offset:34816\n" \
"v_mfma_f32_16x16x32_bf16 v[104:107], v[148:151], v[184:187], v[104:107]\n" \
"ds_read_b64_tr_b16 v[140:141], v234 offset:32768\n" \
"v_mfma_f32_16x16x32_bf16 v[108:111], v[148:151], v[188:191], v[108:111]\n" \
"ds_read_b64_tr_b16 v[142:143], v234 offset:34816\n" \
"s_waitcnt lgkmcnt(8)\n" \
"v_mfma_f32_16x16x32_bf16 v[112:115], v[152:155], v[184:187], v[112:115]\n" \
"ds_read_b64_tr_b16 v[144:145], v235 offset:32768\n" \
"v_mfma_f32_16x16x32_bf16 v[116:119], v[152:155], v[188:191], v[116:119]\n" \
"ds_read_b64_tr_b16 v[146:147], v235 offset:34816\n" \
"v_mfma_f32_16x16x32_bf16 v[120:123], v[156:159], v[184:187], v[120:123]\n" \
"ds_read_b64_tr_b16 v[148:149], v236 offset:32768\n" \
"v_mfma_f32_16x16x32_bf16 v[124:127], v[156:159], v[188:191], v[124:127]\n" \
"ds_read_b64_tr_b16 v[150:151], v236 offset:34816\n" \
"s_waitcnt lgkmcnt(8)\n" \
"s_add_u32 s83, s55, 49152\n" \
"buffer_store_dwordx4 v[212:215], v224, %[rs], s83 offen offset:0 sc1\n" \
"v_mfma_f32_16x16x32_bf16 v[0:3], v[128:131], v[192:195], v[0:3]\n" \
"ds_read_b64_tr_b16 v[152:153], v237 offset:32768\n" \
"v_accvgpr_read_b32 v208, a216\n" \
"v_accvgpr_read_b32 v209, a217\n" \
"v_mfma_f32_16x16x32_bf16 v[4:7], v[128:131], v[196:199], v[4:7]\n" \
"ds_read_b64_tr_b16 v[154:155], v237 offset:34816\n" \
"v_accvgpr_read_b32 v210, a218\n" \
"v_accvgpr_read_b32 v211, a219\n" \
"v_mfma_f32_16x16x32_bf16 v[8:11], v[132:135], v[192:195], v[8:11]\n" \
"ds_read_b64_tr_b16 v[156:157], v238 offset:32768\n" \
"v_cvt_pk_bf16_f32 v212, v208, v209\n" \
"v_cvt_pk_bf16_f32 v213, v210, v211\n" \
"v_mfma_f32_16x16x32_bf16 v[12:15], v[132:135], v[196:199], v[12:15]\n" \
"ds_read_b64_tr_b16 v[158:159], v238 offset:34816\n" \
"s_waitcnt lgkmcnt(8)\n" \
"v_accvgpr_read_b32 v208, a220\n" \
"v_mfma_f32_16x16x32_bf16 v[16:19], v[136:139], v[192:195], v[16:19]\n" \
"ds_read_b64_tr_b16 v[128:129], v231 offset:33024\n" \
"v_accvgpr_read_b32 v209, a221\n" \
"v_accvgpr_read_b32 v210, a222\n" \
"v_mfma_f32_16x16x32_bf16 v[20:23], v[136:139], v[196:199], v[20:23]\n" \
"ds_read_b64_tr_b16 v[130:131], v231 offset:35072\n" \
"v_accvgpr_read_b32 v211, a223\n" \
"v_cvt_pk_bf16_f32 v214, v208, v209\n" \
"v_mfma_f32_16x16x32_bf16 v[24:27], v[140:143], v[192:195], v[24:27]\n" \
"ds_read_b64_tr_b16 v[132:133], v232 offset:33024\n" \
"v_cvt_pk_bf16_f32 v215, v210, v211\n" \
"v_mfma_f32_16x16x32_bf16 v[28:31], v[140:143], v[196:199], v[28:31]\n" \
"ds_read_b64_tr_b16 v[134:135], v232 offset:35072\n" \
"s_waitcnt lgkmcnt(8)\n" \
"v_mfma_f32_16x16x32_bf16 v[32:35], v[144:147], v[192:195], v[32:35]\n" \
"ds_read_b64_tr_b16 v[136:137], v233 offset:33024\n" \
"v_mfma_f32_16x16x32_bf16 v[36:39], v[144:147], v[196:199], v[36:39]\n" \
"ds_read_b64_tr_b16 v[138:139], v233 offset:35072\n" \
"v_mfma_f32_16x16x32_bf16 v[40:43], v[148:151], v[192:195], v[40:43]\n" \
"ds_read_b64_tr_b16 v[140:141], v234 offset:33024\n" \
"v_mfma_f32_16x16x32_bf16 v[44:47], v[148:151], v[196:199], v[44:47]\n" \
"ds_read_b64_tr_b16 v[142:143], v234 offset:35072\n" \
"s_waitcnt lgkmcnt(8)\n" \
"v_mfma_f32_16x16x32_bf16 v[48:51], v[152:155], v[192:195], v[48:51]\n" \
"ds_read_b64_tr_b16 v[144:145], v235 offset:33024\n" \
"v_mfma_f32_16x16x32_bf16 v[52:55], v[152:155], v[196:199], v[52:55]\n" \
"ds_read_b64_tr_b16 v[146:147], v235 offset:35072\n" \
"buffer_store_dwordx4 v[216:219], v224, %[rs], s83 offen offset:2048 sc1\n" \
"v_accvgpr_read_b32 v208, a224\n" \
"v_mfma_f32_16x16x32_bf16 v[56:59], v[156:159], v[192:195], v[56:59]\n" \
"ds_read_b64_tr_b16 v[148:149], v236 offset:33024\n" \
"v_accvgpr_read_b32 v209, a225\n" \
"v_accvgpr_read_b32 v210, a226\n" \
"v_mfma_f32_16x16x32_bf16 v[60:63], v[156:159], v[196:199], v[60:63]\n" \
"ds_read_b64_tr_b16 v[150:151], v236 offset:35072\n" \
"s_waitcnt lgkmcnt(8)\n" \
"v_accvgpr_read_b32 v211, a227\n" \
"v_mfma_f32_16x16x32_bf16 v[64:67], v[128:131], v[192:195], v[64:67]\n" \
"ds_read_b64_tr_b16 v[152:153], v237 offset:33024\n" \
"v_cvt_pk_bf16_f32 v216, v208, v209\n" \
"v_cvt_pk_bf16_f32 v217, v210, v211\n" \
"v_mfma_f32_16x16x32_bf16 v[68:71], v[128:131], v[196:199], v[68:71]\n" \
"ds_read_b64_tr_b16 v[154:155], v237 offset:35072\n" \
"v_accvgpr_read_b32 v208, a228\n" \
"v_accvgpr_read_b32 v209, a229\n" \
"v_mfma_f32_16x16x32_bf16 v[72:75], v[132:135], v[192:195], v[72:75]\n" \
"ds_read_b64_tr_b16 v[156:157], v238 offset:33024\n" \
"v_accvgpr_read_b32 v210, a230\n" \
"v_accvgpr_read_b32 v211, a231\n" \
"v_mfma_f32_16x16x32_bf16 v[76:79], v[132:135], v[196:199], v[76:79]\n" \
"ds_read_b64_tr_b16 v[158:159], v238 offset:35072\n" \
"s_waitcnt lgkmcnt(8)\n" \
"v_cvt_pk_bf16_f32 v218, v208, v209\n" \
"s_waitcnt vmcnt(13)\n" \
"s_barrier\n" \
"v_mfma_f32_16x16x32_bf16 v[80:83], v[136:139], v[192:195], v[80:83]\n" \
"ds_read_b64_tr_b16 v[128:129], v231 offset:49152\n" \
"v_cvt_pk_bf16_f32 v219, v210, v211\n" \
"v_mfma_f32_16x16x32_bf16 v[84:87], v[136:139], v[196:199], v[84:87]\n" \
"ds_read_b64_tr_b16 v[130:131], v231 offset:51200\n" \
"v_mfma_f32_16x16x32_bf16 v[88:91], v[140:143], v[192:195], v[88:91]\n" \
"ds_read_b64_tr_b16 v[132:133], v232 offset:49152\n" \
"v_mfma_f32_16x16x32_bf16 v[92:95], v[140:143], v[196:199], v[92:95]\n" \
"ds_read_b64_tr_b16 v[134:135], v232 offset:51200\n" \
"s_waitcnt lgkmcnt(8)\n" \
"v_mfma_f32_16x16x32_bf16 v[96:99], v[144:147], v[192:195], v[96:99]\n" \
"ds_read_b64_tr_b16 v[136:137], v233 offset:49152\n" \
"v_mfma_f32_16x16x32_bf16 v[100:103], v[144:147], v[196:199], v[100:103]\n" \
"ds_read_b64_tr_b16 v[138:139], v233 offset:51200\n" \
"v_mfma_f32_16x16x32_bf16 v[104:107], v[148:151], v[192:195], v[104:107]\n" \
"ds_read_b64_tr_b16 v[140:141], v234 offset:49152\n" \
"v_mfma_f32_16x16x32_bf16 v[108:111], v[148:151], v[196:199], v[108:111]\n" \
"ds_read_b64_tr_b16 v[142:143], v234 offset:51200\n" \
"s_waitcnt lgkmcnt(8)\n" \
"s_add_u32 s83, s55, 53248\n" \
"buffer_store_dwordx4 v[220:223], v224, %[rs], s83 offen offset:0 sc1\n" \
"v_mfma_f32_16x16x32_bf16 v[112:115], v[152:155], v[192:195], v[112:115]\n" \
"ds_read_b64_tr_b16 v[144:145], v235 offset:49152\n" \
"v_accvgpr_read_b32 v208, a232\n" \
"v_accvgpr_read_b32 v209, a233\n" \
"v_mfma_f32_16x16x32_bf16 v[116:119], v[152:155], v[196:199], v[116:119]\n" \
"ds_read_b64_tr_b16 v[146:147], v235 offset:51200\n" \
"v_accvgpr_read_b32 v210, a234\n" \
"v_accvgpr_read_b32 v211, a235\n" \
"v_mfma_f32_16x16x32_bf16 v[120:123], v[156:159], v[192:195], v[120:123]\n" \
"ds_read_b64_tr_b16 v[148:149], v236 offset:49152\n" \
"v_cvt_pk_bf16_f32 v220, v208, v209\n" \
"v_cvt_pk_bf16_f32 v221, v210, v211\n" \
"v_mfma_f32_16x16x32_bf16 v[124:127], v[156:159], v[196:199], v[124:127]\n" \
"ds_read_b64_tr_b16 v[150:151], v236 offset:51200\n" \
"s_waitcnt lgkmcnt(8)\n" \
"v_accvgpr_read_b32 v208, a236\n" \
"v_mfma_f32_16x16x32_bf16 v[0:3], v[128:131], v[200:203], v[0:3]\n" \
"ds_read_b64_tr_b16 v[152:153], v237 offset:49152\n" \
"v_accvgpr_read_b32 v209, a237\n" \
"v_accvgpr_read_b32 v210, a238\n" \
"v_mfma_f32_16x16x32_bf16 v[4:7], v[128:131], v[204:207], v[4:7]\n" \
"ds_read_b64_tr_b16 v[154:155], v237 offset:51200\n" \
"v_accvgpr_read_b32 v211, a239\n" \
"v_cvt_pk_bf16_f32 v222, v208, v209\n" \
"v_mfma_f32_16x16x32_bf16 v[8:11], v[132:135], v[200:203], v[8:11]\n" \
"ds_read_b64_tr_b16 v[156:157], v238 offset:49152\n" \
"v_cvt_pk_bf16_f32 v223, v210, v211\n" \
"v_mfma_f32_16x16x32_bf16 v[12:15], v[132:135], v[204:207], v[12:15]\n" \
"ds_read_b64_tr_b16 v[158:159], v238 offset:51200\n" \
"s_waitcnt lgkmcnt(8)\n" \
"v_mfma_f32_16x16x32_bf16 v[16:19], v[136:139], v[200:203], v[16:19]\n" \
"ds_read_b64_tr_b16 v[128:129], v231 offset:49408\n" \
"v_mfma_f32_16x16x32_bf16 v[20:23], v[136:139], v[204:207], v[20:23]\n" \
"ds_read_b64_tr_b16 v[130:131], v231 offset:51456\n" \
"v_mfma_f32_16x16x32_bf16 v[24:27], v[140:143], v[200:203], v[24:27]\n" \
"ds_read_b64_tr_b16 v[132:133], v232 offset:49408\n" \
"v_add_u32 v231, 0x10000, v231\n" \
"v_mfma_f32_16x16x32_bf16 v[28:31], v[140:143], v[204:207], v[28:31]\n" \
"ds_read_b64_tr_b16 v[134:135], v232 offset:51456\n" \
"s_waitcnt lgkmcnt(8)\n" \
"v_mfma_f32_16x16x32_bf16 v[32:35], v[144:147], v[200:203], v[32:35]\n" \
"ds_read_b64_tr_b16 v[136:137], v233 offset:49408\n" \
"v_add_u32 v232, 0x10000, v232\n" \
"v_mfma_f32_16x16x32_bf16 v[36:39], v[144:147], v[204:207], v[36:39]\n" \
"ds_read_b64_tr_b16 v[138:139], v233 offset:51456\n" \
"buffer_store_dwordx4 v[212:215], v224, %[rs], s83 offen offset:2048 sc1\n" \
"v_accvgpr_read_b32 v208, a240\n" \
"v_mfma_f32_16x16x32_bf16 v[40:43], v[148:151], v[200:203], v[40:43]\n" \
"ds_read_b64_tr_b16 v[140:141], v234 offset:49408\n" \
"v_add_u32 v233, 0x10000, v233\n" \
"v_accvgpr_read_b32 v209, a241\n" \
"v_mfma_f32_16x16x32_bf16 v[44:47], v[148:151], v[204:207], v[44:47]\n" \
"ds_read_b64_tr_b16 v[142:143], v234 offset:51456\n" \
"s_waitcnt lgkmcnt(8)\n" \
"v_accvgpr_read_b32 v210, a242\n" \
"v_mfma_f32_16x16x32_bf16 v[48:51], v[152:155], v[200:203], v[48:51]\n" \
"ds_read_b64_tr_b16 v[144:145], v235 offset:49408\n" \
"v_add_u32 v234, 0x10000, v234\n" \
"v_accvgpr_read_b32 v211, a243\n" \
"v_mfma_f32_16x16x32_bf16 v[52:55], v[152:155], v[204:207], v[52:55]\n" \
"ds_read_b64_tr_b16 v[146:147], v235 offset:51456\n" \
"v_cvt_pk_bf16_f32 v212, v208, v209\n" \
"v_cvt_pk_bf16_f32 v213, v210, v211\n" \
"v_mfma_f32_16x16x32_bf16 v[56:59], v[156:159], v[200:203], v[56:59]\n" \
"ds_read_b64_tr_b16 v[148:149], v236 offset:49408\n" \
"v_add_u32 v235, 0x10000, v235\n" \
"v_accvgpr_read_b32 v208, a244\n" \
"v_mfma_f32_16x16x32_bf16 v[60:63], v[156:159], v[204:207], v[60:63]\n" \
"ds_read_b64_tr_b16 v[150:151], v236 offset:51456\n" \
"s_waitcnt lgkmcnt(8)\n" \
"v_accvgpr_read_b32 v209, a245\n" \
"v_mfma_f32_16x16x32_bf16 v[64:67], v[128:131], v[200:203], v[64:67]\n" \
"ds_read_b64_tr_b16 v[152:153], v237 offset:49408\n" \
"v_add_u32 v236, 0x10000, v236\n" \
"v_accvgpr_read_b32 v210, a246\n" \
"v_mfma_f32_16x16x32_bf16 v[68:71], v[128:131], v[204:207], v[68:71]\n" \
"ds_read_b64_tr_b16 v[154:155], v237 offset:51456\n" \
"v_accvgpr_read_b32 v211, a247\n" \
"v_cvt_pk_bf16_f32 v214, v208, v209\n" \
"v_mfma_f32_16x16x32_bf16 v[72:75], v[132:135], v[200:203], v[72:75]\n" \
"ds_read_b64_tr_b16 v[156:157], v238 offset:49408\n" \
"v_add_u32 v237, 0x10000, v237\n" \
"v_cvt_pk_bf16_f32 v215, v210, v211\n" \
"v_mfma_f32_16x16x32_bf16 v[76:79], v[132:135], v[204:207], v[76:79]\n" \
"ds_read_b64_tr_b16 v[158:159], v238 offset:51456\n" \
"s_waitcnt lgkmcnt(8)\n" \
"s_waitcnt vmcnt(11)\n" \
"s_barrier\n" \
"v_mfma_f32_16x16x32_bf16 v[80:83], v[136:139], v[200:203], v[80:83]\n" \
"ds_read_b64_tr_b16 v[128:129], v231 offset:0\n" \
"v_add_u32 v238, 0x10000, v238\n" \
"v_mfma_f32_16x16x32_bf16 v[84:87], v[136:139], v[204:207], v[84:87]\n" \
"ds_read_b64_tr_b16 v[130:131], v231 offset:2048\n" \
"v_mfma_f32_16x16x32_bf16 v[88:91], v[140:143], v[200:203], v[88:91]\n" \
"ds_read_b64_tr_b16 v[132:133], v232 offset:0\n" \
"v_mfma_f32_16x16x32_bf16 v[92:95], v[140:143], v[204:207], v[92:95]\n" \
"ds_read_b64_tr_b16 v[134:135], v232 offset:2048\n" \
"s_waitcnt lgkmcnt(8)\n" \
"s_add_u32 s83, s55, 49152\n" \
"buffer_store_dwordx4 v[216:219], v224, %[rs], s83 offen offset:1024 sc1\n" \
"v_mfma_f32_16x16x32_bf16 v[96:99], v[144:147], v[200:203], v[96:99]\n" \
"ds_read_b64_tr_b16 v[136:137], v233 offset:0\n" \
"v_accvgpr_read_b32 v208, a248\n" \
"v_accvgpr_read_b32 v209, a249\n" \
"v_mfma_f32_16x16x32_bf16 v[100:103], v[144:147], v[204:207], v[100:103]\n" \
"ds_read_b64_tr_b16 v[138:139], v233 offset:2048\n" \
"v_accvgpr_read_b32 v210, a250\n" \
"v_accvgpr_read_b32 v211, a251\n" \
"v_mfma_f32_16x16x32_bf16 v[104:107], v[148:151], v[200:203], v[104:107]\n" \
"ds_read_b64_tr_b16 v[140:141], v234 offset:0\n" \
"v_cvt_pk_bf16_f32 v216, v208, v209\n" \
"v_cvt_pk_bf16_f32 v217, v210, v211\n" \
"v_mfma_f32_16x16x32_bf16 v[108:111], v[148:151], v[204:207], v[108:111]\n" \
"ds_read_b64_tr_b16 v[142:143], v234 offset:2048\n" \
"s_waitcnt lgkmcnt(8)\n" \
"v_accvgpr_read_b32 v208, a252\n" \
"v_mfma_f32_16x16x32_bf16 v[112:115], v[152:155], v[200:203], v[112:115]\n" \
"ds_read_b64_tr_b16 v[144:145], v235 offset:0\n" \
"v_accvgpr_read_b32 v209, a253\n" \
"v_accvgpr_read_b32 v210, a254\n" \
"v_mfma_f32_16x16x32_bf16 v[116:119], v[152:155], v[204:207], v[116:119]\n" \
"ds_read_b64_tr_b16 v[146:147], v235 offset:2048\n" \
"v_accvgpr_read_b32 v211, a255\n" \
"v_cvt_pk_bf16_f32 v218, v208, v209\n" \
"v_mfma_f32_16x16x32_bf16 v[120:123], v[156:159], v[200:203], v[120:123]\n" \
"ds_read_b64_tr_b16 v[148:149], v236 offset:0\n" \
"v_cvt_pk_bf16_f32 v219, v210, v211\n" \
"v_mfma_f32_16x16x32_bf16 v[124:127], v[156:159], v[204:207], v[124:127]\n" \
"ds_read_b64_tr_b16 v[150:151], v236 offset:2048\n" \
"s_waitcnt lgkmcnt(8)\n" \
"s_mov_b32 s63, s54\n" \
"s_lshr_b32 s80, s17, 1\n" \
"s_lshl_b32 s62, s16, 3\n" \
"s_add_u32 s62, s62, s80\n" \
"s_lshl_b32 s60, s62, 2\n" \
"s_add_u32 s60, s60, s36\n" \
"s_mul_i32 s61, s60, 17408\n" \
"s_add_u32 s61, s61, 45088768\n" \
"s_lshl_b32 s60, s60, 14\n" \
"s_add_u32 s60, s60, 36700160\n" \
"s_cmp_eq_u32 s63, 0\n" \
"s_cselect_b32 s68, s60, 0x80000000\n" \
"s_lshl_b32 s59, s16, 8\n" \
"s_add_u32 s59, s59, 4096\n" \
"v_mfma_f32_16x16x32_bf16 v[0:3], v[128:131], v[160:163], v[0:3]\n" \
"ds_read_b64_tr_b16 v[152:153], v237 offset:0\n" \
"v_mfma_f32_16x16x32_bf16 v[4:7], v[128:131], v[164:167], v[4:7]\n" \
"ds_read_b64_tr_b16 v[154:155], v237 offset:2048\n" \
"v_mfma_f32_16x16x32_bf16 v[8:11], v[132:135], v[160:163], v[8:11]\n" \
"ds_read_b64_tr_b16 v[156:157], v238 offset:0\n" \
"v_mfma_f32_16x16x32_bf16 v[12:15], v[132:135], v[164:167], v[12:15]\n" \
"ds_read_b64_tr_b16 v[158:159], v238 offset:2048\n" \
"s_waitcnt lgkmcnt(8)\n" \
"v_mfma_f32_16x16x32_bf16 v[16:19], v[136:139], v[160:163], v[16:19]\n" \
"ds_read_b64_tr_b16 v[128:129], v231 offset:256\n" \
"v_mfma_f32_16x16x32_bf16 v[20:23], v[136:139], v[164:167], v[20:23]\n" \
"ds_read_b64_tr_b16 v[130:131], v231 offset:2304\n" \
"buffer_store_dwordx4 v[220:223], v224, %[rs], s83 offen offset:3072 sc1\n" \
"v_mfma_f32_16x16x32_bf16 v[24:27], v[140:143], v[160:163], v[24:27]\n" \
"ds_read_b64_tr_b16 v[132:133], v232 offset:256\n" \
"v_mfma_f32_16x16x32_bf16 v[28:31], v[140:143], v[164:167], v[28:31]\n" \
"ds_read_b64_tr_b16 v[134:135], v232 offset:2304\n" \
"s_waitcnt lgkmcnt(8)\n" \
"v_mfma_f32_16x16x32_bf16 v[32:35], v[144:147], v[160:163], v[32:35]\n" \
"ds_read_b64_tr_b16 v[136:137], v233 offset:256\n" \
"v_mfma_f32_16x16x32_bf16 v[36:39], v[144:147], v[164:167], v[36:39]\n" \
"ds_read_b64_tr_b16 v[138:139], v233 offset:2304\n" \
"v_mfma_f32_16x16x32_bf16 v[40:43], v[148:151], v[160:163], v[40:43]\n" \
"ds_read_b64_tr_b16 v[140:141], v234 offset:256\n" \
"v_mfma_f32_16x16x32_bf16 v[44:47], v[148:151], v[164:167], v[44:47]\n" \
"ds_read_b64_tr_b16 v[142:143], v234 offset:2304\n" \
"s_waitcnt lgkmcnt(8)\n" \
"v_mfma_f32_16x16x32_bf16 v[48:51], v[152:155], v[160:163], v[48:51]\n" \
"ds_read_b64_tr_b16 v[144:145], v235 offset:256\n" \
"v_mfma_f32_16x16x32_bf16 v[52:55], v[152:155], v[164:167], v[52:55]\n" \
"ds_read_b64_tr_b16 v[146:147], v235 offset:2304\n" \
"v_mfma_f32_16x16x32_bf16 v[56:59], v[156:159], v[160:163], v[56:59]\n" \
"ds_read_b64_tr_b16 v[148:149], v236 offset:256\n" \
"v_mfma_f32_16x16x32_bf16 v[60:63], v[156:159], v[164:167], v[60:63]\n" \
"ds_read_b64_tr_b16 v[150:151], v236 offset:2304\n" \
"s_waitcnt lgkmcnt(8)\n" \
"v_mfma_f32_16x16x32_bf16 v[64:67], v[128:131], v[160:163], v[64:67]\n" \
"ds_read_b64_tr_b16 v[152:153], v237 offset:256\n" \
"v_mfma_f32_16x16x32_bf16 v[68:71], v[128:131], v[164:167], v[68:71]\n" \
"ds_read_b64_tr_b16 v[154:155], v237 offset:2304\n" \
"v_mfma_f32_16x16x32_bf16 v[72:75], v[132:135], v[160:163], v[72:75]\n" \
"ds_read_b64_tr_b16 v[156:157], v238 offset:256\n" \
"v_mfma_f32_16x16x32_bf16 v[76:79], v[132:135], v[164:167], v[76:79]\n" \
"ds_read_b64_tr_b16 v[158:159], v238 offset:2304\n" \
"s_waitcnt lgkmcnt(8)\n" \
"s_add_u32 s83, s55, 53248\n" \
"buffer_store_dwordx4 v[212:215], v224, %[rs], s83 offen offset:1024 sc1\n" \
"s_waitcnt vmcnt(10)\n" \
"s_barrier\n" \
"v_mfma_f32_16x16x32_bf16 v[80:83], v[136:139], v[160:163], v[80:83]\n" \
"ds_read_b64_tr_b16 v[128:129], v231 offset:16384\n" \
"v_mfma_f32_16x16x32_bf16 v[84:87], v[136:139], v[164:167], v[84:87]\n" \
"ds_read_b64_tr_b16 v[130:131], v231 offset:18432\n" \
"v_mfma_f32_16x16x32_bf16 v[88:91], v[140:143], v[160:163], v[88:91]\n" \
"ds_read_b64_tr_b16 v[132:133], v232 offset:16384\n" \
"v_mfma_f32_16x16x32_bf16 v[92:95], v[140:143], v[164:167], v[92:95]\n" \
"ds_read_b64_tr_b16 v[134:135], v232 offset:18432\n" \
"s_waitcnt lgkmcnt(8)\n" \
"v_mfma_f32_16x16x32_bf16 v[96:99], v[144:147], v[160:163], v[96:99]\n" \
"ds_read_b64_tr_b16 v[136:137], v233 offset:16384\n" \
"v_mfma_f32_16x16x32_bf16 v[100:103], v[144:147], v[164:167], v[100:103]\n" \
"ds_read_b64_tr_b16 v[138:139], v233 offset:18432\n" \
"v_mfma_f32_16x16x32_bf16 v[104:107], v[148:151], v[160:163], v[104:107]\n" \
"ds_read_b64_tr_b16 v[140:141], v234 offset:16384\n" \
"v_mfma_f32_16x16x32_bf16 v[108:111], v[148:151], v[164:167], v[108:111]\n" \
"ds_read_b64_tr_b16 v[142:143], v234 offset:18432\n" \
"s_waitcnt lgkmcnt(8)\n" \
"v_mfma_f32_16x16x32_bf16 v[112:115], v[152:155], v[160:163], v[112:115]\n" \
"ds_read_b64_tr_b16 v[144:145], v235 offset:16384\n" \
"v_mfma_f32_16x16x32_bf16 v[116:119], v[152:155], v[164:167], v[116:119]\n" \
"ds_read_b64_tr_b16 v[146:147], v235 offset:18432\n" \
"v_mfma_f32_16x16x32_bf16 v[120:123], v[156:159], v[160:163], v[120:123]\n" \
"ds_read_b64_tr_b16 v[148:149], v236 offset:16384\n" \
"v_mfma_f32_16x16x32_bf16 v[124:127], v[156:159], v[164:167], v[124:127]\n" \
"ds_read_b64_tr_b16 v[150:151], v236 offset:18432\n" \
"s_waitcnt lgkmcnt(8)\n" \
"v_mfma_f32_16x16x32_bf16 v[0:3], v[128:131], v[168:171], v[0:3]\n" \
"ds_read_b64_tr_b16 v[152:153], v237 offset:16384\n" \
"buffer_store_dwordx4 v[216:219], v224, %[rs], s83 offen offset:3072 sc1\n" \
"v_mfma_f32_16x16x32_bf16 v[4:7], v[128:131], v[172:175], v[4:7]\n" \
"ds_read_b64_tr_b16 v[154:155], v237 offset:18432\n" \
"v_mfma_f32_16x16x32_bf16 v[8:11], v[132:135], v[168:171], v[8:11]\n" \
"ds_read_b64_tr_b16 v[156:157], v238 offset:16384\n" \
"v_mfma_f32_16x16x32_bf16 v[12:15], v[132:135], v[172:175], v[12:15]\n" \
"ds_read_b64_tr_b16 v[158:159], v238 offset:18432\n" \
"s_waitcnt lgkmcnt(8)\n" \
"v_mfma_f32_16x16x32_bf16 v[16:19], v[136:139], v[168:171], v[16:19]\n" \
"ds_read_b64_tr_b16 v[128:129], v231 offset:16640\n" \
"v_mfma_f32_16x16x32_bf16 v[20:23], v[136:139], v[172:175], v[20:23]\n" \
"ds_read_b64_tr_b16 v[130:131], v231 offset:18688\n" \
"v_mfma_f32_16x16x32_bf16 v[24:27], v[140:143], v[168:171], v[24:27]\n" \
"ds_read_b64_tr_b16 v[132:133], v232 offset:16640\n" \
"v_mfma_f32_16x16x32_bf16 v[28:31], v[140:143], v[172:175], v[28:31]\n" \
"ds_read_b64_tr_b16 v[134:135], v232 offset:18688\n" \
"s_waitcnt lgkmcnt(8)\n" \
"v_mfma_f32_16x16x32_bf16 v[32:35], v[144:147], v[168:171], v[32:35]\n" \
"ds_read_b64_tr_b16 v[136:137], v233 offset:16640\n" \
"v_cvt_pk_bf16_f32 v0, v0, v1\n" \
"v_cvt_pk_bf16_f32 v1, v2, v3\n" \
"v_mfma_f32_16x16x32_bf16 v[36:39], v[144:147], v[172:175], v[36:39]\n" \
"ds_read_b64_tr_b16 v[138:139], v233 offset:18688\n" \
"v_cvt_pk_bf16_f32 v2, v8, v9\n" \
"v_cvt_pk_bf16_f32 v3, v10, v11\n" \
"v_mfma_f32_16x16x32_bf16 v[40:43], v[148:151], v[168:171], v[40:43]\n" \
"ds_read_b64_tr_b16 v[140:141], v234 offset:16640\n" \
"s_add_u32 s94, s68, 0\n" \
"buffer_store_dwordx4 v[0:3], v224, %[rs], s94 offen sc1\n" \
"v_mfma_f32_16x16x32_bf16 v[44:47], v[148:151], v[172:175], v[44:47]\n" \
"ds_read_b64_tr_b16 v[142:143], v234 offset:18688\n" \
"s_waitcnt lgkmcnt(8)\n" \
"v_cvt_pk_bf16_f32 v4, v4, v5\n" \
"v_mfma_f32_16x16x32_bf16 v[48:51], v[152:155], v[168:171], v[48:51]\n" \
"ds_read_b64_tr_b16 v[144:145], v235 offset:16640\n" \
"v_cvt_pk_bf16_f32 v5, v6, v7\n" \
"v_cvt_pk_bf16_f32 v6, v12, v13\n" \
"v_mfma_f32_16x16x32_bf16 v[52:55], v[152:155], v[172:175], v[52:55]\n" \
"ds_read_b64_tr_b16 v[146:147], v235 offset:18688\n" \
"v_cvt_pk_bf16_f32 v7, v14, v15\n" \
"v_cvt_pk_bf16_f32 v16, v16, v17\n" \
"v_mfma_f32_16x16x32_bf16 v[56:59], v[156:159], v[168:171], v[56:59]\n" \
"ds_read_b64_tr_b16 v[148:149], v236 offset:16640\n" \
"s_add_u32 s94, s68, 8192\n" \
"buffer_store_dwordx4 v[4:7], v224, %[rs], s94 offen sc1\n" \
"v_mfma_f32_16x16x32_bf16 v[60:63], v[156:159], v[172:175], v[60:63]\n" \
"ds_read_b64_tr_b16 v[150:151], v236 offset:18688\n" \
"s_waitcnt lgkmcnt(8)\n" \
"v_cvt_pk_bf16_f32 v17, v18, v19\n" \
"v_mfma_f32_16x16x32_bf16 v[64:67], v[128:131], v[168:171], v[64:67]\n" \
"ds_read_b64_tr_b16 v[152:153], v237 offset:16640\n" \
"v_cvt_pk_bf16_f32 v18, v24, v25\n" \
"v_cvt_pk_bf16_f32 v19, v26, v27\n" \
"v_mfma_f32_16x16x32_bf16 v[68:71], v[128:131], v[172:175], v[68:71]\n" \
"ds_read_b64_tr_b16 v[154:155], v237 offset:18688\n" \
"v_cvt_pk_bf16_f32 v20, v20, v21\n" \
"v_cvt_pk_bf16_f32 v21, v22, v23\n" \
"v_mfma_f32_16x16x32_bf16 v[72:75], v[132:135], v[168:171], v[72:75]\n" \
"ds_read_b64_tr_b16 v[156:157], v238 offset:16640\n" \
"s_add_u32 s94, s68, 1024\n" \
"buffer_store_dwordx4 v[16:19], v224, %[rs], s94 offen sc1\n" \
"v_mfma_f32_16x16x32_bf16 v[76:79], v[132:135], v[172:175], v[76:79]\n" \
"ds_read_b64_tr_b16 v[158:159], v238 offset:18688\n" \
"s_waitcnt lgkmcnt(8)\n" \
"v_cvt_pk_bf16_f32 v22, v28, v29\n" \
"v_mfma_f32_16x16x32_bf16 v[80:83], v[136:139], v[168:171], v[80:83]\n" \
"v_cvt_pk_bf16_f32 v23, v30, v31\n" \
"v_cvt_pk_bf16_f32 v32, v32, v33\n" \
"v_cvt_pk_bf16_f32 v33, v34, v35\n" \
"v_mfma_f32_16x16x32_bf16 v[84:87], v[136:139], v[172:175], v[84:87]\n" \
"v_cvt_pk_bf16_f32 v34, v40, v41\n" \
"v_cvt_pk_bf16_f32 v35, v42, v43\n" \
"v_cvt_pk_bf16_f32 v36, v36, v37\n" \
"v_mfma_f32_16x16x32_bf16 v[88:91], v[140:143], v[168:171], v[88:91]\n" \
"s_add_u32 s94, s68, 9216\n" \
"buffer_store_dwordx4 v[20:23], v224, %[rs], s94 offen sc1\n" \
"v_cvt_pk_bf16_f32 v37, v38, v39\n" \
"v_mfma_f32_16x16x32_bf16 v[92:95], v[140:143], v[172:175], v[92:95]\n" \
"s_waitcnt lgkmcnt(4)\n" \
"v_cvt_pk_bf16_f32 v38, v44, v45\n" \
"v_cvt_pk_bf16_f32 v39, v46, v47\n" \
"v_mfma_f32_16x16x32_bf16 v[96:99], v[144:147], v[168:171], v[96:99]\n" \
"v_cvt_pk_bf16_f32 v48, v48, v49\n" \
"v_cvt_pk_bf16_f32 v49, v50, v51\n" \
"v_cvt_pk_bf16_f32 v50, v56, v57\n" \
"v_mfma_f32_16x16x32_bf16 v[100:103], v[144:147], v[172:175], v[100:103]\n" \
"v_cvt_pk_bf16_f32 v51, v58, v59\n" \
"v_cvt_pk_bf16_f32 v52, v52, v53\n" \
"v_cvt_pk_bf16_f32 v53, v54, v55\n" \
"v_mfma_f32_16x16x32_bf16 v[104:107], v[148:151], v[168:171], v[104:107]\n" \
"s_add_u32 s94, s68, 2048\n" \
"buffer_store_dwordx4 v[32:35], v224, %[rs], s94 offen sc1\n" \
"v_cvt_pk_bf16_f32 v54, v60, v61\n" \
"v_mfma_f32_16x16x32_bf16 v[108:111], v[148:151], v[172:175], v[108:111]\n" \
"s_waitcnt lgkmcnt(0)\n" \
"v_cvt_pk_bf16_f32 v55, v62, v63\n" \
"v_cvt_pk_bf16_f32 v64, v64, v65\n" \
"v_mfma_f32_16x16x32_bf16 v[112:115], v[152:155], v[168:171], v[112:115]\n" \
"v_cvt_pk_bf16_f32 v65, v66, v67\n" \
"v_cvt_pk_bf16_f32 v66, v72, v73\n" \
"v_cvt_pk_bf16_f32 v67, v74, v75\n" \
"v_mfma_f32_16x16x32_bf16 v[116:119], v[152:155], v[172:175], v[116:119]\n" \
"v_cvt_pk_bf16_f32 v68, v68, v69\n" \
"v_cvt_pk_bf16_f32 v69, v70, v71\n" \
"v_cvt_pk_bf16_f32 v70, v76, v77\n" \
"v_mfma_f32_16x16x32_bf16 v[120:123], v[156:159], v[168:171], v[120:123]\n" \
"s_add_u32 s94, s68, 10240\n" \
"buffer_store_dwordx4 v[36:39], v224, %[rs], s94 offen sc1\n" \
"v_cvt_pk_bf16_f32 v71, v78, v79\n" \
"v_mfma_f32_16x16x32_bf16 v[124:127], v[156:159], v[172:175], v[124:127]\n" \
"v_cvt_pk_bf16_f32 v80, v80, v81\n" \
"v_cvt_pk_bf16_f32 v81, v82, v83\n" \
"s_add_u32 s94, s68, 3072\n" \
"buffer_store_dwordx4 v[48:51], v224, %[rs], s94 offen sc1\n" \
"s_add_u32 s94, s68, 11264\n" \
"buffer_store_dwordx4 v[52:55], v224, %[rs], s94 offen sc1\n" \
"s_add_u32 s94, s68, 4096\n" \
"buffer_store_dwordx4 v[64:67], v224, %[rs], s94 offen sc1\n" \
"s_add_u32 s94, s68, 12288\n" \
"buffer_store_dwordx4 v[68:71], v224, %[rs], s94 offen sc1\n" \
"v_cvt_pk_bf16_f32 v82, v88, v89\n" \
"v_cvt_pk_bf16_f32 v83, v90, v91\n" \
"s_add_u32 s94, s68, 5120\n" \
"buffer_store_dwordx4 v[80:83], v224, %[rs], s94 offen sc1\n" \
"v_cvt_pk_bf16_f32 v84, v84, v85\n" \
"v_cvt_pk_bf16_f32 v85, v86, v87\n" \
"v_cvt_pk_bf16_f32 v86, v92, v93\n" \
"v_cvt_pk_bf16_f32 v87, v94, v95\n" \
"s_add_u32 s94, s68, 13312\n" \
"buffer_store_dwordx4 v[84:87], v224, %[rs], s94 offen sc1\n" \
"v_cvt_pk_bf16_f32 v96, v96, v97\n" \
"v_cvt_pk_bf16_f32 v97, v98, v99\n" \
"v_cvt_pk_bf16_f32 v98, v104, v105\n" \
"v_cvt_pk_bf16_f32 v99, v106, v107\n" \
"s_add_u32 s94, s68, 6144\n" \
"buffer_store_dwordx4 v[96:99], v224, %[rs], s94 offen sc1\n" \
"v_cvt_pk_bf16_f32 v100, v100, v101\n" \
"v_cvt_pk_bf16_f32 v101, v102, v103\n" \
"v_cvt_pk_bf16_f32 v102, v108, v109\n" \
"v_cvt_pk_bf16_f32 v103, v110, v111\n" \
"s_add_u32 s94, s68, 14336\n" \
"buffer_store_dwordx4 v[100:103], v224, %[rs], s94 offen sc1\n" \
"v_cvt_pk_bf16_f32 v112, v112, v113\n" \
"v_cvt_pk_bf16_f32 v113, v114, v115\n" \
"v_cvt_pk_bf16_f32 v114, v120, v121\n" \
"v_cvt_pk_bf16_f32 v115, v122, v123\n" \
"s_add_u32 s94, s68, 7168\n" \
"buffer_store_dwordx4 v[112:115], v224, %[rs], s94 offen sc1\n" \
"v_cvt_pk_bf16_f32 v116, v116, v117\n" \
"v_cvt_pk_bf16_f32 v117, v118, v119\n" \
"v_cvt_pk_bf16_f32 v118, v124, v125\n" \
"v_cvt_pk_bf16_f32 v119, v126, v127\n" \
"s_add_u32 s94, s68, 15360\n" \
"buffer_store_dwordx4 v[116:119], v224, %[rs], s94 offen sc1\n" \
"s_branch LQ%=\n" \
"LV%=:\n" \
"s_waitcnt vmcnt(12) lgkmcnt(0)\n" \
"s_barrier\n" \
"v_mfma_f32_16x16x32_bf16 a[0:3], v[0:3], v[32:35], 0\n" \
"ds_read_b128 v[64:67], v230\n" \
"v_lshlrev_b32 v152, 16, v144\n" \
"v_mfma_f32_16x16x32_bf16 a[4:7], v[0:3], v[36:39], 0\n" \
"s_add_u32 s52, s39, 256\n" \
"s_add_u32 m0, s37, 65536\n" \
"v_mfma_f32_16x16x32_bf16 a[8:11], v[0:3], v[40:43], 0\n" \
"ds_read_b64_tr_b16 v[96:97], v231 offset:16384\n" \
"v_lshlrev_b32 v160, 16, v148\n" \
"v_mfma_f32_16x16x32_bf16 a[12:15], v[0:3], v[44:47], 0\n" \
"buffer_load_dwordx4 v225, %[rc], s52 offen lds\n" \
"v_and_b32 v153, 0xffff0000, v144\n" \
"v_mfma_f32_16x16x32_bf16 a[16:19], v[0:3], v[48:51], 0\n" \
"ds_read_b64_tr_b16 v[98:99], v231 offset:18432\n" \
"v_and_b32 v161, 0xffff0000, v148\n" \
"v_mfma_f32_16x16x32_bf16 a[20:23], v[0:3], v[52:55], 0\n" \
"s_add_u32 s52, s40, 786432\n" \
"s_add_u32 m0, s38, 147456\n" \
"v_mfma_f32_16x16x32_bf16 a[24:27], v[0:3], v[56:59], 0\n" \
"ds_read_b64_tr_b16 v[100:101], v232 offset:16384\n" \
"v_lshlrev_b32 v154, 16, v145\n" \
"v_mfma_f32_16x16x32_bf16 a[28:31], v[0:3], v[60:63], 0\n" \
"buffer_load_dwordx4 v227, %[rw], s52 offen lds\n" \
"v_lshlrev_b32 v162, 16, v149\n" \
"v_mfma_f32_16x16x32_bf16 a[32:35], v[4:7], v[32:35], 0\n" \
"ds_read_b64_tr_b16 v[102:103], v232 offset:18432\n" \
"v_and_b32 v155, 0xffff0000, v145\n" \
"v_mfma_f32_16x16x32_bf16 a[36:39], v[4:7], v[36:39], 0\n" \
"s_add_u32 s52, s39, 16640\n" \
"s_add_u32 m0, s37, 66560\n" \
"v_mfma_f32_16x16x32_bf16 a[40:43], v[4:7], v[40:43], 0\n" \
"ds_read_b64_tr_b16 v[104:105], v233 offset:16384\n" \
"v_and_b32 v163, 0xffff0000, v149\n" \
"v_mfma_f32_16x16x32_bf16 a[44:47], v[4:7], v[44:47], 0\n" \
"buffer_load_dwordx4 v226, %[rc], s52 offen lds\n" \
"v_lshlrev_b32 v156, 16, v146\n" \
"v_mfma_f32_16x16x32_bf16 a[48:51], v[4:7], v[48:51], 0\n" \
"ds_read_b64_tr_b16 v[106:107], v233 offset:18432\n" \
"v_lshlrev_b32 v164, 16, v150\n" \
"v_mfma_f32_16x16x32_bf16 a[52:55], v[4:7], v[52:55], 0\n" \
"s_add_u32 s52, s40, 802816\n" \
"s_add_u32 m0, s38, 148480\n" \
"v_mfma_f32_16x16x32_bf16 a[56:59], v[4:7], v[56:59], 0\n" \
"ds_read_b64_tr_b16 v[108:109], v234 offset:16384\n" \
"v_and_b32 v157, 0xffff0000, v146\n" \
"v_mfma_f32_16x16x32_bf16 a[60:63], v[4:7], v[60:63], 0\n" \
"buffer_load_dwordx4 v228, %[rw], s52 offen lds\n" \
"v_and_b32 v165, 0xffff0000, v150\n" \
"v_mfma_f32_16x16x32_bf16 a[64:67], v[8:11], v[32:35], 0\n" \
"ds_read_b64_tr_b16 v[110:111], v234 offset:18432\n" \
"v_lshlrev_b32 v158, 16, v147\n" \
"v_mfma_f32_16x16x32_bf16 a[68:71], v[8:11], v[36:39], 0\n" \
"s_add_u32 s52, s39, 33024\n" \
"s_add_u32 m0, s37, 67584\n" \
"v_mfma_f32_16x16x32_bf16 a[72:75], v[8:11], v[40:43], 0\n" \
"ds_read_b64_tr_b16 v[112:113], v235 offset:16384\n" \
"v_lshlrev_b32 v166, 16, v151\n" \
"v_mfma_f32_16x16x32_bf16 a[76:79], v[8:11], v[44:47], 0\n" \
"buffer_load_dwordx4 v225, %[rc], s52 offen lds\n" \
"v_and_b32 v159, 0xffff0000, v147\n" \
"v_mfma_f32_16x16x32_bf16 a[80:83], v[8:11], v[48:51], 0\n" \
"ds_read_b64_tr_b16 v[114:115], v235 offset:18432\n" \
"v_and_b32 v167, 0xffff0000, v151\n" \
"v_mfma_f32_16x16x32_bf16 a[84:87], v[8:11], v[52:55], 0\n" \
"s_add_u32 s52, s40, 819200\n" \
"s_add_u32 m0, s38, 149504\n" \
"v_mfma_f32_16x16x32_bf16 a[88:91], v[8:11], v[56:59], 0\n" \
"ds_read_b64_tr_b16 v[116:117], v236 offset:16384\n" \
"v_add_f32 v168, v152, v153\n" \
"v_mfma_f32_16x16x32_bf16 a[92:95], v[8:11], v[60:63], 0\n" \
"buffer_load_dwordx4 v227, %[rw], s52 offen lds\n" \
"v_add_f32 v169, v160, v161\n" \
"v_mfma_f32_16x16x32_bf16 a[96:99], v[12:15], v[32:35], 0\n" \
"ds_read_b64_tr_b16 v[118:119], v236 offset:18432\n" \
"v_add_f32 v168, v168, v154\n" \
"v_mfma_f32_16x16x32_bf16 a[100:103], v[12:15], v[36:39], 0\n" \
"s_add_u32 s52, s39, 49408\n" \
"s_add_u32 m0, s37, 68608\n" \
"v_mfma_f32_16x16x32_bf16 a[104:107], v[12:15], v[40:43], 0\n" \
"ds_read_b64_tr_b16 v[120:121], v237 offset:16384\n" \
"v_add_f32 v169, v169, v162\n" \
"v_mfma_f32_16x16x32_bf16 a[108:111], v[12:15], v[44:47], 0\n" \
"buffer_load_dwordx4 v226, %[rc], s52 offen lds\n" \
"v_add_f32 v168, v168, v155\n" \
"v_mfma_f32_16x16x32_bf16 a[112:115], v[12:15], v[48:51], 0\n" \
"ds_read_b64_tr_b16 v[122:123], v237 offset:18432\n" \
"v_add_f32 v169, v169, v163\n" \
"v_mfma_f32_16x16x32_bf16 a[116:119], v[12:15], v[52:55], 0\n" \
"s_add_u32 s52, s40, 835584\n" \
"s_add_u32 m0, s38, 150528\n" \
"v_mfma_f32_16x16x32_bf16 a[120:123], v[12:15], v[56:59], 0\n" \
"ds_read_b64_tr_b16 v[124:125], v238 offset:16384\n" \
"v_add_f32 v168, v168, v156\n" \
"v_mfma_f32_16x16x32_bf16 a[124:127], v[12:15], v[60:63], 0\n" \
"buffer_load_dwordx4 v228, %[rw], s52 offen lds\n" \
"v_add_f32 v169, v169, v164\n" \
"v_mfma_f32_16x16x32_bf16 a[128:131], v[16:19], v[32:35], 0\n" \
"ds_read_b64_tr_b16 v[126:127], v238 offset:18432\n" \
"v_add_f32 v168, v168, v157\n" \
"v_mfma_f32_16x16x32_bf16 a[132:135], v[16:19], v[36:39], 0\n" \
"s_add_u32 s52, s43, 65536\n" \
"s_add_u32 m0, s41, 0\n" \
"v_mfma_f32_16x16x32_bf16 a[136:139], v[16:19], v[40:43], 0\n" \
"ds_read_b128 v[68:71], v230 offset:2048\n" \
"v_add_f32 v169, v169, v165\n" \
"v_mfma_f32_16x16x32_bf16 a[140:143], v[16:19], v[44:47], 0\n" \
"buffer_load_dwordx4 v225, %[rc], s52 offen lds\n" \
"v_add_f32 v168, v168, v158\n" \
"v_mfma_f32_16x16x32_bf16 a[144:147], v[16:19], v[48:51], 0\n" \
"ds_read_b128 v[72:75], v230 offset:4096\n" \
"v_add_f32 v169, v169, v166\n" \
"v_mfma_f32_16x16x32_bf16 a[148:151], v[16:19], v[52:55], 0\n" \
"s_add_u32 s52, s47, 0\n" \
"s_add_u32 m0, s45, 0\n" \
"v_mfma_f32_16x16x32_bf16 a[152:155], v[16:19], v[56:59], 0\n" \
"ds_read_b128 v[76:79], v230 offset:6144\n" \
"v_add_f32 v168, v168, v159\n" \
"v_mfma_f32_16x16x32_bf16 a[156:159], v[16:19], v[60:63], 0\n" \
"buffer_load_dwordx4 v227, %[rw], s52 offen lds\n" \
"v_add_f32 v169, v169, v167\n" \
"v_mfma_f32_16x16x32_bf16 a[160:163], v[20:23], v[32:35], 0\n" \
"ds_read_b128 v[80:83], v230 offset:8192\n" \
"v_add_f32_dpp v168, v168, v168 quad_perm:[1,0,3,2] row_mask:0xf bank_mask:0xf\n" \
"v_mfma_f32_16x16x32_bf16 a[164:167], v[20:23], v[36:39], 0\n" \
"s_add_u32 s52, s43, 81920\n" \
"s_add_u32 m0, s41, 1024\n" \
"v_mfma_f32_16x16x32_bf16 a[168:171], v[20:23], v[40:43], 0\n" \
"ds_read_b128 v[84:87], v230 offset:10240\n" \
"v_add_f32_dpp v169, v169, v169 quad_perm:[1,0,3,2] row_mask:0xf bank_mask:0xf\n" \
"v_mfma_f32_16x16x32_bf16 a[172:175], v[20:23], v[44:47], 0\n" \
"buffer_load_dwordx4 v226, %[rc], s52 offen lds\n" \
"v_add_f32_dpp v168, v168, v168 quad_perm:[2,3,0,1] row_mask:0xf bank_mask:0xf\n" \
"v_mfma_f32_16x16x32_bf16 a[176:179], v[20:23], v[48:51], 0\n" \
"ds_read_b128 v[88:91], v230 offset:12288\n" \
"v_add_f32_dpp v169, v169, v169 quad_perm:[2,3,0,1] row_mask:0xf bank_mask:0xf\n" \
"v_mfma_f32_16x16x32_bf16 a[180:183], v[20:23], v[52:55], 0\n" \
"s_add_u32 s52, s47, 16384\n" \
"s_add_u32 m0, s45, 1024\n" \
"v_mfma_f32_16x16x32_bf16 a[184:187], v[20:23], v[56:59], 0\n" \
"ds_read_b128 v[92:95], v230 offset:14336\n" \
"v_add_f32_dpp v168, v168, v168 row_half_mirror row_mask:0xf bank_mask:0xf\n" \
"v_mfma_f32_16x16x32_bf16 a[188:191], v[20:23], v[60:63], 0\n" \
"buffer_load_dwordx4 v228, %[rw], s52 offen lds\n" \
"v_add_f32_dpp v169, v169, v169 row_half_mirror row_mask:0xf bank_mask:0xf\n" \
"v_mfma_f32_16x16x32_bf16 a[192:195], v[24:27], v[32:35], 0\n" \
"s_add_u32 s42, s42, s49\n" \
"v_add_f32_dpp v168, v168, v168 row_mirror row_mask:0xf bank_mask:0xf\n" \
"v_mfma_f32_16x16x32_bf16 a[196:199], v[24:27], v[36:39], 0\n" \
"s_add_u32 s52, s43, 98304\n" \
"s_add_u32 m0, s41, 2048\n" \
"v_mfma_f32_16x16x32_bf16 a[200:203], v[24:27], v[40:43], 0\n" \
"s_add_u32 s44, s44, 128\n" \
"v_add_u32 v230, s50, v230\n" \
"v_mfma_f32_16x16x32_bf16 a[204:207], v[24:27], v[44:47], 0\n" \
"buffer_load_dwordx4 v225, %[rc], s52 offen lds\n" \
"v_add_f32_dpp v169, v169, v169 row_mirror row_mask:0xf bank_mask:0xf\n" \
"v_mfma_f32_16x16x32_bf16 a[208:211], v[24:27], v[48:51], 0\n" \
"s_xor_b32 s46, s46, 0x38000\n" \
"v_mov_b32 v170, v168\n" \
"v_mfma_f32_16x16x32_bf16 a[212:215], v[24:27], v[52:55], 0\n" \
"s_add_u32 s52, s47, 32768\n" \
"s_add_u32 m0, s45, 2048\n" \
"v_mfma_f32_16x16x32_bf16 a[216:219], v[24:27], v[56:59], 0\n" \
"s_add_u32 s48, s48, 524288\n" \
"v_mov_b32 v171, v169\n" \
"v_mfma_f32_16x16x32_bf16 a[220:223], v[24:27], v[60:63], 0\n" \
"buffer_load_dwordx4 v227, %[rw], s52 offen lds\n" \
"v_permlane16_swap_b32 v170, v168\n" \
"v_mfma_f32_16x16x32_bf16 a[224:227], v[28:31], v[32:35], 0\n" \
"s_add_u32 s51, s49, s50\n" \
"v_permlane16_swap_b32 v171, v169\n" \
"v_mfma_f32_16x16x32_bf16 a[228:231], v[28:31], v[36:39], 0\n" \
"s_add_u32 s52, s43, 114688\n" \
"s_add_u32 m0, s41, 3072\n" \
"v_mfma_f32_16x16x32_bf16 a[232:235], v[28:31], v[40:43], 0\n" \
"s_sub_u32 s51, 0, s51\n" \
"v_add_f32 v168, v168, v170\n" \
"v_mfma_f32_16x16x32_bf16 a[236:239], v[28:31], v[44:47], 0\n" \
"buffer_load_dwordx4 v226, %[rc], s52 offen lds\n" \
"v_add_f32 v169, v169, v171\n" \
"v_mfma_f32_16x16x32_bf16 a[240:243], v[28:31], v[48:51], 0\n" \
"v_mov_b32 v170, v168\n" \
"v_mov_b32 v171, v169\n" \
"v_mfma_f32_16x16x32_bf16 a[244:247], v[28:31], v[52:55], 0\n" \
"s_add_u32 s52, s47, 49152\n" \
"s_add_u32 m0, s45, 3072\n" \
"v_mfma_f32_16x16x32_bf16 a[248:251], v[28:31], v[56:59], 0\n" \
"v_permlane32_swap_b32 v170, v168\n" \
"v_permlane32_swap_b32 v171, v169\n" \
"v_mfma_f32_16x16x32_bf16 a[252:255], v[28:31], v[60:63], 0\n" \
"buffer_load_dwordx4 v228, %[rw], s52 offen lds\n" \
"v_add_f32 v168, v168, v170\n" \
"s_waitcnt vmcnt(16) lgkmcnt(0)\n" \
"s_barrier\n" \
"v_mfma_f32_16x16x32_bf16 a[0:3], v[64:67], v[96:99], a[0:3]\n" \
"ds_read_b128 v[0:3], v229\n" \
"v_xor_b32 v231, 0x38000, v231\n" \
"v_mfma_f32_16x16x32_bf16 a[4:7], v[64:67], v[100:103], a[4:7]\n" \
"s_add_u32 s52, s44, 0\n" \
"v_add_f32 v169, v169, v171\n" \
"v_mfma_f32_16x16x32_bf16 a[8:11], v[64:67], v[104:107], a[8:11]\n" \
"ds_read_b64_tr_b16 v[32:33], v231 offset:0\n" \
"v_mul_f32 v168, 0x3b124925, v168\n" \
"v_mfma_f32_16x16x32_bf16 a[12:15], v[64:67], v[108:111], a[12:15]\n" \
"s_add_u32 m0, s42, 0\n" \
"v_mul_f32 v169, 0x3b124925, v169\n" \
"v_mfma_f32_16x16x32_bf16 a[16:19], v[64:67], v[112:115], a[16:19]\n" \
"ds_read_b64_tr_b16 v[34:35], v231 offset:2048\n" \
"v_xor_b32 v232, 0x38000, v232\n" \
"v_mfma_f32_16x16x32_bf16 a[20:23], v[64:67], v[116:119], a[20:23]\n" \
"buffer_load_dwordx4 v225, %[rc], s52 offen lds\n" \
"v_sub_f32 v152, v152, v168\n" \
"v_mfma_f32_16x16x32_bf16 a[24:27], v[64:67], v[120:123], a[24:27]\n" \
"ds_read_b64_tr_b16 v[36:37], v232 offset:0\n" \
"v_sub_f32 v160, v160, v169\n" \
"v_mfma_f32_16x16x32_bf16 a[28:31], v[64:67], v[124:127], a[28:31]\n" \
"s_add_u32 s41, s42, 4096\n" \
"v_sub_f32 v153, v153, v168\n" \
"v_mfma_f32_16x16x32_bf16 a[32:35], v[68:71], v[96:99], a[32:35]\n" \
"ds_read_b64_tr_b16 v[38:39], v232 offset:2048\n" \
"v_xor_b32 v233, 0x38000, v233\n" \
"v_mfma_f32_16x16x32_bf16 a[36:39], v[68:71], v[100:103], a[36:39]\n" \
"s_add_u32 s52, s48, 0\n" \
"buffer_load_dword v188, v185, s[20:23], s64 offen\n" \
"v_mfma_f32_16x16x32_bf16 a[40:43], v[68:71], v[104:107], a[40:43]\n" \
"ds_read_b64_tr_b16 v[40:41], v233 offset:0\n" \
"v_sub_f32 v161, v161, v169\n" \
"v_mfma_f32_16x16x32_bf16 a[44:47], v[68:71], v[108:111], a[44:47]\n" \
"s_add_u32 m0, s46, 0\n" \
"v_sub_f32 v154, v154, v168\n" \
"v_mfma_f32_16x16x32_bf16 a[48:51], v[68:71], v[112:115], a[48:51]\n" \
"ds_read_b64_tr_b16 v[42:43], v233 offset:2048\n" \
"v_xor_b32 v234, 0x38000, v234\n" \
"v_mfma_f32_16x16x32_bf16 a[52:55], v[68:71], v[116:119], a[52:55]\n" \
"buffer_load_dwordx4 v227, %[rw], s52 offen lds\n" \
"v_sub_f32 v162, v162, v169\n" \
"v_mfma_f32_16x16x32_bf16 a[56:59], v[68:71], v[120:123], a[56:59]\n" \
"ds_read_b64_tr_b16 v[44:45], v234 offset:0\n" \
"v_sub_f32 v155, v155, v168\n" \
"v_mfma_f32_16x16x32_bf16 a[60:63], v[68:71], v[124:127], a[60:63]\n" \
"s_add_u32 s43, s43, 128\n" \
"v_sub_f32 v163, v163, v169\n" \
"v_mfma_f32_16x16x32_bf16 a[64:67], v[72:75], v[96:99], a[64:67]\n" \
"ds_read_b64_tr_b16 v[46:47], v234 offset:2048\n" \
"v_xor_b32 v235, 0x38000, v235\n" \
"v_mfma_f32_16x16x32_bf16 a[68:71], v[72:75], v[100:103], a[68:71]\n" \
"s_add_u32 s52, s44, 16384\n" \
"v_sub_f32 v156, v156, v168\n" \
"v_mfma_f32_16x16x32_bf16 a[72:75], v[72:75], v[104:107], a[72:75]\n" \
"ds_read_b64_tr_b16 v[48:49], v235 offset:0\n" \
"v_sub_f32 v164, v164, v169\n" \
"v_mfma_f32_16x16x32_bf16 a[76:79], v[72:75], v[108:111], a[76:79]\n" \
"s_add_u32 m0, s42, 1024\n" \
"v_sub_f32 v157, v157, v168\n" \
"v_mfma_f32_16x16x32_bf16 a[80:83], v[72:75], v[112:115], a[80:83]\n" \
"ds_read_b64_tr_b16 v[50:51], v235 offset:2048\n" \
"v_xor_b32 v236, 0x38000, v236\n" \
"v_mfma_f32_16x16x32_bf16 a[84:87], v[72:75], v[116:119], a[84:87]\n" \
"buffer_load_dwordx4 v226, %[rc], s52 offen lds\n" \
"v_sub_f32 v165, v165, v169\n" \
"v_mfma_f32_16x16x32_bf16 a[88:91], v[72:75], v[120:123], a[88:91]\n" \
"ds_read_b64_tr_b16 v[52:53], v236 offset:0\n" \
"v_sub_f32 v158, v158, v168\n" \
"v_mfma_f32_16x16x32_bf16 a[92:95], v[72:75], v[124:127], a[92:95]\n" \
"s_xor_b32 s45, s45, 0x38000\n" \
"v_sub_f32 v166, v166, v169\n" \
"v_mfma_f32_16x16x32_bf16 a[96:99], v[76:79], v[96:99], a[96:99]\n" \
"ds_read_b64_tr_b16 v[54:55], v236 offset:2048\n" \
"v_xor_b32 v237, 0x38000, v237\n" \
"v_mfma_f32_16x16x32_bf16 a[100:103], v[76:79], v[100:103], a[100:103]\n" \
"s_add_u32 s52, s48, 16384\n" \
"buffer_load_dword v189, v186, s[28:31], s65 offen\n" \
"v_mfma_f32_16x16x32_bf16 a[104:107], v[76:79], v[104:107], a[104:107]\n" \
"ds_read_b64_tr_b16 v[56:57], v237 offset:0\n" \
"v_sub_f32 v159, v159, v168\n" \
"v_mfma_f32_16x16x32_bf16 a[108:111], v[76:79], v[108:111], a[108:111]\n" \
"s_add_u32 m0, s46, 1024\n" \
"v_sub_f32 v167, v167, v169\n" \
"v_mfma_f32_16x16x32_bf16 a[112:115], v[76:79], v[112:115], a[112:115]\n" \
"ds_read_b64_tr_b16 v[58:59], v237 offset:2048\n" \
"v_xor_b32 v238, 0x38000, v238\n" \
"v_mfma_f32_16x16x32_bf16 a[116:119], v[76:79], v[116:119], a[116:119]\n" \
"buffer_load_dwordx4 v228, %[rw], s52 offen lds\n" \
"v_mul_f32 v168, v152, v152\n" \
"v_mfma_f32_16x16x32_bf16 a[120:123], v[76:79], v[120:123], a[120:123]\n" \
"ds_read_b64_tr_b16 v[60:61], v238 offset:0\n" \
"v_mul_f32 v169, v160, v160\n" \
"v_mfma_f32_16x16x32_bf16 a[124:127], v[76:79], v[124:127], a[124:127]\n" \
"s_add_u32 s47, s47, 524288\n" \
"v_fmac_f32 v168, v153, v153\n" \
"v_mfma_f32_16x16x32_bf16 a[128:131], v[80:83], v[96:99], a[128:131]\n" \
"ds_read_b64_tr_b16 v[62:63], v238 offset:2048\n" \
"v_fmac_f32 v169, v161, v161\n" \
"v_mfma_f32_16x16x32_bf16 a[132:135], v[80:83], v[100:103], a[132:135]\n" \
"s_add_u32 s52, s44, 32768\n" \
"v_fmac_f32 v168, v154, v154\n" \
"v_mfma_f32_16x16x32_bf16 a[136:139], v[80:83], v[104:107], a[136:139]\n" \
"ds_read_b128 v[4:7], v229 offset:2048\n" \
"v_fmac_f32 v169, v162, v162\n" \
"v_mfma_f32_16x16x32_bf16 a[140:143], v[80:83], v[108:111], a[140:143]\n" \
"s_add_u32 m0, s42, 2048\n" \
"v_fmac_f32 v168, v155, v155\n" \
"v_mfma_f32_16x16x32_bf16 a[144:147], v[80:83], v[112:115], a[144:147]\n" \
"ds_read_b128 v[8:11], v229 offset:4096\n" \
"v_fmac_f32 v169, v163, v163\n" \
"v_mfma_f32_16x16x32_bf16 a[148:151], v[80:83], v[116:119], a[148:151]\n" \
"buffer_load_dwordx4 v225, %[rc], s52 offen lds\n" \
"v_fmac_f32 v168, v156, v156\n" \
"v_mfma_f32_16x16x32_bf16 a[152:155], v[80:83], v[120:123], a[152:155]\n" \
"ds_read_b128 v[12:15], v229 offset:6144\n" \
"v_fmac_f32 v169, v164, v164\n" \
"v_mfma_f32_16x16x32_bf16 a[156:159], v[80:83], v[124:127], a[156:159]\n" \
"s_mov_b32 s49, s50\n" \
"v_fmac_f32 v168, v157, v157\n" \
"v_mfma_f32_16x16x32_bf16 a[160:163], v[84:87], v[96:99], a[160:163]\n" \
"ds_read_b128 v[16:19], v229 offset:8192\n" \
"v_fmac_f32 v169, v165, v165\n" \
"v_mfma_f32_16x16x32_bf16 a[164:167], v[84:87], v[100:103], a[164:167]\n" \
"s_add_u32 s52, s48, 32768\n" \
"buffer_load_dword v191, v190, s[88:91], s66 offen\n" \
"v_mfma_f32_16x16x32_bf16 a[168:171], v[84:87], v[104:107], a[168:171]\n" \
"ds_read_b128 v[20:23], v229 offset:10240\n" \
"v_fmac_f32 v168, v158, v158\n" \
"v_mfma_f32_16x16x32_bf16 a[172:175], v[84:87], v[108:111], a[172:175]\n" \
"s_add_u32 m0, s46, 2048\n" \
"v_fmac_f32 v169, v166, v166\n" \
"v_mfma_f32_16x16x32_bf16 a[176:179], v[84:87], v[112:115], a[176:179]\n" \
"ds_read_b128 v[24:27], v229 offset:12288\n" \
"v_fmac_f32 v168, v159, v159\n" \
"v_mfma_f32_16x16x32_bf16 a[180:183], v[84:87], v[116:119], a[180:183]\n" \
"buffer_load_dwordx4 v227, %[rw], s52 offen lds\n" \
"v_fmac_f32 v169, v167, v167\n" \
"v_mfma_f32_16x16x32_bf16 a[184:187], v[84:87], v[120:123], a[184:187]\n" \
"ds_read_b128 v[28:31], v229 offset:14336\n" \
"v_cndmask_b32 v168, 0, v168, vcc\n" \
"v_mfma_f32_16x16x32_bf16 a[188:191], v[84:87], v[124:127], a[188:191]\n" \
"s_mov_b32 s50, s51\n" \
"v_cndmask_b32 v169, 0, v169, vcc\n" \
"v_mfma_f32_16x16x32_bf16 a[192:195], v[88:91], v[96:99], a[192:195]\n" \
"s_add_u32 s53, s53, 1\n" \
"v_add_f32_dpp v168, v168, v168 quad_perm:[1,0,3,2] row_mask:0xf bank_mask:0xf\n" \
"v_mfma_f32_16x16x32_bf16 a[196:199], v[88:91], v[100:103], a[196:199]\n" \
"s_add_u32 s52, s44, 49152\n" \
"v_add_f32_dpp v169, v169, v169 quad_perm:[1,0,3,2] row_mask:0xf bank_mask:0xf\n" \
"v_mfma_f32_16x16x32_bf16 a[200:203], v[88:91], v[104:107], a[200:203]\n" \
"v_add_u32 v229, s51, v229\n" \
"v_add_f32_dpp v168, v168, v168 quad_perm:[2,3,0,1] row_mask:0xf bank_mask:0xf\n" \
"v_mfma_f32_16x16x32_bf16 a[204:207], v[88:91], v[108:111], a[204:207]\n" \
"s_add_u32 m0, s42, 3072\n" \
"v_add_f32_dpp v169, v169, v169 quad_perm:[2,3,0,1] row_mask:0xf bank_mask:0xf\n" \
"v_mfma_f32_16x16x32_bf16 a[208:211], v[88:91], v[112:115], a[208:211]\n" \
"v_add_f32_dpp v168, v168, v168 row_half_mirror row_mask:0xf bank_mask:0xf\n" \
"v_add_f32_dpp v169, v169, v169 row_half_mirror row_mask:0xf bank_mask:0xf\n" \
"v_mfma_f32_16x16x32_bf16 a[212:215], v[88:91], v[116:119], a[212:215]\n" \
"buffer_load_dwordx4 v226, %[rc], s52 offen lds\n" \
"v_add_f32_dpp v168, v168, v168 row_mirror row_mask:0xf bank_mask:0xf\n" \
"v_mfma_f32_16x16x32_bf16 a[216:219], v[88:91], v[120:123], a[216:219]\n" \
"v_add_f32_dpp v169, v169, v169 row_mirror row_mask:0xf bank_mask:0xf\n" \
"v_mov_b32 v170, v168\n" \
"v_mfma_f32_16x16x32_bf16 a[220:223], v[88:91], v[124:127], a[220:223]\n" \
"v_mov_b32 v171, v169\n" \
"v_permlane16_swap_b32 v170, v168\n" \
"v_mfma_f32_16x16x32_bf16 a[224:227], v[92:95], v[96:99], a[224:227]\n" \
"v_permlane16_swap_b32 v171, v169\n" \
"v_add_f32 v168, v168, v170\n" \
"v_mfma_f32_16x16x32_bf16 a[228:231], v[92:95], v[100:103], a[228:231]\n" \
"s_add_u32 s52, s48, 49152\n" \
"buffer_load_dword v192, v190, s[32:35], s66 offen\n" \
"v_mfma_f32_16x16x32_bf16 a[232:235], v[92:95], v[104:107], a[232:235]\n" \
"v_add_f32 v169, v169, v171\n" \
"v_mov_b32 v170, v168\n" \
"v_mfma_f32_16x16x32_bf16 a[236:239], v[92:95], v[108:111], a[236:239]\n" \
"s_add_u32 m0, s46, 3072\n" \
"v_mov_b32 v171, v169\n" \
"v_mfma_f32_16x16x32_bf16 a[240:243], v[92:95], v[112:115], a[240:243]\n" \
"v_permlane32_swap_b32 v170, v168\n" \
"v_permlane32_swap_b32 v171, v169\n" \
"v_mfma_f32_16x16x32_bf16 a[244:247], v[92:95], v[116:119], a[244:247]\n" \
"buffer_load_dwordx4 v228, %[rw], s52 offen lds\n" \
"v_add_f32 v168, v168, v170\n" \
"v_mfma_f32_16x16x32_bf16 a[248:251], v[92:95], v[120:123], a[248:251]\n" \
"v_add_f32 v169, v169, v171\n" \
"v_mov_b32 v172, 0x3727c5ac\n" \
"v_mfma_f32_16x16x32_bf16 a[252:255], v[92:95], v[124:127], a[252:255]\n" \
"v_mov_b32 v173, 0x3727c5ac\n" \
"v_fmac_f32 v172, 0x3b124925, v168\n" \
"v_fmac_f32 v173, 0x3b124925, v169\n" \
"v_rsq_f32 v172, v172\n" \
"v_rsq_f32 v173, v173\n" \
"v_mul_f32 v172, 0x3db8aa3b, v172\n" \
"v_mul_f32 v173, 0x3db8aa3b, v173\n" \
"v_mul_f32 v152, v152, v172\n" \
"v_mul_f32 v160, v160, v173\n" \
"v_mul_f32 v153, v153, v172\n" \
"v_mul_f32 v161, v161, v173\n" \
"v_mul_f32 v154, v154, v172\n" \
"v_mul_f32 v162, v162, v173\n" \
"v_mul_f32 v155, v155, v172\n" \
"v_mul_f32 v163, v163, v173\n" \
"v_mul_f32 v156, v156, v172\n" \
"v_mul_f32 v164, v164, v173\n" \
"v_mul_f32 v157, v157, v172\n" \
"v_mul_f32 v165, v165, v173\n" \
"v_mul_f32 v158, v158, v172\n" \
"v_mul_f32 v166, v166, v173\n" \
"v_mul_f32 v159, v159, v172\n" \
"v_mul_f32 v167, v167, v173\n" \
"v_cvt_pk_bf16_f32 v144, v152, v153\n" \
"v_cvt_pk_bf16_f32 v148, v160, v161\n" \
"v_cvt_pk_bf16_f32 v145, v154, v155\n" \
"v_cvt_pk_bf16_f32 v149, v162, v163\n" \
"v_cvt_pk_bf16_f32 v146, v156, v157\n" \
"v_cvt_pk_bf16_f32 v150, v164, v165\n" \
"v_cvt_pk_bf16_f32 v147, v158, v159\n" \
"v_cvt_pk_bf16_f32 v151, v166, v167\n" \
"buffer_store_dwordx4 v[144:147], v175, %[rs], s68 offen sc1\n" \
"s_nop 0\n" \
"buffer_store_dwordx4 v[148:151], v175, %[rs], s68 offen offset:16 sc1\n" \
"s_lshr_b32 s80, s17, 1\n" \
"s_lshl_b32 s81, s16, 3\n" \
"s_add_u32 s81, s81, s80\n" \
"s_mul_i32 s83, s81, 28672\n" \
"s_lshl_b32 s80, s80, 12\n" \
"s_mul_i32 s94, s16, 229376\n" \
"s_add_u32 s94, s94, s80\n" \
"s_add_u32 s94, s94, 54525952\n" \
"s_lshr_b32 s80, s17, 1\n" \
"s_lshl_b32 s80, s80, 17\n" \
"s_lshr_b32 s81, s36, 1\n" \
"s_lshl_b32 s81, s81, 16\n" \
"s_add_u32 s80, s80, s81\n" \
"s_lshl_b32 s81, s16, 20\n" \
"s_add_u32 s80, s80, s81\n" \
"s_and_b32 s81, s36, 1\n" \
"s_lshl_b32 s81, s81, 12\n" \
"s_lshl_b32 s81, s81, 1\n" \
"s_add_u32 s80, s80, s81\n" \
"s_lshl_b32 s81, s54, 24\n" \
"s_add_u32 s80, s80, s81\n" \
"s_add_u32 s55, s80, 1048576\n" \
"s_lshr_b32 s80, s95, 4\n" \
"s_lshl_b32 s56, s80, 8\n" \
"s_and_b32 s80, s95, 15\n" \
"s_lshl_b32 s80, s80, 2\n" \
"s_add_u32 s56, s56, s80\n" \
"s_lshl_b32 s57, s18, 8\n" \
"v_bfe_u32 v240, v224, 4, 4\n" \
"v_min_u32 v241, 13, v240\n" \
"v_lshlrev_b32 v240, 2, v240\n" \
"v_lshlrev_b32 v241, 2, v241\n" \
"s_lshl_b32 s80, s18, 3\n" \
"s_add_u32 s81, s80, s36\n" \
"s_mul_i32 s58, s81, 14336\n" \
"s_add_u32 s58, s58, 34603008\n" \
"s_sub_u32 s81, s80, s36\n" \
"s_mul_i32 s71, s81, 14336\n" \
"s_add_u32 s71, s71, 34703360\n" \
"s_lshl_b32 s80, s36, 2\n" \
"s_add_u32 s80, s80, 0\n" \
"s_min_u32 s80, s80, 13\n" \
"s_mul_i32 s81, s54, 14\n" \
"s_add_u32 s80, s80, s81\n" \
"s_lshl_b32 s72, s80, 5\n" \
"s_add_u32 s72, s72, s83\n" \
"s_lshr_b32 s81, s80, 2\n" \
"s_lshl_b32 s81, s81, 15\n" \
"s_and_b32 s80, s80, 3\n" \
"s_lshl_b32 s80, s80, 10\n" \
"s_add_u32 s80, s80, s81\n" \
"s_add_u32 s76, s80, s94\n" \
"s_lshl_b32 s80, s36, 2\n" \
"s_add_u32 s80, s80, 1\n" \
"s_min_u32 s80, s80, 13\n" \
"s_mul_i32 s81, s54, 14\n" \
"s_add_u32 s80, s80, s81\n" \
"s_lshl_b32 s73, s80, 5\n" \
"s_add_u32 s73, s73, s83\n" \
"s_lshr_b32 s81, s80, 2\n" \
"s_lshl_b32 s81, s81, 15\n" \
"s_and_b32 s80, s80, 3\n" \
"s_lshl_b32 s80, s80, 10\n" \
"s_add_u32 s80, s80, s81\n" \
"s_add_u32 s77, s80, s94\n" \
"s_lshl_b32 s80, s36, 2\n" \
"s_add_u32 s80, s80, 2\n" \
"s_min_u32 s80, s80, 13\n" \
"s_mul_i32 s81, s54, 14\n" \
"s_add_u32 s80, s80, s81\n" \
"s_lshl_b32 s74, s80, 5\n" \
"s_add_u32 s74, s74, s83\n" \
"s_lshr_b32 s81, s80, 2\n" \
"s_lshl_b32 s81, s81, 15\n" \
"s_and_b32 s80, s80, 3\n" \
"s_lshl_b32 s80, s80, 10\n" \
"s_add_u32 s80, s80, s81\n" \
"s_add_u32 s78, s80, s94\n" \
"s_lshl_b32 s80, s36, 2\n" \
"s_add_u32 s80, s80, 3\n" \
"s_min_u32 s80, s80, 13\n" \
"s_mul_i32 s81, s54, 14\n" \
"s_add_u32 s80, s80, s81\n" \
"s_lshl_b32 s75, s80, 5\n" \
"s_add_u32 s75, s75, s83\n" \
"s_lshr_b32 s81, s80, 2\n" \
"s_lshl_b32 s81, s81, 15\n" \
"s_and_b32 s80, s80, 3\n" \
"s_lshl_b32 s80, s80, 10\n" \
"s_add_u32 s80, s80, s81\n" \
"s_add_u32 s79, s80, s94\n" \
"s_and_b32 s82, s36, 1\n" \
"s_lshl_b32 s82, s82, 8\n" \
"s_sub_u32 s82, 0xfffe8000, s82\n" \
".p2align 6\n" \
"LkvV%=:\n" \
"s_waitcnt vmcnt(16) lgkmcnt(0)\n" \
"s_barrier\n" \
"v_mfma_f32_16x16x32_bf16 a[0:3], v[0:3], v[32:35], a[0:3]\n" \
"ds_read_b128 v[64:67], v230\n" \
"v_mfma_f32_16x16x32_bf16 a[4:7], v[0:3], v[36:39], a[4:7]\n" \
"s_add_u32 s52, s43, 65536\n" \
"v_mfma_f32_16x16x32_bf16 a[8:11], v[0:3], v[40:43], a[8:11]\n" \
"ds_read_b64_tr_b16 v[96:97], v231 offset:16384\n" \
"v_mfma_f32_16x16x32_bf16 a[12:15], v[0:3], v[44:47], a[12:15]\n" \
"s_add_u32 m0, s41, 0\n" \
"v_mfma_f32_16x16x32_bf16 a[16:19], v[0:3], v[48:51], a[16:19]\n" \
"ds_read_b64_tr_b16 v[98:99], v231 offset:18432\n" \
"v_mfma_f32_16x16x32_bf16 a[20:23], v[0:3], v[52:55], a[20:23]\n" \
"buffer_load_dwordx4 v225, %[rc], s52 offen lds\n" \
"v_mfma_f32_16x16x32_bf16 a[24:27], v[0:3], v[56:59], a[24:27]\n" \
"ds_read_b64_tr_b16 v[100:101], v232 offset:16384\n" \
"v_mfma_f32_16x16x32_bf16 a[28:31], v[0:3], v[60:63], a[28:31]\n" \
"s_add_u32 s42, s42, s49\n" \
"v_mfma_f32_16x16x32_bf16 a[32:35], v[4:7], v[32:35], a[32:35]\n" \
"ds_read_b64_tr_b16 v[102:103], v232 offset:18432\n" \
"v_mfma_f32_16x16x32_bf16 a[36:39], v[4:7], v[36:39], a[36:39]\n" \
"s_add_u32 s52, s47, 0\n" \
"v_mfma_f32_16x16x32_bf16 a[40:43], v[4:7], v[40:43], a[40:43]\n" \
"ds_read_b64_tr_b16 v[104:105], v233 offset:16384\n" \
"v_mfma_f32_16x16x32_bf16 a[44:47], v[4:7], v[44:47], a[44:47]\n" \
"s_add_u32 m0, s45, 0\n" \
"v_mfma_f32_16x16x32_bf16 a[48:51], v[4:7], v[48:51], a[48:51]\n" \
"ds_read_b64_tr_b16 v[106:107], v233 offset:18432\n" \
"v_mfma_f32_16x16x32_bf16 a[52:55], v[4:7], v[52:55], a[52:55]\n" \
"buffer_load_dwordx4 v227, %[rw], s52 offen lds\n" \
"v_mfma_f32_16x16x32_bf16 a[56:59], v[4:7], v[56:59], a[56:59]\n" \
"ds_read_b64_tr_b16 v[108:109], v234 offset:16384\n" \
"v_mfma_f32_16x16x32_bf16 a[60:63], v[4:7], v[60:63], a[60:63]\n" \
"s_add_u32 s44, s44, 128\n" \
"v_mfma_f32_16x16x32_bf16 a[64:67], v[8:11], v[32:35], a[64:67]\n" \
"ds_read_b64_tr_b16 v[110:111], v234 offset:18432\n" \
"v_mfma_f32_16x16x32_bf16 a[68:71], v[8:11], v[36:39], a[68:71]\n" \
"s_add_u32 s52, s43, 81920\n" \
"v_mfma_f32_16x16x32_bf16 a[72:75], v[8:11], v[40:43], a[72:75]\n" \
"ds_read_b64_tr_b16 v[112:113], v235 offset:16384\n" \
"v_mfma_f32_16x16x32_bf16 a[76:79], v[8:11], v[44:47], a[76:79]\n" \
"s_add_u32 m0, s41, 1024\n" \
"v_mfma_f32_16x16x32_bf16 a[80:83], v[8:11], v[48:51], a[80:83]\n" \
"ds_read_b64_tr_b16 v[114:115], v235 offset:18432\n" \
"v_mfma_f32_16x16x32_bf16 a[84:87], v[8:11], v[52:55], a[84:87]\n" \
"buffer_load_dwordx4 v226, %[rc], s52 offen lds\n" \
"v_mfma_f32_16x16x32_bf16 a[88:91], v[8:11], v[56:59], a[88:91]\n" \
"ds_read_b64_tr_b16 v[116:117], v236 offset:16384\n" \
"v_mfma_f32_16x16x32_bf16 a[92:95], v[8:11], v[60:63], a[92:95]\n" \
"s_xor_b32 s46, s46, 0x38000\n" \
"v_mfma_f32_16x16x32_bf16 a[96:99], v[12:15], v[32:35], a[96:99]\n" \
"ds_read_b64_tr_b16 v[118:119], v236 offset:18432\n" \
"v_mfma_f32_16x16x32_bf16 a[100:103], v[12:15], v[36:39], a[100:103]\n" \
"s_add_u32 s52, s47, 16384\n" \
"v_mfma_f32_16x16x32_bf16 a[104:107], v[12:15], v[40:43], a[104:107]\n" \
"ds_read_b64_tr_b16 v[120:121], v237 offset:16384\n" \
"v_mfma_f32_16x16x32_bf16 a[108:111], v[12:15], v[44:47], a[108:111]\n" \
"s_add_u32 m0, s45, 1024\n" \
"v_mfma_f32_16x16x32_bf16 a[112:115], v[12:15], v[48:51], a[112:115]\n" \
"ds_read_b64_tr_b16 v[122:123], v237 offset:18432\n" \
"v_mfma_f32_16x16x32_bf16 a[116:119], v[12:15], v[52:55], a[116:119]\n" \
"buffer_load_dwordx4 v228, %[rw], s52 offen lds\n" \
"v_mfma_f32_16x16x32_bf16 a[120:123], v[12:15], v[56:59], a[120:123]\n" \
"ds_read_b64_tr_b16 v[124:125], v238 offset:16384\n" \
"v_mfma_f32_16x16x32_bf16 a[124:127], v[12:15], v[60:63], a[124:127]\n" \
"s_add_u32 s48, s48, 524288\n" \
"v_mfma_f32_16x16x32_bf16 a[128:131], v[16:19], v[32:35], a[128:131]\n" \
"ds_read_b64_tr_b16 v[126:127], v238 offset:18432\n" \
"v_mfma_f32_16x16x32_bf16 a[132:135], v[16:19], v[36:39], a[132:135]\n" \
"s_add_u32 s52, s43, 98304\n" \
"v_mfma_f32_16x16x32_bf16 a[136:139], v[16:19], v[40:43], a[136:139]\n" \
"ds_read_b128 v[68:71], v230 offset:2048\n" \
"v_mfma_f32_16x16x32_bf16 a[140:143], v[16:19], v[44:47], a[140:143]\n" \
"s_add_u32 m0, s41, 2048\n" \
"v_mfma_f32_16x16x32_bf16 a[144:147], v[16:19], v[48:51], a[144:147]\n" \
"ds_read_b128 v[72:75], v230 offset:4096\n" \
"v_mfma_f32_16x16x32_bf16 a[148:151], v[16:19], v[52:55], a[148:151]\n" \
"buffer_load_dwordx4 v225, %[rc], s52 offen lds\n" \
"v_mfma_f32_16x16x32_bf16 a[152:155], v[16:19], v[56:59], a[152:155]\n" \
"ds_read_b128 v[76:79], v230 offset:6144\n" \
"v_mfma_f32_16x16x32_bf16 a[156:159], v[16:19], v[60:63], a[156:159]\n" \
"s_add_u32 s51, s49, s50\n" \
"v_mfma_f32_16x16x32_bf16 a[160:163], v[20:23], v[32:35], a[160:163]\n" \
"ds_read_b128 v[80:83], v230 offset:8192\n" \
"v_mfma_f32_16x16x32_bf16 a[164:167], v[20:23], v[36:39], a[164:167]\n" \
"s_add_u32 s52, s47, 32768\n" \
"v_mfma_f32_16x16x32_bf16 a[168:171], v[20:23], v[40:43], a[168:171]\n" \
"ds_read_b128 v[84:87], v230 offset:10240\n" \
"v_mfma_f32_16x16x32_bf16 a[172:175], v[20:23], v[44:47], a[172:175]\n" \
"s_add_u32 m0, s45, 2048\n" \
"v_mfma_f32_16x16x32_bf16 a[176:179], v[20:23], v[48:51], a[176:179]\n" \
"ds_read_b128 v[88:91], v230 offset:12288\n" \
"v_mfma_f32_16x16x32_bf16 a[180:183], v[20:23], v[52:55], a[180:183]\n" \
"buffer_load_dwordx4 v227, %[rw], s52 offen lds\n" \
"v_mfma_f32_16x16x32_bf16 a[184:187], v[20:23], v[56:59], a[184:187]\n" \
"ds_read_b128 v[92:95], v230 offset:14336\n" \
"v_mfma_f32_16x16x32_bf16 a[188:191], v[20:23], v[60:63], a[188:191]\n" \
"s_sub_u32 s51, 0, s51\n" \
"v_mfma_f32_16x16x32_bf16 a[192:195], v[24:27], v[32:35], a[192:195]\n" \
"v_mfma_f32_16x16x32_bf16 a[196:199], v[24:27], v[36:39], a[196:199]\n" \
"s_add_u32 s52, s43, 114688\n" \
"v_mfma_f32_16x16x32_bf16 a[200:203], v[24:27], v[40:43], a[200:203]\n" \
"v_add_u32 v230, s50, v230\n" \
"v_mfma_f32_16x16x32_bf16 a[204:207], v[24:27], v[44:47], a[204:207]\n" \
"s_add_u32 m0, s41, 3072\n" \
"v_mfma_f32_16x16x32_bf16 a[208:211], v[24:27], v[48:51], a[208:211]\n" \
"v_mfma_f32_16x16x32_bf16 a[212:215], v[24:27], v[52:55], a[212:215]\n" \
"buffer_load_dwordx4 v226, %[rc], s52 offen lds\n" \
"v_mfma_f32_16x16x32_bf16 a[216:219], v[24:27], v[56:59], a[216:219]\n" \
"v_mfma_f32_16x16x32_bf16 a[220:223], v[24:27], v[60:63], a[220:223]\n" \
"v_mfma_f32_16x16x32_bf16 a[224:227], v[28:31], v[32:35], a[224:227]\n" \
"v_mfma_f32_16x16x32_bf16 a[228:231], v[28:31], v[36:39], a[228:231]\n" \
"s_add_u32 s52, s47, 49152\n" \
"v_mfma_f32_16x16x32_bf16 a[232:235], v[28:31], v[40:43], a[232:235]\n" \
"v_mfma_f32_16x16x32_bf16 a[236:239], v[28:31], v[44:47], a[236:239]\n" \
"s_add_u32 m0, s45, 3072\n" \
"v_mfma_f32_16x16x32_bf16 a[240:243], v[28:31], v[48:51], a[240:243]\n" \
"v_mfma_f32_16x16x32_bf16 a[244:247], v[28:31], v[52:55], a[244:247]\n" \
"buffer_load_dwordx4 v228, %[rw], s52 offen lds\n" \
"v_mfma_f32_16x16x32_bf16 a[248:251], v[28:31], v[56:59], a[248:251]\n" \
"v_mfma_f32_16x16x32_bf16 a[252:255], v[28:31], v[60:63], a[252:255]\n" \
"s_waitcnt vmcnt(16) lgkmcnt(0)\n" \
"s_barrier\n" \
"v_mfma_f32_16x16x32_bf16 a[0:3], v[64:67], v[96:99], a[0:3]\n" \
"ds_read_b128 v[0:3], v229\n" \
"v_xor_b32 v231, 0x38000, v231\n" \
"v_mfma_f32_16x16x32_bf16 a[4:7], v[64:67], v[100:103], a[4:7]\n" \
"s_add_u32 s52, s44, 0\n" \
"v_mfma_f32_16x16x32_bf16 a[8:11], v[64:67], v[104:107], a[8:11]\n" \
"ds_read_b64_tr_b16 v[32:33], v231 offset:0\n" \
"v_mfma_f32_16x16x32_bf16 a[12:15], v[64:67], v[108:111], a[12:15]\n" \
"s_add_u32 m0, s42, 0\n" \
"v_mfma_f32_16x16x32_bf16 a[16:19], v[64:67], v[112:115], a[16:19]\n" \
"ds_read_b64_tr_b16 v[34:35], v231 offset:2048\n" \
"v_xor_b32 v232, 0x38000, v232\n" \
"v_mfma_f32_16x16x32_bf16 a[20:23], v[64:67], v[116:119], a[20:23]\n" \
"buffer_load_dwordx4 v225, %[rc], s52 offen lds\n" \
"v_mfma_f32_16x16x32_bf16 a[24:27], v[64:67], v[120:123], a[24:27]\n" \
"ds_read_b64_tr_b16 v[36:37], v232 offset:0\n" \
"v_mfma_f32_16x16x32_bf16 a[28:31], v[64:67], v[124:127], a[28:31]\n" \
"s_add_u32 s41, s42, 4096\n" \
"v_mfma_f32_16x16x32_bf16 a[32:35], v[68:71], v[96:99], a[32:35]\n" \
"ds_read_b64_tr_b16 v[38:39], v232 offset:2048\n" \
"v_xor_b32 v233, 0x38000, v233\n" \
"v_mfma_f32_16x16x32_bf16 a[36:39], v[68:71], v[100:103], a[36:39]\n" \
"s_add_u32 s52, s48, 0\n" \
"v_mfma_f32_16x16x32_bf16 a[40:43], v[68:71], v[104:107], a[40:43]\n" \
"ds_read_b64_tr_b16 v[40:41], v233 offset:0\n" \
"v_mfma_f32_16x16x32_bf16 a[44:47], v[68:71], v[108:111], a[44:47]\n" \
"s_add_u32 m0, s46, 0\n" \
"v_mfma_f32_16x16x32_bf16 a[48:51], v[68:71], v[112:115], a[48:51]\n" \
"ds_read_b64_tr_b16 v[42:43], v233 offset:2048\n" \
"v_xor_b32 v234, 0x38000, v234\n" \
"v_mfma_f32_16x16x32_bf16 a[52:55], v[68:71], v[116:119], a[52:55]\n" \
"buffer_load_dwordx4 v227, %[rw], s52 offen lds\n" \
"v_mfma_f32_16x16x32_bf16 a[56:59], v[68:71], v[120:123], a[56:59]\n" \
"ds_read_b64_tr_b16 v[44:45], v234 offset:0\n" \
"v_mfma_f32_16x16x32_bf16 a[60:63], v[68:71], v[124:127], a[60:63]\n" \
"s_add_u32 s43, s43, 128\n" \
"v_mfma_f32_16x16x32_bf16 a[64:67], v[72:75], v[96:99], a[64:67]\n" \
"ds_read_b64_tr_b16 v[46:47], v234 offset:2048\n" \
"v_xor_b32 v235, 0x38000, v235\n" \
"v_mfma_f32_16x16x32_bf16 a[68:71], v[72:75], v[100:103], a[68:71]\n" \
"s_add_u32 s52, s44, 16384\n" \
"v_mfma_f32_16x16x32_bf16 a[72:75], v[72:75], v[104:107], a[72:75]\n" \
"ds_read_b64_tr_b16 v[48:49], v235 offset:0\n" \
"v_mfma_f32_16x16x32_bf16 a[76:79], v[72:75], v[108:111], a[76:79]\n" \
"s_add_u32 m0, s42, 1024\n" \
"v_mfma_f32_16x16x32_bf16 a[80:83], v[72:75], v[112:115], a[80:83]\n" \
"ds_read_b64_tr_b16 v[50:51], v235 offset:2048\n" \
"v_xor_b32 v236, 0x38000, v236\n" \
"v_mfma_f32_16x16x32_bf16 a[84:87], v[72:75], v[116:119], a[84:87]\n" \
"buffer_load_dwordx4 v226, %[rc], s52 offen lds\n" \
"v_mfma_f32_16x16x32_bf16 a[88:91], v[72:75], v[120:123], a[88:91]\n" \
"ds_read_b64_tr_b16 v[52:53], v236 offset:0\n" \
"v_mfma_f32_16x16x32_bf16 a[92:95], v[72:75], v[124:127], a[92:95]\n" \
"s_xor_b32 s45, s45, 0x38000\n" \
"v_mfma_f32_16x16x32_bf16 a[96:99], v[76:79], v[96:99], a[96:99]\n" \
"ds_read_b64_tr_b16 v[54:55], v236 offset:2048\n" \
"v_xor_b32 v237, 0x38000, v237\n" \
"v_mfma_f32_16x16x32_bf16 a[100:103], v[76:79], v[100:103], a[100:103]\n" \
"s_add_u32 s52, s48, 16384\n" \
"v_mfma_f32_16x16x32_bf16 a[104:107], v[76:79], v[104:107], a[104:107]\n" \
"ds_read_b64_tr_b16 v[56:57], v237 offset:0\n" \
"v_mfma_f32_16x16x32_bf16 a[108:111], v[76:79], v[108:111], a[108:111]\n" \
"s_add_u32 m0, s46, 1024\n" \
"v_mfma_f32_16x16x32_bf16 a[112:115], v[76:79], v[112:115], a[112:115]\n" \
"ds_read_b64_tr_b16 v[58:59], v237 offset:2048\n" \
"v_xor_b32 v238, 0x38000, v238\n" \
"v_mfma_f32_16x16x32_bf16 a[116:119], v[76:79], v[116:119], a[116:119]\n" \
"buffer_load_dwordx4 v228, %[rw], s52 offen lds\n" \
"v_mfma_f32_16x16x32_bf16 a[120:123], v[76:79], v[120:123], a[120:123]\n" \
"ds_read_b64_tr_b16 v[60:61], v238 offset:0\n" \
"v_mfma_f32_16x16x32_bf16 a[124:127], v[76:79], v[124:127], a[124:127]\n" \
"s_add_u32 s47, s47, 524288\n" \
"v_mfma_f32_16x16x32_bf16 a[128:131], v[80:83], v[96:99], a[128:131]\n" \
"ds_read_b64_tr_b16 v[62:63], v238 offset:2048\n" \
"v_mfma_f32_16x16x32_bf16 a[132:135], v[80:83], v[100:103], a[132:135]\n" \
"s_add_u32 s52, s44, 32768\n" \
"v_mfma_f32_16x16x32_bf16 a[136:139], v[80:83], v[104:107], a[136:139]\n" \
"ds_read_b128 v[4:7], v229 offset:2048\n" \
"v_mfma_f32_16x16x32_bf16 a[140:143], v[80:83], v[108:111], a[140:143]\n" \
"s_add_u32 m0, s42, 2048\n" \
"v_mfma_f32_16x16x32_bf16 a[144:147], v[80:83], v[112:115], a[144:147]\n" \
"ds_read_b128 v[8:11], v229 offset:4096\n" \
"v_mfma_f32_16x16x32_bf16 a[148:151], v[80:83], v[116:119], a[148:151]\n" \
"buffer_load_dwordx4 v225, %[rc], s52 offen lds\n" \
"v_mfma_f32_16x16x32_bf16 a[152:155], v[80:83], v[120:123], a[152:155]\n" \
"ds_read_b128 v[12:15], v229 offset:6144\n" \
"v_mfma_f32_16x16x32_bf16 a[156:159], v[80:83], v[124:127], a[156:159]\n" \
"s_mov_b32 s49, s50\n" \
"v_mfma_f32_16x16x32_bf16 a[160:163], v[84:87], v[96:99], a[160:163]\n" \
"ds_read_b128 v[16:19], v229 offset:8192\n" \
"v_mfma_f32_16x16x32_bf16 a[164:167], v[84:87], v[100:103], a[164:167]\n" \
"s_add_u32 s52, s48, 32768\n" \
"v_mfma_f32_16x16x32_bf16 a[168:171], v[84:87], v[104:107], a[168:171]\n" \
"ds_read_b128 v[20:23], v229 offset:10240\n" \
"v_mfma_f32_16x16x32_bf16 a[172:175], v[84:87], v[108:111], a[172:175]\n" \
"s_add_u32 m0, s46, 2048\n" \
"v_mfma_f32_16x16x32_bf16 a[176:179], v[84:87], v[112:115], a[176:179]\n" \
"ds_read_b128 v[24:27], v229 offset:12288\n" \
"v_mfma_f32_16x16x32_bf16 a[180:183], v[84:87], v[116:119], a[180:183]\n" \
"buffer_load_dwordx4 v227, %[rw], s52 offen lds\n" \
"v_mfma_f32_16x16x32_bf16 a[184:187], v[84:87], v[120:123], a[184:187]\n" \
"ds_read_b128 v[28:31], v229 offset:14336\n" \
"v_mfma_f32_16x16x32_bf16 a[188:191], v[84:87], v[124:127], a[188:191]\n" \
"s_mov_b32 s50, s51\n" \
"v_mfma_f32_16x16x32_bf16 a[192:195], v[88:91], v[96:99], a[192:195]\n" \
"s_add_u32 s53, s53, 1\n" \
"v_mfma_f32_16x16x32_bf16 a[196:199], v[88:91], v[100:103], a[196:199]\n" \
"s_add_u32 s52, s44, 49152\n" \
"v_mfma_f32_16x16x32_bf16 a[200:203], v[88:91], v[104:107], a[200:203]\n" \
"v_add_u32 v229, s51, v229\n" \
"v_mfma_f32_16x16x32_bf16 a[204:207], v[88:91], v[108:111], a[204:207]\n" \
"s_add_u32 m0, s42, 3072\n" \
"v_mfma_f32_16x16x32_bf16 a[208:211], v[88:91], v[112:115], a[208:211]\n" \
"v_mfma_f32_16x16x32_bf16 a[212:215], v[88:91], v[116:119], a[212:215]\n" \
"buffer_load_dwordx4 v226, %[rc], s52 offen lds\n" \
"v_mfma_f32_16x16x32_bf16 a[216:219], v[88:91], v[120:123], a[216:219]\n" \
"v_mfma_f32_16x16x32_bf16 a[220:223], v[88:91], v[124:127], a[220:223]\n" \
"v_mfma_f32_16x16x32_bf16 a[224:227], v[92:95], v[96:99], a[224:227]\n" \
"v_mfma_f32_16x16x32_bf16 a[228:231], v[92:95], v[100:103], a[228:231]\n" \
"s_add_u32 s52, s48, 49152\n" \
"v_mfma_f32_16x16x32_bf16 a[232:235], v[92:95], v[104:107], a[232:235]\n" \
"v_mfma_f32_16x16x32_bf16 a[236:239], v[92:95], v[108:111], a[236:239]\n" \
"s_add_u32 m0, s46, 3072\n" \
"v_mfma_f32_16x16x32_bf16 a[240:243], v[92:95], v[112:115], a[240:243]\n" \
"v_mfma_f32_16x16x32_bf16 a[244:247], v[92:95], v[116:119], a[244:247]\n" \
"buffer_load_dwordx4 v228, %[rw], s52 offen lds\n" \
"v_mfma_f32_16x16x32_bf16 a[248:251], v[92:95], v[120:123], a[248:251]\n" \
"s_cmp_lg_u32 s53, 3\n" \
"v_mfma_f32_16x16x32_bf16 a[252:255], v[92:95], v[124:127], a[252:255]\n" \
"s_cbranch_scc1 LnsV%=\n" \
"s_cmp_lg_u32 s36, 0\n" \
"s_cbranch_scc1 LsxV%=\n" \
"s_mov_b64 s[96:97], exec\n" \
"s_mov_b64 exec, 1\n" \
"v_mov_b32 v223, %[tag]\n" \
"buffer_store_dword v223, off, %[rs], s56 sc1\n" \
"s_mov_b64 exec, s[96:97]\n" \
"LsxV%=:\n" \
"LnsV%=:\n" \
"s_cmp_lt_u32 s53, 13\n" \
"s_cbranch_scc1 LkvV%=\n" \
"s_waitcnt vmcnt(16) lgkmcnt(0)\n" \
"s_barrier\n" \
"v_mfma_f32_16x16x32_bf16 a[0:3], v[0:3], v[32:35], a[0:3]\n" \
"ds_read_b128 v[64:67], v230\n" \
"buffer_load_dword v222, v240, %[rs], s57 offen sc1\n" \
"v_mfma_f32_16x16x32_bf16 a[4:7], v[0:3], v[36:39], a[4:7]\n" \
"s_add_u32 s52, s39, 67456\n" \
"v_mfma_f32_16x16x32_bf16 a[8:11], v[0:3], v[40:43], a[8:11]\n" \
"ds_read_b64_tr_b16 v[96:97], v231 offset:16384\n" \
"v_mfma_f32_16x16x32_bf16 a[12:15], v[0:3], v[44:47], a[12:15]\n" \
"s_add_u32 m0, s37, 4096\n" \
"v_mfma_f32_16x16x32_bf16 a[16:19], v[0:3], v[48:51], a[16:19]\n" \
"ds_read_b64_tr_b16 v[98:99], v231 offset:18432\n" \
"v_mfma_f32_16x16x32_bf16 a[20:23], v[0:3], v[52:55], a[20:23]\n" \
"buffer_load_dwordx4 v225, %[rc], s52 offen lds\n" \
"v_mfma_f32_16x16x32_bf16 a[24:27], v[0:3], v[56:59], a[24:27]\n" \
"ds_read_b64_tr_b16 v[100:101], v232 offset:16384\n" \
"v_mfma_f32_16x16x32_bf16 a[28:31], v[0:3], v[60:63], a[28:31]\n" \
"s_add_u32 s94, s72, 0\n" \
"v_mfma_f32_16x16x32_bf16 a[32:35], v[4:7], v[32:35], a[32:35]\n" \
"ds_read_b64_tr_b16 v[102:103], v232 offset:18432\n" \
"v_mfma_f32_16x16x32_bf16 a[36:39], v[4:7], v[36:39], a[36:39]\n" \
"s_add_u32 s52, s40, 7864320\n" \
"v_mfma_f32_16x16x32_bf16 a[40:43], v[4:7], v[40:43], a[40:43]\n" \
"ds_read_b64_tr_b16 v[104:105], v233 offset:16384\n" \
"v_mfma_f32_16x16x32_bf16 a[44:47], v[4:7], v[44:47], a[44:47]\n" \
"s_add_u32 m0, s38, 131072\n" \
"v_mfma_f32_16x16x32_bf16 a[48:51], v[4:7], v[48:51], a[48:51]\n" \
"ds_read_b64_tr_b16 v[106:107], v233 offset:18432\n" \
"v_mfma_f32_16x16x32_bf16 a[52:55], v[4:7], v[52:55], a[52:55]\n" \
"buffer_load_dwordx4 v227, %[rw], s52 offen lds\n" \
"v_mfma_f32_16x16x32_bf16 a[56:59], v[4:7], v[56:59], a[56:59]\n" \
"ds_read_b64_tr_b16 v[108:109], v234 offset:16384\n" \
"v_mfma_f32_16x16x32_bf16 a[60:63], v[4:7], v[60:63], a[60:63]\n" \
"buffer_load_ushort v128, v239, s[28:31], s94 offen offset:0\n" \
"v_mfma_f32_16x16x32_bf16 a[64:67], v[8:11], v[32:35], a[64:67]\n" \
"ds_read_b64_tr_b16 v[110:111], v234 offset:18432\n" \
"v_mfma_f32_16x16x32_bf16 a[68:71], v[8:11], v[36:39], a[68:71]\n" \
"s_add_u32 s52, s39, 83840\n" \
"v_mfma_f32_16x16x32_bf16 a[72:75], v[8:11], v[40:43], a[72:75]\n" \
"ds_read_b64_tr_b16 v[112:113], v235 offset:16384\n" \
"v_mfma_f32_16x16x32_bf16 a[76:79], v[8:11], v[44:47], a[76:79]\n" \
"s_add_u32 m0, s37, 5120\n" \
"v_mfma_f32_16x16x32_bf16 a[80:83], v[8:11], v[48:51], a[80:83]\n" \
"ds_read_b64_tr_b16 v[114:115], v235 offset:18432\n" \
"v_mfma_f32_16x16x32_bf16 a[84:87], v[8:11], v[52:55], a[84:87]\n" \
"buffer_load_dwordx4 v226, %[rc], s52 offen lds\n" \
"v_mfma_f32_16x16x32_bf16 a[88:91], v[8:11], v[56:59], a[88:91]\n" \
"ds_read_b64_tr_b16 v[116:117], v236 offset:16384\n" \
"v_mfma_f32_16x16x32_bf16 a[92:95], v[8:11], v[60:63], a[92:95]\n" \
"buffer_load_ushort v129, v239, s[28:31], s94 offen offset:896\n" \
"v_mfma_f32_16x16x32_bf16 a[96:99], v[12:15], v[32:35], a[96:99]\n" \
"ds_read_b64_tr_b16 v[118:119], v236 offset:18432\n" \
"v_mfma_f32_16x16x32_bf16 a[100:103], v[12:15], v[36:39], a[100:103]\n" \
"s_add_u32 s52, s40, 7880704\n" \
"v_mfma_f32_16x16x32_bf16 a[104:107], v[12:15], v[40:43], a[104:107]\n" \
"ds_read_b64_tr_b16 v[120:121], v237 offset:16384\n" \
"v_mfma_f32_16x16x32_bf16 a[108:111], v[12:15], v[44:47], a[108:111]\n" \
"s_add_u32 m0, s38, 132096\n" \
"v_mfma_f32_16x16x32_bf16 a[112:115], v[12:15], v[48:51], a[112:115]\n" \
"ds_read_b64_tr_b16 v[122:123], v237 offset:18432\n" \
"v_mfma_f32_16x16x32_bf16 a[116:119], v[12:15], v[52:55], a[116:119]\n" \
"buffer_load_dwordx4 v228, %[rw], s52 offen lds\n" \
"v_mfma_f32_16x16x32_bf16 a[120:123], v[12:15], v[56:59], a[120:123]\n" \
"ds_read_b64_tr_b16 v[124:125], v238 offset:16384\n" \
"v_mfma_f32_16x16x32_bf16 a[124:127], v[12:15], v[60:63], a[124:127]\n" \
"buffer_load_ushort v130, v239, s[28:31], s94 offen offset:1792\n" \
"v_mfma_f32_16x16x32_bf16 a[128:131], v[16:19], v[32:35], a[128:131]\n" \
"ds_read_b64_tr_b16 v[126:127], v238 offset:18432\n" \
"v_mfma_f32_16x16x32_bf16 a[132:135], v[16:19], v[36:39], a[132:135]\n" \
"s_add_u32 s52, s39, 100224\n" \
"v_mfma_f32_16x16x32_bf16 a[136:139], v[16:19], v[40:43], a[136:139]\n" \
"ds_read_b128 v[68:71], v230 offset:2048\n" \
"v_mfma_f32_16x16x32_bf16 a[140:143], v[16:19], v[44:47], a[140:143]\n" \
"s_add_u32 m0, s37, 6144\n" \
"v_mfma_f32_16x16x32_bf16 a[144:147], v[16:19], v[48:51], a[144:147]\n" \
"ds_read_b128 v[72:75], v230 offset:4096\n" \
"v_mfma_f32_16x16x32_bf16 a[148:151], v[16:19], v[52:55], a[148:151]\n" \
"buffer_load_dwordx4 v225, %[rc], s52 offen lds\n" \
"v_mfma_f32_16x16x32_bf16 a[152:155], v[16:19], v[56:59], a[152:155]\n" \
"ds_read_b128 v[76:79], v230 offset:6144\n" \
"v_mfma_f32_16x16x32_bf16 a[156:159], v[16:19], v[60:63], a[156:159]\n" \
"buffer_load_ushort v131, v239, s[28:31], s94 offen offset:2688\n" \
"v_mfma_f32_16x16x32_bf16 a[160:163], v[20:23], v[32:35], a[160:163]\n" \
"ds_read_b128 v[80:83], v230 offset:8192\n" \
"v_mfma_f32_16x16x32_bf16 a[164:167], v[20:23], v[36:39], a[164:167]\n" \
"s_add_u32 s52, s40, 7897088\n" \
"v_mfma_f32_16x16x32_bf16 a[168:171], v[20:23], v[40:43], a[168:171]\n" \
"ds_read_b128 v[84:87], v230 offset:10240\n" \
"v_mfma_f32_16x16x32_bf16 a[172:175], v[20:23], v[44:47], a[172:175]\n" \
"s_add_u32 m0, s38, 133120\n" \
"v_mfma_f32_16x16x32_bf16 a[176:179], v[20:23], v[48:51], a[176:179]\n" \
"ds_read_b128 v[88:91], v230 offset:12288\n" \
"v_mfma_f32_16x16x32_bf16 a[180:183], v[20:23], v[52:55], a[180:183]\n" \
"buffer_load_dwordx4 v227, %[rw], s52 offen lds\n" \
"v_mfma_f32_16x16x32_bf16 a[184:187], v[20:23], v[56:59], a[184:187]\n" \
"ds_read_b128 v[92:95], v230 offset:14336\n" \
"v_mfma_f32_16x16x32_bf16 a[188:191], v[20:23], v[60:63], a[188:191]\n" \
"s_add_u32 s94, s72, 14336\n" \
"v_mfma_f32_16x16x32_bf16 a[192:195], v[24:27], v[32:35], a[192:195]\n" \
"buffer_load_ushort v132, v239, s[28:31], s94 offen offset:0\n" \
"v_mfma_f32_16x16x32_bf16 a[196:199], v[24:27], v[36:39], a[196:199]\n" \
"s_add_u32 s52, s39, 116608\n" \
"buffer_load_ushort v133, v239, s[28:31], s94 offen offset:896\n" \
"v_mfma_f32_16x16x32_bf16 a[200:203], v[24:27], v[40:43], a[200:203]\n" \
"v_add_u32 v230, 0x8000, v230\n" \
"buffer_load_ushort v134, v239, s[28:31], s94 offen offset:1792\n" \
"v_mfma_f32_16x16x32_bf16 a[204:207], v[24:27], v[44:47], a[204:207]\n" \
"s_add_u32 m0, s37, 7168\n" \
"buffer_load_ushort v135, v239, s[28:31], s94 offen offset:2688\n" \
"v_mfma_f32_16x16x32_bf16 a[208:211], v[24:27], v[48:51], a[208:211]\n" \
"s_add_u32 s94, s73, 0\n" \
"v_mfma_f32_16x16x32_bf16 a[212:215], v[24:27], v[52:55], a[212:215]\n" \
"buffer_load_dwordx4 v226, %[rc], s52 offen lds\n" \
"v_mfma_f32_16x16x32_bf16 a[216:219], v[24:27], v[56:59], a[216:219]\n" \
"buffer_load_ushort v136, v239, s[28:31], s94 offen offset:0\n" \
"v_mfma_f32_16x16x32_bf16 a[220:223], v[24:27], v[60:63], a[220:223]\n" \
"buffer_load_ushort v137, v239, s[28:31], s94 offen offset:896\n" \
"v_mfma_f32_16x16x32_bf16 a[224:227], v[28:31], v[32:35], a[224:227]\n" \
"buffer_load_ushort v138, v239, s[28:31], s94 offen offset:1792\n" \
"v_mfma_f32_16x16x32_bf16 a[228:231], v[28:31], v[36:39], a[228:231]\n" \
"s_add_u32 s52, s40, 7913472\n" \
"buffer_load_ushort v139, v239, s[28:31], s94 offen offset:2688\n" \
"v_mfma_f32_16x16x32_bf16 a[232:235], v[28:31], v[40:43], a[232:235]\n" \
"s_add_u32 s94, s73, 14336\n" \
"v_mfma_f32_16x16x32_bf16 a[236:239], v[28:31], v[44:47], a[236:239]\n" \
"s_add_u32 m0, s38, 134144\n" \
"buffer_load_ushort v140, v239, s[28:31], s94 offen offset:0\n" \
"v_mfma_f32_16x16x32_bf16 a[240:243], v[28:31], v[48:51], a[240:243]\n" \
"buffer_load_ushort v141, v239, s[28:31], s94 offen offset:896\n" \
"v_mfma_f32_16x16x32_bf16 a[244:247], v[28:31], v[52:55], a[244:247]\n" \
"buffer_load_dwordx4 v228, %[rw], s52 offen lds\n" \
"v_mfma_f32_16x16x32_bf16 a[248:251], v[28:31], v[56:59], a[248:251]\n" \
"buffer_load_ushort v142, v239, s[28:31], s94 offen offset:1792\n" \
"v_mfma_f32_16x16x32_bf16 a[252:255], v[28:31], v[60:63], a[252:255]\n" \
"buffer_load_ushort v143, v239, s[28:31], s94 offen offset:2688\n" \
"s_waitcnt vmcnt(33) lgkmcnt(0)\n" \
"s_barrier\n" \
"v_mfma_f32_16x16x32_bf16 a[0:3], v[64:67], v[96:99], a[0:3]\n" \
"ds_read_b128 v[0:3], v229\n" \
"v_xor_b32 v231, 0x38000, v231\n" \
"v_mfma_f32_16x16x32_bf16 a[4:7], v[64:67], v[100:103], a[4:7]\n" \
"s_add_u32 s52, s40, 0\n" \
"v_mfma_f32_16x16x32_bf16 a[8:11], v[64:67], v[104:107], a[8:11]\n" \
"ds_read_b64_tr_b16 v[32:33], v231 offset:0\n" \
"v_mfma_f32_16x16x32_bf16 a[12:15], v[64:67], v[108:111], a[12:15]\n" \
"s_add_u32 m0, s38, 32768\n" \
"v_mfma_f32_16x16x32_bf16 a[16:19], v[64:67], v[112:115], a[16:19]\n" \
"ds_read_b64_tr_b16 v[34:35], v231 offset:2048\n" \
"v_xor_b32 v232, 0x38000, v232\n" \
"v_mfma_f32_16x16x32_bf16 a[20:23], v[64:67], v[116:119], a[20:23]\n" \
"buffer_load_dwordx4 v227, s[20:23], s52 offen lds\n" \
"v_mfma_f32_16x16x32_bf16 a[24:27], v[64:67], v[120:123], a[24:27]\n" \
"ds_read_b64_tr_b16 v[36:37], v232 offset:0\n" \
"v_mfma_f32_16x16x32_bf16 a[28:31], v[64:67], v[124:127], a[28:31]\n" \
"s_add_u32 s94, s74, 0\n" \
"v_mfma_f32_16x16x32_bf16 a[32:35], v[68:71], v[96:99], a[32:35]\n" \
"ds_read_b64_tr_b16 v[38:39], v232 offset:2048\n" \
"v_xor_b32 v233, 0x38000, v233\n" \
"v_mfma_f32_16x16x32_bf16 a[36:39], v[68:71], v[100:103], a[36:39]\n" \
"s_add_u32 s52, s40, 8126464\n" \
"v_mfma_f32_16x16x32_bf16 a[40:43], v[68:71], v[104:107], a[40:43]\n" \
"ds_read_b64_tr_b16 v[40:41], v233 offset:0\n" \
"v_mfma_f32_16x16x32_bf16 a[44:47], v[68:71], v[108:111], a[44:47]\n" \
"s_add_u32 m0, s38, 147456\n" \
"v_mfma_f32_16x16x32_bf16 a[48:51], v[68:71], v[112:115], a[48:51]\n" \
"ds_read_b64_tr_b16 v[42:43], v233 offset:2048\n" \
"v_xor_b32 v234, 0x38000, v234\n" \
"v_mfma_f32_16x16x32_bf16 a[52:55], v[68:71], v[116:119], a[52:55]\n" \
"buffer_load_dwordx4 v227, %[rw], s52 offen lds\n" \
"v_mfma_f32_16x16x32_bf16 a[56:59], v[68:71], v[120:123], a[56:59]\n" \
"ds_read_b64_tr_b16 v[44:45], v234 offset:0\n" \
"v_mfma_f32_16x16x32_bf16 a[60:63], v[68:71], v[124:127], a[60:63]\n" \
"buffer_load_ushort v144, v239, s[28:31], s94 offen offset:0\n" \
"v_mfma_f32_16x16x32_bf16 a[64:67], v[72:75], v[96:99], a[64:67]\n" \
"ds_read_b64_tr_b16 v[46:47], v234 offset:2048\n" \
"v_xor_b32 v235, 0x38000, v235\n" \
"v_mfma_f32_16x16x32_bf16 a[68:71], v[72:75], v[100:103], a[68:71]\n" \
"s_add_u32 s52, s40, 16384\n" \
"v_mfma_f32_16x16x32_bf16 a[72:75], v[72:75], v[104:107], a[72:75]\n" \
"ds_read_b64_tr_b16 v[48:49], v235 offset:0\n" \
"v_mfma_f32_16x16x32_bf16 a[76:79], v[72:75], v[108:111], a[76:79]\n" \
"s_add_u32 m0, s38, 33792\n" \
"v_mfma_f32_16x16x32_bf16 a[80:83], v[72:75], v[112:115], a[80:83]\n" \
"ds_read_b64_tr_b16 v[50:51], v235 offset:2048\n" \
"v_xor_b32 v236, 0x38000, v236\n" \
"v_mfma_f32_16x16x32_bf16 a[84:87], v[72:75], v[116:119], a[84:87]\n" \
"buffer_load_dwordx4 v228, s[20:23], s52 offen lds\n" \
"v_mfma_f32_16x16x32_bf16 a[88:91], v[72:75], v[120:123], a[88:91]\n" \
"ds_read_b64_tr_b16 v[52:53], v236 offset:0\n" \
"v_mfma_f32_16x16x32_bf16 a[92:95], v[72:75], v[124:127], a[92:95]\n" \
"buffer_load_ushort v145, v239, s[28:31], s94 offen offset:896\n" \
"v_mfma_f32_16x16x32_bf16 a[96:99], v[76:79], v[96:99], a[96:99]\n" \
"ds_read_b64_tr_b16 v[54:55], v236 offset:2048\n" \
"v_xor_b32 v237, 0x38000, v237\n" \
"v_mfma_f32_16x16x32_bf16 a[100:103], v[76:79], v[100:103], a[100:103]\n" \
"s_add_u32 s52, s40, 8142848\n" \
"v_mfma_f32_16x16x32_bf16 a[104:107], v[76:79], v[104:107], a[104:107]\n" \
"ds_read_b64_tr_b16 v[56:57], v237 offset:0\n" \
"v_mfma_f32_16x16x32_bf16 a[108:111], v[76:79], v[108:111], a[108:111]\n" \
"s_add_u32 m0, s38, 148480\n" \
"v_mfma_f32_16x16x32_bf16 a[112:115], v[76:79], v[112:115], a[112:115]\n" \
"ds_read_b64_tr_b16 v[58:59], v237 offset:2048\n" \
"v_xor_b32 v238, 0x38000, v238\n" \
"v_mfma_f32_16x16x32_bf16 a[116:119], v[76:79], v[116:119], a[116:119]\n" \
"buffer_load_dwordx4 v228, %[rw], s52 offen lds\n" \
"v_mfma_f32_16x16x32_bf16 a[120:123], v[76:79], v[120:123], a[120:123]\n" \
"ds_read_b64_tr_b16 v[60:61], v238 offset:0\n" \
"v_mfma_f32_16x16x32_bf16 a[124:127], v[76:79], v[124:127], a[124:127]\n" \
"buffer_load_ushort v146, v239, s[28:31], s94 offen offset:1792\n" \
"v_mfma_f32_16x16x32_bf16 a[128:131], v[80:83], v[96:99], a[128:131]\n" \
"ds_read_b64_tr_b16 v[62:63], v238 offset:2048\n" \
"v_mfma_f32_16x16x32_bf16 a[132:135], v[80:83], v[100:103], a[132:135]\n" \
"s_add_u32 s52, s40, 32768\n" \
"v_mfma_f32_16x16x32_bf16 a[136:139], v[80:83], v[104:107], a[136:139]\n" \
"ds_read_b128 v[4:7], v229 offset:2048\n" \
"v_mfma_f32_16x16x32_bf16 a[140:143], v[80:83], v[108:111], a[140:143]\n" \
"s_add_u32 m0, s38, 34816\n" \
"v_mfma_f32_16x16x32_bf16 a[144:147], v[80:83], v[112:115], a[144:147]\n" \
"ds_read_b128 v[8:11], v229 offset:4096\n" \
"v_mfma_f32_16x16x32_bf16 a[148:151], v[80:83], v[116:119], a[148:151]\n" \
"buffer_load_dwordx4 v227, s[20:23], s52 offen lds\n" \
"v_mfma_f32_16x16x32_bf16 a[152:155], v[80:83], v[120:123], a[152:155]\n" \
"ds_read_b128 v[12:15], v229 offset:6144\n" \
"v_mfma_f32_16x16x32_bf16 a[156:159], v[80:83], v[124:127], a[156:159]\n" \
"buffer_load_ushort v147, v239, s[28:31], s94 offen offset:2688\n" \
"v_mfma_f32_16x16x32_bf16 a[160:163], v[84:87], v[96:99], a[160:163]\n" \
"ds_read_b128 v[16:19], v229 offset:8192\n" \
"v_mfma_f32_16x16x32_bf16 a[164:167], v[84:87], v[100:103], a[164:167]\n" \
"s_add_u32 s52, s40, 8159232\n" \
"v_mfma_f32_16x16x32_bf16 a[168:171], v[84:87], v[104:107], a[168:171]\n" \
"ds_read_b128 v[20:23], v229 offset:10240\n" \
"v_mfma_f32_16x16x32_bf16 a[172:175], v[84:87], v[108:111], a[172:175]\n" \
"s_add_u32 m0, s38, 149504\n" \
"v_mfma_f32_16x16x32_bf16 a[176:179], v[84:87], v[112:115], a[176:179]\n" \
"ds_read_b128 v[24:27], v229 offset:12288\n" \
"v_mfma_f32_16x16x32_bf16 a[180:183], v[84:87], v[116:119], a[180:183]\n" \
"buffer_load_dwordx4 v227, %[rw], s52 offen lds\n" \
"v_mfma_f32_16x16x32_bf16 a[184:187], v[84:87], v[120:123], a[184:187]\n" \
"ds_read_b128 v[28:31], v229 offset:14336\n" \
"v_mfma_f32_16x16x32_bf16 a[188:191], v[84:87], v[124:127], a[188:191]\n" \
"s_add_u32 s94, s74, 14336\n" \
"v_mfma_f32_16x16x32_bf16 a[192:195], v[88:91], v[96:99], a[192:195]\n" \
"buffer_load_ushort v148, v239, s[28:31], s94 offen offset:0\n" \
"v_mfma_f32_16x16x32_bf16 a[196:199], v[88:91], v[100:103], a[196:199]\n" \
"s_add_u32 s52, s40, 49152\n" \
"buffer_load_ushort v149, v239, s[28:31], s94 offen offset:896\n" \
"v_mfma_f32_16x16x32_bf16 a[200:203], v[88:91], v[104:107], a[200:203]\n" \
"v_add_u32 v229, 0xffff0000, v229\n" \
"buffer_load_ushort v150, v239, s[28:31], s94 offen offset:1792\n" \
"v_mfma_f32_16x16x32_bf16 a[204:207], v[88:91], v[108:111], a[204:207]\n" \
"s_add_u32 m0, s38, 35840\n" \
"buffer_load_ushort v151, v239, s[28:31], s94 offen offset:2688\n" \
"v_mfma_f32_16x16x32_bf16 a[208:211], v[88:91], v[112:115], a[208:211]\n" \
"s_add_u32 s94, s75, 0\n" \
"v_mfma_f32_16x16x32_bf16 a[212:215], v[88:91], v[116:119], a[212:215]\n" \
"buffer_load_dwordx4 v228, s[20:23], s52 offen lds\n" \
"v_mfma_f32_16x16x32_bf16 a[216:219], v[88:91], v[120:123], a[216:219]\n" \
"buffer_load_ushort v152, v239, s[28:31], s94 offen offset:0\n" \
"v_mfma_f32_16x16x32_bf16 a[220:223], v[88:91], v[124:127], a[220:223]\n" \
"buffer_load_ushort v153, v239, s[28:31], s94 offen offset:896\n" \
"v_mfma_f32_16x16x32_bf16 a[224:227], v[92:95], v[96:99], a[224:227]\n" \
"buffer_load_ushort v154, v239, s[28:31], s94 offen offset:1792\n" \
"v_mfma_f32_16x16x32_bf16 a[228:231], v[92:95], v[100:103], a[228:231]\n" \
"s_add_u32 s52, s40, 8175616\n" \
"buffer_load_ushort v155, v239, s[28:31], s94 offen offset:2688\n" \
"v_mfma_f32_16x16x32_bf16 a[232:235], v[92:95], v[104:107], a[232:235]\n" \
"s_add_u32 s94, s75, 14336\n" \
"v_mfma_f32_16x16x32_bf16 a[236:239], v[92:95], v[108:111], a[236:239]\n" \
"s_add_u32 m0, s38, 150528\n" \
"buffer_load_ushort v156, v239, s[28:31], s94 offen offset:0\n" \
"v_mfma_f32_16x16x32_bf16 a[240:243], v[92:95], v[112:115], a[240:243]\n" \
"buffer_load_ushort v157, v239, s[28:31], s94 offen offset:896\n" \
"v_mfma_f32_16x16x32_bf16 a[244:247], v[92:95], v[116:119], a[244:247]\n" \
"buffer_load_dwordx4 v228, %[rw], s52 offen lds\n" \
"v_mfma_f32_16x16x32_bf16 a[248:251], v[92:95], v[120:123], a[248:251]\n" \
"buffer_load_ushort v158, v239, s[28:31], s94 offen offset:1792\n" \
"v_mfma_f32_16x16x32_bf16 a[252:255], v[92:95], v[124:127], a[252:255]\n" \
"buffer_load_ushort v159, v239, s[28:31], s94 offen offset:2688\n" \
"s_waitcnt vmcnt(48)\n" \
"v_cmp_ne_u32 s[96:97], %[tag], v222\n" \
"s_nop 1\n" \
"s_cmp_eq_u64 s[96:97], 0\n" \
"s_cbranch_scc1 LxokV%=\n" \
"s_mov_b32 s81, 0\n" \
"LxwV_p%=:\n" \
"buffer_load_dword v208, v240, %[rs], s57 offen sc1\n" \
"s_waitcnt vmcnt(0)\n" \
"v_cmp_ne_u32 s[96:97], %[tag], v208\n" \
"s_nop 1\n" \
"s_cmp_eq_u64 s[96:97], 0\n" \
"s_cbranch_scc1 LxwV_d%=\n" \
"s_sleep 4\n" \
"s_add_u32 s81, s81, 1\n" \
"s_cmp_lt_u32 s81, 0x20000\n" \
"s_cbranch_scc1 LxwV_p%=\n" \
"LxwV_d%=:\n" \
"LxokV%=:\n" \
"s_waitcnt vmcnt(48) lgkmcnt(0)\n" \
"s_barrier\n" \
"v_mfma_f32_16x16x32_bf16 a[0:3], v[0:3], v[32:35], a[0:3]\n" \
"ds_read_b128 v[64:67], v230\n" \
"v_mfma_f32_16x16x32_bf16 a[4:7], v[0:3], v[36:39], a[4:7]\n" \
"s_add_u32 s52, s40, 262144\n" \
"v_mfma_f32_16x16x32_bf16 a[8:11], v[0:3], v[40:43], a[8:11]\n" \
"ds_read_b64_tr_b16 v[96:97], v231 offset:16384\n" \
"v_mfma_f32_16x16x32_bf16 a[12:15], v[0:3], v[44:47], a[12:15]\n" \
"s_add_u32 m0, s38, 49152\n" \
"v_mfma_f32_16x16x32_bf16 a[16:19], v[0:3], v[48:51], a[16:19]\n" \
"ds_read_b64_tr_b16 v[98:99], v231 offset:18432\n" \
"v_mfma_f32_16x16x32_bf16 a[20:23], v[0:3], v[52:55], a[20:23]\n" \
"buffer_load_dwordx4 v227, s[20:23], s52 offen lds\n" \
"v_mfma_f32_16x16x32_bf16 a[24:27], v[0:3], v[56:59], a[24:27]\n" \
"ds_read_b64_tr_b16 v[100:101], v232 offset:16384\n" \
"v_mfma_f32_16x16x32_bf16 a[28:31], v[0:3], v[60:63], a[28:31]\n" \
"v_mfma_f32_16x16x32_bf16 a[32:35], v[4:7], v[32:35], a[32:35]\n" \
"ds_read_b64_tr_b16 v[102:103], v232 offset:18432\n" \
"v_mfma_f32_16x16x32_bf16 a[36:39], v[4:7], v[36:39], a[36:39]\n" \
"s_add_u32 s52, s40, 1048576\n" \
"v_mfma_f32_16x16x32_bf16 a[40:43], v[4:7], v[40:43], a[40:43]\n" \
"ds_read_b64_tr_b16 v[104:105], v233 offset:16384\n" \
"v_mfma_f32_16x16x32_bf16 a[44:47], v[4:7], v[44:47], a[44:47]\n" \
"s_add_u32 m0, s38, 98304\n" \
"v_mfma_f32_16x16x32_bf16 a[48:51], v[4:7], v[48:51], a[48:51]\n" \
"ds_read_b64_tr_b16 v[106:107], v233 offset:18432\n" \
"v_mfma_f32_16x16x32_bf16 a[52:55], v[4:7], v[52:55], a[52:55]\n" \
"buffer_load_dwordx4 v227, s[20:23], s52 offen lds\n" \
"v_mfma_f32_16x16x32_bf16 a[56:59], v[4:7], v[56:59], a[56:59]\n" \
"ds_read_b64_tr_b16 v[108:109], v234 offset:16384\n" \
"v_mfma_f32_16x16x32_bf16 a[60:63], v[4:7], v[60:63], a[60:63]\n" \
"s_add_u32 s80, s58, 0\n" \
"v_mfma_f32_16x16x32_bf16 a[64:67], v[8:11], v[32:35], a[64:67]\n" \
"ds_read_b64_tr_b16 v[110:111], v234 offset:18432\n" \
"v_mfma_f32_16x16x32_bf16 a[68:71], v[8:11], v[36:39], a[68:71]\n" \
"s_add_u32 s52, s40, 278528\n" \
"buffer_load_dwordx4 v[160:163], v224, %[rs], s80 offen sc1\n" \
"v_mfma_f32_16x16x32_bf16 a[72:75], v[8:11], v[40:43], a[72:75]\n" \
"ds_read_b64_tr_b16 v[112:113], v235 offset:16384\n" \
"v_mfma_f32_16x16x32_bf16 a[76:79], v[8:11], v[44:47], a[76:79]\n" \
"s_add_u32 m0, s38, 50176\n" \
"v_mfma_f32_16x16x32_bf16 a[80:83], v[8:11], v[48:51], a[80:83]\n" \
"ds_read_b64_tr_b16 v[114:115], v235 offset:18432\n" \
"v_mfma_f32_16x16x32_bf16 a[84:87], v[8:11], v[52:55], a[84:87]\n" \
"buffer_load_dwordx4 v228, s[20:23], s52 offen lds\n" \
"v_mfma_f32_16x16x32_bf16 a[88:91], v[8:11], v[56:59], a[88:91]\n" \
"ds_read_b64_tr_b16 v[116:117], v236 offset:16384\n" \
"v_mfma_f32_16x16x32_bf16 a[92:95], v[8:11], v[60:63], a[92:95]\n" \
"v_mfma_f32_16x16x32_bf16 a[96:99], v[12:15], v[32:35], a[96:99]\n" \
"ds_read_b64_tr_b16 v[118:119], v236 offset:18432\n" \
"v_mfma_f32_16x16x32_bf16 a[100:103], v[12:15], v[36:39], a[100:103]\n" \
"s_add_u32 s52, s40, 1064960\n" \
"v_mfma_f32_16x16x32_bf16 a[104:107], v[12:15], v[40:43], a[104:107]\n" \
"ds_read_b64_tr_b16 v[120:121], v237 offset:16384\n" \
"v_mfma_f32_16x16x32_bf16 a[108:111], v[12:15], v[44:47], a[108:111]\n" \
"s_add_u32 m0, s38, 99328\n" \
"v_mfma_f32_16x16x32_bf16 a[112:115], v[12:15], v[48:51], a[112:115]\n" \
"ds_read_b64_tr_b16 v[122:123], v237 offset:18432\n" \
"v_mfma_f32_16x16x32_bf16 a[116:119], v[12:15], v[52:55], a[116:119]\n" \
"buffer_load_dwordx4 v228, s[20:23], s52 offen lds\n" \
"v_mfma_f32_16x16x32_bf16 a[120:123], v[12:15], v[56:59], a[120:123]\n" \
"ds_read_b64_tr_b16 v[124:125], v238 offset:16384\n" \
"v_mfma_f32_16x16x32_bf16 a[124:127], v[12:15], v[60:63], a[124:127]\n" \
"s_add_u32 s80, s71, 0\n" \
"v_mfma_f32_16x16x32_bf16 a[128:131], v[16:19], v[32:35], a[128:131]\n" \
"ds_read_b64_tr_b16 v[126:127], v238 offset:18432\n" \
"v_mfma_f32_16x16x32_bf16 a[132:135], v[16:19], v[36:39], a[132:135]\n" \
"s_add_u32 s52, s40, 294912\n" \
"buffer_load_dwordx4 v[164:167], v224, %[rs], s80 offen sc1\n" \
"v_mfma_f32_16x16x32_bf16 a[136:139], v[16:19], v[40:43], a[136:139]\n" \
"ds_read_b128 v[68:71], v230 offset:2048\n" \
"v_mfma_f32_16x16x32_bf16 a[140:143], v[16:19], v[44:47], a[140:143]\n" \
"s_add_u32 m0, s38, 51200\n" \
"v_mfma_f32_16x16x32_bf16 a[144:147], v[16:19], v[48:51], a[144:147]\n" \
"ds_read_b128 v[72:75], v230 offset:4096\n" \
"v_mfma_f32_16x16x32_bf16 a[148:151], v[16:19], v[52:55], a[148:151]\n" \
"buffer_load_dwordx4 v227, s[20:23], s52 offen lds\n" \
"v_mfma_f32_16x16x32_bf16 a[152:155], v[16:19], v[56:59], a[152:155]\n" \
"ds_read_b128 v[76:79], v230 offset:6144\n" \
"v_mfma_f32_16x16x32_bf16 a[156:159], v[16:19], v[60:63], a[156:159]\n" \
"v_mfma_f32_16x16x32_bf16 a[160:163], v[20:23], v[32:35], a[160:163]\n" \
"ds_read_b128 v[80:83], v230 offset:8192\n" \
"v_mfma_f32_16x16x32_bf16 a[164:167], v[20:23], v[36:39], a[164:167]\n" \
"s_add_u32 s52, s40, 1081344\n" \
"v_mfma_f32_16x16x32_bf16 a[168:171], v[20:23], v[40:43], a[168:171]\n" \
"ds_read_b128 v[84:87], v230 offset:10240\n" \
"v_mfma_f32_16x16x32_bf16 a[172:175], v[20:23], v[44:47], a[172:175]\n" \
"s_add_u32 m0, s38, 100352\n" \
"v_mfma_f32_16x16x32_bf16 a[176:179], v[20:23], v[48:51], a[176:179]\n" \
"ds_read_b128 v[88:91], v230 offset:12288\n" \
"v_mfma_f32_16x16x32_bf16 a[180:183], v[20:23], v[52:55], a[180:183]\n" \
"buffer_load_dwordx4 v227, s[20:23], s52 offen lds\n" \
"v_mfma_f32_16x16x32_bf16 a[184:187], v[20:23], v[56:59], a[184:187]\n" \
"ds_read_b128 v[92:95], v230 offset:14336\n" \
"v_mfma_f32_16x16x32_bf16 a[188:191], v[20:23], v[60:63], a[188:191]\n" \
"s_add_u32 s80, s58, 1024\n" \
"v_mfma_f32_16x16x32_bf16 a[192:195], v[24:27], v[32:35], a[192:195]\n" \
"v_mfma_f32_16x16x32_bf16 a[196:199], v[24:27], v[36:39], a[196:199]\n" \
"s_add_u32 s52, s40, 311296\n" \
"buffer_load_dwordx4 v[168:171], v224, %[rs], s80 offen sc1\n" \
"v_mfma_f32_16x16x32_bf16 a[200:203], v[24:27], v[40:43], a[200:203]\n" \
"v_add_u32 v230, 0xffff0000, v230\n" \
"v_mfma_f32_16x16x32_bf16 a[204:207], v[24:27], v[44:47], a[204:207]\n" \
"s_add_u32 m0, s38, 52224\n" \
"v_mfma_f32_16x16x32_bf16 a[208:211], v[24:27], v[48:51], a[208:211]\n" \
"v_mfma_f32_16x16x32_bf16 a[212:215], v[24:27], v[52:55], a[212:215]\n" \
"buffer_load_dwordx4 v228, s[20:23], s52 offen lds\n" \
"v_mfma_f32_16x16x32_bf16 a[216:219], v[24:27], v[56:59], a[216:219]\n" \
"v_mfma_f32_16x16x32_bf16 a[220:223], v[24:27], v[60:63], a[220:223]\n" \
"v_mfma_f32_16x16x32_bf16 a[224:227], v[28:31], v[32:35], a[224:227]\n" \
"v_mfma_f32_16x16x32_bf16 a[228:231], v[28:31], v[36:39], a[228:231]\n" \
"s_add_u32 s52, s40, 1097728\n" \
"v_mfma_f32_16x16x32_bf16 a[232:235], v[28:31], v[40:43], a[232:235]\n" \
"v_mfma_f32_16x16x32_bf16 a[236:239], v[28:31], v[44:47], a[236:239]\n" \
"s_add_u32 m0, s38, 101376\n" \
"v_mfma_f32_16x16x32_bf16 a[240:243], v[28:31], v[48:51], a[240:243]\n" \
"v_mfma_f32_16x16x32_bf16 a[244:247], v[28:31], v[52:55], a[244:247]\n" \
"buffer_load_dwordx4 v228, s[20:23], s52 offen lds\n" \
"v_mfma_f32_16x16x32_bf16 a[248:251], v[28:31], v[56:59], a[248:251]\n" \
"v_mfma_f32_16x16x32_bf16 a[252:255], v[28:31], v[60:63], a[252:255]\n" \
"s_waitcnt vmcnt(37) lgkmcnt(0)\n" \
"s_barrier\n" \
"v_mfma_f32_16x16x32_bf16 a[0:3], v[64:67], v[96:99], a[0:3]\n" \
"ds_read_b128 v[0:3], v229\n" \
"v_xor_b32 v231, 0x38000, v231\n" \
"v_mfma_f32_16x16x32_bf16 a[4:7], v[64:67], v[100:103], a[4:7]\n" \
"s_add_u32 s52, s40, 524288\n" \
"v_mfma_f32_16x16x32_bf16 a[8:11], v[64:67], v[104:107], a[8:11]\n" \
"ds_read_b64_tr_b16 v[32:33], v231 offset:0\n" \
"v_mfma_f32_16x16x32_bf16 a[12:15], v[64:67], v[108:111], a[12:15]\n" \
"s_add_u32 m0, s38, 65536\n" \
"v_mfma_f32_16x16x32_bf16 a[16:19], v[64:67], v[112:115], a[16:19]\n" \
"ds_read_b64_tr_b16 v[34:35], v231 offset:2048\n" \
"v_xor_b32 v232, 0x38000, v232\n" \
"v_mfma_f32_16x16x32_bf16 a[20:23], v[64:67], v[116:119], a[20:23]\n" \
"buffer_load_dwordx4 v227, s[20:23], s52 offen lds\n" \
"v_mfma_f32_16x16x32_bf16 a[24:27], v[64:67], v[120:123], a[24:27]\n" \
"ds_read_b64_tr_b16 v[36:37], v232 offset:0\n" \
"v_mfma_f32_16x16x32_bf16 a[28:31], v[64:67], v[124:127], a[28:31]\n" \
"v_mfma_f32_16x16x32_bf16 a[32:35], v[68:71], v[96:99], a[32:35]\n" \
"ds_read_b64_tr_b16 v[38:39], v232 offset:2048\n" \
"v_xor_b32 v233, 0x38000, v233\n" \
"v_mfma_f32_16x16x32_bf16 a[36:39], v[68:71], v[100:103], a[36:39]\n" \
"s_add_u32 s52, s40, 1310720\n" \
"v_mfma_f32_16x16x32_bf16 a[40:43], v[68:71], v[104:107], a[40:43]\n" \
"ds_read_b64_tr_b16 v[40:41], v233 offset:0\n" \
"v_mfma_f32_16x16x32_bf16 a[44:47], v[68:71], v[108:111], a[44:47]\n" \
"s_add_u32 m0, s38, 114688\n" \
"v_mfma_f32_16x16x32_bf16 a[48:51], v[68:71], v[112:115], a[48:51]\n" \
"ds_read_b64_tr_b16 v[42:43], v233 offset:2048\n" \
"v_xor_b32 v234, 0x38000, v234\n" \
"v_mfma_f32_16x16x32_bf16 a[52:55], v[68:71], v[116:119], a[52:55]\n" \
"buffer_load_dwordx4 v227, s[20:23], s52 offen lds\n" \
"v_mfma_f32_16x16x32_bf16 a[56:59], v[68:71], v[120:123], a[56:59]\n" \
"ds_read_b64_tr_b16 v[44:45], v234 offset:0\n" \
"v_mfma_f32_16x16x32_bf16 a[60:63], v[68:71], v[124:127], a[60:63]\n" \
"s_add_u32 s80, s71, 1024\n" \
"v_mfma_f32_16x16x32_bf16 a[64:67], v[72:75], v[96:99], a[64:67]\n" \
"ds_read_b64_tr_b16 v[46:47], v234 offset:2048\n" \
"v_xor_b32 v235, 0x38000, v235\n" \
"v_mfma_f32_16x16x32_bf16 a[68:71], v[72:75], v[100:103], a[68:71]\n" \
"s_add_u32 s52, s40, 540672\n" \
"buffer_load_dwordx4 v[172:175], v224, %[rs], s80 offen sc1\n" \
"v_mfma_f32_16x16x32_bf16 a[72:75], v[72:75], v[104:107], a[72:75]\n" \
"ds_read_b64_tr_b16 v[48:49], v235 offset:0\n" \
"v_mfma_f32_16x16x32_bf16 a[76:79], v[72:75], v[108:111], a[76:79]\n" \
"s_add_u32 m0, s38, 66560\n" \
"v_mfma_f32_16x16x32_bf16 a[80:83], v[72:75], v[112:115], a[80:83]\n" \
"ds_read_b64_tr_b16 v[50:51], v235 offset:2048\n" \
"v_xor_b32 v236, 0x38000, v236\n" \
"v_mfma_f32_16x16x32_bf16 a[84:87], v[72:75], v[116:119], a[84:87]\n" \
"buffer_load_dwordx4 v228, s[20:23], s52 offen lds\n" \
"v_mfma_f32_16x16x32_bf16 a[88:91], v[72:75], v[120:123], a[88:91]\n" \
"ds_read_b64_tr_b16 v[52:53], v236 offset:0\n" \
"v_mfma_f32_16x16x32_bf16 a[92:95], v[72:75], v[124:127], a[92:95]\n" \
"v_mfma_f32_16x16x32_bf16 a[96:99], v[76:79], v[96:99], a[96:99]\n" \
"ds_read_b64_tr_b16 v[54:55], v236 offset:2048\n" \
"v_xor_b32 v237, 0x38000, v237\n" \
"v_mfma_f32_16x16x32_bf16 a[100:103], v[76:79], v[100:103], a[100:103]\n" \
"s_add_u32 s52, s40, 1327104\n" \
"v_mfma_f32_16x16x32_bf16 a[104:107], v[76:79], v[104:107], a[104:107]\n" \
"ds_read_b64_tr_b16 v[56:57], v237 offset:0\n" \
"v_mfma_f32_16x16x32_bf16 a[108:111], v[76:79], v[108:111], a[108:111]\n" \
"s_add_u32 m0, s38, 115712\n" \
"v_mfma_f32_16x16x32_bf16 a[112:115], v[76:79], v[112:115], a[112:115]\n" \
"ds_read_b64_tr_b16 v[58:59], v237 offset:2048\n" \
"v_xor_b32 v238, 0x38000, v238\n" \
"v_mfma_f32_16x16x32_bf16 a[116:119], v[76:79], v[116:119], a[116:119]\n" \
"buffer_load_dwordx4 v228, s[20:23], s52 offen lds\n" \
"v_mfma_f32_16x16x32_bf16 a[120:123], v[76:79], v[120:123], a[120:123]\n" \
"ds_read_b64_tr_b16 v[60:61], v238 offset:0\n" \
"v_mfma_f32_16x16x32_bf16 a[124:127], v[76:79], v[124:127], a[124:127]\n" \
"s_add_u32 s80, s58, 2048\n" \
"v_mfma_f32_16x16x32_bf16 a[128:131], v[80:83], v[96:99], a[128:131]\n" \
"ds_read_b64_tr_b16 v[62:63], v238 offset:2048\n" \
"v_mfma_f32_16x16x32_bf16 a[132:135], v[80:83], v[100:103], a[132:135]\n" \
"s_add_u32 s52, s40, 557056\n" \
"buffer_load_dwordx4 v[176:179], v224, %[rs], s80 offen sc1\n" \
"v_mfma_f32_16x16x32_bf16 a[136:139], v[80:83], v[104:107], a[136:139]\n" \
"ds_read_b128 v[4:7], v229 offset:2048\n" \
"v_mfma_f32_16x16x32_bf16 a[140:143], v[80:83], v[108:111], a[140:143]\n" \
"s_add_u32 m0, s38, 67584\n" \
"v_mfma_f32_16x16x32_bf16 a[144:147], v[80:83], v[112:115], a[144:147]\n" \
"ds_read_b128 v[8:11], v229 offset:4096\n" \
"v_mfma_f32_16x16x32_bf16 a[148:151], v[80:83], v[116:119], a[148:151]\n" \
"buffer_load_dwordx4 v227, s[20:23], s52 offen lds\n" \
"v_mfma_f32_16x16x32_bf16 a[152:155], v[80:83], v[120:123], a[152:155]\n" \
"ds_read_b128 v[12:15], v229 offset:6144\n" \
"v_mfma_f32_16x16x32_bf16 a[156:159], v[80:83], v[124:127], a[156:159]\n" \
"v_mfma_f32_16x16x32_bf16 a[160:163], v[84:87], v[96:99], a[160:163]\n" \
"ds_read_b128 v[16:19], v229 offset:8192\n" \
"v_mfma_f32_16x16x32_bf16 a[164:167], v[84:87], v[100:103], a[164:167]\n" \
"s_add_u32 s52, s40, 1343488\n" \
"v_mfma_f32_16x16x32_bf16 a[168:171], v[84:87], v[104:107], a[168:171]\n" \
"ds_read_b128 v[20:23], v229 offset:10240\n" \
"v_mfma_f32_16x16x32_bf16 a[172:175], v[84:87], v[108:111], a[172:175]\n" \
"s_add_u32 m0, s38, 116736\n" \
"v_mfma_f32_16x16x32_bf16 a[176:179], v[84:87], v[112:115], a[176:179]\n" \
"ds_read_b128 v[24:27], v229 offset:12288\n" \
"v_mfma_f32_16x16x32_bf16 a[180:183], v[84:87], v[116:119], a[180:183]\n" \
"buffer_load_dwordx4 v227, s[20:23], s52 offen lds\n" \
"v_mfma_f32_16x16x32_bf16 a[184:187], v[84:87], v[120:123], a[184:187]\n" \
"ds_read_b128 v[28:31], v229 offset:14336\n" \
"v_mfma_f32_16x16x32_bf16 a[188:191], v[84:87], v[124:127], a[188:191]\n" \
"s_add_u32 s80, s71, 2048\n" \
"v_mfma_f32_16x16x32_bf16 a[192:195], v[88:91], v[96:99], a[192:195]\n" \
"v_mfma_f32_16x16x32_bf16 a[196:199], v[88:91], v[100:103], a[196:199]\n" \
"s_add_u32 s52, s40, 573440\n" \
"buffer_load_dwordx4 v[180:183], v224, %[rs], s80 offen sc1\n" \
"v_mfma_f32_16x16x32_bf16 a[200:203], v[88:91], v[104:107], a[200:203]\n" \
"v_mfma_f32_16x16x32_bf16 a[204:207], v[88:91], v[108:111], a[204:207]\n" \
"s_add_u32 m0, s38, 68608\n" \
"v_mfma_f32_16x16x32_bf16 a[208:211], v[88:91], v[112:115], a[208:211]\n" \
"v_mfma_f32_16x16x32_bf16 a[212:215], v[88:91], v[116:119], a[212:215]\n" \
"buffer_load_dwordx4 v228, s[20:23], s52 offen lds\n" \
"v_mfma_f32_16x16x32_bf16 a[216:219], v[88:91], v[120:123], a[216:219]\n" \
"v_mfma_f32_16x16x32_bf16 a[220:223], v[88:91], v[124:127], a[220:223]\n" \
"v_mfma_f32_16x16x32_bf16 a[224:227], v[92:95], v[96:99], a[224:227]\n" \
"v_mfma_f32_16x16x32_bf16 a[228:231], v[92:95], v[100:103], a[228:231]\n" \
"s_add_u32 s52, s40, 1359872\n" \
"v_mfma_f32_16x16x32_bf16 a[232:235], v[92:95], v[104:107], a[232:235]\n" \
"v_mfma_f32_16x16x32_bf16 a[236:239], v[92:95], v[108:111], a[236:239]\n" \
"s_add_u32 m0, s38, 117760\n" \
"v_mfma_f32_16x16x32_bf16 a[240:243], v[92:95], v[112:115], a[240:243]\n" \
"v_mfma_f32_16x16x32_bf16 a[244:247], v[92:95], v[116:119], a[244:247]\n" \
"buffer_load_dwordx4 v228, s[20:23], s52 offen lds\n" \
"v_mfma_f32_16x16x32_bf16 a[248:251], v[92:95], v[120:123], a[248:251]\n" \
"v_mfma_f32_16x16x32_bf16 a[252:255], v[92:95], v[124:127], a[252:255]\n" \
"s_waitcnt vmcnt(22) lgkmcnt(0)\n" \
"s_barrier\n" \
"v_mfma_f32_16x16x32_bf16 a[0:3], v[0:3], v[32:35], a[0:3]\n" \
"ds_read_b128 v[64:67], v230\n" \
"v_mfma_f32_16x16x32_bf16 a[4:7], v[0:3], v[36:39], a[4:7]\n" \
"s_add_u32 s52, s40, 786432\n" \
"v_mfma_f32_16x16x32_bf16 a[8:11], v[0:3], v[40:43], a[8:11]\n" \
"ds_read_b64_tr_b16 v[96:97], v231 offset:16384\n" \
"v_mfma_f32_16x16x32_bf16 a[12:15], v[0:3], v[44:47], a[12:15]\n" \
"s_add_u32 m0, s38, 81920\n" \
"v_mfma_f32_16x16x32_bf16 a[16:19], v[0:3], v[48:51], a[16:19]\n" \
"ds_read_b64_tr_b16 v[98:99], v231 offset:18432\n" \
"v_mfma_f32_16x16x32_bf16 a[20:23], v[0:3], v[52:55], a[20:23]\n" \
"buffer_load_dwordx4 v227, s[20:23], s52 offen lds\n" \
"v_lshl_or_b32 v210, v133, 16, v132\n" \
"v_lshl_or_b32 v211, v135, 16, v134\n" \
"v_mfma_f32_16x16x32_bf16 a[24:27], v[0:3], v[56:59], a[24:27]\n" \
"ds_read_b64_tr_b16 v[100:101], v232 offset:16384\n" \
"v_mfma_f32_16x16x32_bf16 a[28:31], v[0:3], v[60:63], a[28:31]\n" \
"v_lshl_or_b32 v208, v129, 16, v128\n" \
"v_lshl_or_b32 v209, v131, 16, v130\n" \
"v_mfma_f32_16x16x32_bf16 a[32:35], v[4:7], v[32:35], a[32:35]\n" \
"ds_read_b64_tr_b16 v[102:103], v232 offset:18432\n" \
"v_mfma_f32_16x16x32_bf16 a[36:39], v[4:7], v[36:39], a[36:39]\n" \
"s_add_u32 s52, s40, 1572864\n" \
"buffer_store_dwordx4 v[208:211], v224, %[rs], s76 offen sc1\n" \
"v_mfma_f32_16x16x32_bf16 a[40:43], v[4:7], v[40:43], a[40:43]\n" \
"ds_read_b64_tr_b16 v[104:105], v233 offset:16384\n" \
"v_mfma_f32_16x16x32_bf16 a[44:47], v[4:7], v[44:47], a[44:47]\n" \
"s_add_u32 m0, s38, 131072\n" \
"v_mfma_f32_16x16x32_bf16 a[48:51], v[4:7], v[48:51], a[48:51]\n" \
"ds_read_b64_tr_b16 v[106:107], v233 offset:18432\n" \
"v_mfma_f32_16x16x32_bf16 a[52:55], v[4:7], v[52:55], a[52:55]\n" \
"buffer_load_dwordx4 v227, s[20:23], s52 offen lds\n" \
"v_mfma_f32_16x16x32_bf16 a[56:59], v[4:7], v[56:59], a[56:59]\n" \
"ds_read_b64_tr_b16 v[108:109], v234 offset:16384\n" \
"v_mfma_f32_16x16x32_bf16 a[60:63], v[4:7], v[60:63], a[60:63]\n" \
"s_add_u32 s80, s58, 3072\n" \
"v_mfma_f32_16x16x32_bf16 a[64:67], v[8:11], v[32:35], a[64:67]\n" \
"ds_read_b64_tr_b16 v[110:111], v234 offset:18432\n" \
"v_mfma_f32_16x16x32_bf16 a[68:71], v[8:11], v[36:39], a[68:71]\n" \
"s_add_u32 s52, s40, 802816\n" \
"buffer_load_dwordx4 v[184:187], v224, %[rs], s80 offen sc1\n" \
"v_mfma_f32_16x16x32_bf16 a[72:75], v[8:11], v[40:43], a[72:75]\n" \
"ds_read_b64_tr_b16 v[112:113], v235 offset:16384\n" \
"v_mfma_f32_16x16x32_bf16 a[76:79], v[8:11], v[44:47], a[76:79]\n" \
"s_add_u32 m0, s38, 82944\n" \
"v_mfma_f32_16x16x32_bf16 a[80:83], v[8:11], v[48:51], a[80:83]\n" \
"ds_read_b64_tr_b16 v[114:115], v235 offset:18432\n" \
"v_mfma_f32_16x16x32_bf16 a[84:87], v[8:11], v[52:55], a[84:87]\n" \
"buffer_load_dwordx4 v228, s[20:23], s52 offen lds\n" \
"v_lshl_or_b32 v214, v141, 16, v140\n" \
"v_lshl_or_b32 v215, v143, 16, v142\n" \
"v_mfma_f32_16x16x32_bf16 a[88:91], v[8:11], v[56:59], a[88:91]\n" \
"ds_read_b64_tr_b16 v[116:117], v236 offset:16384\n" \
"v_mfma_f32_16x16x32_bf16 a[92:95], v[8:11], v[60:63], a[92:95]\n" \
"v_lshl_or_b32 v212, v137, 16, v136\n" \
"v_lshl_or_b32 v213, v139, 16, v138\n" \
"v_mfma_f32_16x16x32_bf16 a[96:99], v[12:15], v[32:35], a[96:99]\n" \
"ds_read_b64_tr_b16 v[118:119], v236 offset:18432\n" \
"v_mfma_f32_16x16x32_bf16 a[100:103], v[12:15], v[36:39], a[100:103]\n" \
"s_add_u32 s52, s40, 1589248\n" \
"buffer_store_dwordx4 v[212:215], v224, %[rs], s77 offen sc1\n" \
"v_mfma_f32_16x16x32_bf16 a[104:107], v[12:15], v[40:43], a[104:107]\n" \
"ds_read_b64_tr_b16 v[120:121], v237 offset:16384\n" \
"v_mfma_f32_16x16x32_bf16 a[108:111], v[12:15], v[44:47], a[108:111]\n" \
"s_add_u32 m0, s38, 132096\n" \
"v_mfma_f32_16x16x32_bf16 a[112:115], v[12:15], v[48:51], a[112:115]\n" \
"ds_read_b64_tr_b16 v[122:123], v237 offset:18432\n" \
"v_mfma_f32_16x16x32_bf16 a[116:119], v[12:15], v[52:55], a[116:119]\n" \
"buffer_load_dwordx4 v228, s[20:23], s52 offen lds\n" \
"v_mfma_f32_16x16x32_bf16 a[120:123], v[12:15], v[56:59], a[120:123]\n" \
"ds_read_b64_tr_b16 v[124:125], v238 offset:16384\n" \
"v_mfma_f32_16x16x32_bf16 a[124:127], v[12:15], v[60:63], a[124:127]\n" \
"s_add_u32 s80, s71, 3072\n" \
"v_mfma_f32_16x16x32_bf16 a[128:131], v[16:19], v[32:35], a[128:131]\n" \
"ds_read_b64_tr_b16 v[126:127], v238 offset:18432\n" \
"v_mfma_f32_16x16x32_bf16 a[132:135], v[16:19], v[36:39], a[132:135]\n" \
"s_add_u32 s52, s40, 819200\n" \
"buffer_load_dwordx4 v[188:191], v224, %[rs], s80 offen sc1\n" \
"v_mfma_f32_16x16x32_bf16 a[136:139], v[16:19], v[40:43], a[136:139]\n" \
"ds_read_b128 v[68:71], v230 offset:2048\n" \
"v_mfma_f32_16x16x32_bf16 a[140:143], v[16:19], v[44:47], a[140:143]\n" \
"s_add_u32 m0, s38, 83968\n" \
"v_mfma_f32_16x16x32_bf16 a[144:147], v[16:19], v[48:51], a[144:147]\n" \
"ds_read_b128 v[72:75], v230 offset:4096\n" \
"v_mfma_f32_16x16x32_bf16 a[148:151], v[16:19], v[52:55], a[148:151]\n" \
"buffer_load_dwordx4 v227, s[20:23], s52 offen lds\n" \
"v_lshl_or_b32 v218, v149, 16, v148\n" \
"v_lshl_or_b32 v219, v151, 16, v150\n" \
"v_mfma_f32_16x16x32_bf16 a[152:155], v[16:19], v[56:59], a[152:155]\n" \
"ds_read_b128 v[76:79], v230 offset:6144\n" \
"v_mfma_f32_16x16x32_bf16 a[156:159], v[16:19], v[60:63], a[156:159]\n" \
"v_lshl_or_b32 v216, v145, 16, v144\n" \
"v_lshl_or_b32 v217, v147, 16, v146\n" \
"v_mfma_f32_16x16x32_bf16 a[160:163], v[20:23], v[32:35], a[160:163]\n" \
"ds_read_b128 v[80:83], v230 offset:8192\n" \
"v_mfma_f32_16x16x32_bf16 a[164:167], v[20:23], v[36:39], a[164:167]\n" \
"s_add_u32 s52, s40, 1605632\n" \
"buffer_store_dwordx4 v[216:219], v224, %[rs], s78 offen sc1\n" \
"v_mfma_f32_16x16x32_bf16 a[168:171], v[20:23], v[40:43], a[168:171]\n" \
"ds_read_b128 v[84:87], v230 offset:10240\n" \
"v_mfma_f32_16x16x32_bf16 a[172:175], v[20:23], v[44:47], a[172:175]\n" \
"s_add_u32 m0, s38, 133120\n" \
"v_mfma_f32_16x16x32_bf16 a[176:179], v[20:23], v[48:51], a[176:179]\n" \
"ds_read_b128 v[88:91], v230 offset:12288\n" \
"v_mfma_f32_16x16x32_bf16 a[180:183], v[20:23], v[52:55], a[180:183]\n" \
"buffer_load_dwordx4 v227, s[20:23], s52 offen lds\n" \
"v_mfma_f32_16x16x32_bf16 a[184:187], v[20:23], v[56:59], a[184:187]\n" \
"ds_read_b128 v[92:95], v230 offset:14336\n" \
"v_mfma_f32_16x16x32_bf16 a[188:191], v[20:23], v[60:63], a[188:191]\n" \
"s_add_u32 s80, s58, 4096\n" \
"v_mfma_f32_16x16x32_bf16 a[192:195], v[24:27], v[32:35], a[192:195]\n" \
"v_mfma_f32_16x16x32_bf16 a[196:199], v[24:27], v[36:39], a[196:199]\n" \
"s_add_u32 s52, s40, 835584\n" \
"buffer_load_dwordx4 v[192:195], v224, %[rs], s80 offen sc1\n" \
"v_mfma_f32_16x16x32_bf16 a[200:203], v[24:27], v[40:43], a[200:203]\n" \
"v_lshl_or_b32 v220, v153, 16, v152\n" \
"v_lshl_or_b32 v221, v155, 16, v154\n" \
"v_mfma_f32_16x16x32_bf16 a[204:207], v[24:27], v[44:47], a[204:207]\n" \
"s_add_u32 m0, s38, 84992\n" \
"v_mfma_f32_16x16x32_bf16 a[208:211], v[24:27], v[48:51], a[208:211]\n" \
"v_lshl_or_b32 v222, v157, 16, v156\n" \
"v_lshl_or_b32 v223, v159, 16, v158\n" \
"v_mfma_f32_16x16x32_bf16 a[212:215], v[24:27], v[52:55], a[212:215]\n" \
"buffer_load_dwordx4 v228, s[20:23], s52 offen lds\n" \
"v_mfma_f32_16x16x32_bf16 a[216:219], v[24:27], v[56:59], a[216:219]\n" \
"v_mfma_f32_16x16x32_bf16 a[220:223], v[24:27], v[60:63], a[220:223]\n" \
"v_mfma_f32_16x16x32_bf16 a[224:227], v[28:31], v[32:35], a[224:227]\n" \
"v_mfma_f32_16x16x32_bf16 a[228:231], v[28:31], v[36:39], a[228:231]\n" \
"s_add_u32 s52, s40, 1622016\n" \
"buffer_store_dwordx4 v[220:223], v224, %[rs], s79 offen sc1\n" \
"v_mfma_f32_16x16x32_bf16 a[232:235], v[28:31], v[40:43], a[232:235]\n" \
"v_mfma_f32_16x16x32_bf16 a[236:239], v[28:31], v[44:47], a[236:239]\n" \
"s_add_u32 m0, s38, 134144\n" \
"v_mfma_f32_16x16x32_bf16 a[240:243], v[28:31], v[48:51], a[240:243]\n" \
"v_mfma_f32_16x16x32_bf16 a[244:247], v[28:31], v[52:55], a[244:247]\n" \
"buffer_load_dwordx4 v228, s[20:23], s52 offen lds\n" \
"v_mfma_f32_16x16x32_bf16 a[248:251], v[28:31], v[56:59], a[248:251]\n" \
"v_mfma_f32_16x16x32_bf16 a[252:255], v[28:31], v[60:63], a[252:255]\n" \
"s_waitcnt vmcnt(63) lgkmcnt(0)\n" \
"s_barrier\n" \
"v_mfma_f32_16x16x32_bf16 a[0:3], v[64:67], v[96:99], a[0:3]\n" \
"v_mfma_f32_16x16x32_bf16 a[4:7], v[64:67], v[100:103], a[4:7]\n" \
"s_add_u32 s52, s40, 1835008\n" \
"v_mfma_f32_16x16x32_bf16 a[8:11], v[64:67], v[104:107], a[8:11]\n" \
"v_add_u32 v231, s82, v231\n" \
"v_mfma_f32_16x16x32_bf16 a[12:15], v[64:67], v[108:111], a[12:15]\n" \
"s_add_u32 m0, s38, 147456\n" \
"v_mfma_f32_16x16x32_bf16 a[16:19], v[64:67], v[112:115], a[16:19]\n" \
"v_add_u32 v232, s82, v232\n" \
"v_mfma_f32_16x16x32_bf16 a[20:23], v[64:67], v[116:119], a[20:23]\n" \
"buffer_load_dwordx4 v227, s[20:23], s52 offen lds\n" \
"v_mfma_f32_16x16x32_bf16 a[24:27], v[64:67], v[120:123], a[24:27]\n" \
"v_add_u32 v233, s82, v233\n" \
"v_mfma_f32_16x16x32_bf16 a[28:31], v[64:67], v[124:127], a[28:31]\n" \
"v_mfma_f32_16x16x32_bf16 a[32:35], v[68:71], v[96:99], a[32:35]\n" \
"v_add_u32 v234, s82, v234\n" \
"v_mfma_f32_16x16x32_bf16 a[36:39], v[68:71], v[100:103], a[36:39]\n" \
"s_add_u32 s52, s40, 2097152\n" \
"v_mfma_f32_16x16x32_bf16 a[40:43], v[68:71], v[104:107], a[40:43]\n" \
"v_add_u32 v235, s82, v235\n" \
"v_mfma_f32_16x16x32_bf16 a[44:47], v[68:71], v[108:111], a[44:47]\n" \
"s_add_u32 m0, s38, 0\n" \
"v_mfma_f32_16x16x32_bf16 a[48:51], v[68:71], v[112:115], a[48:51]\n" \
"v_add_u32 v236, s82, v236\n" \
"v_mfma_f32_16x16x32_bf16 a[52:55], v[68:71], v[116:119], a[52:55]\n" \
"buffer_load_dwordx4 v227, s[20:23], s52 offen lds\n" \
"v_accvgpr_read_b32 v208, a0\n" \
"v_accvgpr_read_b32 v209, a1\n" \
"v_mfma_f32_16x16x32_bf16 a[56:59], v[68:71], v[120:123], a[56:59]\n" \
"v_add_u32 v237, s82, v237\n" \
"v_accvgpr_read_b32 v210, a2\n" \
"v_accvgpr_read_b32 v211, a3\n" \
"v_mfma_f32_16x16x32_bf16 a[60:63], v[68:71], v[124:127], a[60:63]\n" \
"s_add_u32 s80, s71, 4096\n" \
"v_cvt_pk_bf16_f32 v212, v208, v209\n" \
"v_cvt_pk_bf16_f32 v213, v210, v211\n" \
"v_mfma_f32_16x16x32_bf16 a[64:67], v[72:75], v[96:99], a[64:67]\n" \
"v_add_u32 v238, s82, v238\n" \
"v_accvgpr_read_b32 v208, a32\n" \
"v_accvgpr_read_b32 v209, a33\n" \
"v_mfma_f32_16x16x32_bf16 a[68:71], v[72:75], v[100:103], a[68:71]\n" \
"s_add_u32 s52, s40, 1851392\n" \
"buffer_load_dwordx4 v[196:199], v224, %[rs], s80 offen sc1\n" \
"v_accvgpr_read_b32 v210, a34\n" \
"v_mfma_f32_16x16x32_bf16 a[72:75], v[72:75], v[104:107], a[72:75]\n" \
"v_accvgpr_read_b32 v211, a35\n" \
"v_cvt_pk_bf16_f32 v214, v208, v209\n" \
"v_cvt_pk_bf16_f32 v215, v210, v211\n" \
"v_mfma_f32_16x16x32_bf16 a[76:79], v[72:75], v[108:111], a[76:79]\n" \
"s_add_u32 m0, s38, 148480\n" \
"v_accvgpr_read_b32 v208, a4\n" \
"v_accvgpr_read_b32 v209, a5\n" \
"v_mfma_f32_16x16x32_bf16 a[80:83], v[72:75], v[112:115], a[80:83]\n" \
"v_accvgpr_read_b32 v210, a6\n" \
"v_accvgpr_read_b32 v211, a7\n" \
"v_cvt_pk_bf16_f32 v216, v208, v209\n" \
"v_mfma_f32_16x16x32_bf16 a[84:87], v[72:75], v[116:119], a[84:87]\n" \
"buffer_load_dwordx4 v228, s[20:23], s52 offen lds\n" \
"v_cvt_pk_bf16_f32 v217, v210, v211\n" \
"v_accvgpr_read_b32 v208, a36\n" \
"v_mfma_f32_16x16x32_bf16 a[88:91], v[72:75], v[120:123], a[88:91]\n" \
"v_accvgpr_read_b32 v209, a37\n" \
"v_accvgpr_read_b32 v210, a38\n" \
"v_accvgpr_read_b32 v211, a39\n" \
"v_mfma_f32_16x16x32_bf16 a[92:95], v[72:75], v[124:127], a[92:95]\n" \
"v_cvt_pk_bf16_f32 v218, v208, v209\n" \
"v_cvt_pk_bf16_f32 v219, v210, v211\n" \
"v_accvgpr_read_b32 v208, a8\n" \
"v_mfma_f32_16x16x32_bf16 a[96:99], v[76:79], v[96:99], a[96:99]\n" \
"v_accvgpr_read_b32 v209, a9\n" \
"v_accvgpr_read_b32 v210, a10\n" \
"v_accvgpr_read_b32 v211, a11\n" \
"v_mfma_f32_16x16x32_bf16 a[100:103], v[76:79], v[100:103], a[100:103]\n" \
"s_add_u32 s52, s40, 2113536\n" \
"s_add_u32 s83, s55, 0\n" \
"buffer_store_dwordx4 v[212:215], v224, %[rs], s83 offen offset:0 sc1\n" \
"v_mfma_f32_16x16x32_bf16 a[104:107], v[76:79], v[104:107], a[104:107]\n" \
"v_cvt_pk_bf16_f32 v220, v208, v209\n" \
"v_cvt_pk_bf16_f32 v221, v210, v211\n" \
"v_accvgpr_read_b32 v208, a40\n" \
"v_mfma_f32_16x16x32_bf16 a[108:111], v[76:79], v[108:111], a[108:111]\n" \
"s_add_u32 m0, s38, 1024\n" \
"v_accvgpr_read_b32 v209, a41\n" \
"v_accvgpr_read_b32 v210, a42\n" \
"v_mfma_f32_16x16x32_bf16 a[112:115], v[76:79], v[112:115], a[112:115]\n" \
"v_accvgpr_read_b32 v211, a43\n" \
"v_cvt_pk_bf16_f32 v222, v208, v209\n" \
"v_cvt_pk_bf16_f32 v223, v210, v211\n" \
"v_mfma_f32_16x16x32_bf16 a[116:119], v[76:79], v[116:119], a[116:119]\n" \
"buffer_load_dwordx4 v228, s[20:23], s52 offen lds\n" \
"v_accvgpr_read_b32 v208, a12\n" \
"v_accvgpr_read_b32 v209, a13\n" \
"v_mfma_f32_16x16x32_bf16 a[120:123], v[76:79], v[120:123], a[120:123]\n" \
"v_accvgpr_read_b32 v210, a14\n" \
"v_accvgpr_read_b32 v211, a15\n" \
"v_cvt_pk_bf16_f32 v212, v208, v209\n" \
"v_mfma_f32_16x16x32_bf16 a[124:127], v[76:79], v[124:127], a[124:127]\n" \
"s_add_u32 s80, s58, 5120\n" \
"v_cvt_pk_bf16_f32 v213, v210, v211\n" \
"v_accvgpr_read_b32 v208, a44\n" \
"v_mfma_f32_16x16x32_bf16 a[128:131], v[80:83], v[96:99], a[128:131]\n" \
"v_accvgpr_read_b32 v209, a45\n" \
"v_accvgpr_read_b32 v210, a46\n" \
"v_accvgpr_read_b32 v211, a47\n" \
"v_mfma_f32_16x16x32_bf16 a[132:135], v[80:83], v[100:103], a[132:135]\n" \
"s_add_u32 s52, s40, 1867776\n" \
"buffer_load_dwordx4 v[200:203], v224, %[rs], s80 offen sc1\n" \
"v_cvt_pk_bf16_f32 v214, v208, v209\n" \
"v_mfma_f32_16x16x32_bf16 a[136:139], v[80:83], v[104:107], a[136:139]\n" \
"v_cvt_pk_bf16_f32 v215, v210, v211\n" \
"v_mfma_f32_16x16x32_bf16 a[140:143], v[80:83], v[108:111], a[140:143]\n" \
"s_add_u32 m0, s38, 149504\n" \
"v_mfma_f32_16x16x32_bf16 a[144:147], v[80:83], v[112:115], a[144:147]\n" \
"v_mfma_f32_16x16x32_bf16 a[148:151], v[80:83], v[116:119], a[148:151]\n" \
"buffer_load_dwordx4 v227, s[20:23], s52 offen lds\n" \
"v_mfma_f32_16x16x32_bf16 a[152:155], v[80:83], v[120:123], a[152:155]\n" \
"v_mfma_f32_16x16x32_bf16 a[156:159], v[80:83], v[124:127], a[156:159]\n" \
"v_mfma_f32_16x16x32_bf16 a[160:163], v[84:87], v[96:99], a[160:163]\n" \
"v_mfma_f32_16x16x32_bf16 a[164:167], v[84:87], v[100:103], a[164:167]\n" \
"s_add_u32 s52, s40, 2129920\n" \
"buffer_store_dwordx4 v[216:219], v224, %[rs], s83 offen offset:1024 sc1\n" \
"v_accvgpr_read_b32 v208, a16\n" \
"v_mfma_f32_16x16x32_bf16 a[168:171], v[84:87], v[104:107], a[168:171]\n" \
"v_accvgpr_read_b32 v209, a17\n" \
"v_accvgpr_read_b32 v210, a18\n" \
"v_accvgpr_read_b32 v211, a19\n" \
"v_mfma_f32_16x16x32_bf16 a[172:175], v[84:87], v[108:111], a[172:175]\n" \
"s_add_u32 m0, s38, 2048\n" \
"v_cvt_pk_bf16_f32 v216, v208, v209\n" \
"v_cvt_pk_bf16_f32 v217, v210, v211\n" \
"v_mfma_f32_16x16x32_bf16 a[176:179], v[84:87], v[112:115], a[176:179]\n" \
"v_accvgpr_read_b32 v208, a48\n" \
"v_accvgpr_read_b32 v209, a49\n" \
"v_accvgpr_read_b32 v210, a50\n" \
"v_mfma_f32_16x16x32_bf16 a[180:183], v[84:87], v[116:119], a[180:183]\n" \
"buffer_load_dwordx4 v227, s[20:23], s52 offen lds\n" \
"v_accvgpr_read_b32 v211, a51\n" \
"v_cvt_pk_bf16_f32 v218, v208, v209\n" \
"v_mfma_f32_16x16x32_bf16 a[184:187], v[84:87], v[120:123], a[184:187]\n" \
"v_cvt_pk_bf16_f32 v219, v210, v211\n" \
"v_mfma_f32_16x16x32_bf16 a[188:191], v[84:87], v[124:127], a[188:191]\n" \
"s_add_u32 s80, s71, 5120\n" \
"v_mfma_f32_16x16x32_bf16 a[192:195], v[88:91], v[96:99], a[192:195]\n" \
"v_mfma_f32_16x16x32_bf16 a[196:199], v[88:91], v[100:103], a[196:199]\n" \
"s_add_u32 s52, s40, 1884160\n" \
"buffer_load_dwordx4 v[204:207], v224, %[rs], s80 offen sc1\n" \
"v_mfma_f32_16x16x32_bf16 a[200:203], v[88:91], v[104:107], a[200:203]\n" \
"ds_read_b64_tr_b16 v[128:129], v231 offset:0\n" \
"v_mfma_f32_16x16x32_bf16 a[204:207], v[88:91], v[108:111], a[204:207]\n" \
"s_add_u32 m0, s38, 150528\n" \
"ds_read_b64_tr_b16 v[130:131], v231 offset:2048\n" \
"v_mfma_f32_16x16x32_bf16 a[208:211], v[88:91], v[112:115], a[208:211]\n" \
"ds_read_b64_tr_b16 v[132:133], v232 offset:0\n" \
"v_mfma_f32_16x16x32_bf16 a[212:215], v[88:91], v[116:119], a[212:215]\n" \
"buffer_load_dwordx4 v228, s[20:23], s52 offen lds\n" \
"ds_read_b64_tr_b16 v[134:135], v232 offset:2048\n" \
"v_mfma_f32_16x16x32_bf16 a[216:219], v[88:91], v[120:123], a[216:219]\n" \
"ds_read_b64_tr_b16 v[136:137], v233 offset:0\n" \
"v_mfma_f32_16x16x32_bf16 a[220:223], v[88:91], v[124:127], a[220:223]\n" \
"ds_read_b64_tr_b16 v[138:139], v233 offset:2048\n" \
"v_mfma_f32_16x16x32_bf16 a[224:227], v[92:95], v[96:99], a[224:227]\n" \
"ds_read_b64_tr_b16 v[140:141], v234 offset:0\n" \
"v_mfma_f32_16x16x32_bf16 a[228:231], v[92:95], v[100:103], a[228:231]\n" \
"s_add_u32 s52, s40, 2146304\n" \
"ds_read_b64_tr_b16 v[142:143], v234 offset:2048\n" \
"buffer_store_dwordx4 v[220:223], v224, %[rs], s83 offen offset:2048 sc1\n" \
"v_mfma_f32_16x16x32_bf16 a[232:235], v[92:95], v[104:107], a[232:235]\n" \
"ds_read_b64_tr_b16 v[144:145], v235 offset:0\n" \
"v_accvgpr_read_b32 v208, a20\n" \
"v_accvgpr_read_b32 v209, a21\n" \
"v_mfma_f32_16x16x32_bf16 a[236:239], v[92:95], v[108:111], a[236:239]\n" \
"s_add_u32 m0, s38, 3072\n" \
"ds_read_b64_tr_b16 v[146:147], v235 offset:2048\n" \
"v_accvgpr_read_b32 v210, a22\n" \
"v_mfma_f32_16x16x32_bf16 a[240:243], v[92:95], v[112:115], a[240:243]\n" \
"ds_read_b64_tr_b16 v[148:149], v236 offset:0\n" \
"v_accvgpr_read_b32 v211, a23\n" \
"v_cvt_pk_bf16_f32 v220, v208, v209\n" \
"v_mfma_f32_16x16x32_bf16 a[244:247], v[92:95], v[116:119], a[244:247]\n" \
"buffer_load_dwordx4 v228, s[20:23], s52 offen lds\n" \
"ds_read_b64_tr_b16 v[150:151], v236 offset:2048\n" \
"v_cvt_pk_bf16_f32 v221, v210, v211\n" \
"v_mfma_f32_16x16x32_bf16 a[248:251], v[92:95], v[120:123], a[248:251]\n" \
"v_accvgpr_read_b32 v208, a52\n" \
"v_accvgpr_read_b32 v209, a53\n" \
"v_accvgpr_read_b32 v210, a54\n" \
"v_mfma_f32_16x16x32_bf16 a[252:255], v[92:95], v[124:127], a[252:255]\n" \
"v_accvgpr_read_b32 v211, a55\n" \
"v_cvt_pk_bf16_f32 v222, v208, v209\n" \
"v_cvt_pk_bf16_f32 v223, v210, v211\n" \
"s_waitcnt vmcnt(45)\n" \
"s_waitcnt lgkmcnt(8)\n" \
"v_mfma_f32_16x16x32_bf16 v[0:3], v[128:131], v[160:163], 0\n" \
"ds_read_b64_tr_b16 v[152:153], v237 offset:0\n" \
"s_add_u32 s52, s40, 2359296\n" \
"v_mfma_f32_16x16x32_bf16 v[4:7], v[128:131], v[164:167], 0\n" \
"ds_read_b64_tr_b16 v[154:155], v237 offset:2048\n" \
"s_add_u32 m0, s38, 16384\n" \
"v_mfma_f32_16x16x32_bf16 v[8:11], v[132:135], v[160:163], 0\n" \
"ds_read_b64_tr_b16 v[156:157], v238 offset:0\n" \
"v_mfma_f32_16x16x32_bf16 v[12:15], v[132:135], v[164:167], 0\n" \
"ds_read_b64_tr_b16 v[158:159], v238 offset:2048\n" \
"s_waitcnt lgkmcnt(8)\n" \
"buffer_load_dwordx4 v227, s[20:23], s52 offen lds\n" \
"v_mfma_f32_16x16x32_bf16 v[16:19], v[136:139], v[160:163], 0\n" \
"ds_read_b64_tr_b16 v[128:129], v231 offset:256\n" \
"v_mfma_f32_16x16x32_bf16 v[20:23], v[136:139], v[164:167], 0\n" \
"ds_read_b64_tr_b16 v[130:131], v231 offset:2304\n" \
"s_add_u32 s52, s40, 2375680\n" \
"v_mfma_f32_16x16x32_bf16 v[24:27], v[140:143], v[160:163], 0\n" \
"ds_read_b64_tr_b16 v[132:133], v232 offset:256\n" \
"s_add_u32 m0, s38, 17408\n" \
"v_mfma_f32_16x16x32_bf16 v[28:31], v[140:143], v[164:167], 0\n" \
"ds_read_b64_tr_b16 v[134:135], v232 offset:2304\n" \
"s_waitcnt lgkmcnt(8)\n" \
"buffer_store_dwordx4 v[212:215], v224, %[rs], s83 offen offset:3072 sc1\n" \
"v_mfma_f32_16x16x32_bf16 v[32:35], v[144:147], v[160:163], 0\n" \
"ds_read_b64_tr_b16 v[136:137], v233 offset:256\n" \
"buffer_load_dwordx4 v228, s[20:23], s52 offen lds\n" \
"v_accvgpr_read_b32 v208, a24\n" \
"v_mfma_f32_16x16x32_bf16 v[36:39], v[144:147], v[164:167], 0\n" \
"ds_read_b64_tr_b16 v[138:139], v233 offset:2304\n" \
"v_accvgpr_read_b32 v209, a25\n" \
"v_accvgpr_read_b32 v210, a26\n" \
"v_mfma_f32_16x16x32_bf16 v[40:43], v[148:151], v[160:163], 0\n" \
"ds_read_b64_tr_b16 v[140:141], v234 offset:256\n" \
"s_add_u32 s52, s40, 2392064\n" \
"v_accvgpr_read_b32 v211, a27\n" \
"v_mfma_f32_16x16x32_bf16 v[44:47], v[148:151], v[164:167], 0\n" \
"ds_read_b64_tr_b16 v[142:143], v234 offset:2304\n" \
"s_waitcnt lgkmcnt(8)\n" \
"s_add_u32 m0, s38, 18432\n" \
"v_mfma_f32_16x16x32_bf16 v[48:51], v[152:155], v[160:163], 0\n" \
"ds_read_b64_tr_b16 v[144:145], v235 offset:256\n" \
"v_cvt_pk_bf16_f32 v212, v208, v209\n" \
"v_cvt_pk_bf16_f32 v213, v210, v211\n" \
"v_mfma_f32_16x16x32_bf16 v[52:55], v[152:155], v[164:167], 0\n" \
"ds_read_b64_tr_b16 v[146:147], v235 offset:2304\n" \
"buffer_load_dwordx4 v227, s[20:23], s52 offen lds\n" \
"v_accvgpr_read_b32 v208, a56\n" \
"v_mfma_f32_16x16x32_bf16 v[56:59], v[156:159], v[160:163], 0\n" \
"ds_read_b64_tr_b16 v[148:149], v236 offset:256\n" \
"v_accvgpr_read_b32 v209, a57\n" \
"v_accvgpr_read_b32 v210, a58\n" \
"v_mfma_f32_16x16x32_bf16 v[60:63], v[156:159], v[164:167], 0\n" \
"ds_read_b64_tr_b16 v[150:151], v236 offset:2304\n" \
"s_waitcnt lgkmcnt(8)\n" \
"s_add_u32 s52, s40, 2408448\n" \
"v_mfma_f32_16x16x32_bf16 v[64:67], v[128:131], v[160:163], 0\n" \
"ds_read_b64_tr_b16 v[152:153], v237 offset:256\n" \
"s_add_u32 m0, s38, 19456\n" \
"v_accvgpr_read_b32 v211, a59\n" \
"v_mfma_f32_16x16x32_bf16 v[68:71], v[128:131], v[164:167], 0\n" \
"ds_read_b64_tr_b16 v[154:155], v237 offset:2304\n" \
"v_cvt_pk_bf16_f32 v214, v208, v209\n" \
"v_cvt_pk_bf16_f32 v215, v210, v211\n" \
"v_mfma_f32_16x16x32_bf16 v[72:75], v[132:135], v[160:163], 0\n" \
"ds_read_b64_tr_b16 v[156:157], v238 offset:256\n" \
"buffer_load_dwordx4 v228, s[20:23], s52 offen lds\n" \
"v_mfma_f32_16x16x32_bf16 v[76:79], v[132:135], v[164:167], 0\n" \
"ds_read_b64_tr_b16 v[158:159], v238 offset:2304\n" \
"s_waitcnt lgkmcnt(8)\n" \
"s_waitcnt vmcnt(42)\n" \
"s_barrier\n" \
"v_mfma_f32_16x16x32_bf16 v[80:83], v[136:139], v[160:163], 0\n" \
"ds_read_b64_tr_b16 v[128:129], v231 offset:16384\n" \
"v_mfma_f32_16x16x32_bf16 v[84:87], v[136:139], v[164:167], 0\n" \
"ds_read_b64_tr_b16 v[130:131], v231 offset:18432\n" \
"v_mfma_f32_16x16x32_bf16 v[88:91], v[140:143], v[160:163], 0\n" \
"ds_read_b64_tr_b16 v[132:133], v232 offset:16384\n" \
"s_add_u32 s83, s55, 4096\n" \
"buffer_store_dwordx4 v[216:219], v224, %[rs], s83 offen offset:0 sc1\n" \
"v_mfma_f32_16x16x32_bf16 v[92:95], v[140:143], v[164:167], 0\n" \
"ds_read_b64_tr_b16 v[134:135], v232 offset:18432\n" \
"s_waitcnt lgkmcnt(8)\n" \
"v_accvgpr_read_b32 v208, a28\n" \
"v_mfma_f32_16x16x32_bf16 v[96:99], v[144:147], v[160:163], 0\n" \
"ds_read_b64_tr_b16 v[136:137], v233 offset:16384\n" \
"v_accvgpr_read_b32 v209, a29\n" \
"v_accvgpr_read_b32 v210, a30\n" \
"v_mfma_f32_16x16x32_bf16 v[100:103], v[144:147], v[164:167], 0\n" \
"ds_read_b64_tr_b16 v[138:139], v233 offset:18432\n" \
"v_accvgpr_read_b32 v211, a31\n" \
"v_cvt_pk_bf16_f32 v216, v208, v209\n" \
"v_mfma_f32_16x16x32_bf16 v[104:107], v[148:151], v[160:163], 0\n" \
"ds_read_b64_tr_b16 v[140:141], v234 offset:16384\n" \
"v_cvt_pk_bf16_f32 v217, v210, v211\n" \
"v_accvgpr_read_b32 v208, a60\n" \
"v_mfma_f32_16x16x32_bf16 v[108:111], v[148:151], v[164:167], 0\n" \
"ds_read_b64_tr_b16 v[142:143], v234 offset:18432\n" \
"s_waitcnt lgkmcnt(8)\n" \
"v_accvgpr_read_b32 v209, a61\n" \
"v_mfma_f32_16x16x32_bf16 v[112:115], v[152:155], v[160:163], 0\n" \
"ds_read_b64_tr_b16 v[144:145], v235 offset:16384\n" \
"v_accvgpr_read_b32 v210, a62\n" \
"v_accvgpr_read_b32 v211, a63\n" \
"v_mfma_f32_16x16x32_bf16 v[116:119], v[152:155], v[164:167], 0\n" \
"ds_read_b64_tr_b16 v[146:147], v235 offset:18432\n" \
"v_cvt_pk_bf16_f32 v218, v208, v209\n" \
"v_cvt_pk_bf16_f32 v219, v210, v211\n" \
"v_mfma_f32_16x16x32_bf16 v[120:123], v[156:159], v[160:163], 0\n" \
"ds_read_b64_tr_b16 v[148:149], v236 offset:16384\n" \
"v_mfma_f32_16x16x32_bf16 v[124:127], v[156:159], v[164:167], 0\n" \
"ds_read_b64_tr_b16 v[150:151], v236 offset:18432\n" \
"s_waitcnt lgkmcnt(8)\n" \
"v_mfma_f32_16x16x32_bf16 v[0:3], v[128:131], v[168:171], v[0:3]\n" \
"ds_read_b64_tr_b16 v[152:153], v237 offset:16384\n" \
"v_mfma_f32_16x16x32_bf16 v[4:7], v[128:131], v[172:175], v[4:7]\n" \
"ds_read_b64_tr_b16 v[154:155], v237 offset:18432\n" \
"v_mfma_f32_16x16x32_bf16 v[8:11], v[132:135], v[168:171], v[8:11]\n" \
"ds_read_b64_tr_b16 v[156:157], v238 offset:16384\n" \
"v_mfma_f32_16x16x32_bf16 v[12:15], v[132:135], v[172:175], v[12:15]\n" \
"ds_read_b64_tr_b16 v[158:159], v238 offset:18432\n" \
"s_waitcnt lgkmcnt(8)\n" \
"v_mfma_f32_16x16x32_bf16 v[16:19], v[136:139], v[168:171], v[16:19]\n" \
"ds_read_b64_tr_b16 v[128:129], v231 offset:16640\n" \
"buffer_store_dwordx4 v[220:223], v224, %[rs], s83 offen offset:1024 sc1\n" \
"v_accvgpr_read_b32 v208, a64\n" \
"v_mfma_f32_16x16x32_bf16 v[20:23], v[136:139], v[172:175], v[20:23]\n" \
"ds_read_b64_tr_b16 v[130:131], v231 offset:18688\n" \
"v_accvgpr_read_b32 v209, a65\n" \
"v_accvgpr_read_b32 v210, a66\n" \
"v_mfma_f32_16x16x32_bf16 v[24:27], v[140:143], v[168:171], v[24:27]\n" \
"ds_read_b64_tr_b16 v[132:133], v232 offset:16640\n" \
"v_accvgpr_read_b32 v211, a67\n" \
"v_cvt_pk_bf16_f32 v220, v208, v209\n" \
"v_mfma_f32_16x16x32_bf16 v[28:31], v[140:143], v[172:175], v[28:31]\n" \
"ds_read_b64_tr_b16 v[134:135], v232 offset:18688\n" \
"s_waitcnt lgkmcnt(8)\n" \
"v_cvt_pk_bf16_f32 v221, v210, v211\n" \
"v_mfma_f32_16x16x32_bf16 v[32:35], v[144:147], v[168:171], v[32:35]\n" \
"ds_read_b64_tr_b16 v[136:137], v233 offset:16640\n" \
"v_accvgpr_read_b32 v208, a96\n" \
"v_accvgpr_read_b32 v209, a97\n" \
"v_mfma_f32_16x16x32_bf16 v[36:39], v[144:147], v[172:175], v[36:39]\n" \
"ds_read_b64_tr_b16 v[138:139], v233 offset:18688\n" \
"s_add_u32 s80, s58, 6144\n" \
"v_accvgpr_read_b32 v210, a98\n" \
"v_mfma_f32_16x16x32_bf16 v[40:43], v[148:151], v[168:171], v[40:43]\n" \
"ds_read_b64_tr_b16 v[140:141], v234 offset:16640\n" \
"v_accvgpr_read_b32 v211, a99\n" \
"v_cvt_pk_bf16_f32 v222, v208, v209\n" \
"v_mfma_f32_16x16x32_bf16 v[44:47], v[148:151], v[172:175], v[44:47]\n" \
"ds_read_b64_tr_b16 v[142:143], v234 offset:18688\n" \
"s_waitcnt lgkmcnt(8)\n" \
"buffer_load_dwordx4 v[160:163], v224, %[rs], s80 offen sc1\n" \
"v_mfma_f32_16x16x32_bf16 v[48:51], v[152:155], v[168:171], v[48:51]\n" \
"ds_read_b64_tr_b16 v[144:145], v235 offset:16640\n" \
"v_cvt_pk_bf16_f32 v223, v210, v211\n" \
"v_mfma_f32_16x16x32_bf16 v[52:55], v[152:155], v[172:175], v[52:55]\n" \
"ds_read_b64_tr_b16 v[146:147], v235 offset:18688\n" \
"v_mfma_f32_16x16x32_bf16 v[56:59], v[156:159], v[168:171], v[56:59]\n" \
"ds_read_b64_tr_b16 v[148:149], v236 offset:16640\n" \
"v_mfma_f32_16x16x32_bf16 v[60:63], v[156:159], v[172:175], v[60:63]\n" \
"ds_read_b64_tr_b16 v[150:151], v236 offset:18688\n" \
"s_waitcnt lgkmcnt(8)\n" \
"s_add_u32 s80, s71, 6144\n" \
"v_mfma_f32_16x16x32_bf16 v[64:67], v[128:131], v[168:171], v[64:67]\n" \
"ds_read_b64_tr_b16 v[152:153], v237 offset:16640\n" \
"v_mfma_f32_16x16x32_bf16 v[68:71], v[128:131], v[172:175], v[68:71]\n" \
"ds_read_b64_tr_b16 v[154:155], v237 offset:18688\n" \
"buffer_load_dwordx4 v[164:167], v224, %[rs], s80 offen sc1\n" \
"v_mfma_f32_16x16x32_bf16 v[72:75], v[132:135], v[168:171], v[72:75]\n" \
"ds_read_b64_tr_b16 v[156:157], v238 offset:16640\n" \
"v_mfma_f32_16x16x32_bf16 v[76:79], v[132:135], v[172:175], v[76:79]\n" \
"ds_read_b64_tr_b16 v[158:159], v238 offset:18688\n" \
"s_waitcnt lgkmcnt(8)\n" \
"s_waitcnt vmcnt(39)\n" \
"s_barrier\n" \
"v_mfma_f32_16x16x32_bf16 v[80:83], v[136:139], v[168:171], v[80:83]\n" \
"ds_read_b64_tr_b16 v[128:129], v231 offset:32768\n" \
"s_add_u32 s52, s40, 2621440\n" \
"v_mfma_f32_16x16x32_bf16 v[84:87], v[136:139], v[172:175], v[84:87]\n" \
"ds_read_b64_tr_b16 v[130:131], v231 offset:34816\n" \
"s_add_u32 m0, s38, 32768\n" \
"buffer_store_dwordx4 v[212:215], v224, %[rs], s83 offen offset:2048 sc1\n" \
"v_mfma_f32_16x16x32_bf16 v[88:91], v[140:143], v[168:171], v[88:91]\n" \
"ds_read_b64_tr_b16 v[132:133], v232 offset:32768\n" \
"v_accvgpr_read_b32 v208, a68\n" \
"v_accvgpr_read_b32 v209, a69\n" \
"v_mfma_f32_16x16x32_bf16 v[92:95], v[140:143], v[172:175], v[92:95]\n" \
"ds_read_b64_tr_b16 v[134:135], v232 offset:34816\n" \
"s_waitcnt lgkmcnt(8)\n" \
"buffer_load_dwordx4 v227, s[20:23], s52 offen lds\n" \
"v_mfma_f32_16x16x32_bf16 v[96:99], v[144:147], v[168:171], v[96:99]\n" \
"ds_read_b64_tr_b16 v[136:137], v233 offset:32768\n" \
"v_accvgpr_read_b32 v210, a70\n" \
"v_accvgpr_read_b32 v211, a71\n" \
"v_mfma_f32_16x16x32_bf16 v[100:103], v[144:147], v[172:175], v[100:103]\n" \
"ds_read_b64_tr_b16 v[138:139], v233 offset:34816\n" \
"s_add_u32 s52, s40, 2637824\n" \
"v_cvt_pk_bf16_f32 v212, v208, v209\n" \
"v_mfma_f32_16x16x32_bf16 v[104:107], v[148:151], v[168:171], v[104:107]\n" \
"ds_read_b64_tr_b16 v[140:141], v234 offset:32768\n" \
"s_add_u32 m0, s38, 33792\n" \
"v_cvt_pk_bf16_f32 v213, v210, v211\n" \
"v_mfma_f32_16x16x32_bf16 v[108:111], v[148:151], v[172:175], v[108:111]\n" \
"ds_read_b64_tr_b16 v[142:143], v234 offset:34816\n" \
"s_waitcnt lgkmcnt(8)\n" \
"v_accvgpr_read_b32 v208, a100\n" \
"v_mfma_f32_16x16x32_bf16 v[112:115], v[152:155], v[168:171], v[112:115]\n" \
"ds_read_b64_tr_b16 v[144:145], v235 offset:32768\n" \
"buffer_load_dwordx4 v228, s[20:23], s52 offen lds\n" \
"v_accvgpr_read_b32 v209, a101\n" \
"v_mfma_f32_16x16x32_bf16 v[116:119], v[152:155], v[172:175], v[116:119]\n" \
"ds_read_b64_tr_b16 v[146:147], v235 offset:34816\n" \
"v_accvgpr_read_b32 v210, a102\n" \
"v_accvgpr_read_b32 v211, a103\n" \
"v_mfma_f32_16x16x32_bf16 v[120:123], v[156:159], v[168:171], v[120:123]\n" \
"ds_read_b64_tr_b16 v[148:149], v236 offset:32768\n" \
"s_add_u32 s52, s40, 2654208\n" \
"v_cvt_pk_bf16_f32 v214, v208, v209\n" \
"v_mfma_f32_16x16x32_bf16 v[124:127], v[156:159], v[172:175], v[124:127]\n" \
"ds_read_b64_tr_b16 v[150:151], v236 offset:34816\n" \
"s_waitcnt lgkmcnt(8)\n" \
"s_add_u32 m0, s38, 34816\n" \
"v_mfma_f32_16x16x32_bf16 v[0:3], v[128:131], v[176:179], v[0:3]\n" \
"ds_read_b64_tr_b16 v[152:153], v237 offset:32768\n" \
"v_cvt_pk_bf16_f32 v215, v210, v211\n" \
"v_mfma_f32_16x16x32_bf16 v[4:7], v[128:131], v[180:183], v[4:7]\n" \
"ds_read_b64_tr_b16 v[154:155], v237 offset:34816\n" \
"buffer_load_dwordx4 v227, s[20:23], s52 offen lds\n" \
"v_mfma_f32_16x16x32_bf16 v[8:11], v[132:135], v[176:179], v[8:11]\n" \
"ds_read_b64_tr_b16 v[156:157], v238 offset:32768\n" \
"v_mfma_f32_16x16x32_bf16 v[12:15], v[132:135], v[180:183], v[12:15]\n" \
"ds_read_b64_tr_b16 v[158:159], v238 offset:34816\n" \
"s_waitcnt lgkmcnt(8)\n" \
"s_add_u32 s52, s40, 2670592\n" \
"v_mfma_f32_16x16x32_bf16 v[16:19], v[136:139], v[176:179], v[16:19]\n" \
"ds_read_b64_tr_b16 v[128:129], v231 offset:33024\n" \
"s_add_u32 m0, s38, 35840\n" \
"v_mfma_f32_16x16x32_bf16 v[20:23], v[136:139], v[180:183], v[20:23]\n" \
"ds_read_b64_tr_b16 v[130:131], v231 offset:35072\n" \
"buffer_store_dwordx4 v[216:219], v224, %[rs], s83 offen offset:3072 sc1\n" \
"v_accvgpr_read_b32 v208, a72\n" \
"v_mfma_f32_16x16x32_bf16 v[24:27], v[140:143], v[176:179], v[24:27]\n" \
"ds_read_b64_tr_b16 v[132:133], v232 offset:33024\n" \
"buffer_load_dwordx4 v228, s[20:23], s52 offen lds\n" \
"v_accvgpr_read_b32 v209, a73\n" \
"v_mfma_f32_16x16x32_bf16 v[28:31], v[140:143], v[180:183], v[28:31]\n" \
"ds_read_b64_tr_b16 v[134:135], v232 offset:35072\n" \
"s_waitcnt lgkmcnt(8)\n" \
"v_accvgpr_read_b32 v210, a74\n" \
"v_mfma_f32_16x16x32_bf16 v[32:35], v[144:147], v[176:179], v[32:35]\n" \
"ds_read_b64_tr_b16 v[136:137], v233 offset:33024\n" \
"v_accvgpr_read_b32 v211, a75\n" \
"v_cvt_pk_bf16_f32 v216, v208, v209\n" \
"v_mfma_f32_16x16x32_bf16 v[36:39], v[144:147], v[180:183], v[36:39]\n" \
"ds_read_b64_tr_b16 v[138:139], v233 offset:35072\n" \
"s_add_u32 s80, s58, 7168\n" \
"v_cvt_pk_bf16_f32 v217, v210, v211\n" \
"v_mfma_f32_16x16x32_bf16 v[40:43], v[148:151], v[176:179], v[40:43]\n" \
"ds_read_b64_tr_b16 v[140:141], v234 offset:33024\n" \
"v_accvgpr_read_b32 v208, a104\n" \
"v_accvgpr_read_b32 v209, a105\n" \
"v_mfma_f32_16x16x32_bf16 v[44:47], v[148:151], v[180:183], v[44:47]\n" \
"ds_read_b64_tr_b16 v[142:143], v234 offset:35072\n" \
"s_waitcnt lgkmcnt(8)\n" \
"buffer_load_dwordx4 v[168:171], v224, %[rs], s80 offen sc1\n" \
"v_mfma_f32_16x16x32_bf16 v[48:51], v[152:155], v[176:179], v[48:51]\n" \
"ds_read_b64_tr_b16 v[144:145], v235 offset:33024\n" \
"v_accvgpr_read_b32 v210, a106\n" \
"v_accvgpr_read_b32 v211, a107\n" \
"v_mfma_f32_16x16x32_bf16 v[52:55], v[152:155], v[180:183], v[52:55]\n" \
"ds_read_b64_tr_b16 v[146:147], v235 offset:35072\n" \
"v_cvt_pk_bf16_f32 v218, v208, v209\n" \
"v_cvt_pk_bf16_f32 v219, v210, v211\n" \
"v_mfma_f32_16x16x32_bf16 v[56:59], v[156:159], v[176:179], v[56:59]\n" \
"ds_read_b64_tr_b16 v[148:149], v236 offset:33024\n" \
"v_mfma_f32_16x16x32_bf16 v[60:63], v[156:159], v[180:183], v[60:63]\n" \
"ds_read_b64_tr_b16 v[150:151], v236 offset:35072\n" \
"s_waitcnt lgkmcnt(8)\n" \
"s_add_u32 s80, s71, 7168\n" \
"v_mfma_f32_16x16x32_bf16 v[64:67], v[128:131], v[176:179], v[64:67]\n" \
"ds_read_b64_tr_b16 v[152:153], v237 offset:33024\n" \
"v_mfma_f32_16x16x32_bf16 v[68:71], v[128:131], v[180:183], v[68:71]\n" \
"ds_read_b64_tr_b16 v[154:155], v237 offset:35072\n" \
"buffer_load_dwordx4 v[172:175], v224, %[rs], s80 offen sc1\n" \
"v_mfma_f32_16x16x32_bf16 v[72:75], v[132:135], v[176:179], v[72:75]\n" \
"ds_read_b64_tr_b16 v[156:157], v238 offset:33024\n" \
"v_mfma_f32_16x16x32_bf16 v[76:79], v[132:135], v[180:183], v[76:79]\n" \
"ds_read_b64_tr_b16 v[158:159], v238 offset:35072\n" \
"s_waitcnt lgkmcnt(8)\n" \
"s_waitcnt vmcnt(33)\n" \
"s_barrier\n" \
"v_mfma_f32_16x16x32_bf16 v[80:83], v[136:139], v[176:179], v[80:83]\n" \
"ds_read_b64_tr_b16 v[128:129], v231 offset:49152\n" \
"s_add_u32 s52, s40, 2883584\n" \
"v_mfma_f32_16x16x32_bf16 v[84:87], v[136:139], v[180:183], v[84:87]\n" \
"ds_read_b64_tr_b16 v[130:131], v231 offset:51200\n" \
"s_add_u32 m0, s38, 49152\n" \
"s_add_u32 s83, s55, 16384\n" \
"buffer_store_dwordx4 v[220:223], v224, %[rs], s83 offen offset:0 sc1\n" \
"v_mfma_f32_16x16x32_bf16 v[88:91], v[140:143], v[176:179], v[88:91]\n" \
"ds_read_b64_tr_b16 v[132:133], v232 offset:49152\n" \
"v_accvgpr_read_b32 v208, a76\n" \
"v_accvgpr_read_b32 v209, a77\n" \
"v_mfma_f32_16x16x32_bf16 v[92:95], v[140:143], v[180:183], v[92:95]\n" \
"ds_read_b64_tr_b16 v[134:135], v232 offset:51200\n" \
"s_waitcnt lgkmcnt(8)\n" \
"buffer_load_dwordx4 v227, s[20:23], s52 offen lds\n" \
"v_mfma_f32_16x16x32_bf16 v[96:99], v[144:147], v[176:179], v[96:99]\n" \
"ds_read_b64_tr_b16 v[136:137], v233 offset:49152\n" \
"v_accvgpr_read_b32 v210, a78\n" \
"v_accvgpr_read_b32 v211, a79\n" \
"v_mfma_f32_16x16x32_bf16 v[100:103], v[144:147], v[180:183], v[100:103]\n" \
"ds_read_b64_tr_b16 v[138:139], v233 offset:51200\n" \
"s_add_u32 s52, s40, 2899968\n" \
"v_cvt_pk_bf16_f32 v220, v208, v209\n" \
"v_mfma_f32_16x16x32_bf16 v[104:107], v[148:151], v[176:179], v[104:107]\n" \
"ds_read_b64_tr_b16 v[140:141], v234 offset:49152\n" \
"s_add_u32 m0, s38, 50176\n" \
"v_cvt_pk_bf16_f32 v221, v210, v211\n" \
"v_mfma_f32_16x16x32_bf16 v[108:111], v[148:151], v[180:183], v[108:111]\n" \
"ds_read_b64_tr_b16 v[142:143], v234 offset:51200\n" \
"s_waitcnt lgkmcnt(8)\n" \
"v_accvgpr_read_b32 v208, a108\n" \
"v_mfma_f32_16x16x32_bf16 v[112:115], v[152:155], v[176:179], v[112:115]\n" \
"ds_read_b64_tr_b16 v[144:145], v235 offset:49152\n" \
"buffer_load_dwordx4 v228, s[20:23], s52 offen lds\n" \
"v_accvgpr_read_b32 v209, a109\n" \
"v_mfma_f32_16x16x32_bf16 v[116:119], v[152:155], v[180:183], v[116:119]\n" \
"ds_read_b64_tr_b16 v[146:147], v235 offset:51200\n" \
"v_accvgpr_read_b32 v210, a110\n" \
"v_accvgpr_read_b32 v211, a111\n" \
"v_mfma_f32_16x16x32_bf16 v[120:123], v[156:159], v[176:179], v[120:123]\n" \
"ds_read_b64_tr_b16 v[148:149], v236 offset:49152\n" \
"s_add_u32 s52, s40, 2916352\n" \
"v_cvt_pk_bf16_f32 v222, v208, v209\n" \
"v_mfma_f32_16x16x32_bf16 v[124:127], v[156:159], v[180:183], v[124:127]\n" \
"ds_read_b64_tr_b16 v[150:151], v236 offset:51200\n" \
"s_waitcnt lgkmcnt(8)\n" \
"s_add_u32 m0, s38, 51200\n" \
"v_mfma_f32_16x16x32_bf16 v[0:3], v[128:131], v[184:187], v[0:3]\n" \
"ds_read_b64_tr_b16 v[152:153], v237 offset:49152\n" \
"v_cvt_pk_bf16_f32 v223, v210, v211\n" \
"v_mfma_f32_16x16x32_bf16 v[4:7], v[128:131], v[188:191], v[4:7]\n" \
"ds_read_b64_tr_b16 v[154:155], v237 offset:51200\n" \
"buffer_load_dwordx4 v227, s[20:23], s52 offen lds\n" \
"v_mfma_f32_16x16x32_bf16 v[8:11], v[132:135], v[184:187], v[8:11]\n" \
"ds_read_b64_tr_b16 v[156:157], v238 offset:49152\n" \
"v_mfma_f32_16x16x32_bf16 v[12:15], v[132:135], v[188:191], v[12:15]\n" \
"ds_read_b64_tr_b16 v[158:159], v238 offset:51200\n" \
"s_waitcnt lgkmcnt(8)\n" \
"s_add_u32 s52, s40, 2932736\n" \
"v_mfma_f32_16x16x32_bf16 v[16:19], v[136:139], v[184:187], v[16:19]\n" \
"ds_read_b64_tr_b16 v[128:129], v231 offset:49408\n" \
"s_add_u32 m0, s38, 52224\n" \
"v_mfma_f32_16x16x32_bf16 v[20:23], v[136:139], v[188:191], v[20:23]\n" \
"ds_read_b64_tr_b16 v[130:131], v231 offset:51456\n" \
"buffer_store_dwordx4 v[212:215], v224, %[rs], s83 offen offset:1024 sc1\n" \
"v_accvgpr_read_b32 v208, a80\n" \
"v_mfma_f32_16x16x32_bf16 v[24:27], v[140:143], v[184:187], v[24:27]\n" \
"ds_read_b64_tr_b16 v[132:133], v232 offset:49408\n" \
"v_add_u32 v231, 0x10000, v231\n" \
"buffer_load_dwordx4 v228, s[20:23], s52 offen lds\n" \
"v_mfma_f32_16x16x32_bf16 v[28:31], v[140:143], v[188:191], v[28:31]\n" \
"ds_read_b64_tr_b16 v[134:135], v232 offset:51456\n" \
"s_waitcnt lgkmcnt(8)\n" \
"v_accvgpr_read_b32 v209, a81\n" \
"v_mfma_f32_16x16x32_bf16 v[32:35], v[144:147], v[184:187], v[32:35]\n" \
"ds_read_b64_tr_b16 v[136:137], v233 offset:49408\n" \
"v_add_u32 v232, 0x10000, v232\n" \
"v_accvgpr_read_b32 v210, a82\n" \
"v_mfma_f32_16x16x32_bf16 v[36:39], v[144:147], v[188:191], v[36:39]\n" \
"ds_read_b64_tr_b16 v[138:139], v233 offset:51456\n" \
"s_add_u32 s80, s58, 8192\n" \
"v_accvgpr_read_b32 v211, a83\n" \
"v_mfma_f32_16x16x32_bf16 v[40:43], v[148:151], v[184:187], v[40:43]\n" \
"ds_read_b64_tr_b16 v[140:141], v234 offset:49408\n" \
"v_add_u32 v233, 0x10000, v233\n" \
"v_cvt_pk_bf16_f32 v212, v208, v209\n" \
"v_mfma_f32_16x16x32_bf16 v[44:47], v[148:151], v[188:191], v[44:47]\n" \
"ds_read_b64_tr_b16 v[142:143], v234 offset:51456\n" \
"s_waitcnt lgkmcnt(8)\n" \
"buffer_load_dwordx4 v[176:179], v224, %[rs], s80 offen sc1\n" \
"v_mfma_f32_16x16x32_bf16 v[48:51], v[152:155], v[184:187], v[48:51]\n" \
"ds_read_b64_tr_b16 v[144:145], v235 offset:49408\n" \
"v_add_u32 v234, 0x10000, v234\n" \
"v_cvt_pk_bf16_f32 v213, v210, v211\n" \
"v_mfma_f32_16x16x32_bf16 v[52:55], v[152:155], v[188:191], v[52:55]\n" \
"ds_read_b64_tr_b16 v[146:147], v235 offset:51456\n" \
"v_accvgpr_read_b32 v208, a112\n" \
"v_accvgpr_read_b32 v209, a113\n" \
"v_mfma_f32_16x16x32_bf16 v[56:59], v[156:159], v[184:187], v[56:59]\n" \
"ds_read_b64_tr_b16 v[148:149], v236 offset:49408\n" \
"v_add_u32 v235, 0x10000, v235\n" \
"v_accvgpr_read_b32 v210, a114\n" \
"v_mfma_f32_16x16x32_bf16 v[60:63], v[156:159], v[188:191], v[60:63]\n" \
"ds_read_b64_tr_b16 v[150:151], v236 offset:51456\n" \
"s_waitcnt lgkmcnt(8)\n" \
"s_add_u32 s80, s71, 8192\n" \
"v_mfma_f32_16x16x32_bf16 v[64:67], v[128:131], v[184:187], v[64:67]\n" \
"ds_read_b64_tr_b16 v[152:153], v237 offset:49408\n" \
"v_add_u32 v236, 0x10000, v236\n" \
"v_accvgpr_read_b32 v211, a115\n" \
"v_mfma_f32_16x16x32_bf16 v[68:71], v[128:131], v[188:191], v[68:71]\n" \
"ds_read_b64_tr_b16 v[154:155], v237 offset:51456\n" \
"buffer_load_dwordx4 v[180:183], v224, %[rs], s80 offen sc1\n" \
"v_cvt_pk_bf16_f32 v214, v208, v209\n" \
"v_mfma_f32_16x16x32_bf16 v[72:75], v[132:135], v[184:187], v[72:75]\n" \
"ds_read_b64_tr_b16 v[156:157], v238 offset:49408\n" \
"v_add_u32 v237, 0x10000, v237\n" \
"v_cvt_pk_bf16_f32 v215, v210, v211\n" \
"v_mfma_f32_16x16x32_bf16 v[76:79], v[132:135], v[188:191], v[76:79]\n" \
"ds_read_b64_tr_b16 v[158:159], v238 offset:51456\n" \
"s_waitcnt lgkmcnt(8)\n" \
"s_waitcnt vmcnt(36)\n" \
"s_barrier\n" \
"v_mfma_f32_16x16x32_bf16 v[80:83], v[136:139], v[184:187], v[80:83]\n" \
"ds_read_b64_tr_b16 v[128:129], v231 offset:0\n" \
"v_add_u32 v238, 0x10000, v238\n" \
"s_add_u32 s52, s40, 3145728\n" \
"v_mfma_f32_16x16x32_bf16 v[84:87], v[136:139], v[188:191], v[84:87]\n" \
"ds_read_b64_tr_b16 v[130:131], v231 offset:2048\n" \
"s_add_u32 m0, s38, 65536\n" \
"buffer_store_dwordx4 v[216:219], v224, %[rs], s83 offen offset:2048 sc1\n" \
"v_mfma_f32_16x16x32_bf16 v[88:91], v[140:143], v[184:187], v[88:91]\n" \
"ds_read_b64_tr_b16 v[132:133], v232 offset:0\n" \
"v_accvgpr_read_b32 v208, a84\n" \
"v_accvgpr_read_b32 v209, a85\n" \
"v_mfma_f32_16x16x32_bf16 v[92:95], v[140:143], v[188:191], v[92:95]\n" \
"ds_read_b64_tr_b16 v[134:135], v232 offset:2048\n" \
"s_waitcnt lgkmcnt(8)\n" \
"buffer_load_dwordx4 v227, s[20:23], s52 offen lds\n" \
"v_mfma_f32_16x16x32_bf16 v[96:99], v[144:147], v[184:187], v[96:99]\n" \
"ds_read_b64_tr_b16 v[136:137], v233 offset:0\n" \
"v_accvgpr_read_b32 v210, a86\n" \
"v_accvgpr_read_b32 v211, a87\n" \
"v_mfma_f32_16x16x32_bf16 v[100:103], v[144:147], v[188:191], v[100:103]\n" \
"ds_read_b64_tr_b16 v[138:139], v233 offset:2048\n" \
"s_add_u32 s52, s40, 3162112\n" \
"v_cvt_pk_bf16_f32 v216, v208, v209\n" \
"v_mfma_f32_16x16x32_bf16 v[104:107], v[148:151], v[184:187], v[104:107]\n" \
"ds_read_b64_tr_b16 v[140:141], v234 offset:0\n" \
"s_add_u32 m0, s38, 66560\n" \
"v_cvt_pk_bf16_f32 v217, v210, v211\n" \
"v_mfma_f32_16x16x32_bf16 v[108:111], v[148:151], v[188:191], v[108:111]\n" \
"ds_read_b64_tr_b16 v[142:143], v234 offset:2048\n" \
"s_waitcnt lgkmcnt(8)\n" \
"v_accvgpr_read_b32 v208, a116\n" \
"v_mfma_f32_16x16x32_bf16 v[112:115], v[152:155], v[184:187], v[112:115]\n" \
"ds_read_b64_tr_b16 v[144:145], v235 offset:0\n" \
"buffer_load_dwordx4 v228, s[20:23], s52 offen lds\n" \
"v_accvgpr_read_b32 v209, a117\n" \
"v_mfma_f32_16x16x32_bf16 v[116:119], v[152:155], v[188:191], v[116:119]\n" \
"ds_read_b64_tr_b16 v[146:147], v235 offset:2048\n" \
"v_accvgpr_read_b32 v210, a118\n" \
"v_accvgpr_read_b32 v211, a119\n" \
"v_mfma_f32_16x16x32_bf16 v[120:123], v[156:159], v[184:187], v[120:123]\n" \
"ds_read_b64_tr_b16 v[148:149], v236 offset:0\n" \
"s_add_u32 s52, s40, 3178496\n" \
"v_cvt_pk_bf16_f32 v218, v208, v209\n" \
"v_mfma_f32_16x16x32_bf16 v[124:127], v[156:159], v[188:191], v[124:127]\n" \
"ds_read_b64_tr_b16 v[150:151], v236 offset:2048\n" \
"s_waitcnt lgkmcnt(8)\n" \
"s_add_u32 m0, s38, 67584\n" \
"v_mfma_f32_16x16x32_bf16 v[0:3], v[128:131], v[192:195], v[0:3]\n" \
"ds_read_b64_tr_b16 v[152:153], v237 offset:0\n" \
"v_cvt_pk_bf16_f32 v219, v210, v211\n" \
"v_mfma_f32_16x16x32_bf16 v[4:7], v[128:131], v[196:199], v[4:7]\n" \
"ds_read_b64_tr_b16 v[154:155], v237 offset:2048\n" \
"buffer_load_dwordx4 v227, s[20:23], s52 offen lds\n" \
"v_mfma_f32_16x16x32_bf16 v[8:11], v[132:135], v[192:195], v[8:11]\n" \
"ds_read_b64_tr_b16 v[156:157], v238 offset:0\n" \
"v_mfma_f32_16x16x32_bf16 v[12:15], v[132:135], v[196:199], v[12:15]\n" \
"ds_read_b64_tr_b16 v[158:159], v238 offset:2048\n" \
"s_waitcnt lgkmcnt(8)\n" \
"s_add_u32 s52, s40, 3194880\n" \
"v_mfma_f32_16x16x32_bf16 v[16:19], v[136:139], v[192:195], v[16:19]\n" \
"ds_read_b64_tr_b16 v[128:129], v231 offset:256\n" \
"s_add_u32 m0, s38, 68608\n" \
"v_mfma_f32_16x16x32_bf16 v[20:23], v[136:139], v[196:199], v[20:23]\n" \
"ds_read_b64_tr_b16 v[130:131], v231 offset:2304\n" \
"buffer_store_dwordx4 v[220:223], v224, %[rs], s83 offen offset:3072 sc1\n" \
"v_accvgpr_read_b32 v208, a88\n" \
"v_mfma_f32_16x16x32_bf16 v[24:27], v[140:143], v[192:195], v[24:27]\n" \
"ds_read_b64_tr_b16 v[132:133], v232 offset:256\n" \
"buffer_load_dwordx4 v228, s[20:23], s52 offen lds\n" \
"v_accvgpr_read_b32 v209, a89\n" \
"v_mfma_f32_16x16x32_bf16 v[28:31], v[140:143], v[196:199], v[28:31]\n" \
"ds_read_b64_tr_b16 v[134:135], v232 offset:2304\n" \
"s_waitcnt lgkmcnt(8)\n" \
"v_accvgpr_read_b32 v210, a90\n" \
"v_mfma_f32_16x16x32_bf16 v[32:35], v[144:147], v[192:195], v[32:35]\n" \
"ds_read_b64_tr_b16 v[136:137], v233 offset:256\n" \
"v_accvgpr_read_b32 v211, a91\n" \
"v_cvt_pk_bf16_f32 v220, v208, v209\n" \
"v_mfma_f32_16x16x32_bf16 v[36:39], v[144:147], v[196:199], v[36:39]\n" \
"ds_read_b64_tr_b16 v[138:139], v233 offset:2304\n" \
"s_add_u32 s80, s58, 9216\n" \
"v_cvt_pk_bf16_f32 v221, v210, v211\n" \
"v_mfma_f32_16x16x32_bf16 v[40:43], v[148:151], v[192:195], v[40:43]\n" \
"ds_read_b64_tr_b16 v[140:141], v234 offset:256\n" \
"v_accvgpr_read_b32 v208, a120\n" \
"v_accvgpr_read_b32 v209, a121\n" \
"v_mfma_f32_16x16x32_bf16 v[44:47], v[148:151], v[196:199], v[44:47]\n" \
"ds_read_b64_tr_b16 v[142:143], v234 offset:2304\n" \
"s_waitcnt lgkmcnt(8)\n" \
"buffer_load_dwordx4 v[184:187], v224, %[rs], s80 offen sc1\n" \
"v_mfma_f32_16x16x32_bf16 v[48:51], v[152:155], v[192:195], v[48:51]\n" \
"ds_read_b64_tr_b16 v[144:145], v235 offset:256\n" \
"v_accvgpr_read_b32 v210, a122\n" \
"v_accvgpr_read_b32 v211, a123\n" \
"v_mfma_f32_16x16x32_bf16 v[52:55], v[152:155], v[196:199], v[52:55]\n" \
"ds_read_b64_tr_b16 v[146:147], v235 offset:2304\n" \
"v_cvt_pk_bf16_f32 v222, v208, v209\n" \
"v_cvt_pk_bf16_f32 v223, v210, v211\n" \
"v_mfma_f32_16x16x32_bf16 v[56:59], v[156:159], v[192:195], v[56:59]\n" \
"ds_read_b64_tr_b16 v[148:149], v236 offset:256\n" \
"v_mfma_f32_16x16x32_bf16 v[60:63], v[156:159], v[196:199], v[60:63]\n" \
"ds_read_b64_tr_b16 v[150:151], v236 offset:2304\n" \
"s_waitcnt lgkmcnt(8)\n" \
"s_add_u32 s80, s71, 9216\n" \
"v_mfma_f32_16x16x32_bf16 v[64:67], v[128:131], v[192:195], v[64:67]\n" \
"ds_read_b64_tr_b16 v[152:153], v237 offset:256\n" \
"v_mfma_f32_16x16x32_bf16 v[68:71], v[128:131], v[196:199], v[68:71]\n" \
"ds_read_b64_tr_b16 v[154:155], v237 offset:2304\n" \
"buffer_load_dwordx4 v[188:191], v224, %[rs], s80 offen sc1\n" \
"v_mfma_f32_16x16x32_bf16 v[72:75], v[132:135], v[192:195], v[72:75]\n" \
"ds_read_b64_tr_b16 v[156:157], v238 offset:256\n" \
"v_mfma_f32_16x16x32_bf16 v[76:79], v[132:135], v[196:199], v[76:79]\n" \
"ds_read_b64_tr_b16 v[158:159], v238 offset:2304\n" \
"s_waitcnt lgkmcnt(8)\n" \
"s_waitcnt vmcnt(36)\n" \
"s_barrier\n" \
"v_mfma_f32_16x16x32_bf16 v[80:83], v[136:139], v[192:195], v[80:83]\n" \
"ds_read_b64_tr_b16 v[128:129], v231 offset:16384\n" \
"s_add_u32 s52, s40, 3407872\n" \
"v_mfma_f32_16x16x32_bf16 v[84:87], v[136:139], v[196:199], v[84:87]\n" \
"ds_read_b64_tr_b16 v[130:131], v231 offset:18432\n" \
"s_add_u32 m0, s38, 81920\n" \
"s_add_u32 s83, s55, 20480\n" \
"buffer_store_dwordx4 v[212:215], v224, %[rs], s83 offen offset:0 sc1\n" \
"v_mfma_f32_16x16x32_bf16 v[88:91], v[140:143], v[192:195], v[88:91]\n" \
"ds_read_b64_tr_b16 v[132:133], v232 offset:16384\n" \
"v_accvgpr_read_b32 v208, a92\n" \
"v_accvgpr_read_b32 v209, a93\n" \
"v_mfma_f32_16x16x32_bf16 v[92:95], v[140:143], v[196:199], v[92:95]\n" \
"ds_read_b64_tr_b16 v[134:135], v232 offset:18432\n" \
"s_waitcnt lgkmcnt(8)\n" \
"buffer_load_dwordx4 v227, s[20:23], s52 offen lds\n" \
"v_mfma_f32_16x16x32_bf16 v[96:99], v[144:147], v[192:195], v[96:99]\n" \
"ds_read_b64_tr_b16 v[136:137], v233 offset:16384\n" \
"v_accvgpr_read_b32 v210, a94\n" \
"v_accvgpr_read_b32 v211, a95\n" \
"v_mfma_f32_16x16x32_bf16 v[100:103], v[144:147], v[196:199], v[100:103]\n" \
"ds_read_b64_tr_b16 v[138:139], v233 offset:18432\n" \
"s_add_u32 s52, s40, 3424256\n" \
"v_cvt_pk_bf16_f32 v212, v208, v209\n" \
"v_mfma_f32_16x16x32_bf16 v[104:107], v[148:151], v[192:195], v[104:107]\n" \
"ds_read_b64_tr_b16 v[140:141], v234 offset:16384\n" \
"s_add_u32 m0, s38, 82944\n" \
"v_cvt_pk_bf16_f32 v213, v210, v211\n" \
"v_mfma_f32_16x16x32_bf16 v[108:111], v[148:151], v[196:199], v[108:111]\n" \
"ds_read_b64_tr_b16 v[142:143], v234 offset:18432\n" \
"s_waitcnt lgkmcnt(8)\n" \
"v_accvgpr_read_b32 v208, a124\n" \
"v_mfma_f32_16x16x32_bf16 v[112:115], v[152:155], v[192:195], v[112:115]\n" \
"ds_read_b64_tr_b16 v[144:145], v235 offset:16384\n" \
"buffer_load_dwordx4 v228, s[20:23], s52 offen lds\n" \
"v_accvgpr_read_b32 v209, a125\n" \
"v_mfma_f32_16x16x32_bf16 v[116:119], v[152:155], v[196:199], v[116:119]\n" \
"ds_read_b64_tr_b16 v[146:147], v235 offset:18432\n" \
"v_accvgpr_read_b32 v210, a126\n" \
"v_accvgpr_read_b32 v211, a127\n" \
"v_mfma_f32_16x16x32_bf16 v[120:123], v[156:159], v[192:195], v[120:123]\n" \
"ds_read_b64_tr_b16 v[148:149], v236 offset:16384\n" \
"s_add_u32 s52, s40, 3440640\n" \
"v_cvt_pk_bf16_f32 v214, v208, v209\n" \
"v_mfma_f32_16x16x32_bf16 v[124:127], v[156:159], v[196:199], v[124:127]\n" \
"ds_read_b64_tr_b16 v[150:151], v236 offset:18432\n" \
"s_waitcnt lgkmcnt(8)\n" \
"s_add_u32 m0, s38, 83968\n" \
"v_mfma_f32_16x16x32_bf16 v[0:3], v[128:131], v[200:203], v[0:3]\n" \
"ds_read_b64_tr_b16 v[152:153], v237 offset:16384\n" \
"v_cvt_pk_bf16_f32 v215, v210, v211\n" \
"v_mfma_f32_16x16x32_bf16 v[4:7], v[128:131], v[204:207], v[4:7]\n" \
"ds_read_b64_tr_b16 v[154:155], v237 offset:18432\n" \
"buffer_load_dwordx4 v227, s[20:23], s52 offen lds\n" \
"v_mfma_f32_16x16x32_bf16 v[8:11], v[132:135], v[200:203], v[8:11]\n" \
"ds_read_b64_tr_b16 v[156:157], v238 offset:16384\n" \
"v_mfma_f32_16x16x32_bf16 v[12:15], v[132:135], v[204:207], v[12:15]\n" \
"ds_read_b64_tr_b16 v[158:159], v238 offset:18432\n" \
"s_waitcnt lgkmcnt(8)\n" \
"s_add_u32 s52, s40, 3457024\n" \
"v_mfma_f32_16x16x32_bf16 v[16:19], v[136:139], v[200:203], v[16:19]\n" \
"ds_read_b64_tr_b16 v[128:129], v231 offset:16640\n" \
"s_add_u32 m0, s38, 84992\n" \
"v_mfma_f32_16x16x32_bf16 v[20:23], v[136:139], v[204:207], v[20:23]\n" \
"ds_read_b64_tr_b16 v[130:131], v231 offset:18688\n" \
"buffer_store_dwordx4 v[216:219], v224, %[rs], s83 offen offset:1024 sc1\n" \
"v_accvgpr_read_b32 v208, a128\n" \
"v_mfma_f32_16x16x32_bf16 v[24:27], v[140:143], v[200:203], v[24:27]\n" \
"ds_read_b64_tr_b16 v[132:133], v232 offset:16640\n" \
"buffer_load_dwordx4 v228, s[20:23], s52 offen lds\n" \
"v_accvgpr_read_b32 v209, a129\n" \
"v_mfma_f32_16x16x32_bf16 v[28:31], v[140:143], v[204:207], v[28:31]\n" \
"ds_read_b64_tr_b16 v[134:135], v232 offset:18688\n" \
"s_waitcnt lgkmcnt(8)\n" \
"v_accvgpr_read_b32 v210, a130\n" \
"v_mfma_f32_16x16x32_bf16 v[32:35], v[144:147], v[200:203], v[32:35]\n" \
"ds_read_b64_tr_b16 v[136:137], v233 offset:16640\n" \
"v_accvgpr_read_b32 v211, a131\n" \
"v_cvt_pk_bf16_f32 v216, v208, v209\n" \
"v_mfma_f32_16x16x32_bf16 v[36:39], v[144:147], v[204:207], v[36:39]\n" \
"ds_read_b64_tr_b16 v[138:139], v233 offset:18688\n" \
"s_add_u32 s80, s58, 10240\n" \
"v_cvt_pk_bf16_f32 v217, v210, v211\n" \
"v_mfma_f32_16x16x32_bf16 v[40:43], v[148:151], v[200:203], v[40:43]\n" \
"ds_read_b64_tr_b16 v[140:141], v234 offset:16640\n" \
"v_accvgpr_read_b32 v208, a160\n" \
"v_accvgpr_read_b32 v209, a161\n" \
"v_mfma_f32_16x16x32_bf16 v[44:47], v[148:151], v[204:207], v[44:47]\n" \
"ds_read_b64_tr_b16 v[142:143], v234 offset:18688\n" \
"s_waitcnt lgkmcnt(8)\n" \
"buffer_load_dwordx4 v[192:195], v224, %[rs], s80 offen sc1\n" \
"v_mfma_f32_16x16x32_bf16 v[48:51], v[152:155], v[200:203], v[48:51]\n" \
"ds_read_b64_tr_b16 v[144:145], v235 offset:16640\n" \
"v_accvgpr_read_b32 v210, a162\n" \
"v_accvgpr_read_b32 v211, a163\n" \
"v_mfma_f32_16x16x32_bf16 v[52:55], v[152:155], v[204:207], v[52:55]\n" \
"ds_read_b64_tr_b16 v[146:147], v235 offset:18688\n" \
"v_cvt_pk_bf16_f32 v218, v208, v209\n" \
"v_cvt_pk_bf16_f32 v219, v210, v211\n" \
"v_mfma_f32_16x16x32_bf16 v[56:59], v[156:159], v[200:203], v[56:59]\n" \
"ds_read_b64_tr_b16 v[148:149], v236 offset:16640\n" \
"v_mfma_f32_16x16x32_bf16 v[60:63], v[156:159], v[204:207], v[60:63]\n" \
"ds_read_b64_tr_b16 v[150:151], v236 offset:18688\n" \
"s_waitcnt lgkmcnt(8)\n" \
"s_add_u32 s80, s71, 10240\n" \
"v_mfma_f32_16x16x32_bf16 v[64:67], v[128:131], v[200:203], v[64:67]\n" \
"ds_read_b64_tr_b16 v[152:153], v237 offset:16640\n" \
"v_mfma_f32_16x16x32_bf16 v[68:71], v[128:131], v[204:207], v[68:71]\n" \
"ds_read_b64_tr_b16 v[154:155], v237 offset:18688\n" \
"buffer_load_dwordx4 v[196:199], v224, %[rs], s80 offen sc1\n" \
"v_mfma_f32_16x16x32_bf16 v[72:75], v[132:135], v[200:203], v[72:75]\n" \
"ds_read_b64_tr_b16 v[156:157], v238 offset:16640\n" \
"v_mfma_f32_16x16x32_bf16 v[76:79], v[132:135], v[204:207], v[76:79]\n" \
"ds_read_b64_tr_b16 v[158:159], v238 offset:18688\n" \
"s_waitcnt lgkmcnt(8)\n" \
"s_waitcnt vmcnt(32)\n" \
"s_barrier\n" \
"v_mfma_f32_16x16x32_bf16 v[80:83], v[136:139], v[200:203], v[80:83]\n" \
"ds_read_b64_tr_b16 v[128:129], v231 offset:32768\n" \
"v_mfma_f32_16x16x32_bf16 v[84:87], v[136:139], v[204:207], v[84:87]\n" \
"ds_read_b64_tr_b16 v[130:131], v231 offset:34816\n" \
"buffer_store_dwordx4 v[220:223], v224, %[rs], s83 offen offset:2048 sc1\n" \
"v_accvgpr_read_b32 v208, a132\n" \
"v_mfma_f32_16x16x32_bf16 v[88:91], v[140:143], v[200:203], v[88:91]\n" \
"ds_read_b64_tr_b16 v[132:133], v232 offset:32768\n" \
"v_accvgpr_read_b32 v209, a133\n" \
"v_accvgpr_read_b32 v210, a134\n" \
"v_mfma_f32_16x16x32_bf16 v[92:95], v[140:143], v[204:207], v[92:95]\n" \
"ds_read_b64_tr_b16 v[134:135], v232 offset:34816\n" \
"s_waitcnt lgkmcnt(8)\n" \
"v_accvgpr_read_b32 v211, a135\n" \
"v_mfma_f32_16x16x32_bf16 v[96:99], v[144:147], v[200:203], v[96:99]\n" \
"ds_read_b64_tr_b16 v[136:137], v233 offset:32768\n" \
"v_cvt_pk_bf16_f32 v220, v208, v209\n" \
"v_cvt_pk_bf16_f32 v221, v210, v211\n" \
"v_mfma_f32_16x16x32_bf16 v[100:103], v[144:147], v[204:207], v[100:103]\n" \
"ds_read_b64_tr_b16 v[138:139], v233 offset:34816\n" \
"v_accvgpr_read_b32 v208, a164\n" \
"v_accvgpr_read_b32 v209, a165\n" \
"v_mfma_f32_16x16x32_bf16 v[104:107], v[148:151], v[200:203], v[104:107]\n" \
"ds_read_b64_tr_b16 v[140:141], v234 offset:32768\n" \
"v_accvgpr_read_b32 v210, a166\n" \
"v_accvgpr_read_b32 v211, a167\n" \
"v_mfma_f32_16x16x32_bf16 v[108:111], v[148:151], v[204:207], v[108:111]\n" \
"ds_read_b64_tr_b16 v[142:143], v234 offset:34816\n" \
"s_waitcnt lgkmcnt(8)\n" \
"v_cvt_pk_bf16_f32 v222, v208, v209\n" \
"v_mfma_f32_16x16x32_bf16 v[112:115], v[152:155], v[200:203], v[112:115]\n" \
"ds_read_b64_tr_b16 v[144:145], v235 offset:32768\n" \
"v_cvt_pk_bf16_f32 v223, v210, v211\n" \
"v_mfma_f32_16x16x32_bf16 v[116:119], v[152:155], v[204:207], v[116:119]\n" \
"ds_read_b64_tr_b16 v[146:147], v235 offset:34816\n" \
"v_mfma_f32_16x16x32_bf16 v[120:123], v[156:159], v[200:203], v[120:123]\n" \
"ds_read_b64_tr_b16 v[148:149], v236 offset:32768\n" \
"v_mfma_f32_16x16x32_bf16 v[124:127], v[156:159], v[204:207], v[124:127]\n" \
"ds_read_b64_tr_b16 v[150:151], v236 offset:34816\n" \
"s_waitcnt lgkmcnt(8)\n" \
"v_mfma_f32_16x16x32_bf16 v[0:3], v[128:131], v[160:163], v[0:3]\n" \
"ds_read_b64_tr_b16 v[152:153], v237 offset:32768\n" \
"v_mfma_f32_16x16x32_bf16 v[4:7], v[128:131], v[164:167], v[4:7]\n" \
"ds_read_b64_tr_b16 v[154:155], v237 offset:34816\n" \
"v_mfma_f32_16x16x32_bf16 v[8:11], v[132:135], v[160:163], v[8:11]\n" \
"ds_read_b64_tr_b16 v[156:157], v238 offset:32768\n" \
"v_mfma_f32_16x16x32_bf16 v[12:15], v[132:135], v[164:167], v[12:15]\n" \
"ds_read_b64_tr_b16 v[158:159], v238 offset:34816\n" \
"s_waitcnt lgkmcnt(8)\n" \
"buffer_store_dwordx4 v[212:215], v224, %[rs], s83 offen offset:3072 sc1\n" \
"v_mfma_f32_16x16x32_bf16 v[16:19], v[136:139], v[160:163], v[16:19]\n" \
"ds_read_b64_tr_b16 v[128:129], v231 offset:33024\n" \
"v_accvgpr_read_b32 v208, a136\n" \
"v_accvgpr_read_b32 v209, a137\n" \
"v_mfma_f32_16x16x32_bf16 v[20:23], v[136:139], v[164:167], v[20:23]\n" \
"ds_read_b64_tr_b16 v[130:131], v231 offset:35072\n" \
"v_accvgpr_read_b32 v210, a138\n" \
"v_accvgpr_read_b32 v211, a139\n" \
"v_mfma_f32_16x16x32_bf16 v[24:27], v[140:143], v[160:163], v[24:27]\n" \
"ds_read_b64_tr_b16 v[132:133], v232 offset:33024\n" \
"v_cvt_pk_bf16_f32 v212, v208, v209\n" \
"v_cvt_pk_bf16_f32 v213, v210, v211\n" \
"v_mfma_f32_16x16x32_bf16 v[28:31], v[140:143], v[164:167], v[28:31]\n" \
"ds_read_b64_tr_b16 v[134:135], v232 offset:35072\n" \
"s_waitcnt lgkmcnt(8)\n" \
"v_accvgpr_read_b32 v208, a168\n" \
"v_mfma_f32_16x16x32_bf16 v[32:35], v[144:147], v[160:163], v[32:35]\n" \
"ds_read_b64_tr_b16 v[136:137], v233 offset:33024\n" \
"v_accvgpr_read_b32 v209, a169\n" \
"v_accvgpr_read_b32 v210, a170\n" \
"v_mfma_f32_16x16x32_bf16 v[36:39], v[144:147], v[164:167], v[36:39]\n" \
"ds_read_b64_tr_b16 v[138:139], v233 offset:35072\n" \
"s_add_u32 s80, s58, 11264\n" \
"v_accvgpr_read_b32 v211, a171\n" \
"v_mfma_f32_16x16x32_bf16 v[40:43], v[148:151], v[160:163], v[40:43]\n" \
"ds_read_b64_tr_b16 v[140:141], v234 offset:33024\n" \
"v_cvt_pk_bf16_f32 v214, v208, v209\n" \
"v_cvt_pk_bf16_f32 v215, v210, v211\n" \
"v_mfma_f32_16x16x32_bf16 v[44:47], v[148:151], v[164:167], v[44:47]\n" \
"ds_read_b64_tr_b16 v[142:143], v234 offset:35072\n" \
"s_waitcnt lgkmcnt(8)\n" \
"buffer_load_dwordx4 v[200:203], v224, %[rs], s80 offen sc1\n" \
"v_mfma_f32_16x16x32_bf16 v[48:51], v[152:155], v[160:163], v[48:51]\n" \
"ds_read_b64_tr_b16 v[144:145], v235 offset:33024\n" \
"v_mfma_f32_16x16x32_bf16 v[52:55], v[152:155], v[164:167], v[52:55]\n" \
"ds_read_b64_tr_b16 v[146:147], v235 offset:35072\n" \
"v_mfma_f32_16x16x32_bf16 v[56:59], v[156:159], v[160:163], v[56:59]\n" \
"ds_read_b64_tr_b16 v[148:149], v236 offset:33024\n" \
"v_mfma_f32_16x16x32_bf16 v[60:63], v[156:159], v[164:167], v[60:63]\n" \
"ds_read_b64_tr_b16 v[150:151], v236 offset:35072\n" \
"s_waitcnt lgkmcnt(8)\n" \
"s_add_u32 s80, s71, 11264\n" \
"v_mfma_f32_16x16x32_bf16 v[64:67], v[128:131], v[160:163], v[64:67]\n" \
"ds_read_b64_tr_b16 v[152:153], v237 offset:33024\n" \
"v_mfma_f32_16x16x32_bf16 v[68:71], v[128:131], v[164:167], v[68:71]\n" \
"ds_read_b64_tr_b16 v[154:155], v237 offset:35072\n" \
"buffer_load_dwordx4 v[204:207], v224, %[rs], s80 offen sc1\n" \
"v_mfma_f32_16x16x32_bf16 v[72:75], v[132:135], v[160:163], v[72:75]\n" \
"ds_read_b64_tr_b16 v[156:157], v238 offset:33024\n" \
"v_mfma_f32_16x16x32_bf16 v[76:79], v[132:135], v[164:167], v[76:79]\n" \
"ds_read_b64_tr_b16 v[158:159], v238 offset:35072\n" \
"s_waitcnt lgkmcnt(8)\n" \
"s_waitcnt vmcnt(28)\n" \
"s_barrier\n" \
"v_mfma_f32_16x16x32_bf16 v[80:83], v[136:139], v[160:163], v[80:83]\n" \
"ds_read_b64_tr_b16 v[128:129], v231 offset:49152\n" \
"v_mfma_f32_16x16x32_bf16 v[84:87], v[136:139], v[164:167], v[84:87]\n" \
"ds_read_b64_tr_b16 v[130:131], v231 offset:51200\n" \
"s_add_u32 s83, s55, 32768\n" \
"buffer_store_dwordx4 v[216:219], v224, %[rs], s83 offen offset:0 sc1\n" \
"v_mfma_f32_16x16x32_bf16 v[88:91], v[140:143], v[160:163], v[88:91]\n" \
"ds_read_b64_tr_b16 v[132:133], v232 offset:49152\n" \
"v_accvgpr_read_b32 v208, a140\n" \
"v_accvgpr_read_b32 v209, a141\n" \
"v_mfma_f32_16x16x32_bf16 v[92:95], v[140:143], v[164:167], v[92:95]\n" \
"ds_read_b64_tr_b16 v[134:135], v232 offset:51200\n" \
"s_waitcnt lgkmcnt(8)\n" \
"v_accvgpr_read_b32 v210, a142\n" \
"v_mfma_f32_16x16x32_bf16 v[96:99], v[144:147], v[160:163], v[96:99]\n" \
"ds_read_b64_tr_b16 v[136:137], v233 offset:49152\n" \
"v_accvgpr_read_b32 v211, a143\n" \
"v_cvt_pk_bf16_f32 v216, v208, v209\n" \
"v_mfma_f32_16x16x32_bf16 v[100:103], v[144:147], v[164:167], v[100:103]\n" \
"ds_read_b64_tr_b16 v[138:139], v233 offset:51200\n" \
"v_cvt_pk_bf16_f32 v217, v210, v211\n" \
"v_accvgpr_read_b32 v208, a172\n" \
"v_mfma_f32_16x16x32_bf16 v[104:107], v[148:151], v[160:163], v[104:107]\n" \
"ds_read_b64_tr_b16 v[140:141], v234 offset:49152\n" \
"v_accvgpr_read_b32 v209, a173\n" \
"v_accvgpr_read_b32 v210, a174\n" \
"v_mfma_f32_16x16x32_bf16 v[108:111], v[148:151], v[164:167], v[108:111]\n" \
"ds_read_b64_tr_b16 v[142:143], v234 offset:51200\n" \
"s_waitcnt lgkmcnt(8)\n" \
"v_accvgpr_read_b32 v211, a175\n" \
"v_mfma_f32_16x16x32_bf16 v[112:115], v[152:155], v[160:163], v[112:115]\n" \
"ds_read_b64_tr_b16 v[144:145], v235 offset:49152\n" \
"v_cvt_pk_bf16_f32 v218, v208, v209\n" \
"v_cvt_pk_bf16_f32 v219, v210, v211\n" \
"v_mfma_f32_16x16x32_bf16 v[116:119], v[152:155], v[164:167], v[116:119]\n" \
"ds_read_b64_tr_b16 v[146:147], v235 offset:51200\n" \
"v_mfma_f32_16x16x32_bf16 v[120:123], v[156:159], v[160:163], v[120:123]\n" \
"ds_read_b64_tr_b16 v[148:149], v236 offset:49152\n" \
"v_mfma_f32_16x16x32_bf16 v[124:127], v[156:159], v[164:167], v[124:127]\n" \
"ds_read_b64_tr_b16 v[150:151], v236 offset:51200\n" \
"s_waitcnt lgkmcnt(8)\n" \
"v_mfma_f32_16x16x32_bf16 v[0:3], v[128:131], v[168:171], v[0:3]\n" \
"ds_read_b64_tr_b16 v[152:153], v237 offset:49152\n" \
"v_mfma_f32_16x16x32_bf16 v[4:7], v[128:131], v[172:175], v[4:7]\n" \
"ds_read_b64_tr_b16 v[154:155], v237 offset:51200\n" \
"v_mfma_f32_16x16x32_bf16 v[8:11], v[132:135], v[168:171], v[8:11]\n" \
"ds_read_b64_tr_b16 v[156:157], v238 offset:49152\n" \
"v_mfma_f32_16x16x32_bf16 v[12:15], v[132:135], v[172:175], v[12:15]\n" \
"ds_read_b64_tr_b16 v[158:159], v238 offset:51200\n" \
"s_waitcnt lgkmcnt(8)\n" \
"buffer_store_dwordx4 v[220:223], v224, %[rs], s83 offen offset:1024 sc1\n" \
"v_mfma_f32_16x16x32_bf16 v[16:19], v[136:139], v[168:171], v[16:19]\n" \
"ds_read_b64_tr_b16 v[128:129], v231 offset:49408\n" \
"v_accvgpr_read_b32 v208, a144\n" \
"v_accvgpr_read_b32 v209, a145\n" \
"v_mfma_f32_16x16x32_bf16 v[20:23], v[136:139], v[172:175], v[20:23]\n" \
"ds_read_b64_tr_b16 v[130:131], v231 offset:51456\n" \
"v_accvgpr_read_b32 v210, a146\n" \
"v_accvgpr_read_b32 v211, a147\n" \
"v_mfma_f32_16x16x32_bf16 v[24:27], v[140:143], v[168:171], v[24:27]\n" \
"ds_read_b64_tr_b16 v[132:133], v232 offset:49408\n" \
"v_add_u32 v231, 0xfffe8000, v231\n" \
"v_cvt_pk_bf16_f32 v220, v208, v209\n" \
"v_mfma_f32_16x16x32_bf16 v[28:31], v[140:143], v[172:175], v[28:31]\n" \
"ds_read_b64_tr_b16 v[134:135], v232 offset:51456\n" \
"s_waitcnt lgkmcnt(8)\n" \
"v_cvt_pk_bf16_f32 v221, v210, v211\n" \
"v_mfma_f32_16x16x32_bf16 v[32:35], v[144:147], v[168:171], v[32:35]\n" \
"ds_read_b64_tr_b16 v[136:137], v233 offset:49408\n" \
"v_add_u32 v232, 0xfffe8000, v232\n" \
"v_accvgpr_read_b32 v208, a176\n" \
"v_mfma_f32_16x16x32_bf16 v[36:39], v[144:147], v[172:175], v[36:39]\n" \
"ds_read_b64_tr_b16 v[138:139], v233 offset:51456\n" \
"s_add_u32 s80, s58, 12288\n" \
"v_accvgpr_read_b32 v209, a177\n" \
"v_mfma_f32_16x16x32_bf16 v[40:43], v[148:151], v[168:171], v[40:43]\n" \
"ds_read_b64_tr_b16 v[140:141], v234 offset:49408\n" \
"v_add_u32 v233, 0xfffe8000, v233\n" \
"v_accvgpr_read_b32 v210, a178\n" \
"v_mfma_f32_16x16x32_bf16 v[44:47], v[148:151], v[172:175], v[44:47]\n" \
"ds_read_b64_tr_b16 v[142:143], v234 offset:51456\n" \
"s_waitcnt lgkmcnt(8)\n" \
"buffer_load_dwordx4 v[160:163], v224, %[rs], s80 offen sc1\n" \
"v_mfma_f32_16x16x32_bf16 v[48:51], v[152:155], v[168:171], v[48:51]\n" \
"ds_read_b64_tr_b16 v[144:145], v235 offset:49408\n" \
"v_add_u32 v234, 0xfffe8000, v234\n" \
"v_accvgpr_read_b32 v211, a179\n" \
"v_mfma_f32_16x16x32_bf16 v[52:55], v[152:155], v[172:175], v[52:55]\n" \
"ds_read_b64_tr_b16 v[146:147], v235 offset:51456\n" \
"v_cvt_pk_bf16_f32 v222, v208, v209\n" \
"v_cvt_pk_bf16_f32 v223, v210, v211\n" \
"v_mfma_f32_16x16x32_bf16 v[56:59], v[156:159], v[168:171], v[56:59]\n" \
"ds_read_b64_tr_b16 v[148:149], v236 offset:49408\n" \
"v_add_u32 v235, 0xfffe8000, v235\n" \
"v_mfma_f32_16x16x32_bf16 v[60:63], v[156:159], v[172:175], v[60:63]\n" \
"ds_read_b64_tr_b16 v[150:151], v236 offset:51456\n" \
"s_waitcnt lgkmcnt(8)\n" \
"s_add_u32 s80, s71, 12288\n" \
"v_mfma_f32_16x16x32_bf16 v[64:67], v[128:131], v[168:171], v[64:67]\n" \
"ds_read_b64_tr_b16 v[152:153], v237 offset:49408\n" \
"v_add_u32 v236, 0xfffe8000, v236\n" \
"v_mfma_f32_16x16x32_bf16 v[68:71], v[128:131], v[172:175], v[68:71]\n" \
"ds_read_b64_tr_b16 v[154:155], v237 offset:51456\n" \
"buffer_load_dwordx4 v[164:167], v224, %[rs], s80 offen sc1\n" \
"v_mfma_f32_16x16x32_bf16 v[72:75], v[132:135], v[168:171], v[72:75]\n" \
"ds_read_b64_tr_b16 v[156:157], v238 offset:49408\n" \
"v_add_u32 v237, 0xfffe8000, v237\n" \
"v_mfma_f32_16x16x32_bf16 v[76:79], v[132:135], v[172:175], v[76:79]\n" \
"ds_read_b64_tr_b16 v[158:159], v238 offset:51456\n" \
"s_waitcnt lgkmcnt(8)\n" \
"s_waitcnt vmcnt(24)\n" \
"s_barrier\n" \
"v_mfma_f32_16x16x32_bf16 v[80:83], v[136:139], v[168:171], v[80:83]\n" \
"ds_read_b64_tr_b16 v[128:129], v231 offset:0\n" \
"v_add_u32 v238, 0xfffe8000, v238\n" \
"v_mfma_f32_16x16x32_bf16 v[84:87], v[136:139], v[172:175], v[84:87]\n" \
"ds_read_b64_tr_b16 v[130:131], v231 offset:2048\n" \
"buffer_store_dwordx4 v[212:215], v224, %[rs], s83 offen offset:2048 sc1\n" \
"v_accvgpr_read_b32 v208, a148\n" \
"v_mfma_f32_16x16x32_bf16 v[88:91], v[140:143], v[168:171], v[88:91]\n" \
"ds_read_b64_tr_b16 v[132:133], v232 offset:0\n" \
"v_accvgpr_read_b32 v209, a149\n" \
"v_accvgpr_read_b32 v210, a150\n" \
"v_mfma_f32_16x16x32_bf16 v[92:95], v[140:143], v[172:175], v[92:95]\n" \
"ds_read_b64_tr_b16 v[134:135], v232 offset:2048\n" \
"s_waitcnt lgkmcnt(8)\n" \
"v_accvgpr_read_b32 v211, a151\n" \
"v_mfma_f32_16x16x32_bf16 v[96:99], v[144:147], v[168:171], v[96:99]\n" \
"ds_read_b64_tr_b16 v[136:137], v233 offset:0\n" \
"v_cvt_pk_bf16_f32 v212, v208, v209\n" \
"v_cvt_pk_bf16_f32 v213, v210, v211\n" \
"v_mfma_f32_16x16x32_bf16 v[100:103], v[144:147], v[172:175], v[100:103]\n" \
"ds_read_b64_tr_b16 v[138:139], v233 offset:2048\n" \
"v_accvgpr_read_b32 v208, a180\n" \
"v_accvgpr_read_b32 v209, a181\n" \
"v_mfma_f32_16x16x32_bf16 v[104:107], v[148:151], v[168:171], v[104:107]\n" \
"ds_read_b64_tr_b16 v[140:141], v234 offset:0\n" \
"v_accvgpr_read_b32 v210, a182\n" \
"v_accvgpr_read_b32 v211, a183\n" \
"v_mfma_f32_16x16x32_bf16 v[108:111], v[148:151], v[172:175], v[108:111]\n" \
"ds_read_b64_tr_b16 v[142:143], v234 offset:2048\n" \
"s_waitcnt lgkmcnt(8)\n" \
"v_cvt_pk_bf16_f32 v214, v208, v209\n" \
"v_mfma_f32_16x16x32_bf16 v[112:115], v[152:155], v[168:171], v[112:115]\n" \
"ds_read_b64_tr_b16 v[144:145], v235 offset:0\n" \
"v_cvt_pk_bf16_f32 v215, v210, v211\n" \
"v_mfma_f32_16x16x32_bf16 v[116:119], v[152:155], v[172:175], v[116:119]\n" \
"ds_read_b64_tr_b16 v[146:147], v235 offset:2048\n" \
"v_mfma_f32_16x16x32_bf16 v[120:123], v[156:159], v[168:171], v[120:123]\n" \
"ds_read_b64_tr_b16 v[148:149], v236 offset:0\n" \
"v_mfma_f32_16x16x32_bf16 v[124:127], v[156:159], v[172:175], v[124:127]\n" \
"ds_read_b64_tr_b16 v[150:151], v236 offset:2048\n" \
"s_waitcnt lgkmcnt(8)\n" \
"v_mfma_f32_16x16x32_bf16 v[0:3], v[128:131], v[176:179], v[0:3]\n" \
"ds_read_b64_tr_b16 v[152:153], v237 offset:0\n" \
"v_mfma_f32_16x16x32_bf16 v[4:7], v[128:131], v[180:183], v[4:7]\n" \
"ds_read_b64_tr_b16 v[154:155], v237 offset:2048\n" \
"v_mfma_f32_16x16x32_bf16 v[8:11], v[132:135], v[176:179], v[8:11]\n" \
"ds_read_b64_tr_b16 v[156:157], v238 offset:0\n" \
"v_mfma_f32_16x16x32_bf16 v[12:15], v[132:135], v[180:183], v[12:15]\n" \
"ds_read_b64_tr_b16 v[158:159], v238 offset:2048\n" \
"s_waitcnt lgkmcnt(8)\n" \
"buffer_store_dwordx4 v[216:219], v224, %[rs], s83 offen offset:3072 sc1\n" \
"v_mfma_f32_16x16x32_bf16 v[16:19], v[136:139], v[176:179], v[16:19]\n" \
"ds_read_b64_tr_b16 v[128:129], v231 offset:256\n" \
"v_accvgpr_read_b32 v208, a152\n" \
"v_accvgpr_read_b32 v209, a153\n" \
"v_mfma_f32_16x16x32_bf16 v[20:23], v[136:139], v[180:183], v[20:23]\n" \
"ds_read_b64_tr_b16 v[130:131], v231 offset:2304\n" \
"v_accvgpr_read_b32 v210, a154\n" \
"v_accvgpr_read_b32 v211, a155\n" \
"v_mfma_f32_16x16x32_bf16 v[24:27], v[140:143], v[176:179], v[24:27]\n" \
"ds_read_b64_tr_b16 v[132:133], v232 offset:256\n" \
"v_cvt_pk_bf16_f32 v216, v208, v209\n" \
"v_cvt_pk_bf16_f32 v217, v210, v211\n" \
"v_mfma_f32_16x16x32_bf16 v[28:31], v[140:143], v[180:183], v[28:31]\n" \
"ds_read_b64_tr_b16 v[134:135], v232 offset:2304\n" \
"s_waitcnt lgkmcnt(8)\n" \
"v_accvgpr_read_b32 v208, a184\n" \
"v_mfma_f32_16x16x32_bf16 v[32:35], v[144:147], v[176:179], v[32:35]\n" \
"ds_read_b64_tr_b16 v[136:137], v233 offset:256\n" \
"v_accvgpr_read_b32 v209, a185\n" \
"v_accvgpr_read_b32 v210, a186\n" \
"v_mfma_f32_16x16x32_bf16 v[36:39], v[144:147], v[180:183], v[36:39]\n" \
"ds_read_b64_tr_b16 v[138:139], v233 offset:2304\n" \
"s_add_u32 s80, s58, 13312\n" \
"v_accvgpr_read_b32 v211, a187\n" \
"v_mfma_f32_16x16x32_bf16 v[40:43], v[148:151], v[176:179], v[40:43]\n" \
"ds_read_b64_tr_b16 v[140:141], v234 offset:256\n" \
"v_cvt_pk_bf16_f32 v218, v208, v209\n" \
"v_cvt_pk_bf16_f32 v219, v210, v211\n" \
"v_mfma_f32_16x16x32_bf16 v[44:47], v[148:151], v[180:183], v[44:47]\n" \
"ds_read_b64_tr_b16 v[142:143], v234 offset:2304\n" \
"s_waitcnt lgkmcnt(8)\n" \
"buffer_load_dwordx4 v[168:171], v224, %[rs], s80 offen sc1\n" \
"v_mfma_f32_16x16x32_bf16 v[48:51], v[152:155], v[176:179], v[48:51]\n" \
"ds_read_b64_tr_b16 v[144:145], v235 offset:256\n" \
"v_mfma_f32_16x16x32_bf16 v[52:55], v[152:155], v[180:183], v[52:55]\n" \
"ds_read_b64_tr_b16 v[146:147], v235 offset:2304\n" \
"v_mfma_f32_16x16x32_bf16 v[56:59], v[156:159], v[176:179], v[56:59]\n" \
"ds_read_b64_tr_b16 v[148:149], v236 offset:256\n" \
"v_mfma_f32_16x16x32_bf16 v[60:63], v[156:159], v[180:183], v[60:63]\n" \
"ds_read_b64_tr_b16 v[150:151], v236 offset:2304\n" \
"s_waitcnt lgkmcnt(8)\n" \
"s_add_u32 s80, s71, 13312\n" \
"v_mfma_f32_16x16x32_bf16 v[64:67], v[128:131], v[176:179], v[64:67]\n" \
"ds_read_b64_tr_b16 v[152:153], v237 offset:256\n" \
"v_mfma_f32_16x16x32_bf16 v[68:71], v[128:131], v[180:183], v[68:71]\n" \
"ds_read_b64_tr_b16 v[154:155], v237 offset:2304\n" \
"buffer_load_dwordx4 v[172:175], v224, %[rs], s80 offen sc1\n" \
"v_mfma_f32_16x16x32_bf16 v[72:75], v[132:135], v[176:179], v[72:75]\n" \
"ds_read_b64_tr_b16 v[156:157], v238 offset:256\n" \
"v_mfma_f32_16x16x32_bf16 v[76:79], v[132:135], v[180:183], v[76:79]\n" \
"ds_read_b64_tr_b16 v[158:159], v238 offset:2304\n" \
"s_waitcnt lgkmcnt(8)\n" \
"s_waitcnt vmcnt(20)\n" \
"s_barrier\n" \
"v_mfma_f32_16x16x32_bf16 v[80:83], v[136:139], v[176:179], v[80:83]\n" \
"ds_read_b64_tr_b16 v[128:129], v231 offset:16384\n" \
"v_mfma_f32_16x16x32_bf16 v[84:87], v[136:139], v[180:183], v[84:87]\n" \
"ds_read_b64_tr_b16 v[130:131], v231 offset:18432\n" \
"s_add_u32 s83, s55, 36864\n" \
"buffer_store_dwordx4 v[220:223], v224, %[rs], s83 offen offset:0 sc1\n" \
"v_mfma_f32_16x16x32_bf16 v[88:91], v[140:143], v[176:179], v[88:91]\n" \
"ds_read_b64_tr_b16 v[132:133], v232 offset:16384\n" \
"v_accvgpr_read_b32 v208, a156\n" \
"v_accvgpr_read_b32 v209, a157\n" \
"v_mfma_f32_16x16x32_bf16 v[92:95], v[140:143], v[180:183], v[92:95]\n" \
"ds_read_b64_tr_b16 v[134:135], v232 offset:18432\n" \
"s_waitcnt lgkmcnt(8)\n" \
"v_accvgpr_read_b32 v210, a158\n" \
"v_mfma_f32_16x16x32_bf16 v[96:99], v[144:147], v[176:179], v[96:99]\n" \
"ds_read_b64_tr_b16 v[136:137], v233 offset:16384\n" \
"v_accvgpr_read_b32 v211, a159\n" \
"v_cvt_pk_bf16_f32 v220, v208, v209\n" \
"v_mfma_f32_16x16x32_bf16 v[100:103], v[144:147], v[180:183], v[100:103]\n" \
"ds_read_b64_tr_b16 v[138:139], v233 offset:18432\n" \
"v_cvt_pk_bf16_f32 v221, v210, v211\n" \
"v_accvgpr_read_b32 v208, a188\n" \
"v_mfma_f32_16x16x32_bf16 v[104:107], v[148:151], v[176:179], v[104:107]\n" \
"ds_read_b64_tr_b16 v[140:141], v234 offset:16384\n" \
"v_accvgpr_read_b32 v209, a189\n" \
"v_accvgpr_read_b32 v210, a190\n" \
"v_mfma_f32_16x16x32_bf16 v[108:111], v[148:151], v[180:183], v[108:111]\n" \
"ds_read_b64_tr_b16 v[142:143], v234 offset:18432\n" \
"s_waitcnt lgkmcnt(8)\n" \
"v_accvgpr_read_b32 v211, a191\n" \
"v_mfma_f32_16x16x32_bf16 v[112:115], v[152:155], v[176:179], v[112:115]\n" \
"ds_read_b64_tr_b16 v[144:145], v235 offset:16384\n" \
"v_cvt_pk_bf16_f32 v222, v208, v209\n" \
"v_cvt_pk_bf16_f32 v223, v210, v211\n" \
"v_mfma_f32_16x16x32_bf16 v[116:119], v[152:155], v[180:183], v[116:119]\n" \
"ds_read_b64_tr_b16 v[146:147], v235 offset:18432\n" \
"v_mfma_f32_16x16x32_bf16 v[120:123], v[156:159], v[176:179], v[120:123]\n" \
"ds_read_b64_tr_b16 v[148:149], v236 offset:16384\n" \
"v_mfma_f32_16x16x32_bf16 v[124:127], v[156:159], v[180:183], v[124:127]\n" \
"ds_read_b64_tr_b16 v[150:151], v236 offset:18432\n" \
"s_waitcnt lgkmcnt(8)\n" \
"v_mfma_f32_16x16x32_bf16 v[0:3], v[128:131], v[184:187], v[0:3]\n" \
"ds_read_b64_tr_b16 v[152:153], v237 offset:16384\n" \
"v_mfma_f32_16x16x32_bf16 v[4:7], v[128:131], v[188:191], v[4:7]\n" \
"ds_read_b64_tr_b16 v[154:155], v237 offset:18432\n" \
"v_mfma_f32_16x16x32_bf16 v[8:11], v[132:135], v[184:187], v[8:11]\n" \
"ds_read_b64_tr_b16 v[156:157], v238 offset:16384\n" \
"v_mfma_f32_16x16x32_bf16 v[12:15], v[132:135], v[188:191], v[12:15]\n" \
"ds_read_b64_tr_b16 v[158:159], v238 offset:18432\n" \
"s_waitcnt lgkmcnt(8)\n" \
"buffer_store_dwordx4 v[212:215], v224, %[rs], s83 offen offset:1024 sc1\n" \
"v_mfma_f32_16x16x32_bf16 v[16:19], v[136:139], v[184:187], v[16:19]\n" \
"ds_read_b64_tr_b16 v[128:129], v231 offset:16640\n" \
"v_accvgpr_read_b32 v208, a192\n" \
"v_accvgpr_read_b32 v209, a193\n" \
"v_mfma_f32_16x16x32_bf16 v[20:23], v[136:139], v[188:191], v[20:23]\n" \
"ds_read_b64_tr_b16 v[130:131], v231 offset:18688\n" \
"v_accvgpr_read_b32 v210, a194\n" \
"v_accvgpr_read_b32 v211, a195\n" \
"v_mfma_f32_16x16x32_bf16 v[24:27], v[140:143], v[184:187], v[24:27]\n" \
"ds_read_b64_tr_b16 v[132:133], v232 offset:16640\n" \
"v_cvt_pk_bf16_f32 v212, v208, v209\n" \
"v_cvt_pk_bf16_f32 v213, v210, v211\n" \
"v_mfma_f32_16x16x32_bf16 v[28:31], v[140:143], v[188:191], v[28:31]\n" \
"ds_read_b64_tr_b16 v[134:135], v232 offset:18688\n" \
"s_waitcnt lgkmcnt(8)\n" \
"v_accvgpr_read_b32 v208, a224\n" \
"v_mfma_f32_16x16x32_bf16 v[32:35], v[144:147], v[184:187], v[32:35]\n" \
"ds_read_b64_tr_b16 v[136:137], v233 offset:16640\n" \
"v_accvgpr_read_b32 v209, a225\n" \
"v_accvgpr_read_b32 v210, a226\n" \
"v_mfma_f32_16x16x32_bf16 v[36:39], v[144:147], v[188:191], v[36:39]\n" \
"ds_read_b64_tr_b16 v[138:139], v233 offset:18688\n" \
"v_accvgpr_read_b32 v211, a227\n" \
"v_cvt_pk_bf16_f32 v214, v208, v209\n" \
"v_mfma_f32_16x16x32_bf16 v[40:43], v[148:151], v[184:187], v[40:43]\n" \
"ds_read_b64_tr_b16 v[140:141], v234 offset:16640\n" \
"v_cvt_pk_bf16_f32 v215, v210, v211\n" \
"v_mfma_f32_16x16x32_bf16 v[44:47], v[148:151], v[188:191], v[44:47]\n" \
"ds_read_b64_tr_b16 v[142:143], v234 offset:18688\n" \
"s_waitcnt lgkmcnt(8)\n" \
"v_mfma_f32_16x16x32_bf16 v[48:51], v[152:155], v[184:187], v[48:51]\n" \
"ds_read_b64_tr_b16 v[144:145], v235 offset:16640\n" \
"v_mfma_f32_16x16x32_bf16 v[52:55], v[152:155], v[188:191], v[52:55]\n" \
"ds_read_b64_tr_b16 v[146:147], v235 offset:18688\n" \
"v_mfma_f32_16x16x32_bf16 v[56:59], v[156:159], v[184:187], v[56:59]\n" \
"ds_read_b64_tr_b16 v[148:149], v236 offset:16640\n" \
"v_mfma_f32_16x16x32_bf16 v[60:63], v[156:159], v[188:191], v[60:63]\n" \
"ds_read_b64_tr_b16 v[150:151], v236 offset:18688\n" \
"s_waitcnt lgkmcnt(8)\n" \
"v_mfma_f32_16x16x32_bf16 v[64:67], v[128:131], v[184:187], v[64:67]\n" \
"ds_read_b64_tr_b16 v[152:153], v237 offset:16640\n" \
"v_mfma_f32_16x16x32_bf16 v[68:71], v[128:131], v[188:191], v[68:71]\n" \
"ds_read_b64_tr_b16 v[154:155], v237 offset:18688\n" \
"buffer_store_dwordx4 v[216:219], v224, %[rs], s83 offen offset:2048 sc1\n" \
"v_accvgpr_read_b32 v208, a196\n" \
"v_mfma_f32_16x16x32_bf16 v[72:75], v[132:135], v[184:187], v[72:75]\n" \
"ds_read_b64_tr_b16 v[156:157], v238 offset:16640\n" \
"v_accvgpr_read_b32 v209, a197\n" \
"v_accvgpr_read_b32 v210, a198\n" \
"v_mfma_f32_16x16x32_bf16 v[76:79], v[132:135], v[188:191], v[76:79]\n" \
"ds_read_b64_tr_b16 v[158:159], v238 offset:18688\n" \
"s_waitcnt lgkmcnt(8)\n" \
"v_accvgpr_read_b32 v211, a199\n" \
"s_waitcnt vmcnt(15)\n" \
"s_barrier\n" \
"v_mfma_f32_16x16x32_bf16 v[80:83], v[136:139], v[184:187], v[80:83]\n" \
"ds_read_b64_tr_b16 v[128:129], v231 offset:32768\n" \
"v_cvt_pk_bf16_f32 v216, v208, v209\n" \
"v_cvt_pk_bf16_f32 v217, v210, v211\n" \
"v_mfma_f32_16x16x32_bf16 v[84:87], v[136:139], v[188:191], v[84:87]\n" \
"ds_read_b64_tr_b16 v[130:131], v231 offset:34816\n" \
"v_accvgpr_read_b32 v208, a228\n" \
"v_accvgpr_read_b32 v209, a229\n" \
"v_mfma_f32_16x16x32_bf16 v[88:91], v[140:143], v[184:187], v[88:91]\n" \
"ds_read_b64_tr_b16 v[132:133], v232 offset:32768\n" \
"v_accvgpr_read_b32 v210, a230\n" \
"v_accvgpr_read_b32 v211, a231\n" \
"v_mfma_f32_16x16x32_bf16 v[92:95], v[140:143], v[188:191], v[92:95]\n" \
"ds_read_b64_tr_b16 v[134:135], v232 offset:34816\n" \
"s_waitcnt lgkmcnt(8)\n" \
"v_cvt_pk_bf16_f32 v218, v208, v209\n" \
"v_mfma_f32_16x16x32_bf16 v[96:99], v[144:147], v[184:187], v[96:99]\n" \
"ds_read_b64_tr_b16 v[136:137], v233 offset:32768\n" \
"v_cvt_pk_bf16_f32 v219, v210, v211\n" \
"v_mfma_f32_16x16x32_bf16 v[100:103], v[144:147], v[188:191], v[100:103]\n" \
"ds_read_b64_tr_b16 v[138:139], v233 offset:34816\n" \
"v_mfma_f32_16x16x32_bf16 v[104:107], v[148:151], v[184:187], v[104:107]\n" \
"ds_read_b64_tr_b16 v[140:141], v234 offset:32768\n" \
"v_mfma_f32_16x16x32_bf16 v[108:111], v[148:151], v[188:191], v[108:111]\n" \
"ds_read_b64_tr_b16 v[142:143], v234 offset:34816\n" \
"s_waitcnt lgkmcnt(8)\n" \
"v_mfma_f32_16x16x32_bf16 v[112:115], v[152:155], v[184:187], v[112:115]\n" \
"ds_read_b64_tr_b16 v[144:145], v235 offset:32768\n" \
"v_mfma_f32_16x16x32_bf16 v[116:119], v[152:155], v[188:191], v[116:119]\n" \
"ds_read_b64_tr_b16 v[146:147], v235 offset:34816\n" \
"v_mfma_f32_16x16x32_bf16 v[120:123], v[156:159], v[184:187], v[120:123]\n" \
"ds_read_b64_tr_b16 v[148:149], v236 offset:32768\n" \
"v_mfma_f32_16x16x32_bf16 v[124:127], v[156:159], v[188:191], v[124:127]\n" \
"ds_read_b64_tr_b16 v[150:151], v236 offset:34816\n" \
"s_waitcnt lgkmcnt(8)\n" \
"buffer_store_dwordx4 v[220:223], v224, %[rs], s83 offen offset:3072 sc1\n" \
"v_mfma_f32_16x16x32_bf16 v[0:3], v[128:131], v[192:195], v[0:3]\n" \
"ds_read_b64_tr_b16 v[152:153], v237 offset:32768\n" \
"v_accvgpr_read_b32 v208, a200\n" \
"v_accvgpr_read_b32 v209, a201\n" \
"v_mfma_f32_16x16x32_bf16 v[4:7], v[128:131], v[196:199], v[4:7]\n" \
"ds_read_b64_tr_b16 v[154:155], v237 offset:34816\n" \
"v_accvgpr_read_b32 v210, a202\n" \
"v_accvgpr_read_b32 v211, a203\n" \
"v_mfma_f32_16x16x32_bf16 v[8:11], v[132:135], v[192:195], v[8:11]\n" \
"ds_read_b64_tr_b16 v[156:157], v238 offset:32768\n" \
"v_cvt_pk_bf16_f32 v220, v208, v209\n" \
"v_cvt_pk_bf16_f32 v221, v210, v211\n" \
"v_mfma_f32_16x16x32_bf16 v[12:15], v[132:135], v[196:199], v[12:15]\n" \
"ds_read_b64_tr_b16 v[158:159], v238 offset:34816\n" \
"s_waitcnt lgkmcnt(8)\n" \
"v_accvgpr_read_b32 v208, a232\n" \
"v_mfma_f32_16x16x32_bf16 v[16:19], v[136:139], v[192:195], v[16:19]\n" \
"ds_read_b64_tr_b16 v[128:129], v231 offset:33024\n" \
"v_accvgpr_read_b32 v209, a233\n" \
"v_accvgpr_read_b32 v210, a234\n" \
"v_mfma_f32_16x16x32_bf16 v[20:23], v[136:139], v[196:199], v[20:23]\n" \
"ds_read_b64_tr_b16 v[130:131], v231 offset:35072\n" \
"v_accvgpr_read_b32 v211, a235\n" \
"v_cvt_pk_bf16_f32 v222, v208, v209\n" \
"v_mfma_f32_16x16x32_bf16 v[24:27], v[140:143], v[192:195], v[24:27]\n" \
"ds_read_b64_tr_b16 v[132:133], v232 offset:33024\n" \
"v_cvt_pk_bf16_f32 v223, v210, v211\n" \
"v_mfma_f32_16x16x32_bf16 v[28:31], v[140:143], v[196:199], v[28:31]\n" \
"ds_read_b64_tr_b16 v[134:135], v232 offset:35072\n" \
"s_waitcnt lgkmcnt(8)\n" \
"v_mfma_f32_16x16x32_bf16 v[32:35], v[144:147], v[192:195], v[32:35]\n" \
"ds_read_b64_tr_b16 v[136:137], v233 offset:33024\n" \
"v_mfma_f32_16x16x32_bf16 v[36:39], v[144:147], v[196:199], v[36:39]\n" \
"ds_read_b64_tr_b16 v[138:139], v233 offset:35072\n" \
"v_mfma_f32_16x16x32_bf16 v[40:43], v[148:151], v[192:195], v[40:43]\n" \
"ds_read_b64_tr_b16 v[140:141], v234 offset:33024\n" \
"v_mfma_f32_16x16x32_bf16 v[44:47], v[148:151], v[196:199], v[44:47]\n" \
"ds_read_b64_tr_b16 v[142:143], v234 offset:35072\n" \
"s_waitcnt lgkmcnt(8)\n" \
"v_mfma_f32_16x16x32_bf16 v[48:51], v[152:155], v[192:195], v[48:51]\n" \
"ds_read_b64_tr_b16 v[144:145], v235 offset:33024\n" \
"v_mfma_f32_16x16x32_bf16 v[52:55], v[152:155], v[196:199], v[52:55]\n" \
"ds_read_b64_tr_b16 v[146:147], v235 offset:35072\n" \
"s_add_u32 s83, s55, 49152\n" \
"buffer_store_dwordx4 v[212:215], v224, %[rs], s83 offen offset:0 sc1\n" \
"v_mfma_f32_16x16x32_bf16 v[56:59], v[156:159], v[192:195], v[56:59]\n" \
"ds_read_b64_tr_b16 v[148:149], v236 offset:33024\n" \
"v_accvgpr_read_b32 v208, a204\n" \
"v_accvgpr_read_b32 v209, a205\n" \
"v_mfma_f32_16x16x32_bf16 v[60:63], v[156:159], v[196:199], v[60:63]\n" \
"ds_read_b64_tr_b16 v[150:151], v236 offset:35072\n" \
"s_waitcnt lgkmcnt(8)\n" \
"v_accvgpr_read_b32 v210, a206\n" \
"v_mfma_f32_16x16x32_bf16 v[64:67], v[128:131], v[192:195], v[64:67]\n" \
"ds_read_b64_tr_b16 v[152:153], v237 offset:33024\n" \
"v_accvgpr_read_b32 v211, a207\n" \
"v_cvt_pk_bf16_f32 v212, v208, v209\n" \
"v_mfma_f32_16x16x32_bf16 v[68:71], v[128:131], v[196:199], v[68:71]\n" \
"ds_read_b64_tr_b16 v[154:155], v237 offset:35072\n" \
"v_cvt_pk_bf16_f32 v213, v210, v211\n" \
"v_accvgpr_read_b32 v208, a236\n" \
"v_mfma_f32_16x16x32_bf16 v[72:75], v[132:135], v[192:195], v[72:75]\n" \
"ds_read_b64_tr_b16 v[156:157], v238 offset:33024\n" \
"v_accvgpr_read_b32 v209, a237\n" \
"v_accvgpr_read_b32 v210, a238\n" \
"v_mfma_f32_16x16x32_bf16 v[76:79], v[132:135], v[196:199], v[76:79]\n" \
"ds_read_b64_tr_b16 v[158:159], v238 offset:35072\n" \
"s_waitcnt lgkmcnt(8)\n" \
"v_accvgpr_read_b32 v211, a239\n" \
"s_waitcnt vmcnt(13)\n" \
"s_barrier\n" \
"v_mfma_f32_16x16x32_bf16 v[80:83], v[136:139], v[192:195], v[80:83]\n" \
"ds_read_b64_tr_b16 v[128:129], v231 offset:49152\n" \
"v_cvt_pk_bf16_f32 v214, v208, v209\n" \
"v_cvt_pk_bf16_f32 v215, v210, v211\n" \
"v_mfma_f32_16x16x32_bf16 v[84:87], v[136:139], v[196:199], v[84:87]\n" \
"ds_read_b64_tr_b16 v[130:131], v231 offset:51200\n" \
"v_mfma_f32_16x16x32_bf16 v[88:91], v[140:143], v[192:195], v[88:91]\n" \
"ds_read_b64_tr_b16 v[132:133], v232 offset:49152\n" \
"v_mfma_f32_16x16x32_bf16 v[92:95], v[140:143], v[196:199], v[92:95]\n" \
"ds_read_b64_tr_b16 v[134:135], v232 offset:51200\n" \
"s_waitcnt lgkmcnt(8)\n" \
"v_mfma_f32_16x16x32_bf16 v[96:99], v[144:147], v[192:195], v[96:99]\n" \
"ds_read_b64_tr_b16 v[136:137], v233 offset:49152\n" \
"v_mfma_f32_16x16x32_bf16 v[100:103], v[144:147], v[196:199], v[100:103]\n" \
"ds_read_b64_tr_b16 v[138:139], v233 offset:51200\n" \
"v_mfma_f32_16x16x32_bf16 v[104:107], v[148:151], v[192:195], v[104:107]\n" \
"ds_read_b64_tr_b16 v[140:141], v234 offset:49152\n" \
"v_mfma_f32_16x16x32_bf16 v[108:111], v[148:151], v[196:199], v[108:111]\n" \
"ds_read_b64_tr_b16 v[142:143], v234 offset:51200\n" \
"s_waitcnt lgkmcnt(8)\n" \
"buffer_store_dwordx4 v[216:219], v224, %[rs], s83 offen offset:1024 sc1\n" \
"v_mfma_f32_16x16x32_bf16 v[112:115], v[152:155], v[192:195], v[112:115]\n" \
"ds_read_b64_tr_b16 v[144:145], v235 offset:49152\n" \
"v_accvgpr_read_b32 v208, a208\n" \
"v_accvgpr_read_b32 v209, a209\n" \
"v_mfma_f32_16x16x32_bf16 v[116:119], v[152:155], v[196:199], v[116:119]\n" \
"ds_read_b64_tr_b16 v[146:147], v235 offset:51200\n" \
"v_accvgpr_read_b32 v210, a210\n" \
"v_accvgpr_read_b32 v211, a211\n" \
"v_mfma_f32_16x16x32_bf16 v[120:123], v[156:159], v[192:195], v[120:123]\n" \
"ds_read_b64_tr_b16 v[148:149], v236 offset:49152\n" \
"v_cvt_pk_bf16_f32 v216, v208, v209\n" \
"v_cvt_pk_bf16_f32 v217, v210, v211\n" \
"v_mfma_f32_16x16x32_bf16 v[124:127], v[156:159], v[196:199], v[124:127]\n" \
"ds_read_b64_tr_b16 v[150:151], v236 offset:51200\n" \
"s_waitcnt lgkmcnt(8)\n" \
"v_accvgpr_read_b32 v208, a240\n" \
"v_mfma_f32_16x16x32_bf16 v[0:3], v[128:131], v[200:203], v[0:3]\n" \
"ds_read_b64_tr_b16 v[152:153], v237 offset:49152\n" \
"v_accvgpr_read_b32 v209, a241\n" \
"v_accvgpr_read_b32 v210, a242\n" \
"v_mfma_f32_16x16x32_bf16 v[4:7], v[128:131], v[204:207], v[4:7]\n" \
"ds_read_b64_tr_b16 v[154:155], v237 offset:51200\n" \
"v_accvgpr_read_b32 v211, a243\n" \
"v_cvt_pk_bf16_f32 v218, v208, v209\n" \
"v_mfma_f32_16x16x32_bf16 v[8:11], v[132:135], v[200:203], v[8:11]\n" \
"ds_read_b64_tr_b16 v[156:157], v238 offset:49152\n" \
"v_cvt_pk_bf16_f32 v219, v210, v211\n" \
"v_mfma_f32_16x16x32_bf16 v[12:15], v[132:135], v[204:207], v[12:15]\n" \
"ds_read_b64_tr_b16 v[158:159], v238 offset:51200\n" \
"s_waitcnt lgkmcnt(8)\n" \
"v_mfma_f32_16x16x32_bf16 v[16:19], v[136:139], v[200:203], v[16:19]\n" \
"ds_read_b64_tr_b16 v[128:129], v231 offset:49408\n" \
"v_mfma_f32_16x16x32_bf16 v[20:23], v[136:139], v[204:207], v[20:23]\n" \
"ds_read_b64_tr_b16 v[130:131], v231 offset:51456\n" \
"v_mfma_f32_16x16x32_bf16 v[24:27], v[140:143], v[200:203], v[24:27]\n" \
"ds_read_b64_tr_b16 v[132:133], v232 offset:49408\n" \
"v_add_u32 v231, 0x10000, v231\n" \
"v_mfma_f32_16x16x32_bf16 v[28:31], v[140:143], v[204:207], v[28:31]\n" \
"ds_read_b64_tr_b16 v[134:135], v232 offset:51456\n" \
"s_waitcnt lgkmcnt(8)\n" \
"v_mfma_f32_16x16x32_bf16 v[32:35], v[144:147], v[200:203], v[32:35]\n" \
"ds_read_b64_tr_b16 v[136:137], v233 offset:49408\n" \
"v_add_u32 v232, 0x10000, v232\n" \
"v_mfma_f32_16x16x32_bf16 v[36:39], v[144:147], v[204:207], v[36:39]\n" \
"ds_read_b64_tr_b16 v[138:139], v233 offset:51456\n" \
"buffer_store_dwordx4 v[220:223], v224, %[rs], s83 offen offset:2048 sc1\n" \
"v_accvgpr_read_b32 v208, a212\n" \
"v_mfma_f32_16x16x32_bf16 v[40:43], v[148:151], v[200:203], v[40:43]\n" \
"ds_read_b64_tr_b16 v[140:141], v234 offset:49408\n" \
"v_add_u32 v233, 0x10000, v233\n" \
"v_accvgpr_read_b32 v209, a213\n" \
"v_mfma_f32_16x16x32_bf16 v[44:47], v[148:151], v[204:207], v[44:47]\n" \
"ds_read_b64_tr_b16 v[142:143], v234 offset:51456\n" \
"s_waitcnt lgkmcnt(8)\n" \
"v_accvgpr_read_b32 v210, a214\n" \
"v_mfma_f32_16x16x32_bf16 v[48:51], v[152:155], v[200:203], v[48:51]\n" \
"ds_read_b64_tr_b16 v[144:145], v235 offset:49408\n" \
"v_add_u32 v234, 0x10000, v234\n" \
"v_accvgpr_read_b32 v211, a215\n" \
"v_mfma_f32_16x16x32_bf16 v[52:55], v[152:155], v[204:207], v[52:55]\n" \
"ds_read_b64_tr_b16 v[146:147], v235 offset:51456\n" \
"v_cvt_pk_bf16_f32 v220, v208, v209\n" \
"v_cvt_pk_bf16_f32 v221, v210, v211\n" \
"v_mfma_f32_16x16x32_bf16 v[56:59], v[156:159], v[200:203], v[56:59]\n" \
"ds_read_b64_tr_b16 v[148:149], v236 offset:49408\n" \
"v_add_u32 v235, 0x10000, v235\n" \
"v_accvgpr_read_b32 v208, a244\n" \
"v_mfma_f32_16x16x32_bf16 v[60:63], v[156:159], v[204:207], v[60:63]\n" \
"ds_read_b64_tr_b16 v[150:151], v236 offset:51456\n" \
"s_waitcnt lgkmcnt(8)\n" \
"v_accvgpr_read_b32 v209, a245\n" \
"v_mfma_f32_16x16x32_bf16 v[64:67], v[128:131], v[200:203], v[64:67]\n" \
"ds_read_b64_tr_b16 v[152:153], v237 offset:49408\n" \
"v_add_u32 v236, 0x10000, v236\n" \
"v_accvgpr_read_b32 v210, a246\n" \
"v_mfma_f32_16x16x32_bf16 v[68:71], v[128:131], v[204:207], v[68:71]\n" \
"ds_read_b64_tr_b16 v[154:155], v237 offset:51456\n" \
"v_accvgpr_read_b32 v211, a247\n" \
"v_cvt_pk_bf16_f32 v222, v208, v209\n" \
"v_mfma_f32_16x16x32_bf16 v[72:75], v[132:135], v[200:203], v[72:75]\n" \
"ds_read_b64_tr_b16 v[156:157], v238 offset:49408\n" \
"v_add_u32 v237, 0x10000, v237\n" \
"v_cvt_pk_bf16_f32 v223, v210, v211\n" \
"v_mfma_f32_16x16x32_bf16 v[76:79], v[132:135], v[204:207], v[76:79]\n" \
"ds_read_b64_tr_b16 v[158:159], v238 offset:51456\n" \
"s_waitcnt lgkmcnt(8)\n" \
"s_waitcnt vmcnt(11)\n" \
"s_barrier\n" \
"v_mfma_f32_16x16x32_bf16 v[80:83], v[136:139], v[200:203], v[80:83]\n" \
"ds_read_b64_tr_b16 v[128:129], v231 offset:0\n" \
"v_add_u32 v238, 0x10000, v238\n" \
"v_mfma_f32_16x16x32_bf16 v[84:87], v[136:139], v[204:207], v[84:87]\n" \
"ds_read_b64_tr_b16 v[130:131], v231 offset:2048\n" \
"v_mfma_f32_16x16x32_bf16 v[88:91], v[140:143], v[200:203], v[88:91]\n" \
"ds_read_b64_tr_b16 v[132:133], v232 offset:0\n" \
"v_mfma_f32_16x16x32_bf16 v[92:95], v[140:143], v[204:207], v[92:95]\n" \
"ds_read_b64_tr_b16 v[134:135], v232 offset:2048\n" \
"s_waitcnt lgkmcnt(8)\n" \
"buffer_store_dwordx4 v[212:215], v224, %[rs], s83 offen offset:3072 sc1\n" \
"v_mfma_f32_16x16x32_bf16 v[96:99], v[144:147], v[200:203], v[96:99]\n" \
"ds_read_b64_tr_b16 v[136:137], v233 offset:0\n" \
"v_accvgpr_read_b32 v208, a216\n" \
"v_accvgpr_read_b32 v209, a217\n" \
"v_mfma_f32_16x16x32_bf16 v[100:103], v[144:147], v[204:207], v[100:103]\n" \
"ds_read_b64_tr_b16 v[138:139], v233 offset:2048\n" \
"v_accvgpr_read_b32 v210, a218\n" \
"v_accvgpr_read_b32 v211, a219\n" \
"v_mfma_f32_16x16x32_bf16 v[104:107], v[148:151], v[200:203], v[104:107]\n" \
"ds_read_b64_tr_b16 v[140:141], v234 offset:0\n" \
"v_cvt_pk_bf16_f32 v212, v208, v209\n" \
"v_cvt_pk_bf16_f32 v213, v210, v211\n" \
"v_mfma_f32_16x16x32_bf16 v[108:111], v[148:151], v[204:207], v[108:111]\n" \
"ds_read_b64_tr_b16 v[142:143], v234 offset:2048\n" \
"s_waitcnt lgkmcnt(8)\n" \
"v_accvgpr_read_b32 v208, a248\n" \
"v_mfma_f32_16x16x32_bf16 v[112:115], v[152:155], v[200:203], v[112:115]\n" \
"ds_read_b64_tr_b16 v[144:145], v235 offset:0\n" \
"v_accvgpr_read_b32 v209, a249\n" \
"v_accvgpr_read_b32 v210, a250\n" \
"v_mfma_f32_16x16x32_bf16 v[116:119], v[152:155], v[204:207], v[116:119]\n" \
"ds_read_b64_tr_b16 v[146:147], v235 offset:2048\n" \
"v_accvgpr_read_b32 v211, a251\n" \
"v_cvt_pk_bf16_f32 v214, v208, v209\n" \
"v_mfma_f32_16x16x32_bf16 v[120:123], v[156:159], v[200:203], v[120:123]\n" \
"ds_read_b64_tr_b16 v[148:149], v236 offset:0\n" \
"v_cvt_pk_bf16_f32 v215, v210, v211\n" \
"v_mfma_f32_16x16x32_bf16 v[124:127], v[156:159], v[204:207], v[124:127]\n" \
"ds_read_b64_tr_b16 v[150:151], v236 offset:2048\n" \
"s_waitcnt lgkmcnt(8)\n" \
"s_mov_b32 s63, s54\n" \
"s_lshr_b32 s80, s17, 1\n" \
"s_lshl_b32 s62, s16, 3\n" \
"s_add_u32 s62, s62, s80\n" \
"s_lshl_b32 s60, s62, 2\n" \
"s_add_u32 s60, s60, s36\n" \
"s_mul_i32 s61, s60, 17408\n" \
"s_add_u32 s61, s61, 45088768\n" \
"s_lshl_b32 s60, s60, 14\n" \
"s_add_u32 s60, s60, 36700160\n" \
"s_cmp_eq_u32 s63, 0\n" \
"s_cselect_b32 s68, s60, 0x80000000\n" \
"s_lshl_b32 s59, s16, 8\n" \
"s_add_u32 s59, s59, 4096\n" \
"v_mfma_f32_16x16x32_bf16 v[0:3], v[128:131], v[160:163], v[0:3]\n" \
"ds_read_b64_tr_b16 v[152:153], v237 offset:0\n" \
"v_mfma_f32_16x16x32_bf16 v[4:7], v[128:131], v[164:167], v[4:7]\n" \
"ds_read_b64_tr_b16 v[154:155], v237 offset:2048\n" \
"v_mfma_f32_16x16x32_bf16 v[8:11], v[132:135], v[160:163], v[8:11]\n" \
"ds_read_b64_tr_b16 v[156:157], v238 offset:0\n" \
"v_mfma_f32_16x16x32_bf16 v[12:15], v[132:135], v[164:167], v[12:15]\n" \
"ds_read_b64_tr_b16 v[158:159], v238 offset:2048\n" \
"s_waitcnt lgkmcnt(8)\n" \
"v_mfma_f32_16x16x32_bf16 v[16:19], v[136:139], v[160:163], v[16:19]\n" \
"ds_read_b64_tr_b16 v[128:129], v231 offset:256\n" \
"v_mfma_f32_16x16x32_bf16 v[20:23], v[136:139], v[164:167], v[20:23]\n" \
"ds_read_b64_tr_b16 v[130:131], v231 offset:2304\n" \
"s_add_u32 s83, s55, 53248\n" \
"buffer_store_dwordx4 v[216:219], v224, %[rs], s83 offen offset:0 sc1\n" \
"v_mfma_f32_16x16x32_bf16 v[24:27], v[140:143], v[160:163], v[24:27]\n" \
"ds_read_b64_tr_b16 v[132:133], v232 offset:256\n" \
"v_accvgpr_read_b32 v208, a220\n" \
"v_accvgpr_read_b32 v209, a221\n" \
"v_mfma_f32_16x16x32_bf16 v[28:31], v[140:143], v[164:167], v[28:31]\n" \
"ds_read_b64_tr_b16 v[134:135], v232 offset:2304\n" \
"s_waitcnt lgkmcnt(8)\n" \
"v_accvgpr_read_b32 v210, a222\n" \
"v_mfma_f32_16x16x32_bf16 v[32:35], v[144:147], v[160:163], v[32:35]\n" \
"ds_read_b64_tr_b16 v[136:137], v233 offset:256\n" \
"v_accvgpr_read_b32 v211, a223\n" \
"v_cvt_pk_bf16_f32 v216, v208, v209\n" \
"v_mfma_f32_16x16x32_bf16 v[36:39], v[144:147], v[164:167], v[36:39]\n" \
"ds_read_b64_tr_b16 v[138:139], v233 offset:2304\n" \
"v_cvt_pk_bf16_f32 v217, v210, v211\n" \
"v_accvgpr_read_b32 v208, a252\n" \
"v_mfma_f32_16x16x32_bf16 v[40:43], v[148:151], v[160:163], v[40:43]\n" \
"ds_read_b64_tr_b16 v[140:141], v234 offset:256\n" \
"v_accvgpr_read_b32 v209, a253\n" \
"v_accvgpr_read_b32 v210, a254\n" \
"v_mfma_f32_16x16x32_bf16 v[44:47], v[148:151], v[164:167], v[44:47]\n" \
"ds_read_b64_tr_b16 v[142:143], v234 offset:2304\n" \
"s_waitcnt lgkmcnt(8)\n" \
"v_accvgpr_read_b32 v211, a255\n" \
"v_mfma_f32_16x16x32_bf16 v[48:51], v[152:155], v[160:163], v[48:51]\n" \
"ds_read_b64_tr_b16 v[144:145], v235 offset:256\n" \
"v_cvt_pk_bf16_f32 v218, v208, v209\n" \
"v_cvt_pk_bf16_f32 v219, v210, v211\n" \
"v_mfma_f32_16x16x32_bf16 v[52:55], v[152:155], v[164:167], v[52:55]\n" \
"ds_read_b64_tr_b16 v[146:147], v235 offset:2304\n" \
"v_mfma_f32_16x16x32_bf16 v[56:59], v[156:159], v[160:163], v[56:59]\n" \
"ds_read_b64_tr_b16 v[148:149], v236 offset:256\n" \
"v_mfma_f32_16x16x32_bf16 v[60:63], v[156:159], v[164:167], v[60:63]\n" \
"ds_read_b64_tr_b16 v[150:151], v236 offset:2304\n" \
"s_waitcnt lgkmcnt(8)\n" \
"v_mfma_f32_16x16x32_bf16 v[64:67], v[128:131], v[160:163], v[64:67]\n" \
"ds_read_b64_tr_b16 v[152:153], v237 offset:256\n" \
"v_mfma_f32_16x16x32_bf16 v[68:71], v[128:131], v[164:167], v[68:71]\n" \
"ds_read_b64_tr_b16 v[154:155], v237 offset:2304\n" \
"v_mfma_f32_16x16x32_bf16 v[72:75], v[132:135], v[160:163], v[72:75]\n" \
"ds_read_b64_tr_b16 v[156:157], v238 offset:256\n" \
"v_mfma_f32_16x16x32_bf16 v[76:79], v[132:135], v[164:167], v[76:79]\n" \
"ds_read_b64_tr_b16 v[158:159], v238 offset:2304\n" \
"s_waitcnt lgkmcnt(8)\n" \
"buffer_store_dwordx4 v[220:223], v224, %[rs], s83 offen offset:1024 sc1\n" \
"s_waitcnt vmcnt(10)\n" \
"s_barrier\n" \
"v_mfma_f32_16x16x32_bf16 v[80:83], v[136:139], v[160:163], v[80:83]\n" \
"ds_read_b64_tr_b16 v[128:129], v231 offset:16384\n" \
"v_mfma_f32_16x16x32_bf16 v[84:87], v[136:139], v[164:167], v[84:87]\n" \
"ds_read_b64_tr_b16 v[130:131], v231 offset:18432\n" \
"v_mfma_f32_16x16x32_bf16 v[88:91], v[140:143], v[160:163], v[88:91]\n" \
"ds_read_b64_tr_b16 v[132:133], v232 offset:16384\n" \
"v_mfma_f32_16x16x32_bf16 v[92:95], v[140:143], v[164:167], v[92:95]\n" \
"ds_read_b64_tr_b16 v[134:135], v232 offset:18432\n" \
"s_waitcnt lgkmcnt(8)\n" \
"v_mfma_f32_16x16x32_bf16 v[96:99], v[144:147], v[160:163], v[96:99]\n" \
"ds_read_b64_tr_b16 v[136:137], v233 offset:16384\n" \
"v_mfma_f32_16x16x32_bf16 v[100:103], v[144:147], v[164:167], v[100:103]\n" \
"ds_read_b64_tr_b16 v[138:139], v233 offset:18432\n" \
"v_mfma_f32_16x16x32_bf16 v[104:107], v[148:151], v[160:163], v[104:107]\n" \
"ds_read_b64_tr_b16 v[140:141], v234 offset:16384\n" \
"v_mfma_f32_16x16x32_bf16 v[108:111], v[148:151], v[164:167], v[108:111]\n" \
"ds_read_b64_tr_b16 v[142:143], v234 offset:18432\n" \
"s_waitcnt lgkmcnt(8)\n" \
"v_mfma_f32_16x16x32_bf16 v[112:115], v[152:155], v[160:163], v[112:115]\n" \
"ds_read_b64_tr_b16 v[144:145], v235 offset:16384\n" \
"v_mfma_f32_16x16x32_bf16 v[116:119], v[152:155], v[164:167], v[116:119]\n" \
"ds_read_b64_tr_b16 v[146:147], v235 offset:18432\n" \
"v_mfma_f32_16x16x32_bf16 v[120:123], v[156:159], v[160:163], v[120:123]\n" \
"ds_read_b64_tr_b16 v[148:149], v236 offset:16384\n" \
"v_mfma_f32_16x16x32_bf16 v[124:127], v[156:159], v[164:167], v[124:127]\n" \
"ds_read_b64_tr_b16 v[150:151], v236 offset:18432\n" \
"s_waitcnt lgkmcnt(8)\n" \
"v_mfma_f32_16x16x32_bf16 v[0:3], v[128:131], v[168:171], v[0:3]\n" \
"ds_read_b64_tr_b16 v[152:153], v237 offset:16384\n" \
"buffer_store_dwordx4 v[212:215], v224, %[rs], s83 offen offset:2048 sc1\n" \
"v_mfma_f32_16x16x32_bf16 v[4:7], v[128:131], v[172:175], v[4:7]\n" \
"ds_read_b64_tr_b16 v[154:155], v237 offset:18432\n" \
"v_mfma_f32_16x16x32_bf16 v[8:11], v[132:135], v[168:171], v[8:11]\n" \
"ds_read_b64_tr_b16 v[156:157], v238 offset:16384\n" \
"v_mfma_f32_16x16x32_bf16 v[12:15], v[132:135], v[172:175], v[12:15]\n" \
"ds_read_b64_tr_b16 v[158:159], v238 offset:18432\n" \
"s_waitcnt lgkmcnt(8)\n" \
"v_mfma_f32_16x16x32_bf16 v[16:19], v[136:139], v[168:171], v[16:19]\n" \
"ds_read_b64_tr_b16 v[128:129], v231 offset:16640\n" \
"buffer_store_dwordx4 v[216:219], v224, %[rs], s83 offen offset:3072 sc1\n" \
"v_mfma_f32_16x16x32_bf16 v[20:23], v[136:139], v[172:175], v[20:23]\n" \
"ds_read_b64_tr_b16 v[130:131], v231 offset:18688\n" \
"v_mfma_f32_16x16x32_bf16 v[24:27], v[140:143], v[168:171], v[24:27]\n" \
"ds_read_b64_tr_b16 v[132:133], v232 offset:16640\n" \
"v_mfma_f32_16x16x32_bf16 v[28:31], v[140:143], v[172:175], v[28:31]\n" \
"ds_read_b64_tr_b16 v[134:135], v232 offset:18688\n" \
"s_waitcnt lgkmcnt(8)\n" \
"v_mfma_f32_16x16x32_bf16 v[32:35], v[144:147], v[168:171], v[32:35]\n" \
"ds_read_b64_tr_b16 v[136:137], v233 offset:16640\n" \
"v_cvt_pk_bf16_f32 v0, v0, v1\n" \
"v_cvt_pk_bf16_f32 v1, v2, v3\n" \
"v_mfma_f32_16x16x32_bf16 v[36:39], v[144:147], v[172:175], v[36:39]\n" \
"ds_read_b64_tr_b16 v[138:139], v233 offset:18688\n" \
"v_cvt_pk_bf16_f32 v2, v8, v9\n" \
"v_cvt_pk_bf16_f32 v3, v10, v11\n" \
"v_mfma_f32_16x16x32_bf16 v[40:43], v[148:151], v[168:171], v[40:43]\n" \
"ds_read_b64_tr_b16 v[140:141], v234 offset:16640\n" \
"s_add_u32 s94, s68, 0\n" \
"buffer_store_dwordx4 v[0:3], v224, %[rs], s94 offen sc1\n" \
"v_mfma_f32_16x16x32_bf16 v[44:47], v[148:151], v[172:175], v[44:47]\n" \
"ds_read_b64_tr_b16 v[142:143], v234 offset:18688\n" \
"s_waitcnt lgkmcnt(8)\n" \
"v_cvt_pk_bf16_f32 v4, v4, v5\n" \
"v_mfma_f32_16x16x32_bf16 v[48:51], v[152:155], v[168:171], v[48:51]\n" \
"ds_read_b64_tr_b16 v[144:145], v235 offset:16640\n" \
"v_cvt_pk_bf16_f32 v5, v6, v7\n" \
"v_cvt_pk_bf16_f32 v6, v12, v13\n" \
"v_mfma_f32_16x16x32_bf16 v[52:55], v[152:155], v[172:175], v[52:55]\n" \
"ds_read_b64_tr_b16 v[146:147], v235 offset:18688\n" \
"v_cvt_pk_bf16_f32 v7, v14, v15\n" \
"v_cvt_pk_bf16_f32 v16, v16, v17\n" \
"v_mfma_f32_16x16x32_bf16 v[56:59], v[156:159], v[168:171], v[56:59]\n" \
"ds_read_b64_tr_b16 v[148:149], v236 offset:16640\n" \
"s_add_u32 s94, s68, 8192\n" \
"buffer_store_dwordx4 v[4:7], v224, %[rs], s94 offen sc1\n" \
"v_mfma_f32_16x16x32_bf16 v[60:63], v[156:159], v[172:175], v[60:63]\n" \
"ds_read_b64_tr_b16 v[150:151], v236 offset:18688\n" \
"s_waitcnt lgkmcnt(8)\n" \
"v_cvt_pk_bf16_f32 v17, v18, v19\n" \
"v_mfma_f32_16x16x32_bf16 v[64:67], v[128:131], v[168:171], v[64:67]\n" \
"ds_read_b64_tr_b16 v[152:153], v237 offset:16640\n" \
"v_cvt_pk_bf16_f32 v18, v24, v25\n" \
"v_cvt_pk_bf16_f32 v19, v26, v27\n" \
"v_mfma_f32_16x16x32_bf16 v[68:71], v[128:131], v[172:175], v[68:71]\n" \
"ds_read_b64_tr_b16 v[154:155], v237 offset:18688\n" \
"v_cvt_pk_bf16_f32 v20, v20, v21\n" \
"v_cvt_pk_bf16_f32 v21, v22, v23\n" \
"v_mfma_f32_16x16x32_bf16 v[72:75], v[132:135], v[168:171], v[72:75]\n" \
"ds_read_b64_tr_b16 v[156:157], v238 offset:16640\n" \
"s_add_u32 s94, s68, 1024\n" \
"buffer_store_dwordx4 v[16:19], v224, %[rs], s94 offen sc1\n" \
"v_mfma_f32_16x16x32_bf16 v[76:79], v[132:135], v[172:175], v[76:79]\n" \
"ds_read_b64_tr_b16 v[158:159], v238 offset:18688\n" \
"s_waitcnt lgkmcnt(8)\n" \
"v_cvt_pk_bf16_f32 v22, v28, v29\n" \
"v_mfma_f32_16x16x32_bf16 v[80:83], v[136:139], v[168:171], v[80:83]\n" \
"v_cvt_pk_bf16_f32 v23, v30, v31\n" \
"v_cvt_pk_bf16_f32 v32, v32, v33\n" \
"v_cvt_pk_bf16_f32 v33, v34, v35\n" \
"v_mfma_f32_16x16x32_bf16 v[84:87], v[136:139], v[172:175], v[84:87]\n" \
"v_cvt_pk_bf16_f32 v34, v40, v41\n" \
"v_cvt_pk_bf16_f32 v35, v42, v43\n" \
"v_cvt_pk_bf16_f32 v36, v36, v37\n" \
"v_mfma_f32_16x16x32_bf16 v[88:91], v[140:143], v[168:171], v[88:91]\n" \
"s_add_u32 s94, s68, 9216\n" \
"buffer_store_dwordx4 v[20:23], v224, %[rs], s94 offen sc1\n" \
"v_cvt_pk_bf16_f32 v37, v38, v39\n" \
"v_mfma_f32_16x16x32_bf16 v[92:95], v[140:143], v[172:175], v[92:95]\n" \
"s_waitcnt lgkmcnt(4)\n" \
"v_cvt_pk_bf16_f32 v38, v44, v45\n" \
"v_cvt_pk_bf16_f32 v39, v46, v47\n" \
"v_mfma_f32_16x16x32_bf16 v[96:99], v[144:147], v[168:171], v[96:99]\n" \
"v_cvt_pk_bf16_f32 v48, v48, v49\n" \
"v_cvt_pk_bf16_f32 v49, v50, v51\n" \
"v_cvt_pk_bf16_f32 v50, v56, v57\n" \
"v_mfma_f32_16x16x32_bf16 v[100:103], v[144:147], v[172:175], v[100:103]\n" \
"v_cvt_pk_bf16_f32 v51, v58, v59\n" \
"v_cvt_pk_bf16_f32 v52, v52, v53\n" \
"v_cvt_pk_bf16_f32 v53, v54, v55\n" \
"v_mfma_f32_16x16x32_bf16 v[104:107], v[148:151], v[168:171], v[104:107]\n" \
"s_add_u32 s94, s68, 2048\n" \
"buffer_store_dwordx4 v[32:35], v224, %[rs], s94 offen sc1\n" \
"v_cvt_pk_bf16_f32 v54, v60, v61\n" \
"v_mfma_f32_16x16x32_bf16 v[108:111], v[148:151], v[172:175], v[108:111]\n" \
"s_waitcnt lgkmcnt(0)\n" \
"v_cvt_pk_bf16_f32 v55, v62, v63\n" \
"v_cvt_pk_bf16_f32 v64, v64, v65\n" \
"v_mfma_f32_16x16x32_bf16 v[112:115], v[152:155], v[168:171], v[112:115]\n" \
"v_cvt_pk_bf16_f32 v65, v66, v67\n" \
"v_cvt_pk_bf16_f32 v66, v72, v73\n" \
"v_cvt_pk_bf16_f32 v67, v74, v75\n" \
"v_mfma_f32_16x16x32_bf16 v[116:119], v[152:155], v[172:175], v[116:119]\n" \
"v_cvt_pk_bf16_f32 v68, v68, v69\n" \
"v_cvt_pk_bf16_f32 v69, v70, v71\n" \
"v_cvt_pk_bf16_f32 v70, v76, v77\n" \
"v_mfma_f32_16x16x32_bf16 v[120:123], v[156:159], v[168:171], v[120:123]\n" \
"s_add_u32 s94, s68, 10240\n" \
"buffer_store_dwordx4 v[36:39], v224, %[rs], s94 offen sc1\n" \
"v_cvt_pk_bf16_f32 v71, v78, v79\n" \
"v_mfma_f32_16x16x32_bf16 v[124:127], v[156:159], v[172:175], v[124:127]\n" \
"v_cvt_pk_bf16_f32 v80, v80, v81\n" \
"v_cvt_pk_bf16_f32 v81, v82, v83\n" \
"s_add_u32 s94, s68, 3072\n" \
"buffer_store_dwordx4 v[48:51], v224, %[rs], s94 offen sc1\n" \
"s_add_u32 s94, s68, 11264\n" \
"buffer_store_dwordx4 v[52:55], v224, %[rs], s94 offen sc1\n" \
"s_add_u32 s94, s68, 4096\n" \
"buffer_store_dwordx4 v[64:67], v224, %[rs], s94 offen sc1\n" \
"s_add_u32 s94, s68, 12288\n" \
"buffer_store_dwordx4 v[68:71], v224, %[rs], s94 offen sc1\n" \
"v_cvt_pk_bf16_f32 v82, v88, v89\n" \
"v_cvt_pk_bf16_f32 v83, v90, v91\n" \
"s_add_u32 s94, s68, 5120\n" \
"buffer_store_dwordx4 v[80:83], v224, %[rs], s94 offen sc1\n" \
"v_cvt_pk_bf16_f32 v84, v84, v85\n" \
"v_cvt_pk_bf16_f32 v85, v86, v87\n" \
"v_cvt_pk_bf16_f32 v86, v92, v93\n" \
"v_cvt_pk_bf16_f32 v87, v94, v95\n" \
"s_add_u32 s94, s68, 13312\n" \
"buffer_store_dwordx4 v[84:87], v224, %[rs], s94 offen sc1\n" \
"v_cvt_pk_bf16_f32 v96, v96, v97\n" \
"v_cvt_pk_bf16_f32 v97, v98, v99\n" \
"v_cvt_pk_bf16_f32 v98, v104, v105\n" \
"v_cvt_pk_bf16_f32 v99, v106, v107\n" \
"s_add_u32 s94, s68, 6144\n" \
"buffer_store_dwordx4 v[96:99], v224, %[rs], s94 offen sc1\n" \
"v_cvt_pk_bf16_f32 v100, v100, v101\n" \
"v_cvt_pk_bf16_f32 v101, v102, v103\n" \
"v_cvt_pk_bf16_f32 v102, v108, v109\n" \
"v_cvt_pk_bf16_f32 v103, v110, v111\n" \
"s_add_u32 s94, s68, 14336\n" \
"buffer_store_dwordx4 v[100:103], v224, %[rs], s94 offen sc1\n" \
"v_cvt_pk_bf16_f32 v112, v112, v113\n" \
"v_cvt_pk_bf16_f32 v113, v114, v115\n" \
"v_cvt_pk_bf16_f32 v114, v120, v121\n" \
"v_cvt_pk_bf16_f32 v115, v122, v123\n" \
"s_add_u32 s94, s68, 7168\n" \
"buffer_store_dwordx4 v[112:115], v224, %[rs], s94 offen sc1\n" \
"v_cvt_pk_bf16_f32 v116, v116, v117\n" \
"v_cvt_pk_bf16_f32 v117, v118, v119\n" \
"v_cvt_pk_bf16_f32 v118, v124, v125\n" \
"v_cvt_pk_bf16_f32 v119, v126, v127\n" \
"s_add_u32 s94, s68, 15360\n" \
"buffer_store_dwordx4 v[116:119], v224, %[rs], s94 offen sc1\n" \
"LQ%=:\n" \
"s_cmp_lg_u32 s63, 0\n" \
"s_cbranch_scc1 LqY%=\n" \
"v_accvgpr_write_b32 a128, v0\n" \
"v_accvgpr_write_b32 a129, v1\n" \
"v_accvgpr_write_b32 a130, v2\n" \
"v_accvgpr_write_b32 a131, v3\n" \
"v_accvgpr_write_b32 a132, v16\n" \
"v_accvgpr_write_b32 a133, v17\n" \
"v_accvgpr_write_b32 a134, v18\n" \
"v_accvgpr_write_b32 a135, v19\n" \
"v_accvgpr_write_b32 a136, v32\n" \
"v_accvgpr_write_b32 a137, v33\n" \
"v_accvgpr_write_b32 a138, v34\n" \
"v_accvgpr_write_b32 a139, v35\n" \
"v_accvgpr_write_b32 a140, v48\n" \
"v_accvgpr_write_b32 a141, v49\n" \
"v_accvgpr_write_b32 a142, v50\n" \
"v_accvgpr_write_b32 a143, v51\n" \
"v_accvgpr_write_b32 a144, v64\n" \
"v_accvgpr_write_b32 a145, v65\n" \
"v_accvgpr_write_b32 a146, v66\n" \
"v_accvgpr_write_b32 a147, v67\n" \
"v_accvgpr_write_b32 a148, v80\n" \
"v_accvgpr_write_b32 a149, v81\n" \
"v_accvgpr_write_b32 a150, v82\n" \
"v_accvgpr_write_b32 a151, v83\n" \
"v_accvgpr_write_b32 a152, v96\n" \
"v_accvgpr_write_b32 a153, v97\n" \
"v_accvgpr_write_b32 a154, v98\n" \
"v_accvgpr_write_b32 a155, v99\n" \
"v_accvgpr_write_b32 a156, v112\n" \
"v_accvgpr_write_b32 a157, v113\n" \
"v_accvgpr_write_b32 a158, v114\n" \
"v_accvgpr_write_b32 a159, v115\n" \
"v_accvgpr_write_b32 a160, v4\n" \
"v_accvgpr_write_b32 a161, v5\n" \
"v_accvgpr_write_b32 a162, v6\n" \
"v_accvgpr_write_b32 a163, v7\n" \
"v_accvgpr_write_b32 a164, v20\n" \
"v_accvgpr_write_b32 a165, v21\n" \
"v_accvgpr_write_b32 a166, v22\n" \
"v_accvgpr_write_b32 a167, v23\n" \
"v_accvgpr_write_b32 a168, v36\n" \
"v_accvgpr_write_b32 a169, v37\n" \
"v_accvgpr_write_b32 a170, v38\n" \
"v_accvgpr_write_b32 a171, v39\n" \
"v_accvgpr_write_b32 a172, v52\n" \
"v_accvgpr_write_b32 a173, v53\n" \
"v_accvgpr_write_b32 a174, v54\n" \
"v_accvgpr_write_b32 a175, v55\n" \
"v_accvgpr_write_b32 a176, v68\n" \
"v_accvgpr_write_b32 a177, v69\n" \
"v_accvgpr_write_b32 a178, v70\n" \
"v_accvgpr_write_b32 a179, v71\n" \
"v_accvgpr_write_b32 a180, v84\n" \
"v_accvgpr_write_b32 a181, v85\n" \
"v_accvgpr_write_b32 a182, v86\n" \
"v_accvgpr_write_b32 a183, v87\n" \
"v_accvgpr_write_b32 a184, v100\n" \
"v_accvgpr_write_b32 a185, v101\n" \
"v_accvgpr_write_b32 a186, v102\n" \
"v_accvgpr_write_b32 a187, v103\n" \
"v_accvgpr_write_b32 a188, v116\n" \
"v_accvgpr_write_b32 a189, v117\n" \
"v_accvgpr_write_b32 a190, v118\n" \
"v_accvgpr_write_b32 a191, v119\n" \
"s_branch LqD%=\n" \
"LqY%=:\n" \
"v_accvgpr_write_b32 a192, v0\n" \
"v_accvgpr_write_b32 a193, v1\n" \
"v_accvgpr_write_b32 a194, v2\n" \
"v_accvgpr_write_b32 a195, v3\n" \
"v_accvgpr_write_b32 a196, v16\n" \
"v_accvgpr_write_b32 a197, v17\n" \
"v_accvgpr_write_b32 a198, v18\n" \
"v_accvgpr_write_b32 a199, v19\n" \
"v_accvgpr_write_b32 a200, v32\n" \
"v_accvgpr_write_b32 a201, v33\n" \
"v_accvgpr_write_b32 a202, v34\n" \
"v_accvgpr_write_b32 a203, v35\n" \
"v_accvgpr_write_b32 a204, v48\n" \
"v_accvgpr_write_b32 a205, v49\n" \
"v_accvgpr_write_b32 a206, v50\n" \
"v_accvgpr_write_b32 a207, v51\n" \
"v_accvgpr_write_b32 a208, v64\n" \
"v_accvgpr_write_b32 a209, v65\n" \
"v_accvgpr_write_b32 a210, v66\n" \
"v_accvgpr_write_b32 a211, v67\n" \
"v_accvgpr_write_b32 a212, v80\n" \
"v_accvgpr_write_b32 a213, v81\n" \
"v_accvgpr_write_b32 a214, v82\n" \
"v_accvgpr_write_b32 a215, v83\n" \
"v_accvgpr_write_b32 a216, v96\n" \
"v_accvgpr_write_b32 a217, v97\n" \
"v_accvgpr_write_b32 a218, v98\n" \
"v_accvgpr_write_b32 a219, v99\n" \
"v_accvgpr_write_b32 a220, v112\n" \
"v_accvgpr_write_b32 a221, v113\n" \
"v_accvgpr_write_b32 a222, v114\n" \
"v_accvgpr_write_b32 a223, v115\n" \
"v_accvgpr_write_b32 a224, v4\n" \
"v_accvgpr_write_b32 a225, v5\n" \
"v_accvgpr_write_b32 a226, v6\n" \
"v_accvgpr_write_b32 a227, v7\n" \
"v_accvgpr_write_b32 a228, v20\n" \
"v_accvgpr_write_b32 a229, v21\n" \
"v_accvgpr_write_b32 a230, v22\n" \
"v_accvgpr_write_b32 a231, v23\n" \
"v_accvgpr_write_b32 a232, v36\n" \
"v_accvgpr_write_b32 a233, v37\n" \
"v_accvgpr_write_b32 a234, v38\n" \
"v_accvgpr_write_b32 a235, v39\n" \
"v_accvgpr_write_b32 a236, v52\n" \
"v_accvgpr_write_b32 a237, v53\n" \
"v_accvgpr_write_b32 a238, v54\n" \
"v_accvgpr_write_b32 a239, v55\n" \
"v_accvgpr_write_b32 a240, v68\n" \
"v_accvgpr_write_b32 a241, v69\n" \
"v_accvgpr_write_b32 a242, v70\n" \
"v_accvgpr_write_b32 a243, v71\n" \
"v_accvgpr_write_b32 a244, v84\n" \
"v_accvgpr_write_b32 a245, v85\n" \
"v_accvgpr_write_b32 a246, v86\n" \
"v_accvgpr_write_b32 a247, v87\n" \
"v_accvgpr_write_b32 a248, v100\n" \
"v_accvgpr_write_b32 a249, v101\n" \
"v_accvgpr_write_b32 a250, v102\n" \
"v_accvgpr_write_b32 a251, v103\n" \
"v_accvgpr_write_b32 a252, v116\n" \
"v_accvgpr_write_b32 a253, v117\n" \
"v_accvgpr_write_b32 a254, v118\n" \
"v_accvgpr_write_b32 a255, v119\n" \
"LqD%=:\n" \
"s_lshr_b32 s80, s17, 1\n" \
"s_lshl_b32 s62, s62, 8\n" \
"s_add_u32 s62, s62, 8192\n" \
"s_lshl_b32 s65, s80, 1\n" \
"s_sub_u32 s81, 32, s65\n" \
"s_add_u32 s65, s65, 2\n" \
"s_sub_u32 s83, s81, 1\n" \
"s_min_u32 s83, s83, 17\n" \
"s_sub_u32 s81, s81, s83\n" \
"s_cmp_eq_u32 s63, 0\n" \
"s_cselect_b32 s64, s83, s81\n" \
"s_cselect_b32 s83, 0, s83\n" \
"s_cselect_b32 s94, 0, s65\n" \
"s_lshl_b32 s80, s80, 7\n" \
"s_sub_u32 s67, 0, s80\n" \
"s_lshl_b32 s66, s83, 6\n" \
"s_add_u32 s66, s66, s80\n" \
"s_sub_u32 s66, s66, 1920\n" \
"s_lshl_b32 s80, s16, 7\n" \
"s_lshl_b32 s81, s18, 3\n" \
"s_add_u32 s80, s80, s81\n" \
"s_add_u32 s81, s80, s36\n" \
"s_mul_i32 s69, s81, 14336\n" \
"s_add_u32 s69, s69, 58720256\n" \
"s_sub_u32 s81, s80, s36\n" \
"s_mul_i32 s71, s81, 14336\n" \
"s_add_u32 s71, s71, 58820608\n" \
"s_lshl_b32 s80, s19, 2\n" \
"s_add_u32 s80, s80, s36\n" \
"s_mul_i32 s70, s80, 12544\n" \
"v_lshrrev_b32 v192, 4, v224\n" \
"v_add_u32 v193, 3, v192\n" \
"v_and_b32 v193, 63, v193\n" \
"v_add_u32 v192, 1, v192\n" \
"v_mul_u32_u24 v195, 0x334, v192\n" \
"v_lshrrev_b32 v195, 12, v195\n" \
"v_mul_u32_u24 v195, 5, v195\n" \
"v_sub_u32 v192, v192, v195\n" \
"v_lshlrev_b32 v238, 15, v192\n" \
"s_lshl_b32 s80, s64, 1\n" \
"s_lshl_b32 s81, s94, 1\n" \
"v_cmp_le_u32 vcc, s80, v193\n" \
"v_subrev_u32 v195, s80, v193\n" \
"s_nop 0\n" \
"v_cndmask_b32 v193, v193, v195, vcc\n" \
"v_mov_b32 v194, s80\n" \
"v_mov_b32 v195, s81\n" \
"v_cndmask_b32 v194, v194, v195, vcc\n" \
"v_mov_b32 v195, s83\n" \
"v_cndmask_b32 v195, v195, 0, vcc\n" \
"v_add_u32 v192, 1, v193\n" \
"v_cmp_eq_u32 s[84:85], v192, v194\n" \
"v_and_b32 v196, 1, v192\n" \
"v_cmp_lt_u32 s[86:87], 1, v192\n" \
"s_nop 1\n" \
"v_cndmask_b32 v196, 0, v196, s[86:87]\n" \
"v_cndmask_b32 v196, v196, 1, s[84:85]\n" \
"v_lshrrev_b32 v192, 1, v192\n" \
"v_add_u32 v192, v192, v195\n" \
"v_sub_u32 v192, v192, v196\n" \
"v_lshlrev_b32 v192, 15, v192\n" \
"v_lshl_add_u32 v192, v196, 24, v192\n" \
"s_lshl_b32 s80, s16, 20\n" \
"s_add_u32 s80, s80, s37\n" \
"s_add_u32 s80, s80, 1048576\n" \
"v_add_u32 v192, s80, v192\n" \
"v_cmp_ge_u32 s[84:85], v193, v194\n" \
"v_sub_u32 v193, v193, v194\n" \
"s_mul_i32 s80, s16, 229376\n" \
"s_add_u32 s80, s80, s37\n" \
"s_add_u32 s80, s80, 54525952\n" \
"v_lshlrev_b32 v195, 15, v193\n" \
"v_add_u32 v195, s80, v195\n" \
"v_cmp_gt_u32 vcc, 7, v193\n" \
"v_bfrev_b32 v196, 1\n" \
"s_nop 0\n" \
"v_cndmask_b32 v195, v196, v195, vcc\n" \
"v_cndmask_b32 v239, v192, v195, s[84:85]\n" \
"v_lshrrev_b32 v192, 8, v224\n" \
"v_bfe_u32 v193, v224, 4, 4\n" \
"v_lshlrev_b32 v195, 1, v192\n" \
"v_xor_b32 v193, v193, v195\n" \
"v_mul_u32_u24 v192, 0xe00, v192\n" \
"v_lshl_add_u32 v237, v193, 4, v192\n" \
"s_load_dwordx2 s[28:29], %[ka], 48\n" \
"s_load_dwordx2 s[32:33], %[ka], 56\n" \
"s_load_dwordx2 s[24:25], %[ka], 0\n" \
"s_load_dwordx2 s[20:21], %[ka], 72\n" \
"s_lshr_b32 s39, s16, 1\n" \
"s_xor_b32 s80, s63, 1\n" \
"s_lshl_b32 s80, s80, 3\n" \
"s_add_u32 s39, s39, s80\n" \
"s_and_b32 s40, s16, 1\n" \
"s_lshl_b32 s40, s40, 3\n" \
"s_lshr_b32 s80, s17, 1\n" \
"s_add_u32 s40, s40, s80\n" \
"s_lshl_b32 s80, s40, 8\n" \
"s_sub_u32 s30, 1605632, s80\n" \
"s_cmp_lt_u32 s40, 14\n" \
"s_cselect_b32 s30, s30, 0\n" \
"s_waitcnt lgkmcnt(0)\n" \
"s_add_u32 s28, s28, s80\n" \
"s_addc_u32 s29, s29, 0\n" \
"s_and_b32 s29, s29, 0xffff\n" \
"s_nop 0\n" \
"s_mov_b32 s31, 0x20000\n" \
"s_and_b32 s33, s33, 0xffff\n" \
"s_mov_b32 s34, 1605632\n" \
"s_mov_b32 s35, 0x20000\n" \
"s_and_b32 s25, s25, 0xffff\n" \
"s_mov_b32 s26, 1835008\n" \
"s_mov_b32 s27, 0x20000\n" \
"s_and_b32 s21, s21, 0xffff\n" \
"s_mov_b32 s22, 1835008\n" \
"s_mov_b32 s23, 0x20000\n" \
"s_waitcnt vmcnt(0) lgkmcnt(0)\n" \
"s_barrier\n" \
"s_lshl_b32 s80, s17, 2\n" \
"s_add_u32 s80, s80, s59\n" \
"s_cmp_lg_u32 s36, 0\n" \
"s_cbranch_scc1 Lsh%=\n" \
"s_mov_b64 s[96:97], exec\n" \
"s_mov_b64 exec, 1\n" \
"v_mov_b32 v223, %[tag]\n" \
"buffer_store_dword v223, off, %[rs], s80 sc1\n" \
"s_mov_b64 exec, s[96:97]\n" \
"Lsh%=:\n" \
"s_mov_b32 s51, 0x44800000\n" \
"v_lshrrev_b32 v184, 4, v224\n" \
"v_and_b32 v185, 15, v184\n" \
"v_lshrrev_b32 v184, 4, v184\n" \
"v_lshlrev_b32 v184, 2, v184\n" \
"v_sub_u32 v184, v184, v185\n" \
"v_cmp_lt_i32 s[84:85], 0, v184\n" \
"v_cmp_lt_i32 s[86:87], -1, v184\n" \
"v_cmp_lt_i32 s[88:89], -2, v184\n" \
"v_cmp_lt_i32 s[90:91], -3, v184\n" \
"s_mov_b32 s41, 0\n" \
"s_mov_b32 s44, 0x20000\n" \
"s_mov_b32 s42, 0\n" \
"s_mov_b32 s50, 0\n" \
"s_mov_b32 s49, 0\n" \
"s_mov_b32 s46, s64\n" \
"s_mov_b32 s47, s66\n" \
"v_mov_b32 v178, v224\n" \
"v_accvgpr_write_b32 a0, 0\n" \
"v_accvgpr_write_b32 a1, 0\n" \
"v_accvgpr_write_b32 a2, 0\n" \
"v_accvgpr_write_b32 a3, 0\n" \
"v_accvgpr_write_b32 a4, 0\n" \
"v_accvgpr_write_b32 a5, 0\n" \
"v_accvgpr_write_b32 a6, 0\n" \
"v_accvgpr_write_b32 a7, 0\n" \
"v_accvgpr_write_b32 a8, 0\n" \
"v_accvgpr_write_b32 a9, 0\n" \
"v_accvgpr_write_b32 a10, 0\n" \
"v_accvgpr_write_b32 a11, 0\n" \
"v_accvgpr_write_b32 a12, 0\n" \
"v_accvgpr_write_b32 a13, 0\n" \
"v_accvgpr_write_b32 a14, 0\n" \
"v_accvgpr_write_b32 a15, 0\n" \
"v_accvgpr_write_b32 a16, 0\n" \
"v_accvgpr_write_b32 a17, 0\n" \
"v_accvgpr_write_b32 a18, 0\n" \
"v_accvgpr_write_b32 a19, 0\n" \
"v_accvgpr_write_b32 a20, 0\n" \
"v_accvgpr_write_b32 a21, 0\n" \
"v_accvgpr_write_b32 a22, 0\n" \
"v_accvgpr_write_b32 a23, 0\n" \
"v_accvgpr_write_b32 a24, 0\n" \
"v_accvgpr_write_b32 a25, 0\n" \
"v_accvgpr_write_b32 a26, 0\n" \
"v_accvgpr_write_b32 a27, 0\n" \
"v_accvgpr_write_b32 a28, 0\n" \
"v_accvgpr_write_b32 a29, 0\n" \
"v_accvgpr_write_b32 a30, 0\n" \
"v_accvgpr_write_b32 a31, 0\n" \
"v_accvgpr_write_b32 a32, 0\n" \
"v_accvgpr_write_b32 a33, 0\n" \
"v_accvgpr_write_b32 a34, 0\n" \
"v_accvgpr_write_b32 a35, 0\n" \
"v_accvgpr_write_b32 a36, 0\n" \
"v_accvgpr_write_b32 a37, 0\n" \
"v_accvgpr_write_b32 a38, 0\n" \
"v_accvgpr_write_b32 a39, 0\n" \
"v_accvgpr_write_b32 a40, 0\n" \
"v_accvgpr_write_b32 a41, 0\n" \
"v_accvgpr_write_b32 a42, 0\n" \
"v_accvgpr_write_b32 a43, 0\n" \
"v_accvgpr_write_b32 a44, 0\n" \
"v_accvgpr_write_b32 a45, 0\n" \
"v_accvgpr_write_b32 a46, 0\n" \
"v_accvgpr_write_b32 a47, 0\n" \
"v_accvgpr_write_b32 a48, 0\n" \
"v_accvgpr_write_b32 a49, 0\n" \
"v_accvgpr_write_b32 a50, 0\n" \
"v_accvgpr_write_b32 a51, 0\n" \
"v_accvgpr_write_b32 a52, 0\n" \
"v_accvgpr_write_b32 a53, 0\n" \
"v_accvgpr_write_b32 a54, 0\n" \
"v_accvgpr_write_b32 a55, 0\n" \
"v_accvgpr_write_b32 a56, 0\n" \
"v_accvgpr_write_b32 a57, 0\n" \
"v_accvgpr_write_b32 a58, 0\n" \
"v_accvgpr_write_b32 a59, 0\n" \
"v_accvgpr_write_b32 a60, 0\n" \
"v_accvgpr_write_b32 a61, 0\n" \
"v_accvgpr_write_b32 a62, 0\n" \
"v_accvgpr_write_b32 a63, 0\n" \
"v_accvgpr_write_b32 a64, 0\n" \
"v_accvgpr_write_b32 a65, 0\n" \
"v_accvgpr_write_b32 a66, 0\n" \
"v_accvgpr_write_b32 a67, 0\n" \
"v_accvgpr_write_b32 a68, 0\n" \
"v_accvgpr_write_b32 a69, 0\n" \
"v_accvgpr_write_b32 a70, 0\n" \
"v_accvgpr_write_b32 a71, 0\n" \
"v_accvgpr_write_b32 a72, 0\n" \
"v_accvgpr_write_b32 a73, 0\n" \
"v_accvgpr_write_b32 a74, 0\n" \
"v_accvgpr_write_b32 a75, 0\n" \
"v_accvgpr_write_b32 a76, 0\n" \
"v_accvgpr_write_b32 a77, 0\n" \
"v_accvgpr_write_b32 a78, 0\n" \
"v_accvgpr_write_b32 a79, 0\n" \
"v_accvgpr_write_b32 a80, 0\n" \
"v_accvgpr_write_b32 a81, 0\n" \
"v_accvgpr_write_b32 a82, 0\n" \
"v_accvgpr_write_b32 a83, 0\n" \
"v_accvgpr_write_b32 a84, 0\n" \
"v_accvgpr_write_b32 a85, 0\n" \
"v_accvgpr_write_b32 a86, 0\n" \
"v_accvgpr_write_b32 a87, 0\n" \
"v_accvgpr_write_b32 a88, 0\n" \
"v_accvgpr_write_b32 a89, 0\n" \
"v_accvgpr_write_b32 a90, 0\n" \
"v_accvgpr_write_b32 a91, 0\n" \
"v_accvgpr_write_b32 a92, 0\n" \
"v_accvgpr_write_b32 a93, 0\n" \
"v_accvgpr_write_b32 a94, 0\n" \
"v_accvgpr_write_b32 a95, 0\n" \
"v_accvgpr_write_b32 a96, 0\n" \
"v_accvgpr_write_b32 a97, 0\n" \
"v_accvgpr_write_b32 a98, 0\n" \
"v_accvgpr_write_b32 a99, 0\n" \
"v_accvgpr_write_b32 a100, 0\n" \
"v_accvgpr_write_b32 a101, 0\n" \
"v_accvgpr_write_b32 a102, 0\n" \
"v_accvgpr_write_b32 a103, 0\n" \
"v_accvgpr_write_b32 a104, 0\n" \
"v_accvgpr_write_b32 a105, 0\n" \
"v_accvgpr_write_b32 a106, 0\n" \
"v_accvgpr_write_b32 a107, 0\n" \
"v_accvgpr_write_b32 a108, 0\n" \
"v_accvgpr_write_b32 a109, 0\n" \
"v_accvgpr_write_b32 a110, 0\n" \
"v_accvgpr_write_b32 a111, 0\n" \
"v_accvgpr_write_b32 a112, 0\n" \
"v_accvgpr_write_b32 a113, 0\n" \
"v_accvgpr_write_b32 a114, 0\n" \
"v_accvgpr_write_b32 a115, 0\n" \
"v_accvgpr_write_b32 a116, 0\n" \
"v_accvgpr_write_b32 a117, 0\n" \
"v_accvgpr_write_b32 a118, 0\n" \
"v_accvgpr_write_b32 a119, 0\n" \
"v_accvgpr_write_b32 a120, 0\n" \
"v_accvgpr_write_b32 a121, 0\n" \
"v_accvgpr_write_b32 a122, 0\n" \
"v_accvgpr_write_b32 a123, 0\n" \
"v_accvgpr_write_b32 a124, 0\n" \
"v_accvgpr_write_b32 a125, 0\n" \
"v_accvgpr_write_b32 a126, 0\n" \
"v_accvgpr_write_b32 a127, 0\n" \
"s_mov_b32 s81, 0\n" \
"Lhw_p%=:\n" \
"buffer_load_dword v243, v240, %[rs], s59 offen sc1\n" \
"s_waitcnt vmcnt(0)\n" \
"v_cmp_ne_u32 s[96:97], %[tag], v243\n" \
"s_nop 1\n" \
"s_cmp_eq_u64 s[96:97], 0\n" \
"s_cbranch_scc1 Lhw_d%=\n" \
"s_sleep 4\n" \
"s_add_u32 s81, s81, 1\n" \
"s_cmp_lt_u32 s81, 0x20000\n" \
"s_cbranch_scc1 Lhw_p%=\n" \
"Lhw_d%=:\n" \
"v_readlane_b32 s43, v239, 61\n" \
"s_add_u32 m0, s37, 0\n" \
"s_nop 3\n" \
"buffer_load_dwordx4 v224, %[rs], s43 offen offset:0 sc1 lds\n" \
"buffer_load_dwordx4 v224, %[rs], s43 offen offset:1024 sc1 lds\n" \
"buffer_load_dwordx4 v224, %[rs], s43 offen offset:2048 sc1 lds\n" \
"buffer_load_dwordx4 v224, %[rs], s43 offen offset:3072 sc1 lds\n" \
"s_add_u32 m0, s37, 4096\n" \
"s_add_u32 s43, s43, 0x1000\n" \
"buffer_load_dwordx4 v224, %[rs], s43 offen offset:0 sc1 lds\n" \
"buffer_load_dwordx4 v224, %[rs], s43 offen offset:1024 sc1 lds\n" \
"buffer_load_dwordx4 v224, %[rs], s43 offen offset:2048 sc1 lds\n" \
"buffer_load_dwordx4 v224, %[rs], s43 offen offset:3072 sc1 lds\n" \
"s_cmp_eq_u32 s63, 0\n" \
"s_cbranch_scc1 LnoQ%=\n" \
"s_add_u32 s83, s60, 0\n" \
"buffer_load_dwordx4 a[128:131], v224, %[rs], s83 offen offset:0 sc1\n" \
"buffer_load_dwordx4 a[132:135], v224, %[rs], s83 offen offset:1024 sc1\n" \
"buffer_load_dwordx4 a[136:139], v224, %[rs], s83 offen offset:2048 sc1\n" \
"buffer_load_dwordx4 a[140:143], v224, %[rs], s83 offen offset:3072 sc1\n" \
"s_add_u32 s83, s60, 4096\n" \
"buffer_load_dwordx4 a[144:147], v224, %[rs], s83 offen offset:0 sc1\n" \
"buffer_load_dwordx4 a[148:151], v224, %[rs], s83 offen offset:1024 sc1\n" \
"buffer_load_dwordx4 a[152:155], v224, %[rs], s83 offen offset:2048 sc1\n" \
"buffer_load_dwordx4 a[156:159], v224, %[rs], s83 offen offset:3072 sc1\n" \
"s_add_u32 s83, s60, 8192\n" \
"buffer_load_dwordx4 a[160:163], v224, %[rs], s83 offen offset:0 sc1\n" \
"buffer_load_dwordx4 a[164:167], v224, %[rs], s83 offen offset:1024 sc1\n" \
"buffer_load_dwordx4 a[168:171], v224, %[rs], s83 offen offset:2048 sc1\n" \
"buffer_load_dwordx4 a[172:175], v224, %[rs], s83 offen offset:3072 sc1\n" \
"s_add_u32 s83, s60, 12288\n" \
"buffer_load_dwordx4 a[176:179], v224, %[rs], s83 offen offset:0 sc1\n" \
"buffer_load_dwordx4 a[180:183], v224, %[rs], s83 offen offset:1024 sc1\n" \
"buffer_load_dwordx4 a[184:187], v224, %[rs], s83 offen offset:2048 sc1\n" \
"buffer_load_dwordx4 a[188:191], v224, %[rs], s83 offen offset:3072 sc1\n" \
"LnoQ%=:\n" \
"v_readlane_b32 s43, v239, 62\n" \
"s_add_u32 m0, s37, 32768\n" \
"s_nop 3\n" \
"buffer_load_dwordx4 v224, %[rs], s43 offen offset:0 sc1 lds\n" \
"buffer_load_dwordx4 v224, %[rs], s43 offen offset:1024 sc1 lds\n" \
"buffer_load_dwordx4 v224, %[rs], s43 offen offset:2048 sc1 lds\n" \
"buffer_load_dwordx4 v224, %[rs], s43 offen offset:3072 sc1 lds\n" \
"s_add_u32 m0, s37, 36864\n" \
"s_add_u32 s43, s43, 0x1000\n" \
"buffer_load_dwordx4 v224, %[rs], s43 offen offset:0 sc1 lds\n" \
"buffer_load_dwordx4 v224, %[rs], s43 offen offset:1024 sc1 lds\n" \
"buffer_load_dwordx4 v224, %[rs], s43 offen offset:2048 sc1 lds\n" \
"buffer_load_dwordx4 v224, %[rs], s43 offen offset:3072 sc1 lds\n" \
"v_readlane_b32 s43, v239, 63\n" \
"s_add_u32 m0, s37, 65536\n" \
"s_nop 3\n" \
"buffer_load_dwordx4 v224, %[rs], s43 offen offset:0 sc1 lds\n" \
"buffer_load_dwordx4 v224, %[rs], s43 offen offset:1024 sc1 lds\n" \
"buffer_load_dwordx4 v224, %[rs], s43 offen offset:2048 sc1 lds\n" \
"buffer_load_dwordx4 v224, %[rs], s43 offen offset:3072 sc1 lds\n" \
"s_add_u32 m0, s37, 69632\n" \
"s_add_u32 s43, s43, 0x1000\n" \
"buffer_load_dwordx4 v224, %[rs], s43 offen offset:0 sc1 lds\n" \
"buffer_load_dwordx4 v224, %[rs], s43 offen offset:1024 sc1 lds\n" \
"buffer_load_dwordx4 v224, %[rs], s43 offen offset:2048 sc1 lds\n" \
"buffer_load_dwordx4 v224, %[rs], s43 offen offset:3072 sc1 lds\n" \
"v_readlane_b32 s43, v239, 0\n" \
"s_add_u32 m0, s37, 98304\n" \
"s_waitcnt vmcnt(16)\n" \
"s_barrier\n" \
"ds_read_b128 v[96:99], v178 offset:0\n" \
"ds_read_b128 v[100:103], v178 offset:1024\n" \
"ds_read_b128 v[104:107], v178 offset:2048\n" \
"ds_read_b128 v[108:111], v178 offset:3072\n" \
"ds_read_b128 v[112:115], v178 offset:4096\n" \
"ds_read_b128 v[116:119], v178 offset:5120\n" \
"ds_read_b128 v[120:123], v178 offset:6144\n" \
"ds_read_b128 v[124:127], v178 offset:7168\n" \
"s_waitcnt lgkmcnt(4)\n" \
"v_mov_b32 v160, 0\n" \
"v_mov_b32 v161, 0\n" \
"v_mov_b32 v162, 0\n" \
"v_mov_b32 v163, 0\n" \
"v_mov_b32 v164, 0\n" \
"v_mov_b32 v165, 0\n" \
"v_mov_b32 v166, 0\n" \
"v_mov_b32 v167, 0\n" \
"v_mov_b32 v170, 0\n" \
"v_mov_b32 v171, 0\n" \
"v_mov_b32 v181, 0\n" \
"s_mov_b32 s45, 1\n" \
"s_sub_u32 s52, s46, 1\n" \
"v_mfma_f32_16x16x32_bf16 v[0:3], v[96:99], a[128:131], v[160:163]\n" \
"ds_read_b128 v[128:131], v178 offset:8192\n" \
"buffer_load_dwordx4 v224, %[rs], s43 offen offset:0 sc1 lds\n" \
"v_mfma_f32_16x16x32_bf16 v[4:7], v[96:99], a[160:163], v[164:167]\n" \
"v_mfma_f32_16x16x32_bf16 v[8:11], v[100:103], a[128:131], v[160:163]\n" \
"ds_read_b128 v[132:135], v178 offset:9216\n" \
"v_readlane_b32 s48, v238, s42\n" \
"v_mfma_f32_16x16x32_bf16 v[12:15], v[100:103], a[160:163], v[164:167]\n" \
"v_mfma_f32_16x16x32_bf16 v[0:3], v[104:107], a[132:135], v[0:3]\n" \
"ds_read_b128 v[136:139], v178 offset:10240\n" \
"buffer_load_dwordx4 v224, %[rs], s43 offen offset:1024 sc1 lds\n" \
"v_mfma_f32_16x16x32_bf16 v[4:7], v[104:107], a[164:167], v[4:7]\n" \
"v_mfma_f32_16x16x32_bf16 v[8:11], v[108:111], a[132:135], v[8:11]\n" \
"ds_read_b128 v[140:143], v178 offset:11264\n" \
"s_waitcnt lgkmcnt(4)\n" \
"v_mfma_f32_16x16x32_bf16 v[12:15], v[108:111], a[164:167], v[12:15]\n" \
"v_mfma_f32_16x16x32_bf16 v[0:3], v[112:115], a[136:139], v[0:3]\n" \
"ds_read_b128 v[144:147], v178 offset:12288\n" \
"s_add_u32 s42, s42, 1\n" \
"v_mfma_f32_16x16x32_bf16 v[4:7], v[112:115], a[168:171], v[4:7]\n" \
"v_mfma_f32_16x16x32_bf16 v[8:11], v[116:119], a[136:139], v[8:11]\n" \
"ds_read_b128 v[148:151], v178 offset:13312\n" \
"buffer_load_dwordx4 v224, %[rs], s43 offen offset:2048 sc1 lds\n" \
"v_mfma_f32_16x16x32_bf16 v[12:15], v[116:119], a[168:171], v[12:15]\n" \
"v_mfma_f32_16x16x32_bf16 v[0:3], v[120:123], a[140:143], v[0:3]\n" \
"ds_read_b128 v[152:155], v178 offset:14336\n" \
"v_add_u32 v179, s48, v224\n" \
"v_mfma_f32_16x16x32_bf16 v[4:7], v[120:123], a[172:175], v[4:7]\n" \
"v_mfma_f32_16x16x32_bf16 v[8:11], v[124:127], a[140:143], v[8:11]\n" \
"ds_read_b128 v[156:159], v178 offset:15360\n" \
"s_waitcnt lgkmcnt(4)\n" \
"v_mfma_f32_16x16x32_bf16 v[12:15], v[124:127], a[172:175], v[12:15]\n" \
"v_mfma_f32_16x16x32_bf16 v[0:3], v[128:131], a[144:147], v[0:3]\n" \
"ds_read_b128 v[96:99], v178 offset:16384\n" \
"buffer_load_dwordx4 v224, %[rs], s43 offen offset:3072 sc1 lds\n" \
"v_mfma_f32_16x16x32_bf16 v[4:7], v[128:131], a[176:179], v[4:7]\n" \
"v_mfma_f32_16x16x32_bf16 v[8:11], v[132:135], a[144:147], v[8:11]\n" \
"ds_read_b128 v[100:103], v178 offset:17408\n" \
"s_add_u32 m0, m0, 0x1000\n" \
"s_add_u32 s43, s43, 0x1000\n" \
"v_mfma_f32_16x16x32_bf16 v[12:15], v[132:135], a[176:179], v[12:15]\n" \
"v_mfma_f32_16x16x32_bf16 v[0:3], v[136:139], a[148:151], v[0:3]\n" \
"ds_read_b128 v[104:107], v178 offset:18432\n" \
"v_mfma_f32_16x16x32_bf16 v[4:7], v[136:139], a[180:183], v[4:7]\n" \
"v_mfma_f32_16x16x32_bf16 v[8:11], v[140:143], a[148:151], v[8:11]\n" \
"ds_read_b128 v[108:111], v178 offset:19456\n" \
"s_waitcnt lgkmcnt(4)\n" \
"v_mfma_f32_16x16x32_bf16 v[12:15], v[140:143], a[180:183], v[12:15]\n" \
"v_mfma_f32_16x16x32_bf16 v[0:3], v[144:147], a[152:155], v[0:3]\n" \
"ds_read_b128 v[112:115], v178 offset:20480\n" \
"buffer_load_dwordx4 v224, %[rs], s43 offen offset:0 sc1 lds\n" \
"v_mfma_f32_16x16x32_bf16 v[4:7], v[144:147], a[184:187], v[4:7]\n" \
"v_mfma_f32_16x16x32_bf16 v[8:11], v[148:151], a[152:155], v[8:11]\n" \
"ds_read_b128 v[116:119], v178 offset:21504\n" \
"v_mfma_f32_16x16x32_bf16 v[12:15], v[148:151], a[184:187], v[12:15]\n" \
"v_mfma_f32_16x16x32_bf16 v[0:3], v[152:155], a[156:159], v[0:3]\n" \
"ds_read_b128 v[120:123], v178 offset:22528\n" \
"buffer_load_dwordx4 v224, %[rs], s43 offen offset:1024 sc1 lds\n" \
"v_mfma_f32_16x16x32_bf16 v[4:7], v[152:155], a[188:191], v[4:7]\n" \
"v_mfma_f32_16x16x32_bf16 v[8:11], v[156:159], a[156:159], v[8:11]\n" \
"ds_read_b128 v[124:127], v178 offset:23552\n" \
"s_waitcnt lgkmcnt(4)\n" \
"v_mfma_f32_16x16x32_bf16 v[12:15], v[156:159], a[188:191], v[12:15]\n" \
"v_mfma_f32_16x16x32_bf16 v[16:19], v[96:99], a[128:131], v[160:163]\n" \
"ds_read_b128 v[128:131], v178 offset:24576\n" \
"v_mfma_f32_16x16x32_bf16 v[20:23], v[96:99], a[160:163], v[164:167]\n" \
"v_mfma_f32_16x16x32_bf16 v[24:27], v[100:103], a[128:131], v[160:163]\n" \
"ds_read_b128 v[132:135], v178 offset:25600\n" \
"buffer_load_dwordx4 v224, %[rs], s43 offen offset:2048 sc1 lds\n" \
"v_mfma_f32_16x16x32_bf16 v[28:31], v[100:103], a[160:163], v[164:167]\n" \
"v_mfma_f32_16x16x32_bf16 v[16:19], v[104:107], a[132:135], v[16:19]\n" \
"ds_read_b128 v[136:139], v178 offset:26624\n" \
"v_mfma_f32_16x16x32_bf16 v[20:23], v[104:107], a[164:167], v[20:23]\n" \
"v_mfma_f32_16x16x32_bf16 v[24:27], v[108:111], a[132:135], v[24:27]\n" \
"ds_read_b128 v[140:143], v178 offset:27648\n" \
"s_waitcnt lgkmcnt(4)\n" \
"v_mfma_f32_16x16x32_bf16 v[28:31], v[108:111], a[164:167], v[28:31]\n" \
"v_mfma_f32_16x16x32_bf16 v[16:19], v[112:115], a[136:139], v[16:19]\n" \
"ds_read_b128 v[144:147], v178 offset:28672\n" \
"buffer_load_dwordx4 v224, %[rs], s43 offen offset:3072 sc1 lds\n" \
"v_mfma_f32_16x16x32_bf16 v[20:23], v[112:115], a[168:171], v[20:23]\n" \
"v_mfma_f32_16x16x32_bf16 v[24:27], v[116:119], a[136:139], v[24:27]\n" \
"ds_read_b128 v[148:151], v178 offset:29696\n" \
"v_readlane_b32 s43, v239, s42\n" \
"v_mfma_f32_16x16x32_bf16 v[28:31], v[116:119], a[168:171], v[28:31]\n" \
"v_mfma_f32_16x16x32_bf16 v[16:19], v[120:123], a[140:143], v[16:19]\n" \
"ds_read_b128 v[152:155], v178 offset:30720\n" \
"s_add_u32 m0, s44, s37\n" \
"v_mfma_f32_16x16x32_bf16 v[20:23], v[120:123], a[172:175], v[20:23]\n" \
"v_mfma_f32_16x16x32_bf16 v[24:27], v[124:127], a[140:143], v[24:27]\n" \
"ds_read_b128 v[156:159], v178 offset:31744\n" \
"s_waitcnt lgkmcnt(4)\n" \
"v_mfma_f32_16x16x32_bf16 v[28:31], v[124:127], a[172:175], v[28:31]\n" \
"s_waitcnt vmcnt(16)\n" \
"s_barrier\n" \
"v_mfma_f32_16x16x32_bf16 v[16:19], v[128:131], a[144:147], v[16:19]\n" \
"ds_read_b128 v[96:99], v179 offset:0\n" \
"v_mfma_f32_16x16x32_bf16 v[20:23], v[128:131], a[176:179], v[20:23]\n" \
"v_mfma_f32_16x16x32_bf16 v[24:27], v[132:135], a[144:147], v[24:27]\n" \
"ds_read_b128 v[100:103], v179 offset:1024\n" \
"s_mov_b32 s44, s41\n" \
"v_mfma_f32_16x16x32_bf16 v[28:31], v[132:135], a[176:179], v[28:31]\n" \
"v_mfma_f32_16x16x32_bf16 v[16:19], v[136:139], a[148:151], v[16:19]\n" \
"ds_read_b128 v[104:107], v179 offset:2048\n" \
"s_mov_b32 s41, s48\n" \
"v_mfma_f32_16x16x32_bf16 v[20:23], v[136:139], a[180:183], v[20:23]\n" \
"v_mfma_f32_16x16x32_bf16 v[24:27], v[140:143], a[148:151], v[24:27]\n" \
"ds_read_b128 v[108:111], v179 offset:3072\n" \
"s_waitcnt lgkmcnt(4)\n" \
"v_mfma_f32_16x16x32_bf16 v[28:31], v[140:143], a[180:183], v[28:31]\n" \
"v_mfma_f32_16x16x32_bf16 v[16:19], v[144:147], a[152:155], v[16:19]\n" \
"ds_read_b128 v[112:115], v179 offset:4096\n" \
"v_mfma_f32_16x16x32_bf16 v[20:23], v[144:147], a[184:187], v[20:23]\n" \
"v_mfma_f32_16x16x32_bf16 v[24:27], v[148:151], a[152:155], v[24:27]\n" \
"ds_read_b128 v[116:119], v179 offset:5120\n" \
"v_mfma_f32_16x16x32_bf16 v[28:31], v[148:151], a[184:187], v[28:31]\n" \
"v_mfma_f32_16x16x32_bf16 v[16:19], v[152:155], a[156:159], v[16:19]\n" \
"ds_read_b128 v[120:123], v179 offset:6144\n" \
"v_mfma_f32_16x16x32_bf16 v[20:23], v[152:155], a[188:191], v[20:23]\n" \
"v_mfma_f32_16x16x32_bf16 v[24:27], v[156:159], a[156:159], v[24:27]\n" \
"ds_read_b128 v[124:127], v179 offset:7168\n" \
"s_waitcnt lgkmcnt(4)\n" \
"v_mfma_f32_16x16x32_bf16 v[28:31], v[156:159], a[188:191], v[28:31]\n" \
"v_mov_b32 v178, v179\n" \
"Litem%=:\n" \
"s_call_b64 s[92:93], Lspec0%=\n" \
"s_nop 7\n" \
"v_max3_f32 v172, v0, v1, v2\n" \
"v_max3_f32 v184, v3, v8, v9\n" \
"v_max3_f32 v172, v172, v10, v11\n" \
"v_max3_f32 v184, v184, v16, v17\n" \
"v_max3_f32 v172, v172, v18, v19\n" \
"v_max3_f32 v184, v184, v24, v25\n" \
"v_max3_f32 v172, v172, v26, v27\n" \
"v_max_f32 v172, v172, v184\n" \
"v_mov_b32 v184, v172\n" \
"s_nop 1\n" \
"v_permlane16_swap_b32 v184, v172\n" \
"v_max_f32 v172, v172, v184\n" \
"v_mov_b32 v184, v172\n" \
"s_nop 1\n" \
"v_permlane32_swap_b32 v184, v172\n" \
"v_max_f32 v172, v172, v184\n" \
"v_max_f32 v172, 0xf149f2ca, v172\n" \
"v_sub_f32 v160, 0, v172\n" \
"v_sub_f32 v161, 0, v172\n" \
"v_sub_f32 v162, 0, v172\n" \
"v_sub_f32 v163, 0, v172\n" \
"v_sub_f32 v0, v0, v172\n" \
"v_sub_f32 v1, v1, v172\n" \
"v_sub_f32 v2, v2, v172\n" \
"v_sub_f32 v3, v3, v172\n" \
"v_sub_f32 v8, v8, v172\n" \
"v_sub_f32 v9, v9, v172\n" \
"v_sub_f32 v10, v10, v172\n" \
"v_sub_f32 v11, v11, v172\n" \
"v_sub_f32 v16, v16, v172\n" \
"v_sub_f32 v17, v17, v172\n" \
"v_sub_f32 v18, v18, v172\n" \
"v_sub_f32 v19, v19, v172\n" \
"v_sub_f32 v24, v24, v172\n" \
"v_sub_f32 v25, v25, v172\n" \
"v_sub_f32 v26, v26, v172\n" \
"v_sub_f32 v27, v27, v172\n" \
"v_max3_f32 v173, v4, v5, v6\n" \
"v_max3_f32 v184, v7, v12, v13\n" \
"v_max3_f32 v173, v173, v14, v15\n" \
"v_max3_f32 v184, v184, v20, v21\n" \
"v_max3_f32 v173, v173, v22, v23\n" \
"v_max3_f32 v184, v184, v28, v29\n" \
"v_max3_f32 v173, v173, v30, v31\n" \
"v_max_f32 v173, v173, v184\n" \
"v_mov_b32 v184, v173\n" \
"s_nop 1\n" \
"v_permlane16_swap_b32 v184, v173\n" \
"v_max_f32 v173, v173, v184\n" \
"v_mov_b32 v184, v173\n" \
"s_nop 1\n" \
"v_permlane32_swap_b32 v184, v173\n" \
"v_max_f32 v173, v173, v184\n" \
"v_max_f32 v173, 0xf149f2ca, v173\n" \
"v_sub_f32 v164, 0, v173\n" \
"v_sub_f32 v165, 0, v173\n" \
"v_sub_f32 v166, 0, v173\n" \
"v_sub_f32 v167, 0, v173\n" \
"v_sub_f32 v4, v4, v173\n" \
"v_sub_f32 v5, v5, v173\n" \
"v_sub_f32 v6, v6, v173\n" \
"v_sub_f32 v7, v7, v173\n" \
"v_sub_f32 v12, v12, v173\n" \
"v_sub_f32 v13, v13, v173\n" \
"v_sub_f32 v14, v14, v173\n" \
"v_sub_f32 v15, v15, v173\n" \
"v_sub_f32 v20, v20, v173\n" \
"v_sub_f32 v21, v21, v173\n" \
"v_sub_f32 v22, v22, v173\n" \
"v_sub_f32 v23, v23, v173\n" \
"v_sub_f32 v28, v28, v173\n" \
"v_sub_f32 v29, v29, v173\n" \
"v_sub_f32 v30, v30, v173\n" \
"v_sub_f32 v31, v31, v173\n" \
"v_exp_f32 v168, v0\n" \
"v_exp_f32 v175, v1\n" \
"s_nop 0\n" \
"v_cvt_pk_bf16_f32 v64, v168, v175\n" \
"v_add_f32 v168, v168, v175\n" \
"v_exp_f32 v176, v2\n" \
"v_exp_f32 v177, v3\n" \
"s_nop 0\n" \
"v_cvt_pk_bf16_f32 v65, v176, v177\n" \
"v_add_f32 v168, v168, v176\n" \
"v_add_f32 v168, v168, v177\n" \
"v_exp_f32 v169, v4\n" \
"v_exp_f32 v175, v5\n" \
"s_nop 0\n" \
"v_cvt_pk_bf16_f32 v72, v169, v175\n" \
"v_add_f32 v169, v169, v175\n" \
"v_exp_f32 v176, v6\n" \
"v_exp_f32 v177, v7\n" \
"s_nop 0\n" \
"v_cvt_pk_bf16_f32 v73, v176, v177\n" \
"v_add_f32 v169, v169, v176\n" \
"v_add_f32 v169, v169, v177\n" \
"v_exp_f32 v174, v8\n" \
"v_exp_f32 v175, v9\n" \
"s_nop 0\n" \
"v_cvt_pk_bf16_f32 v66, v174, v175\n" \
"v_add_f32 v168, v168, v174\n" \
"v_add_f32 v168, v168, v175\n" \
"v_exp_f32 v176, v10\n" \
"v_exp_f32 v177, v11\n" \
"s_nop 0\n" \
"v_cvt_pk_bf16_f32 v67, v176, v177\n" \
"v_add_f32 v168, v168, v176\n" \
"v_add_f32 v168, v168, v177\n" \
"v_exp_f32 v174, v12\n" \
"v_exp_f32 v175, v13\n" \
"s_nop 0\n" \
"v_cvt_pk_bf16_f32 v74, v174, v175\n" \
"v_add_f32 v169, v169, v174\n" \
"v_add_f32 v169, v169, v175\n" \
"v_exp_f32 v176, v14\n" \
"v_exp_f32 v177, v15\n" \
"s_nop 0\n" \
"v_cvt_pk_bf16_f32 v75, v176, v177\n" \
"v_add_f32 v169, v169, v176\n" \
"v_add_f32 v169, v169, v177\n" \
"v_exp_f32 v174, v16\n" \
"v_exp_f32 v175, v17\n" \
"s_nop 0\n" \
"v_cvt_pk_bf16_f32 v68, v174, v175\n" \
"v_add_f32 v168, v168, v174\n" \
"v_add_f32 v168, v168, v175\n" \
"s_add_u32 s47, s47, 64\n" \
"s_cmp_eq_u32 s46, 1\n" \
"s_cbranch_scc1 Ldr0%=\n" \
"s_cmp_eq_u32 s47, 64\n" \
"s_cbranch_scc1 LAH1%=\n" \
".p2align 6\n" \
"LA1%=:\n" \
"v_mfma_f32_16x16x32_bf16 v[32:35], v[96:99], a[128:131], v[160:163]\n" \
"ds_read_b128 v[128:131], v178 offset:8192\n" \
"buffer_load_dwordx4 v224, %[rs], s43 offen offset:0 sc1 lds\n" \
"v_mfma_f32_16x16x32_bf16 v[36:39], v[96:99], a[160:163], v[164:167]\n" \
"v_exp_f32 v176, v18\n" \
"v_mfma_f32_16x16x32_bf16 v[40:43], v[100:103], a[128:131], v[160:163]\n" \
"ds_read_b128 v[132:135], v178 offset:9216\n" \
"v_readlane_b32 s48, v238, s42\n" \
"v_mfma_f32_16x16x32_bf16 v[44:47], v[100:103], a[160:163], v[164:167]\n" \
"v_exp_f32 v177, v19\n" \
"v_mfma_f32_16x16x32_bf16 v[32:35], v[104:107], a[132:135], v[32:35]\n" \
"ds_read_b128 v[136:139], v178 offset:10240\n" \
"buffer_load_dwordx4 v224, %[rs], s43 offen offset:1024 sc1 lds\n" \
"v_mfma_f32_16x16x32_bf16 v[36:39], v[104:107], a[164:167], v[36:39]\n" \
"v_cvt_pk_bf16_f32 v69, v176, v177\n" \
"v_add_f32 v168, v168, v176\n" \
"v_mfma_f32_16x16x32_bf16 v[40:43], v[108:111], a[132:135], v[40:43]\n" \
"ds_read_b128 v[140:143], v178 offset:11264\n" \
"s_waitcnt lgkmcnt(4)\n" \
"v_mfma_f32_16x16x32_bf16 v[44:47], v[108:111], a[164:167], v[44:47]\n" \
"v_exp_f32 v174, v20\n" \
"v_mfma_f32_16x16x32_bf16 v[32:35], v[112:115], a[136:139], v[32:35]\n" \
"ds_read_b128 v[144:147], v178 offset:12288\n" \
"s_add_u32 s42, s42, 1\n" \
"v_add_f32 v168, v168, v177\n" \
"v_mfma_f32_16x16x32_bf16 v[36:39], v[112:115], a[168:171], v[36:39]\n" \
"v_exp_f32 v175, v21\n" \
"v_mfma_f32_16x16x32_bf16 v[40:43], v[116:119], a[136:139], v[40:43]\n" \
"ds_read_b128 v[148:151], v178 offset:13312\n" \
"buffer_load_dwordx4 v224, %[rs], s43 offen offset:2048 sc1 lds\n" \
"v_mfma_f32_16x16x32_bf16 v[44:47], v[116:119], a[168:171], v[44:47]\n" \
"v_cvt_pk_bf16_f32 v76, v174, v175\n" \
"v_add_f32 v169, v169, v174\n" \
"v_mfma_f32_16x16x32_bf16 v[32:35], v[120:123], a[140:143], v[32:35]\n" \
"ds_read_b128 v[152:155], v178 offset:14336\n" \
"v_add_u32 v179, s48, v224\n" \
"v_mfma_f32_16x16x32_bf16 v[36:39], v[120:123], a[172:175], v[36:39]\n" \
"v_exp_f32 v176, v22\n" \
"v_mfma_f32_16x16x32_bf16 v[40:43], v[124:127], a[140:143], v[40:43]\n" \
"ds_read_b128 v[156:159], v178 offset:15360\n" \
"s_waitcnt lgkmcnt(4)\n" \
"v_add_f32 v169, v169, v175\n" \
"v_mfma_f32_16x16x32_bf16 v[44:47], v[124:127], a[172:175], v[44:47]\n" \
"v_exp_f32 v177, v23\n" \
"v_mfma_f32_16x16x32_bf16 v[32:35], v[128:131], a[144:147], v[32:35]\n" \
"ds_read_b128 v[96:99], v178 offset:16384\n" \
"buffer_load_dwordx4 v224, %[rs], s43 offen offset:3072 sc1 lds\n" \
"v_mfma_f32_16x16x32_bf16 v[36:39], v[128:131], a[176:179], v[36:39]\n" \
"v_cvt_pk_bf16_f32 v77, v176, v177\n" \
"v_add_f32 v169, v169, v176\n" \
"v_mfma_f32_16x16x32_bf16 v[40:43], v[132:135], a[144:147], v[40:43]\n" \
"ds_read_b128 v[100:103], v178 offset:17408\n" \
"s_add_u32 m0, m0, 0x1000\n" \
"s_add_u32 s43, s43, 0x1000\n" \
"v_mfma_f32_16x16x32_bf16 v[44:47], v[132:135], a[176:179], v[44:47]\n" \
"v_exp_f32 v174, v24\n" \
"v_mfma_f32_16x16x32_bf16 v[32:35], v[136:139], a[148:151], v[32:35]\n" \
"ds_read_b128 v[104:107], v178 offset:18432\n" \
"v_add_f32 v169, v169, v177\n" \
"v_mfma_f32_16x16x32_bf16 v[36:39], v[136:139], a[180:183], v[36:39]\n" \
"v_exp_f32 v175, v25\n" \
"v_mfma_f32_16x16x32_bf16 v[40:43], v[140:143], a[148:151], v[40:43]\n" \
"ds_read_b128 v[108:111], v178 offset:19456\n" \
"s_waitcnt lgkmcnt(4)\n" \
"v_mfma_f32_16x16x32_bf16 v[44:47], v[140:143], a[180:183], v[44:47]\n" \
"v_cvt_pk_bf16_f32 v70, v174, v175\n" \
"v_add_f32 v168, v168, v174\n" \
"v_mfma_f32_16x16x32_bf16 v[32:35], v[144:147], a[152:155], v[32:35]\n" \
"ds_read_b128 v[112:115], v178 offset:20480\n" \
"buffer_load_dwordx4 v224, %[rs], s43 offen offset:0 sc1 lds\n" \
"v_mfma_f32_16x16x32_bf16 v[36:39], v[144:147], a[184:187], v[36:39]\n" \
"v_exp_f32 v176, v26\n" \
"v_mfma_f32_16x16x32_bf16 v[40:43], v[148:151], a[152:155], v[40:43]\n" \
"ds_read_b128 v[116:119], v178 offset:21504\n" \
"v_add_f32 v168, v168, v175\n" \
"v_mfma_f32_16x16x32_bf16 v[44:47], v[148:151], a[184:187], v[44:47]\n" \
"v_exp_f32 v177, v27\n" \
"v_mfma_f32_16x16x32_bf16 v[32:35], v[152:155], a[156:159], v[32:35]\n" \
"ds_read_b128 v[120:123], v178 offset:22528\n" \
"buffer_load_dwordx4 v224, %[rs], s43 offen offset:1024 sc1 lds\n" \
"v_mfma_f32_16x16x32_bf16 v[36:39], v[152:155], a[188:191], v[36:39]\n" \
"v_cvt_pk_bf16_f32 v71, v176, v177\n" \
"v_add_f32 v168, v168, v176\n" \
"v_mfma_f32_16x16x32_bf16 v[40:43], v[156:159], a[156:159], v[40:43]\n" \
"ds_read_b128 v[124:127], v178 offset:23552\n" \
"s_waitcnt lgkmcnt(4)\n" \
"v_mfma_f32_16x16x32_bf16 v[44:47], v[156:159], a[188:191], v[44:47]\n" \
"v_exp_f32 v174, v28\n" \
"v_mfma_f32_16x16x32_bf16 v[48:51], v[96:99], a[128:131], v[160:163]\n" \
"ds_read_b128 v[128:131], v178 offset:24576\n" \
"v_add_f32 v168, v168, v177\n" \
"v_mfma_f32_16x16x32_bf16 v[52:55], v[96:99], a[160:163], v[164:167]\n" \
"v_exp_f32 v175, v29\n" \
"v_mfma_f32_16x16x32_bf16 v[56:59], v[100:103], a[128:131], v[160:163]\n" \
"ds_read_b128 v[132:135], v178 offset:25600\n" \
"buffer_load_dwordx4 v224, %[rs], s43 offen offset:2048 sc1 lds\n" \
"v_mfma_f32_16x16x32_bf16 v[60:63], v[100:103], a[160:163], v[164:167]\n" \
"v_cvt_pk_bf16_f32 v78, v174, v175\n" \
"v_add_f32 v169, v169, v174\n" \
"v_mfma_f32_16x16x32_bf16 v[48:51], v[104:107], a[132:135], v[48:51]\n" \
"ds_read_b128 v[136:139], v178 offset:26624\n" \
"v_add_f32 v169, v169, v175\n" \
"v_mfma_f32_16x16x32_bf16 v[52:55], v[104:107], a[164:167], v[52:55]\n" \
"v_exp_f32 v176, v30\n" \
"v_mfma_f32_16x16x32_bf16 v[56:59], v[108:111], a[132:135], v[56:59]\n" \
"ds_read_b128 v[140:143], v178 offset:27648\n" \
"s_waitcnt lgkmcnt(4)\n" \
"v_mfma_f32_16x16x32_bf16 v[60:63], v[108:111], a[164:167], v[60:63]\n" \
"v_exp_f32 v177, v31\n" \
"v_mfma_f32_16x16x32_bf16 v[48:51], v[112:115], a[136:139], v[48:51]\n" \
"ds_read_b128 v[144:147], v178 offset:28672\n" \
"buffer_load_dwordx4 v224, %[rs], s43 offen offset:3072 sc1 lds\n" \
"v_mfma_f32_16x16x32_bf16 v[52:55], v[112:115], a[168:171], v[52:55]\n" \
"v_cvt_pk_bf16_f32 v79, v176, v177\n" \
"v_add_f32 v169, v169, v176\n" \
"v_mfma_f32_16x16x32_bf16 v[56:59], v[116:119], a[136:139], v[56:59]\n" \
"ds_read_b128 v[148:151], v178 offset:29696\n" \
"v_readlane_b32 s43, v239, s42\n" \
"v_mfma_f32_16x16x32_bf16 v[60:63], v[116:119], a[168:171], v[60:63]\n" \
"v_mfma_f32_16x16x32_bf16 v[48:51], v[120:123], a[140:143], v[48:51]\n" \
"ds_read_b128 v[152:155], v178 offset:30720\n" \
"s_add_u32 m0, s44, s37\n" \
"v_add_f32 v169, v169, v177\n" \
"v_mfma_f32_16x16x32_bf16 v[52:55], v[120:123], a[172:175], v[52:55]\n" \
"v_add_f32 v180, v168, v169\n" \
"v_mfma_f32_16x16x32_bf16 v[56:59], v[124:127], a[140:143], v[56:59]\n" \
"ds_read_b128 v[156:159], v178 offset:31744\n" \
"s_waitcnt lgkmcnt(4)\n" \
"v_mfma_f32_16x16x32_bf16 v[60:63], v[124:127], a[172:175], v[60:63]\n" \
"v_cmp_nge_f32 vcc, s51, v180\n" \
"s_waitcnt vmcnt(16)\n" \
"s_barrier\n" \
"v_mfma_f32_16x16x32_bf16 v[48:51], v[128:131], a[144:147], v[48:51]\n" \
"ds_read_b128 v[96:99], v179 offset:0\n" \
"v_mfma_f32_16x16x32_bf16 v[52:55], v[128:131], a[176:179], v[52:55]\n" \
"v_mfma_f32_16x16x32_bf16 v[56:59], v[132:135], a[144:147], v[56:59]\n" \
"ds_read_b128 v[100:103], v179 offset:1024\n" \
"s_mov_b32 s44, s41\n" \
"v_mfma_f32_16x16x32_bf16 v[60:63], v[132:135], a[176:179], v[60:63]\n" \
"v_mfma_f32_16x16x32_bf16 v[48:51], v[136:139], a[148:151], v[48:51]\n" \
"ds_read_b128 v[104:107], v179 offset:2048\n" \
"s_mov_b32 s41, s48\n" \
"v_mfma_f32_16x16x32_bf16 v[52:55], v[136:139], a[180:183], v[52:55]\n" \
"v_mfma_f32_16x16x32_bf16 v[56:59], v[140:143], a[148:151], v[56:59]\n" \
"ds_read_b128 v[108:111], v179 offset:3072\n" \
"s_waitcnt lgkmcnt(4)\n" \
"v_mfma_f32_16x16x32_bf16 v[60:63], v[140:143], a[180:183], v[60:63]\n" \
"v_mfma_f32_16x16x32_bf16 v[48:51], v[144:147], a[152:155], v[48:51]\n" \
"ds_read_b128 v[112:115], v179 offset:4096\n" \
"v_mfma_f32_16x16x32_bf16 v[52:55], v[144:147], a[184:187], v[52:55]\n" \
"s_cmp_ge_i32 s47, 0\n" \
"v_mfma_f32_16x16x32_bf16 v[56:59], v[148:151], a[152:155], v[56:59]\n" \
"ds_read_b128 v[116:119], v179 offset:5120\n" \
"v_mfma_f32_16x16x32_bf16 v[60:63], v[148:151], a[184:187], v[60:63]\n" \
"s_cselect_b32 s95, 1, s49\n" \
"v_mfma_f32_16x16x32_bf16 v[48:51], v[152:155], a[156:159], v[48:51]\n" \
"ds_read_b128 v[120:123], v179 offset:6144\n" \
"v_mfma_f32_16x16x32_bf16 v[52:55], v[152:155], a[188:191], v[52:55]\n" \
"s_cmp_lg_u32 s95, 0\n" \
"v_mfma_f32_16x16x32_bf16 v[56:59], v[156:159], a[156:159], v[56:59]\n" \
"ds_read_b128 v[124:127], v179 offset:7168\n" \
"s_waitcnt lgkmcnt(4)\n" \
"v_mfma_f32_16x16x32_bf16 v[60:63], v[156:159], a[188:191], v[60:63]\n" \
"v_mov_b32 v178, v179\n" \
"LaF%=:\n" \
"s_cbranch_scc1 LaS1%=\n" \
"LaR1%=:\n" \
"s_cbranch_vccnz LaV1%=\n" \
"LaC1%=:\n" \
"v_mfma_f32_16x16x32_bf16 a[0:3], v[96:99], v[64:67], a[0:3]\n" \
"ds_read_b128 v[128:131], v178 offset:8192\n" \
"buffer_load_dwordx4 v224, %[rs], s43 offen offset:0 sc1 lds\n" \
"v_mfma_f32_16x16x32_bf16 a[4:7], v[96:99], v[72:75], a[4:7]\n" \
"v_add_f32 v170, v170, v168\n" \
"v_add_f32 v171, v171, v169\n" \
"v_mfma_f32_16x16x32_bf16 a[8:11], v[100:103], v[64:67], a[8:11]\n" \
"ds_read_b128 v[132:135], v178 offset:9216\n" \
"v_readlane_b32 s48, v238, s42\n" \
"v_mfma_f32_16x16x32_bf16 a[12:15], v[100:103], v[72:75], a[12:15]\n" \
"v_exp_f32 v168, v32\n" \
"v_mfma_f32_16x16x32_bf16 a[16:19], v[104:107], v[64:67], a[16:19]\n" \
"ds_read_b128 v[136:139], v178 offset:10240\n" \
"buffer_load_dwordx4 v224, %[rs], s43 offen offset:1024 sc1 lds\n" \
"v_mfma_f32_16x16x32_bf16 a[20:23], v[104:107], v[72:75], a[20:23]\n" \
"v_exp_f32 v175, v33\n" \
"v_mfma_f32_16x16x32_bf16 a[24:27], v[108:111], v[64:67], a[24:27]\n" \
"ds_read_b128 v[140:143], v178 offset:11264\n" \
"s_waitcnt lgkmcnt(4)\n" \
"v_mfma_f32_16x16x32_bf16 a[28:31], v[108:111], v[72:75], a[28:31]\n" \
"v_cvt_pk_bf16_f32 v80, v168, v175\n" \
"v_add_f32 v168, v168, v175\n" \
"v_mfma_f32_16x16x32_bf16 a[32:35], v[112:115], v[64:67], a[32:35]\n" \
"ds_read_b128 v[144:147], v178 offset:12288\n" \
"s_add_u32 s42, s42, 1\n" \
"v_mfma_f32_16x16x32_bf16 a[36:39], v[112:115], v[72:75], a[36:39]\n" \
"v_exp_f32 v176, v34\n" \
"v_mfma_f32_16x16x32_bf16 a[40:43], v[116:119], v[64:67], a[40:43]\n" \
"ds_read_b128 v[148:151], v178 offset:13312\n" \
"buffer_load_dwordx4 v224, %[rs], s43 offen offset:2048 sc1 lds\n" \
"v_mfma_f32_16x16x32_bf16 a[44:47], v[116:119], v[72:75], a[44:47]\n" \
"v_exp_f32 v177, v35\n" \
"v_mfma_f32_16x16x32_bf16 a[48:51], v[120:123], v[64:67], a[48:51]\n" \
"ds_read_b128 v[152:155], v178 offset:14336\n" \
"v_add_u32 v179, s48, v224\n" \
"v_mfma_f32_16x16x32_bf16 a[52:55], v[120:123], v[72:75], a[52:55]\n" \
"v_cvt_pk_bf16_f32 v81, v176, v177\n" \
"v_add_f32 v168, v168, v176\n" \
"v_mfma_f32_16x16x32_bf16 a[56:59], v[124:127], v[64:67], a[56:59]\n" \
"ds_read_b128 v[156:159], v178 offset:15360\n" \
"s_waitcnt lgkmcnt(4)\n" \
"v_mfma_f32_16x16x32_bf16 a[60:63], v[124:127], v[72:75], a[60:63]\n" \
"v_exp_f32 v169, v36\n" \
"v_mfma_f32_16x16x32_bf16 a[64:67], v[128:131], v[64:67], a[64:67]\n" \
"ds_read_b128 v[96:99], v178 offset:16384\n" \
"buffer_load_dwordx4 v224, %[rs], s43 offen offset:3072 sc1 lds\n" \
"v_add_f32 v168, v168, v177\n" \
"v_mfma_f32_16x16x32_bf16 a[68:71], v[128:131], v[72:75], a[68:71]\n" \
"v_exp_f32 v175, v37\n" \
"v_mfma_f32_16x16x32_bf16 a[72:75], v[132:135], v[64:67], a[72:75]\n" \
"ds_read_b128 v[100:103], v178 offset:17408\n" \
"s_add_u32 m0, m0, 0x1000\n" \
"s_add_u32 s43, s43, 0x1000\n" \
"v_mfma_f32_16x16x32_bf16 a[76:79], v[132:135], v[72:75], a[76:79]\n" \
"v_cvt_pk_bf16_f32 v88, v169, v175\n" \
"v_add_f32 v169, v169, v175\n" \
"v_mfma_f32_16x16x32_bf16 a[80:83], v[136:139], v[64:67], a[80:83]\n" \
"ds_read_b128 v[104:107], v178 offset:18432\n" \
"v_mfma_f32_16x16x32_bf16 a[84:87], v[136:139], v[72:75], a[84:87]\n" \
"v_exp_f32 v176, v38\n" \
"v_mfma_f32_16x16x32_bf16 a[88:91], v[140:143], v[64:67], a[88:91]\n" \
"ds_read_b128 v[108:111], v178 offset:19456\n" \
"s_waitcnt lgkmcnt(4)\n" \
"v_mfma_f32_16x16x32_bf16 a[92:95], v[140:143], v[72:75], a[92:95]\n" \
"v_exp_f32 v177, v39\n" \
"v_mfma_f32_16x16x32_bf16 a[96:99], v[144:147], v[64:67], a[96:99]\n" \
"ds_read_b128 v[112:115], v178 offset:20480\n" \
"buffer_load_dwordx4 v224, %[rs], s43 offen offset:0 sc1 lds\n" \
"v_mfma_f32_16x16x32_bf16 a[100:103], v[144:147], v[72:75], a[100:103]\n" \
"v_cvt_pk_bf16_f32 v89, v176, v177\n" \
"v_add_f32 v169, v169, v176\n" \
"v_mfma_f32_16x16x32_bf16 a[104:107], v[148:151], v[64:67], a[104:107]\n" \
"ds_read_b128 v[116:119], v178 offset:21504\n" \
"v_add_f32 v169, v169, v177\n" \
"v_mfma_f32_16x16x32_bf16 a[108:111], v[148:151], v[72:75], a[108:111]\n" \
"v_exp_f32 v174, v40\n" \
"v_mfma_f32_16x16x32_bf16 a[112:115], v[152:155], v[64:67], a[112:115]\n" \
"ds_read_b128 v[120:123], v178 offset:22528\n" \
"buffer_load_dwordx4 v224, %[rs], s43 offen offset:1024 sc1 lds\n" \
"v_mfma_f32_16x16x32_bf16 a[116:119], v[152:155], v[72:75], a[116:119]\n" \
"v_exp_f32 v175, v41\n" \
"v_mfma_f32_16x16x32_bf16 a[120:123], v[156:159], v[64:67], a[120:123]\n" \
"ds_read_b128 v[124:127], v178 offset:23552\n" \
"s_waitcnt lgkmcnt(4)\n" \
"v_mfma_f32_16x16x32_bf16 a[124:127], v[156:159], v[72:75], a[124:127]\n" \
"v_cvt_pk_bf16_f32 v82, v174, v175\n" \
"v_add_f32 v168, v168, v174\n" \
"v_mfma_f32_16x16x32_bf16 a[0:3], v[96:99], v[68:71], a[0:3]\n" \
"ds_read_b128 v[128:131], v178 offset:24576\n" \
"v_add_f32 v168, v168, v175\n" \
"v_mfma_f32_16x16x32_bf16 a[4:7], v[96:99], v[76:79], a[4:7]\n" \
"v_exp_f32 v176, v42\n" \
"v_mfma_f32_16x16x32_bf16 a[8:11], v[100:103], v[68:71], a[8:11]\n" \
"ds_read_b128 v[132:135], v178 offset:25600\n" \
"buffer_load_dwordx4 v224, %[rs], s43 offen offset:2048 sc1 lds\n" \
"v_mfma_f32_16x16x32_bf16 a[12:15], v[100:103], v[76:79], a[12:15]\n" \
"v_exp_f32 v177, v43\n" \
"v_mfma_f32_16x16x32_bf16 a[16:19], v[104:107], v[68:71], a[16:19]\n" \
"ds_read_b128 v[136:139], v178 offset:26624\n" \
"v_mfma_f32_16x16x32_bf16 a[20:23], v[104:107], v[76:79], a[20:23]\n" \
"v_cvt_pk_bf16_f32 v83, v176, v177\n" \
"v_add_f32 v168, v168, v176\n" \
"v_mfma_f32_16x16x32_bf16 a[24:27], v[108:111], v[68:71], a[24:27]\n" \
"ds_read_b128 v[140:143], v178 offset:27648\n" \
"s_waitcnt lgkmcnt(4)\n" \
"v_mfma_f32_16x16x32_bf16 a[28:31], v[108:111], v[76:79], a[28:31]\n" \
"v_exp_f32 v174, v44\n" \
"v_mfma_f32_16x16x32_bf16 a[32:35], v[112:115], v[68:71], a[32:35]\n" \
"ds_read_b128 v[144:147], v178 offset:28672\n" \
"buffer_load_dwordx4 v224, %[rs], s43 offen offset:3072 sc1 lds\n" \
"v_add_f32 v168, v168, v177\n" \
"v_mfma_f32_16x16x32_bf16 a[36:39], v[112:115], v[76:79], a[36:39]\n" \
"v_exp_f32 v175, v45\n" \
"v_mfma_f32_16x16x32_bf16 a[40:43], v[116:119], v[68:71], a[40:43]\n" \
"ds_read_b128 v[148:151], v178 offset:29696\n" \
"v_readlane_b32 s43, v239, s42\n" \
"v_mfma_f32_16x16x32_bf16 a[44:47], v[116:119], v[76:79], a[44:47]\n" \
"v_cvt_pk_bf16_f32 v90, v174, v175\n" \
"v_add_f32 v169, v169, v174\n" \
"v_mfma_f32_16x16x32_bf16 a[48:51], v[120:123], v[68:71], a[48:51]\n" \
"ds_read_b128 v[152:155], v178 offset:30720\n" \
"s_add_u32 m0, s44, s37\n" \
"v_mfma_f32_16x16x32_bf16 a[52:55], v[120:123], v[76:79], a[52:55]\n" \
"v_exp_f32 v176, v46\n" \
"v_mfma_f32_16x16x32_bf16 a[56:59], v[124:127], v[68:71], a[56:59]\n" \
"ds_read_b128 v[156:159], v178 offset:31744\n" \
"s_waitcnt lgkmcnt(4)\n" \
"v_add_f32 v169, v169, v175\n" \
"v_mfma_f32_16x16x32_bf16 a[60:63], v[124:127], v[76:79], a[60:63]\n" \
"v_exp_f32 v177, v47\n" \
"s_waitcnt vmcnt(16)\n" \
"s_barrier\n" \
"v_mfma_f32_16x16x32_bf16 a[64:67], v[128:131], v[68:71], a[64:67]\n" \
"ds_read_b128 v[96:99], v179 offset:0\n" \
"v_mfma_f32_16x16x32_bf16 a[68:71], v[128:131], v[76:79], a[68:71]\n" \
"v_cvt_pk_bf16_f32 v91, v176, v177\n" \
"v_add_f32 v169, v169, v176\n" \
"v_mfma_f32_16x16x32_bf16 a[72:75], v[132:135], v[68:71], a[72:75]\n" \
"ds_read_b128 v[100:103], v179 offset:1024\n" \
"s_mov_b32 s44, s41\n" \
"v_mfma_f32_16x16x32_bf16 a[76:79], v[132:135], v[76:79], a[76:79]\n" \
"v_exp_f32 v174, v48\n" \
"v_mfma_f32_16x16x32_bf16 a[80:83], v[136:139], v[68:71], a[80:83]\n" \
"ds_read_b128 v[104:107], v179 offset:2048\n" \
"s_mov_b32 s41, s48\n" \
"v_add_f32 v169, v169, v177\n" \
"v_mfma_f32_16x16x32_bf16 a[84:87], v[136:139], v[76:79], a[84:87]\n" \
"v_exp_f32 v175, v49\n" \
"v_mfma_f32_16x16x32_bf16 a[88:91], v[140:143], v[68:71], a[88:91]\n" \
"ds_read_b128 v[108:111], v179 offset:3072\n" \
"s_waitcnt lgkmcnt(4)\n" \
"v_mfma_f32_16x16x32_bf16 a[92:95], v[140:143], v[76:79], a[92:95]\n" \
"v_cvt_pk_bf16_f32 v84, v174, v175\n" \
"v_add_f32 v168, v168, v174\n" \
"v_mfma_f32_16x16x32_bf16 a[96:99], v[144:147], v[68:71], a[96:99]\n" \
"ds_read_b128 v[112:115], v179 offset:4096\n" \
"v_add_f32 v168, v168, v175\n" \
"v_mfma_f32_16x16x32_bf16 a[100:103], v[144:147], v[76:79], a[100:103]\n" \
"v_mfma_f32_16x16x32_bf16 a[104:107], v[148:151], v[68:71], a[104:107]\n" \
"ds_read_b128 v[116:119], v179 offset:5120\n" \
"s_add_u32 s45, s45, 1\n" \
"v_mfma_f32_16x16x32_bf16 a[108:111], v[148:151], v[76:79], a[108:111]\n" \
"s_add_u32 s47, s47, 64\n" \
"v_mfma_f32_16x16x32_bf16 a[112:115], v[152:155], v[68:71], a[112:115]\n" \
"ds_read_b128 v[120:123], v179 offset:6144\n" \
"v_mfma_f32_16x16x32_bf16 a[116:119], v[152:155], v[76:79], a[116:119]\n" \
"s_cmp_ge_u32 s45, s52\n" \
"v_mfma_f32_16x16x32_bf16 a[120:123], v[156:159], v[68:71], a[120:123]\n" \
"ds_read_b128 v[124:127], v179 offset:7168\n" \
"s_waitcnt lgkmcnt(4)\n" \
"v_mfma_f32_16x16x32_bf16 a[124:127], v[156:159], v[76:79], a[124:127]\n" \
"v_mov_b32 v178, v179\n" \
"s_cbranch_scc1 Lt1%=\n" \
"LA0%=:\n" \
"v_mfma_f32_16x16x32_bf16 v[0:3], v[96:99], a[128:131], v[160:163]\n" \
"ds_read_b128 v[128:131], v178 offset:8192\n" \
"buffer_load_dwordx4 v224, %[rs], s43 offen offset:0 sc1 lds\n" \
"v_mfma_f32_16x16x32_bf16 v[4:7], v[96:99], a[160:163], v[164:167]\n" \
"v_exp_f32 v176, v50\n" \
"v_mfma_f32_16x16x32_bf16 v[8:11], v[100:103], a[128:131], v[160:163]\n" \
"ds_read_b128 v[132:135], v178 offset:9216\n" \
"v_readlane_b32 s48, v238, s42\n" \
"v_mfma_f32_16x16x32_bf16 v[12:15], v[100:103], a[160:163], v[164:167]\n" \
"v_exp_f32 v177, v51\n" \
"v_mfma_f32_16x16x32_bf16 v[0:3], v[104:107], a[132:135], v[0:3]\n" \
"ds_read_b128 v[136:139], v178 offset:10240\n" \
"buffer_load_dwordx4 v224, %[rs], s43 offen offset:1024 sc1 lds\n" \
"v_mfma_f32_16x16x32_bf16 v[4:7], v[104:107], a[164:167], v[4:7]\n" \
"v_cvt_pk_bf16_f32 v85, v176, v177\n" \
"v_add_f32 v168, v168, v176\n" \
"v_mfma_f32_16x16x32_bf16 v[8:11], v[108:111], a[132:135], v[8:11]\n" \
"ds_read_b128 v[140:143], v178 offset:11264\n" \
"s_waitcnt lgkmcnt(4)\n" \
"v_mfma_f32_16x16x32_bf16 v[12:15], v[108:111], a[164:167], v[12:15]\n" \
"v_exp_f32 v174, v52\n" \
"v_mfma_f32_16x16x32_bf16 v[0:3], v[112:115], a[136:139], v[0:3]\n" \
"ds_read_b128 v[144:147], v178 offset:12288\n" \
"s_add_u32 s42, s42, 1\n" \
"v_add_f32 v168, v168, v177\n" \
"v_mfma_f32_16x16x32_bf16 v[4:7], v[112:115], a[168:171], v[4:7]\n" \
"v_exp_f32 v175, v53\n" \
"v_mfma_f32_16x16x32_bf16 v[8:11], v[116:119], a[136:139], v[8:11]\n" \
"ds_read_b128 v[148:151], v178 offset:13312\n" \
"buffer_load_dwordx4 v224, %[rs], s43 offen offset:2048 sc1 lds\n" \
"v_mfma_f32_16x16x32_bf16 v[12:15], v[116:119], a[168:171], v[12:15]\n" \
"v_cvt_pk_bf16_f32 v92, v174, v175\n" \
"v_add_f32 v169, v169, v174\n" \
"v_mfma_f32_16x16x32_bf16 v[0:3], v[120:123], a[140:143], v[0:3]\n" \
"ds_read_b128 v[152:155], v178 offset:14336\n" \
"v_add_u32 v179, s48, v224\n" \
"v_mfma_f32_16x16x32_bf16 v[4:7], v[120:123], a[172:175], v[4:7]\n" \
"v_exp_f32 v176, v54\n" \
"v_mfma_f32_16x16x32_bf16 v[8:11], v[124:127], a[140:143], v[8:11]\n" \
"ds_read_b128 v[156:159], v178 offset:15360\n" \
"s_waitcnt lgkmcnt(4)\n" \
"v_add_f32 v169, v169, v175\n" \
"v_mfma_f32_16x16x32_bf16 v[12:15], v[124:127], a[172:175], v[12:15]\n" \
"v_exp_f32 v177, v55\n" \
"v_mfma_f32_16x16x32_bf16 v[0:3], v[128:131], a[144:147], v[0:3]\n" \
"ds_read_b128 v[96:99], v178 offset:16384\n" \
"buffer_load_dwordx4 v224, %[rs], s43 offen offset:3072 sc1 lds\n" \
"v_mfma_f32_16x16x32_bf16 v[4:7], v[128:131], a[176:179], v[4:7]\n" \
"v_cvt_pk_bf16_f32 v93, v176, v177\n" \
"v_add_f32 v169, v169, v176\n" \
"v_mfma_f32_16x16x32_bf16 v[8:11], v[132:135], a[144:147], v[8:11]\n" \
"ds_read_b128 v[100:103], v178 offset:17408\n" \
"s_add_u32 m0, m0, 0x1000\n" \
"s_add_u32 s43, s43, 0x1000\n" \
"v_mfma_f32_16x16x32_bf16 v[12:15], v[132:135], a[176:179], v[12:15]\n" \
"v_exp_f32 v174, v56\n" \
"v_mfma_f32_16x16x32_bf16 v[0:3], v[136:139], a[148:151], v[0:3]\n" \
"ds_read_b128 v[104:107], v178 offset:18432\n" \
"v_add_f32 v169, v169, v177\n" \
"v_mfma_f32_16x16x32_bf16 v[4:7], v[136:139], a[180:183], v[4:7]\n" \
"v_exp_f32 v175, v57\n" \
"v_mfma_f32_16x16x32_bf16 v[8:11], v[140:143], a[148:151], v[8:11]\n" \
"ds_read_b128 v[108:111], v178 offset:19456\n" \
"s_waitcnt lgkmcnt(4)\n" \
"v_mfma_f32_16x16x32_bf16 v[12:15], v[140:143], a[180:183], v[12:15]\n" \
"v_cvt_pk_bf16_f32 v86, v174, v175\n" \
"v_add_f32 v168, v168, v174\n" \
"v_mfma_f32_16x16x32_bf16 v[0:3], v[144:147], a[152:155], v[0:3]\n" \
"ds_read_b128 v[112:115], v178 offset:20480\n" \
"buffer_load_dwordx4 v224, %[rs], s43 offen offset:0 sc1 lds\n" \
"v_mfma_f32_16x16x32_bf16 v[4:7], v[144:147], a[184:187], v[4:7]\n" \
"v_exp_f32 v176, v58\n" \
"v_mfma_f32_16x16x32_bf16 v[8:11], v[148:151], a[152:155], v[8:11]\n" \
"ds_read_b128 v[116:119], v178 offset:21504\n" \
"v_add_f32 v168, v168, v175\n" \
"v_mfma_f32_16x16x32_bf16 v[12:15], v[148:151], a[184:187], v[12:15]\n" \
"v_exp_f32 v177, v59\n" \
"v_mfma_f32_16x16x32_bf16 v[0:3], v[152:155], a[156:159], v[0:3]\n" \
"ds_read_b128 v[120:123], v178 offset:22528\n" \
"buffer_load_dwordx4 v224, %[rs], s43 offen offset:1024 sc1 lds\n" \
"v_mfma_f32_16x16x32_bf16 v[4:7], v[152:155], a[188:191], v[4:7]\n" \
"v_cvt_pk_bf16_f32 v87, v176, v177\n" \
"v_add_f32 v168, v168, v176\n" \
"v_mfma_f32_16x16x32_bf16 v[8:11], v[156:159], a[156:159], v[8:11]\n" \
"ds_read_b128 v[124:127], v178 offset:23552\n" \
"s_waitcnt lgkmcnt(4)\n" \
"v_mfma_f32_16x16x32_bf16 v[12:15], v[156:159], a[188:191], v[12:15]\n" \
"v_exp_f32 v174, v60\n" \
"v_mfma_f32_16x16x32_bf16 v[16:19], v[96:99], a[128:131], v[160:163]\n" \
"ds_read_b128 v[128:131], v178 offset:24576\n" \
"v_add_f32 v168, v168, v177\n" \
"v_mfma_f32_16x16x32_bf16 v[20:23], v[96:99], a[160:163], v[164:167]\n" \
"v_exp_f32 v175, v61\n" \
"v_mfma_f32_16x16x32_bf16 v[24:27], v[100:103], a[128:131], v[160:163]\n" \
"ds_read_b128 v[132:135], v178 offset:25600\n" \
"buffer_load_dwordx4 v224, %[rs], s43 offen offset:2048 sc1 lds\n" \
"v_mfma_f32_16x16x32_bf16 v[28:31], v[100:103], a[160:163], v[164:167]\n" \
"v_cvt_pk_bf16_f32 v94, v174, v175\n" \
"v_add_f32 v169, v169, v174\n" \
"v_mfma_f32_16x16x32_bf16 v[16:19], v[104:107], a[132:135], v[16:19]\n" \
"ds_read_b128 v[136:139], v178 offset:26624\n" \
"v_add_f32 v169, v169, v175\n" \
"v_mfma_f32_16x16x32_bf16 v[20:23], v[104:107], a[164:167], v[20:23]\n" \
"v_exp_f32 v176, v62\n" \
"v_mfma_f32_16x16x32_bf16 v[24:27], v[108:111], a[132:135], v[24:27]\n" \
"ds_read_b128 v[140:143], v178 offset:27648\n" \
"s_waitcnt lgkmcnt(4)\n" \
"v_mfma_f32_16x16x32_bf16 v[28:31], v[108:111], a[164:167], v[28:31]\n" \
"v_exp_f32 v177, v63\n" \
"v_mfma_f32_16x16x32_bf16 v[16:19], v[112:115], a[136:139], v[16:19]\n" \
"ds_read_b128 v[144:147], v178 offset:28672\n" \
"buffer_load_dwordx4 v224, %[rs], s43 offen offset:3072 sc1 lds\n" \
"v_mfma_f32_16x16x32_bf16 v[20:23], v[112:115], a[168:171], v[20:23]\n" \
"v_cvt_pk_bf16_f32 v95, v176, v177\n" \
"v_add_f32 v169, v169, v176\n" \
"v_mfma_f32_16x16x32_bf16 v[24:27], v[116:119], a[136:139], v[24:27]\n" \
"ds_read_b128 v[148:151], v178 offset:29696\n" \
"v_readlane_b32 s43, v239, s42\n" \
"v_mfma_f32_16x16x32_bf16 v[28:31], v[116:119], a[168:171], v[28:31]\n" \
"v_mfma_f32_16x16x32_bf16 v[16:19], v[120:123], a[140:143], v[16:19]\n" \
"ds_read_b128 v[152:155], v178 offset:30720\n" \
"s_add_u32 m0, s44, s37\n" \
"v_add_f32 v169, v169, v177\n" \
"v_mfma_f32_16x16x32_bf16 v[20:23], v[120:123], a[172:175], v[20:23]\n" \
"v_add_f32 v180, v168, v169\n" \
"v_mfma_f32_16x16x32_bf16 v[24:27], v[124:127], a[140:143], v[24:27]\n" \
"ds_read_b128 v[156:159], v178 offset:31744\n" \
"s_waitcnt lgkmcnt(4)\n" \
"v_mfma_f32_16x16x32_bf16 v[28:31], v[124:127], a[172:175], v[28:31]\n" \
"v_cmp_nge_f32 vcc, s51, v180\n" \
"s_waitcnt vmcnt(16)\n" \
"s_barrier\n" \
"v_mfma_f32_16x16x32_bf16 v[16:19], v[128:131], a[144:147], v[16:19]\n" \
"ds_read_b128 v[96:99], v179 offset:0\n" \
"v_mfma_f32_16x16x32_bf16 v[20:23], v[128:131], a[176:179], v[20:23]\n" \
"v_mfma_f32_16x16x32_bf16 v[24:27], v[132:135], a[144:147], v[24:27]\n" \
"ds_read_b128 v[100:103], v179 offset:1024\n" \
"s_mov_b32 s44, s41\n" \
"v_mfma_f32_16x16x32_bf16 v[28:31], v[132:135], a[176:179], v[28:31]\n" \
"v_mfma_f32_16x16x32_bf16 v[16:19], v[136:139], a[148:151], v[16:19]\n" \
"ds_read_b128 v[104:107], v179 offset:2048\n" \
"s_mov_b32 s41, s48\n" \
"v_mfma_f32_16x16x32_bf16 v[20:23], v[136:139], a[180:183], v[20:23]\n" \
"v_mfma_f32_16x16x32_bf16 v[24:27], v[140:143], a[148:151], v[24:27]\n" \
"ds_read_b128 v[108:111], v179 offset:3072\n" \
"s_waitcnt lgkmcnt(4)\n" \
"v_mfma_f32_16x16x32_bf16 v[28:31], v[140:143], a[180:183], v[28:31]\n" \
"v_mfma_f32_16x16x32_bf16 v[16:19], v[144:147], a[152:155], v[16:19]\n" \
"ds_read_b128 v[112:115], v179 offset:4096\n" \
"v_mfma_f32_16x16x32_bf16 v[20:23], v[144:147], a[184:187], v[20:23]\n" \
"s_cmp_ge_i32 s47, 0\n" \
"v_mfma_f32_16x16x32_bf16 v[24:27], v[148:151], a[152:155], v[24:27]\n" \
"ds_read_b128 v[116:119], v179 offset:5120\n" \
"v_mfma_f32_16x16x32_bf16 v[28:31], v[148:151], a[184:187], v[28:31]\n" \
"s_cselect_b32 s95, 1, s49\n" \
"v_mfma_f32_16x16x32_bf16 v[16:19], v[152:155], a[156:159], v[16:19]\n" \
"ds_read_b128 v[120:123], v179 offset:6144\n" \
"v_mfma_f32_16x16x32_bf16 v[20:23], v[152:155], a[188:191], v[20:23]\n" \
"s_cmp_lg_u32 s95, 0\n" \
"v_mfma_f32_16x16x32_bf16 v[24:27], v[156:159], a[156:159], v[24:27]\n" \
"ds_read_b128 v[124:127], v179 offset:7168\n" \
"s_waitcnt lgkmcnt(4)\n" \
"v_mfma_f32_16x16x32_bf16 v[28:31], v[156:159], a[188:191], v[28:31]\n" \
"v_mov_b32 v178, v179\n" \
"LaG%=:\n" \
"s_cbranch_scc1 LaS2%=\n" \
"LaR2%=:\n" \
"s_cbranch_vccnz LaV2%=\n" \
"LaC2%=:\n" \
"v_mfma_f32_16x16x32_bf16 a[0:3], v[96:99], v[80:83], a[0:3]\n" \
"ds_read_b128 v[128:131], v178 offset:8192\n" \
"buffer_load_dwordx4 v224, %[rs], s43 offen offset:0 sc1 lds\n" \
"v_mfma_f32_16x16x32_bf16 a[4:7], v[96:99], v[88:91], a[4:7]\n" \
"v_add_f32 v170, v170, v168\n" \
"v_add_f32 v171, v171, v169\n" \
"v_mfma_f32_16x16x32_bf16 a[8:11], v[100:103], v[80:83], a[8:11]\n" \
"ds_read_b128 v[132:135], v178 offset:9216\n" \
"v_readlane_b32 s48, v238, s42\n" \
"v_mfma_f32_16x16x32_bf16 a[12:15], v[100:103], v[88:91], a[12:15]\n" \
"v_exp_f32 v168, v0\n" \
"v_mfma_f32_16x16x32_bf16 a[16:19], v[104:107], v[80:83], a[16:19]\n" \
"ds_read_b128 v[136:139], v178 offset:10240\n" \
"buffer_load_dwordx4 v224, %[rs], s43 offen offset:1024 sc1 lds\n" \
"v_mfma_f32_16x16x32_bf16 a[20:23], v[104:107], v[88:91], a[20:23]\n" \
"v_exp_f32 v175, v1\n" \
"v_mfma_f32_16x16x32_bf16 a[24:27], v[108:111], v[80:83], a[24:27]\n" \
"ds_read_b128 v[140:143], v178 offset:11264\n" \
"s_waitcnt lgkmcnt(4)\n" \
"v_mfma_f32_16x16x32_bf16 a[28:31], v[108:111], v[88:91], a[28:31]\n" \
"v_cvt_pk_bf16_f32 v64, v168, v175\n" \
"v_add_f32 v168, v168, v175\n" \
"v_mfma_f32_16x16x32_bf16 a[32:35], v[112:115], v[80:83], a[32:35]\n" \
"ds_read_b128 v[144:147], v178 offset:12288\n" \
"s_add_u32 s42, s42, 1\n" \
"v_mfma_f32_16x16x32_bf16 a[36:39], v[112:115], v[88:91], a[36:39]\n" \
"v_exp_f32 v176, v2\n" \
"v_mfma_f32_16x16x32_bf16 a[40:43], v[116:119], v[80:83], a[40:43]\n" \
"ds_read_b128 v[148:151], v178 offset:13312\n" \
"buffer_load_dwordx4 v224, %[rs], s43 offen offset:2048 sc1 lds\n" \
"v_mfma_f32_16x16x32_bf16 a[44:47], v[116:119], v[88:91], a[44:47]\n" \
"v_exp_f32 v177, v3\n" \
"v_mfma_f32_16x16x32_bf16 a[48:51], v[120:123], v[80:83], a[48:51]\n" \
"ds_read_b128 v[152:155], v178 offset:14336\n" \
"v_add_u32 v179, s48, v224\n" \
"v_mfma_f32_16x16x32_bf16 a[52:55], v[120:123], v[88:91], a[52:55]\n" \
"v_cvt_pk_bf16_f32 v65, v176, v177\n" \
"v_add_f32 v168, v168, v176\n" \
"v_mfma_f32_16x16x32_bf16 a[56:59], v[124:127], v[80:83], a[56:59]\n" \
"ds_read_b128 v[156:159], v178 offset:15360\n" \
"s_waitcnt lgkmcnt(4)\n" \
"v_mfma_f32_16x16x32_bf16 a[60:63], v[124:127], v[88:91], a[60:63]\n" \
"v_exp_f32 v169, v4\n" \
"v_mfma_f32_16x16x32_bf16 a[64:67], v[128:131], v[80:83], a[64:67]\n" \
"ds_read_b128 v[96:99], v178 offset:16384\n" \
"buffer_load_dwordx4 v224, %[rs], s43 offen offset:3072 sc1 lds\n" \
"v_add_f32 v168, v168, v177\n" \
"v_mfma_f32_16x16x32_bf16 a[68:71], v[128:131], v[88:91], a[68:71]\n" \
"v_exp_f32 v175, v5\n" \
"v_mfma_f32_16x16x32_bf16 a[72:75], v[132:135], v[80:83], a[72:75]\n" \
"ds_read_b128 v[100:103], v178 offset:17408\n" \
"s_add_u32 m0, m0, 0x1000\n" \
"s_add_u32 s43, s43, 0x1000\n" \
"v_mfma_f32_16x16x32_bf16 a[76:79], v[132:135], v[88:91], a[76:79]\n" \
"v_cvt_pk_bf16_f32 v72, v169, v175\n" \
"v_add_f32 v169, v169, v175\n" \
"v_mfma_f32_16x16x32_bf16 a[80:83], v[136:139], v[80:83], a[80:83]\n" \
"ds_read_b128 v[104:107], v178 offset:18432\n" \
"v_mfma_f32_16x16x32_bf16 a[84:87], v[136:139], v[88:91], a[84:87]\n" \
"v_exp_f32 v176, v6\n" \
"v_mfma_f32_16x16x32_bf16 a[88:91], v[140:143], v[80:83], a[88:91]\n" \
"ds_read_b128 v[108:111], v178 offset:19456\n" \
"s_waitcnt lgkmcnt(4)\n" \
"v_mfma_f32_16x16x32_bf16 a[92:95], v[140:143], v[88:91], a[92:95]\n" \
"v_exp_f32 v177, v7\n" \
"v_mfma_f32_16x16x32_bf16 a[96:99], v[144:147], v[80:83], a[96:99]\n" \
"ds_read_b128 v[112:115], v178 offset:20480\n" \
"buffer_load_dwordx4 v224, %[rs], s43 offen offset:0 sc1 lds\n" \
"v_mfma_f32_16x16x32_bf16 a[100:103], v[144:147], v[88:91], a[100:103]\n" \
"v_cvt_pk_bf16_f32 v73, v176, v177\n" \
"v_add_f32 v169, v169, v176\n" \
"v_mfma_f32_16x16x32_bf16 a[104:107], v[148:151], v[80:83], a[104:107]\n" \
"ds_read_b128 v[116:119], v178 offset:21504\n" \
"v_add_f32 v169, v169, v177\n" \
"v_mfma_f32_16x16x32_bf16 a[108:111], v[148:151], v[88:91], a[108:111]\n" \
"v_exp_f32 v174, v8\n" \
"v_mfma_f32_16x16x32_bf16 a[112:115], v[152:155], v[80:83], a[112:115]\n" \
"ds_read_b128 v[120:123], v178 offset:22528\n" \
"buffer_load_dwordx4 v224, %[rs], s43 offen offset:1024 sc1 lds\n" \
"v_mfma_f32_16x16x32_bf16 a[116:119], v[152:155], v[88:91], a[116:119]\n" \
"v_exp_f32 v175, v9\n" \
"v_mfma_f32_16x16x32_bf16 a[120:123], v[156:159], v[80:83], a[120:123]\n" \
"ds_read_b128 v[124:127], v178 offset:23552\n" \
"s_waitcnt lgkmcnt(4)\n" \
"v_mfma_f32_16x16x32_bf16 a[124:127], v[156:159], v[88:91], a[124:127]\n" \
"v_cvt_pk_bf16_f32 v66, v174, v175\n" \
"v_add_f32 v168, v168, v174\n" \
"v_mfma_f32_16x16x32_bf16 a[0:3], v[96:99], v[84:87], a[0:3]\n" \
"ds_read_b128 v[128:131], v178 offset:24576\n" \
"v_add_f32 v168, v168, v175\n" \
"v_mfma_f32_16x16x32_bf16 a[4:7], v[96:99], v[92:95], a[4:7]\n" \
"v_exp_f32 v176, v10\n" \
"v_mfma_f32_16x16x32_bf16 a[8:11], v[100:103], v[84:87], a[8:11]\n" \
"ds_read_b128 v[132:135], v178 offset:25600\n" \
"buffer_load_dwordx4 v224, %[rs], s43 offen offset:2048 sc1 lds\n" \
"v_mfma_f32_16x16x32_bf16 a[12:15], v[100:103], v[92:95], a[12:15]\n" \
"v_exp_f32 v177, v11\n" \
"v_mfma_f32_16x16x32_bf16 a[16:19], v[104:107], v[84:87], a[16:19]\n" \
"ds_read_b128 v[136:139], v178 offset:26624\n" \
"v_mfma_f32_16x16x32_bf16 a[20:23], v[104:107], v[92:95], a[20:23]\n" \
"v_cvt_pk_bf16_f32 v67, v176, v177\n" \
"v_add_f32 v168, v168, v176\n" \
"v_mfma_f32_16x16x32_bf16 a[24:27], v[108:111], v[84:87], a[24:27]\n" \
"ds_read_b128 v[140:143], v178 offset:27648\n" \
"s_waitcnt lgkmcnt(4)\n" \
"v_mfma_f32_16x16x32_bf16 a[28:31], v[108:111], v[92:95], a[28:31]\n" \
"v_exp_f32 v174, v12\n" \
"v_mfma_f32_16x16x32_bf16 a[32:35], v[112:115], v[84:87], a[32:35]\n" \
"ds_read_b128 v[144:147], v178 offset:28672\n" \
"buffer_load_dwordx4 v224, %[rs], s43 offen offset:3072 sc1 lds\n" \
"v_add_f32 v168, v168, v177\n" \
"v_mfma_f32_16x16x32_bf16 a[36:39], v[112:115], v[92:95], a[36:39]\n" \
"v_exp_f32 v175, v13\n" \
"v_mfma_f32_16x16x32_bf16 a[40:43], v[116:119], v[84:87], a[40:43]\n" \
"ds_read_b128 v[148:151], v178 offset:29696\n" \
"v_readlane_b32 s43, v239, s42\n" \
"v_mfma_f32_16x16x32_bf16 a[44:47], v[116:119], v[92:95], a[44:47]\n" \
"v_cvt_pk_bf16_f32 v74, v174, v175\n" \
"v_add_f32 v169, v169, v174\n" \
"v_mfma_f32_16x16x32_bf16 a[48:51], v[120:123], v[84:87], a[48:51]\n" \
"ds_read_b128 v[152:155], v178 offset:30720\n" \
"s_add_u32 m0, s44, s37\n" \
"v_mfma_f32_16x16x32_bf16 a[52:55], v[120:123], v[92:95], a[52:55]\n" \
"v_exp_f32 v176, v14\n" \
"v_mfma_f32_16x16x32_bf16 a[56:59], v[124:127], v[84:87], a[56:59]\n" \
"ds_read_b128 v[156:159], v178 offset:31744\n" \
"s_waitcnt lgkmcnt(4)\n" \
"v_add_f32 v169, v169, v175\n" \
"v_mfma_f32_16x16x32_bf16 a[60:63], v[124:127], v[92:95], a[60:63]\n" \
"v_exp_f32 v177, v15\n" \
"s_waitcnt vmcnt(16)\n" \
"s_barrier\n" \
"v_mfma_f32_16x16x32_bf16 a[64:67], v[128:131], v[84:87], a[64:67]\n" \
"ds_read_b128 v[96:99], v179 offset:0\n" \
"v_mfma_f32_16x16x32_bf16 a[68:71], v[128:131], v[92:95], a[68:71]\n" \
"v_cvt_pk_bf16_f32 v75, v176, v177\n" \
"v_add_f32 v169, v169, v176\n" \
"v_mfma_f32_16x16x32_bf16 a[72:75], v[132:135], v[84:87], a[72:75]\n" \
"ds_read_b128 v[100:103], v179 offset:1024\n" \
"s_mov_b32 s44, s41\n" \
"v_mfma_f32_16x16x32_bf16 a[76:79], v[132:135], v[92:95], a[76:79]\n" \
"v_exp_f32 v174, v16\n" \
"v_mfma_f32_16x16x32_bf16 a[80:83], v[136:139], v[84:87], a[80:83]\n" \
"ds_read_b128 v[104:107], v179 offset:2048\n" \
"s_mov_b32 s41, s48\n" \
"v_add_f32 v169, v169, v177\n" \
"v_mfma_f32_16x16x32_bf16 a[84:87], v[136:139], v[92:95], a[84:87]\n" \
"v_exp_f32 v175, v17\n" \
"v_mfma_f32_16x16x32_bf16 a[88:91], v[140:143], v[84:87], a[88:91]\n" \
"ds_read_b128 v[108:111], v179 offset:3072\n" \
"s_waitcnt lgkmcnt(4)\n" \
"v_mfma_f32_16x16x32_bf16 a[92:95], v[140:143], v[92:95], a[92:95]\n" \
"v_cvt_pk_bf16_f32 v68, v174, v175\n" \
"v_add_f32 v168, v168, v174\n" \
"v_mfma_f32_16x16x32_bf16 a[96:99], v[144:147], v[84:87], a[96:99]\n" \
"ds_read_b128 v[112:115], v179 offset:4096\n" \
"v_add_f32 v168, v168, v175\n" \
"v_mfma_f32_16x16x32_bf16 a[100:103], v[144:147], v[92:95], a[100:103]\n" \
"v_mfma_f32_16x16x32_bf16 a[104:107], v[148:151], v[84:87], a[104:107]\n" \
"ds_read_b128 v[116:119], v179 offset:5120\n" \
"s_add_u32 s45, s45, 1\n" \
"v_mfma_f32_16x16x32_bf16 a[108:111], v[148:151], v[92:95], a[108:111]\n" \
"s_add_u32 s47, s47, 64\n" \
"v_mfma_f32_16x16x32_bf16 a[112:115], v[152:155], v[84:87], a[112:115]\n" \
"ds_read_b128 v[120:123], v179 offset:6144\n" \
"v_mfma_f32_16x16x32_bf16 a[116:119], v[152:155], v[92:95], a[116:119]\n" \
"s_cmp_ge_u32 s45, s52\n" \
"v_mfma_f32_16x16x32_bf16 a[120:123], v[156:159], v[84:87], a[120:123]\n" \
"ds_read_b128 v[124:127], v179 offset:7168\n" \
"s_waitcnt lgkmcnt(4)\n" \
"v_mfma_f32_16x16x32_bf16 a[124:127], v[156:159], v[92:95], a[124:127]\n" \
"v_mov_b32 v178, v179\n" \
"s_cbranch_scc0 LA1%=\n" \
"s_cmp_eq_u32 s45, s46\n" \
"s_cbranch_scc1 Ldr0%=\n" \
"s_cmp_lg_u32 s63, 0\n" \
"s_cbranch_scc1 LAH1%=\n" \
"s_mov_b32 s49, 1\n" \
"s_branch LA1%=\n" \
"Lt1%=:\n" \
"s_cmp_eq_u32 s45, s46\n" \
"s_cbranch_scc1 Ldr1%=\n" \
"s_cmp_lg_u32 s63, 0\n" \
"s_cbranch_scc1 LAH0%=\n" \
"s_mov_b32 s49, 1\n" \
"s_branch LA0%=\n" \
"LAH1%=:\n" \
"ds_read_b128 v[128:131], v178 offset:8192\n" \
"buffer_load_dwordx4 v224, %[rs], s43 offen offset:0 sc1 lds\n" \
"v_mfma_f32_16x16x32_bf16 v[36:39], v[96:99], a[160:163], v[164:167]\n" \
"v_exp_f32 v176, v18\n" \
"ds_read_b128 v[132:135], v178 offset:9216\n" \
"v_readlane_b32 s48, v238, s42\n" \
"v_mfma_f32_16x16x32_bf16 v[44:47], v[100:103], a[160:163], v[164:167]\n" \
"v_exp_f32 v177, v19\n" \
"ds_read_b128 v[136:139], v178 offset:10240\n" \
"buffer_load_dwordx4 v224, %[rs], s43 offen offset:1024 sc1 lds\n" \
"v_mfma_f32_16x16x32_bf16 v[36:39], v[104:107], a[164:167], v[36:39]\n" \
"v_cvt_pk_bf16_f32 v69, v176, v177\n" \
"v_add_f32 v168, v168, v176\n" \
"ds_read_b128 v[140:143], v178 offset:11264\n" \
"s_waitcnt lgkmcnt(4)\n" \
"v_mfma_f32_16x16x32_bf16 v[44:47], v[108:111], a[164:167], v[44:47]\n" \
"v_exp_f32 v174, v20\n" \
"ds_read_b128 v[144:147], v178 offset:12288\n" \
"s_add_u32 s42, s42, 1\n" \
"v_add_f32 v168, v168, v177\n" \
"v_mfma_f32_16x16x32_bf16 v[36:39], v[112:115], a[168:171], v[36:39]\n" \
"v_exp_f32 v175, v21\n" \
"ds_read_b128 v[148:151], v178 offset:13312\n" \
"buffer_load_dwordx4 v224, %[rs], s43 offen offset:2048 sc1 lds\n" \
"v_mfma_f32_16x16x32_bf16 v[44:47], v[116:119], a[168:171], v[44:47]\n" \
"v_cvt_pk_bf16_f32 v76, v174, v175\n" \
"v_add_f32 v169, v169, v174\n" \
"ds_read_b128 v[152:155], v178 offset:14336\n" \
"v_add_u32 v179, s48, v224\n" \
"v_mfma_f32_16x16x32_bf16 v[36:39], v[120:123], a[172:175], v[36:39]\n" \
"v_exp_f32 v176, v22\n" \
"ds_read_b128 v[156:159], v178 offset:15360\n" \
"s_waitcnt lgkmcnt(4)\n" \
"v_add_f32 v169, v169, v175\n" \
"v_mfma_f32_16x16x32_bf16 v[44:47], v[124:127], a[172:175], v[44:47]\n" \
"v_exp_f32 v177, v23\n" \
"ds_read_b128 v[96:99], v178 offset:16384\n" \
"buffer_load_dwordx4 v224, %[rs], s43 offen offset:3072 sc1 lds\n" \
"v_mfma_f32_16x16x32_bf16 v[36:39], v[128:131], a[176:179], v[36:39]\n" \
"v_cvt_pk_bf16_f32 v77, v176, v177\n" \
"v_add_f32 v169, v169, v176\n" \
"ds_read_b128 v[100:103], v178 offset:17408\n" \
"s_add_u32 m0, m0, 0x1000\n" \
"s_add_u32 s43, s43, 0x1000\n" \
"v_mfma_f32_16x16x32_bf16 v[44:47], v[132:135], a[176:179], v[44:47]\n" \
"v_exp_f32 v174, v24\n" \
"ds_read_b128 v[104:107], v178 offset:18432\n" \
"v_add_f32 v169, v169, v177\n" \
"v_mfma_f32_16x16x32_bf16 v[36:39], v[136:139], a[180:183], v[36:39]\n" \
"v_exp_f32 v175, v25\n" \
"ds_read_b128 v[108:111], v178 offset:19456\n" \
"s_waitcnt lgkmcnt(4)\n" \
"v_mfma_f32_16x16x32_bf16 v[44:47], v[140:143], a[180:183], v[44:47]\n" \
"v_cvt_pk_bf16_f32 v70, v174, v175\n" \
"v_add_f32 v168, v168, v174\n" \
"ds_read_b128 v[112:115], v178 offset:20480\n" \
"buffer_load_dwordx4 v224, %[rs], s43 offen offset:0 sc1 lds\n" \
"v_mfma_f32_16x16x32_bf16 v[36:39], v[144:147], a[184:187], v[36:39]\n" \
"v_exp_f32 v176, v26\n" \
"ds_read_b128 v[116:119], v178 offset:21504\n" \
"v_add_f32 v168, v168, v175\n" \
"v_mfma_f32_16x16x32_bf16 v[44:47], v[148:151], a[184:187], v[44:47]\n" \
"v_exp_f32 v177, v27\n" \
"ds_read_b128 v[120:123], v178 offset:22528\n" \
"buffer_load_dwordx4 v224, %[rs], s43 offen offset:1024 sc1 lds\n" \
"v_mfma_f32_16x16x32_bf16 v[36:39], v[152:155], a[188:191], v[36:39]\n" \
"v_cvt_pk_bf16_f32 v71, v176, v177\n" \
"v_add_f32 v168, v168, v176\n" \
"ds_read_b128 v[124:127], v178 offset:23552\n" \
"s_waitcnt lgkmcnt(4)\n" \
"v_mfma_f32_16x16x32_bf16 v[44:47], v[156:159], a[188:191], v[44:47]\n" \
"v_exp_f32 v174, v28\n" \
"ds_read_b128 v[128:131], v178 offset:24576\n" \
"v_add_f32 v168, v168, v177\n" \
"v_mfma_f32_16x16x32_bf16 v[52:55], v[96:99], a[160:163], v[164:167]\n" \
"v_exp_f32 v175, v29\n" \
"ds_read_b128 v[132:135], v178 offset:25600\n" \
"buffer_load_dwordx4 v224, %[rs], s43 offen offset:2048 sc1 lds\n" \
"v_mfma_f32_16x16x32_bf16 v[60:63], v[100:103], a[160:163], v[164:167]\n" \
"v_cvt_pk_bf16_f32 v78, v174, v175\n" \
"v_add_f32 v169, v169, v174\n" \
"ds_read_b128 v[136:139], v178 offset:26624\n" \
"v_add_f32 v169, v169, v175\n" \
"v_mfma_f32_16x16x32_bf16 v[52:55], v[104:107], a[164:167], v[52:55]\n" \
"v_exp_f32 v176, v30\n" \
"ds_read_b128 v[140:143], v178 offset:27648\n" \
"s_waitcnt lgkmcnt(4)\n" \
"v_mfma_f32_16x16x32_bf16 v[60:63], v[108:111], a[164:167], v[60:63]\n" \
"v_exp_f32 v177, v31\n" \
"ds_read_b128 v[144:147], v178 offset:28672\n" \
"buffer_load_dwordx4 v224, %[rs], s43 offen offset:3072 sc1 lds\n" \
"v_mfma_f32_16x16x32_bf16 v[52:55], v[112:115], a[168:171], v[52:55]\n" \
"v_cvt_pk_bf16_f32 v79, v176, v177\n" \
"v_add_f32 v169, v169, v176\n" \
"ds_read_b128 v[148:151], v178 offset:29696\n" \
"v_readlane_b32 s43, v239, s42\n" \
"v_mfma_f32_16x16x32_bf16 v[60:63], v[116:119], a[168:171], v[60:63]\n" \
"ds_read_b128 v[152:155], v178 offset:30720\n" \
"s_add_u32 m0, s44, s37\n" \
"v_add_f32 v169, v169, v177\n" \
"v_mfma_f32_16x16x32_bf16 v[52:55], v[120:123], a[172:175], v[52:55]\n" \
"v_add_f32 v180, v168, v169\n" \
"ds_read_b128 v[156:159], v178 offset:31744\n" \
"s_waitcnt lgkmcnt(4)\n" \
"v_mfma_f32_16x16x32_bf16 v[60:63], v[124:127], a[172:175], v[60:63]\n" \
"v_cmp_nge_f32 vcc, s51, v180\n" \
"s_waitcnt vmcnt(16)\n" \
"s_barrier\n" \
"ds_read_b128 v[96:99], v179 offset:0\n" \
"v_mfma_f32_16x16x32_bf16 v[52:55], v[128:131], a[176:179], v[52:55]\n" \
"ds_read_b128 v[100:103], v179 offset:1024\n" \
"s_mov_b32 s44, s41\n" \
"v_mfma_f32_16x16x32_bf16 v[60:63], v[132:135], a[176:179], v[60:63]\n" \
"ds_read_b128 v[104:107], v179 offset:2048\n" \
"s_mov_b32 s41, s48\n" \
"v_mfma_f32_16x16x32_bf16 v[52:55], v[136:139], a[180:183], v[52:55]\n" \
"ds_read_b128 v[108:111], v179 offset:3072\n" \
"s_waitcnt lgkmcnt(4)\n" \
"v_mfma_f32_16x16x32_bf16 v[60:63], v[140:143], a[180:183], v[60:63]\n" \
"ds_read_b128 v[112:115], v179 offset:4096\n" \
"v_mfma_f32_16x16x32_bf16 v[52:55], v[144:147], a[184:187], v[52:55]\n" \
"s_cmp_ge_i32 s47, 0\n" \
"ds_read_b128 v[116:119], v179 offset:5120\n" \
"v_mfma_f32_16x16x32_bf16 v[60:63], v[148:151], a[184:187], v[60:63]\n" \
"s_cselect_b32 s95, 1, s49\n" \
"ds_read_b128 v[120:123], v179 offset:6144\n" \
"v_mfma_f32_16x16x32_bf16 v[52:55], v[152:155], a[188:191], v[52:55]\n" \
"s_cmp_lg_u32 s95, 0\n" \
"ds_read_b128 v[124:127], v179 offset:7168\n" \
"s_waitcnt lgkmcnt(4)\n" \
"v_mfma_f32_16x16x32_bf16 v[60:63], v[156:159], a[188:191], v[60:63]\n" \
"v_mov_b32 v178, v179\n" \
"s_branch LaF%=\n" \
"LAH0%=:\n" \
"ds_read_b128 v[128:131], v178 offset:8192\n" \
"buffer_load_dwordx4 v224, %[rs], s43 offen offset:0 sc1 lds\n" \
"v_mfma_f32_16x16x32_bf16 v[4:7], v[96:99], a[160:163], v[164:167]\n" \
"v_exp_f32 v176, v50\n" \
"ds_read_b128 v[132:135], v178 offset:9216\n" \
"v_readlane_b32 s48, v238, s42\n" \
"v_mfma_f32_16x16x32_bf16 v[12:15], v[100:103], a[160:163], v[164:167]\n" \
"v_exp_f32 v177, v51\n" \
"ds_read_b128 v[136:139], v178 offset:10240\n" \
"buffer_load_dwordx4 v224, %[rs], s43 offen offset:1024 sc1 lds\n" \
"v_mfma_f32_16x16x32_bf16 v[4:7], v[104:107], a[164:167], v[4:7]\n" \
"v_cvt_pk_bf16_f32 v85, v176, v177\n" \
"v_add_f32 v168, v168, v176\n" \
"ds_read_b128 v[140:143], v178 offset:11264\n" \
"s_waitcnt lgkmcnt(4)\n" \
"v_mfma_f32_16x16x32_bf16 v[12:15], v[108:111], a[164:167], v[12:15]\n" \
"v_exp_f32 v174, v52\n" \
"ds_read_b128 v[144:147], v178 offset:12288\n" \
"s_add_u32 s42, s42, 1\n" \
"v_add_f32 v168, v168, v177\n" \
"v_mfma_f32_16x16x32_bf16 v[4:7], v[112:115], a[168:171], v[4:7]\n" \
"v_exp_f32 v175, v53\n" \
"ds_read_b128 v[148:151], v178 offset:13312\n" \
"buffer_load_dwordx4 v224, %[rs], s43 offen offset:2048 sc1 lds\n" \
"v_mfma_f32_16x16x32_bf16 v[12:15], v[116:119], a[168:171], v[12:15]\n" \
"v_cvt_pk_bf16_f32 v92, v174, v175\n" \
"v_add_f32 v169, v169, v174\n" \
"ds_read_b128 v[152:155], v178 offset:14336\n" \
"v_add_u32 v179, s48, v224\n" \
"v_mfma_f32_16x16x32_bf16 v[4:7], v[120:123], a[172:175], v[4:7]\n" \
"v_exp_f32 v176, v54\n" \
"ds_read_b128 v[156:159], v178 offset:15360\n" \
"s_waitcnt lgkmcnt(4)\n" \
"v_add_f32 v169, v169, v175\n" \
"v_mfma_f32_16x16x32_bf16 v[12:15], v[124:127], a[172:175], v[12:15]\n" \
"v_exp_f32 v177, v55\n" \
"ds_read_b128 v[96:99], v178 offset:16384\n" \
"buffer_load_dwordx4 v224, %[rs], s43 offen offset:3072 sc1 lds\n" \
"v_mfma_f32_16x16x32_bf16 v[4:7], v[128:131], a[176:179], v[4:7]\n" \
"v_cvt_pk_bf16_f32 v93, v176, v177\n" \
"v_add_f32 v169, v169, v176\n" \
"ds_read_b128 v[100:103], v178 offset:17408\n" \
"s_add_u32 m0, m0, 0x1000\n" \
"s_add_u32 s43, s43, 0x1000\n" \
"v_mfma_f32_16x16x32_bf16 v[12:15], v[132:135], a[176:179], v[12:15]\n" \
"v_exp_f32 v174, v56\n" \
"ds_read_b128 v[104:107], v178 offset:18432\n" \
"v_add_f32 v169, v169, v177\n" \
"v_mfma_f32_16x16x32_bf16 v[4:7], v[136:139], a[180:183], v[4:7]\n" \
"v_exp_f32 v175, v57\n" \
"ds_read_b128 v[108:111], v178 offset:19456\n" \
"s_waitcnt lgkmcnt(4)\n" \
"v_mfma_f32_16x16x32_bf16 v[12:15], v[140:143], a[180:183], v[12:15]\n" \
"v_cvt_pk_bf16_f32 v86, v174, v175\n" \
"v_add_f32 v168, v168, v174\n" \
"ds_read_b128 v[112:115], v178 offset:20480\n" \
"buffer_load_dwordx4 v224, %[rs], s43 offen offset:0 sc1 lds\n" \
"v_mfma_f32_16x16x32_bf16 v[4:7], v[144:147], a[184:187], v[4:7]\n" \
"v_exp_f32 v176, v58\n" \
"ds_read_b128 v[116:119], v178 offset:21504\n" \
"v_add_f32 v168, v168, v175\n" \
"v_mfma_f32_16x16x32_bf16 v[12:15], v[148:151], a[184:187], v[12:15]\n" \
"v_exp_f32 v177, v59\n" \
"ds_read_b128 v[120:123], v178 offset:22528\n" \
"buffer_load_dwordx4 v224, %[rs], s43 offen offset:1024 sc1 lds\n" \
"v_mfma_f32_16x16x32_bf16 v[4:7], v[152:155], a[188:191], v[4:7]\n" \
"v_cvt_pk_bf16_f32 v87, v176, v177\n" \
"v_add_f32 v168, v168, v176\n" \
"ds_read_b128 v[124:127], v178 offset:23552\n" \
"s_waitcnt lgkmcnt(4)\n" \
"v_mfma_f32_16x16x32_bf16 v[12:15], v[156:159], a[188:191], v[12:15]\n" \
"v_exp_f32 v174, v60\n" \
"ds_read_b128 v[128:131], v178 offset:24576\n" \
"v_add_f32 v168, v168, v177\n" \
"v_mfma_f32_16x16x32_bf16 v[20:23], v[96:99], a[160:163], v[164:167]\n" \
"v_exp_f32 v175, v61\n" \
"ds_read_b128 v[132:135], v178 offset:25600\n" \
"buffer_load_dwordx4 v224, %[rs], s43 offen offset:2048 sc1 lds\n" \
"v_mfma_f32_16x16x32_bf16 v[28:31], v[100:103], a[160:163], v[164:167]\n" \
"v_cvt_pk_bf16_f32 v94, v174, v175\n" \
"v_add_f32 v169, v169, v174\n" \
"ds_read_b128 v[136:139], v178 offset:26624\n" \
"v_add_f32 v169, v169, v175\n" \
"v_mfma_f32_16x16x32_bf16 v[20:23], v[104:107], a[164:167], v[20:23]\n" \
"v_exp_f32 v176, v62\n" \
"ds_read_b128 v[140:143], v178 offset:27648\n" \
"s_waitcnt lgkmcnt(4)\n" \
"v_mfma_f32_16x16x32_bf16 v[28:31], v[108:111], a[164:167], v[28:31]\n" \
"v_exp_f32 v177, v63\n" \
"ds_read_b128 v[144:147], v178 offset:28672\n" \
"buffer_load_dwordx4 v224, %[rs], s43 offen offset:3072 sc1 lds\n" \
"v_mfma_f32_16x16x32_bf16 v[20:23], v[112:115], a[168:171], v[20:23]\n" \
"v_cvt_pk_bf16_f32 v95, v176, v177\n" \
"v_add_f32 v169, v169, v176\n" \
"ds_read_b128 v[148:151], v178 offset:29696\n" \
"v_readlane_b32 s43, v239, s42\n" \
"v_mfma_f32_16x16x32_bf16 v[28:31], v[116:119], a[168:171], v[28:31]\n" \
"ds_read_b128 v[152:155], v178 offset:30720\n" \
"s_add_u32 m0, s44, s37\n" \
"v_add_f32 v169, v169, v177\n" \
"v_mfma_f32_16x16x32_bf16 v[20:23], v[120:123], a[172:175], v[20:23]\n" \
"v_add_f32 v180, v168, v169\n" \
"ds_read_b128 v[156:159], v178 offset:31744\n" \
"s_waitcnt lgkmcnt(4)\n" \
"v_mfma_f32_16x16x32_bf16 v[28:31], v[124:127], a[172:175], v[28:31]\n" \
"v_cmp_nge_f32 vcc, s51, v180\n" \
"s_waitcnt vmcnt(16)\n" \
"s_barrier\n" \
"ds_read_b128 v[96:99], v179 offset:0\n" \
"v_mfma_f32_16x16x32_bf16 v[20:23], v[128:131], a[176:179], v[20:23]\n" \
"ds_read_b128 v[100:103], v179 offset:1024\n" \
"s_mov_b32 s44, s41\n" \
"v_mfma_f32_16x16x32_bf16 v[28:31], v[132:135], a[176:179], v[28:31]\n" \
"ds_read_b128 v[104:107], v179 offset:2048\n" \
"s_mov_b32 s41, s48\n" \
"v_mfma_f32_16x16x32_bf16 v[20:23], v[136:139], a[180:183], v[20:23]\n" \
"ds_read_b128 v[108:111], v179 offset:3072\n" \
"s_waitcnt lgkmcnt(4)\n" \
"v_mfma_f32_16x16x32_bf16 v[28:31], v[140:143], a[180:183], v[28:31]\n" \
"ds_read_b128 v[112:115], v179 offset:4096\n" \
"v_mfma_f32_16x16x32_bf16 v[20:23], v[144:147], a[184:187], v[20:23]\n" \
"s_cmp_ge_i32 s47, 0\n" \
"ds_read_b128 v[116:119], v179 offset:5120\n" \
"v_mfma_f32_16x16x32_bf16 v[28:31], v[148:151], a[184:187], v[28:31]\n" \
"s_cselect_b32 s95, 1, s49\n" \
"ds_read_b128 v[120:123], v179 offset:6144\n" \
"v_mfma_f32_16x16x32_bf16 v[20:23], v[152:155], a[188:191], v[20:23]\n" \
"s_cmp_lg_u32 s95, 0\n" \
"ds_read_b128 v[124:127], v179 offset:7168\n" \
"s_waitcnt lgkmcnt(4)\n" \
"v_mfma_f32_16x16x32_bf16 v[28:31], v[156:159], a[188:191], v[28:31]\n" \
"v_mov_b32 v178, v179\n" \
"s_branch LaG%=\n" \
"LaS1%=:\n" \
"s_call_b64 s[92:93], Lspec1%=\n" \
"s_branch LaR1%=\n" \
"LaV1%=:\n" \
"s_call_b64 s[92:93], Lrare0%=\n" \
"s_branch LaC1%=\n" \
"LaS2%=:\n" \
"s_call_b64 s[92:93], Lspec0%=\n" \
"s_branch LaR2%=\n" \
"LaV2%=:\n" \
"s_call_b64 s[92:93], Lrare1%=\n" \
"s_branch LaC2%=\n" \
"Lrare0%=:\n" \
"s_nop 7\n" \
"v_max3_f32 v186, v0, v1, v2\n" \
"v_max3_f32 v184, v3, v8, v9\n" \
"v_max3_f32 v186, v186, v10, v11\n" \
"v_max3_f32 v184, v184, v16, v17\n" \
"v_max3_f32 v186, v186, v18, v19\n" \
"v_max3_f32 v184, v184, v24, v25\n" \
"v_max3_f32 v186, v186, v26, v27\n" \
"v_max_f32 v186, v186, v184\n" \
"v_mov_b32 v184, v186\n" \
"s_nop 1\n" \
"v_permlane16_swap_b32 v184, v186\n" \
"v_max_f32 v186, v186, v184\n" \
"v_mov_b32 v184, v186\n" \
"s_nop 1\n" \
"v_permlane32_swap_b32 v184, v186\n" \
"v_max_f32 v186, v186, v184\n" \
"v_max_f32 v186, 0, v186\n" \
"v_add_f32 v172, v172, v186\n" \
"v_exp_f32 v187, -v186\n" \
"v_sub_f32 v160, 0, v172\n" \
"v_sub_f32 v161, 0, v172\n" \
"v_sub_f32 v162, 0, v172\n" \
"v_sub_f32 v163, 0, v172\n" \
"v_mul_f32 v170, v170, v187\n" \
"v_mov_b32 v168, 0\n" \
"v_sub_f32 v188, v0, v186\n" \
"v_sub_f32 v189, v1, v186\n" \
"v_sub_f32 v190, v2, v186\n" \
"v_sub_f32 v191, v3, v186\n" \
"v_exp_f32 v188, v188\n" \
"v_exp_f32 v189, v189\n" \
"v_exp_f32 v190, v190\n" \
"v_exp_f32 v191, v191\n" \
"v_sub_f32 v32, v32, v186\n" \
"v_sub_f32 v33, v33, v186\n" \
"v_sub_f32 v34, v34, v186\n" \
"v_sub_f32 v35, v35, v186\n" \
"v_add_f32 v168, v168, v188\n" \
"v_add_f32 v168, v168, v189\n" \
"v_add_f32 v168, v168, v190\n" \
"v_add_f32 v168, v168, v191\n" \
"v_cvt_pk_bf16_f32 v64, v188, v189\n" \
"v_cvt_pk_bf16_f32 v65, v190, v191\n" \
"v_sub_f32 v188, v8, v186\n" \
"v_sub_f32 v189, v9, v186\n" \
"v_sub_f32 v190, v10, v186\n" \
"v_sub_f32 v191, v11, v186\n" \
"v_exp_f32 v188, v188\n" \
"v_exp_f32 v189, v189\n" \
"v_exp_f32 v190, v190\n" \
"v_exp_f32 v191, v191\n" \
"v_sub_f32 v40, v40, v186\n" \
"v_sub_f32 v41, v41, v186\n" \
"v_sub_f32 v42, v42, v186\n" \
"v_sub_f32 v43, v43, v186\n" \
"v_add_f32 v168, v168, v188\n" \
"v_add_f32 v168, v168, v189\n" \
"v_add_f32 v168, v168, v190\n" \
"v_add_f32 v168, v168, v191\n" \
"v_cvt_pk_bf16_f32 v66, v188, v189\n" \
"v_cvt_pk_bf16_f32 v67, v190, v191\n" \
"v_sub_f32 v188, v16, v186\n" \
"v_sub_f32 v189, v17, v186\n" \
"v_sub_f32 v190, v18, v186\n" \
"v_sub_f32 v191, v19, v186\n" \
"v_exp_f32 v188, v188\n" \
"v_exp_f32 v189, v189\n" \
"v_exp_f32 v190, v190\n" \
"v_exp_f32 v191, v191\n" \
"v_sub_f32 v48, v48, v186\n" \
"v_sub_f32 v49, v49, v186\n" \
"v_sub_f32 v50, v50, v186\n" \
"v_sub_f32 v51, v51, v186\n" \
"v_add_f32 v168, v168, v188\n" \
"v_add_f32 v168, v168, v189\n" \
"v_add_f32 v168, v168, v190\n" \
"v_add_f32 v168, v168, v191\n" \
"v_cvt_pk_bf16_f32 v68, v188, v189\n" \
"v_cvt_pk_bf16_f32 v69, v190, v191\n" \
"v_sub_f32 v188, v24, v186\n" \
"v_sub_f32 v189, v25, v186\n" \
"v_sub_f32 v190, v26, v186\n" \
"v_sub_f32 v191, v27, v186\n" \
"v_exp_f32 v188, v188\n" \
"v_exp_f32 v189, v189\n" \
"v_exp_f32 v190, v190\n" \
"v_exp_f32 v191, v191\n" \
"v_sub_f32 v56, v56, v186\n" \
"v_sub_f32 v57, v57, v186\n" \
"v_sub_f32 v58, v58, v186\n" \
"v_sub_f32 v59, v59, v186\n" \
"v_add_f32 v168, v168, v188\n" \
"v_add_f32 v168, v168, v189\n" \
"v_add_f32 v168, v168, v190\n" \
"v_add_f32 v168, v168, v191\n" \
"v_cvt_pk_bf16_f32 v70, v188, v189\n" \
"v_cvt_pk_bf16_f32 v71, v190, v191\n" \
"v_accvgpr_read_b32 v192, a0\n" \
"v_accvgpr_read_b32 v193, a1\n" \
"v_accvgpr_read_b32 v194, a2\n" \
"v_accvgpr_read_b32 v195, a3\n" \
"v_mul_f32 v192, v192, v187\n" \
"v_mul_f32 v193, v193, v187\n" \
"v_mul_f32 v194, v194, v187\n" \
"v_mul_f32 v195, v195, v187\n" \
"v_accvgpr_write_b32 a0, v192\n" \
"v_accvgpr_write_b32 a1, v193\n" \
"v_accvgpr_write_b32 a2, v194\n" \
"v_accvgpr_write_b32 a3, v195\n" \
"v_accvgpr_read_b32 v192, a8\n" \
"v_accvgpr_read_b32 v193, a9\n" \
"v_accvgpr_read_b32 v194, a10\n" \
"v_accvgpr_read_b32 v195, a11\n" \
"v_mul_f32 v192, v192, v187\n" \
"v_mul_f32 v193, v193, v187\n" \
"v_mul_f32 v194, v194, v187\n" \
"v_mul_f32 v195, v195, v187\n" \
"v_accvgpr_write_b32 a8, v192\n" \
"v_accvgpr_write_b32 a9, v193\n" \
"v_accvgpr_write_b32 a10, v194\n" \
"v_accvgpr_write_b32 a11, v195\n" \
"v_accvgpr_read_b32 v192, a16\n" \
"v_accvgpr_read_b32 v193, a17\n" \
"v_accvgpr_read_b32 v194, a18\n" \
"v_accvgpr_read_b32 v195, a19\n" \
"v_mul_f32 v192, v192, v187\n" \
"v_mul_f32 v193, v193, v187\n" \
"v_mul_f32 v194, v194, v187\n" \
"v_mul_f32 v195, v195, v187\n" \
"v_accvgpr_write_b32 a16, v192\n" \
"v_accvgpr_write_b32 a17, v193\n" \
"v_accvgpr_write_b32 a18, v194\n" \
"v_accvgpr_write_b32 a19, v195\n" \
"v_accvgpr_read_b32 v192, a24\n" \
"v_accvgpr_read_b32 v193, a25\n" \
"v_accvgpr_read_b32 v194, a26\n" \
"v_accvgpr_read_b32 v195, a27\n" \
"v_mul_f32 v192, v192, v187\n" \
"v_mul_f32 v193, v193, v187\n" \
"v_mul_f32 v194, v194, v187\n" \
"v_mul_f32 v195, v195, v187\n" \
"v_accvgpr_write_b32 a24, v192\n" \
"v_accvgpr_write_b32 a25, v193\n" \
"v_accvgpr_write_b32 a26, v194\n" \
"v_accvgpr_write_b32 a27, v195\n" \
"v_accvgpr_read_b32 v192, a32\n" \
"v_accvgpr_read_b32 v193, a33\n" \
"v_accvgpr_read_b32 v194, a34\n" \
"v_accvgpr_read_b32 v195, a35\n" \
"v_mul_f32 v192, v192, v187\n" \
"v_mul_f32 v193, v193, v187\n" \
"v_mul_f32 v194, v194, v187\n" \
"v_mul_f32 v195, v195, v187\n" \
"v_accvgpr_write_b32 a32, v192\n" \
"v_accvgpr_write_b32 a33, v193\n" \
"v_accvgpr_write_b32 a34, v194\n" \
"v_accvgpr_write_b32 a35, v195\n" \
"v_accvgpr_read_b32 v192, a40\n" \
"v_accvgpr_read_b32 v193, a41\n" \
"v_accvgpr_read_b32 v194, a42\n" \
"v_accvgpr_read_b32 v195, a43\n" \
"v_mul_f32 v192, v192, v187\n" \
"v_mul_f32 v193, v193, v187\n" \
"v_mul_f32 v194, v194, v187\n" \
"v_mul_f32 v195, v195, v187\n" \
"v_accvgpr_write_b32 a40, v192\n" \
"v_accvgpr_write_b32 a41, v193\n" \
"v_accvgpr_write_b32 a42, v194\n" \
"v_accvgpr_write_b32 a43, v195\n" \
"v_accvgpr_read_b32 v192, a48\n" \
"v_accvgpr_read_b32 v193, a49\n" \
"v_accvgpr_read_b32 v194, a50\n" \
"v_accvgpr_read_b32 v195, a51\n" \
"v_mul_f32 v192, v192, v187\n" \
"v_mul_f32 v193, v193, v187\n" \
"v_mul_f32 v194, v194, v187\n" \
"v_mul_f32 v195, v195, v187\n" \
"v_accvgpr_write_b32 a48, v192\n" \
"v_accvgpr_write_b32 a49, v193\n" \
"v_accvgpr_write_b32 a50, v194\n" \
"v_accvgpr_write_b32 a51, v195\n" \
"v_accvgpr_read_b32 v192, a56\n" \
"v_accvgpr_read_b32 v193, a57\n" \
"v_accvgpr_read_b32 v194, a58\n" \
"v_accvgpr_read_b32 v195, a59\n" \
"v_mul_f32 v192, v192, v187\n" \
"v_mul_f32 v193, v193, v187\n" \
"v_mul_f32 v194, v194, v187\n" \
"v_mul_f32 v195, v195, v187\n" \
"v_accvgpr_write_b32 a56, v192\n" \
"v_accvgpr_write_b32 a57, v193\n" \
"v_accvgpr_write_b32 a58, v194\n" \
"v_accvgpr_write_b32 a59, v195\n" \
"v_accvgpr_read_b32 v192, a64\n" \
"v_accvgpr_read_b32 v193, a65\n" \
"v_accvgpr_read_b32 v194, a66\n" \
"v_accvgpr_read_b32 v195, a67\n" \
"v_mul_f32 v192, v192, v187\n" \
"v_mul_f32 v193, v193, v187\n" \
"v_mul_f32 v194, v194, v187\n" \
"v_mul_f32 v195, v195, v187\n" \
"v_accvgpr_write_b32 a64, v192\n" \
"v_accvgpr_write_b32 a65, v193\n" \
"v_accvgpr_write_b32 a66, v194\n" \
"v_accvgpr_write_b32 a67, v195\n" \
"v_accvgpr_read_b32 v192, a72\n" \
"v_accvgpr_read_b32 v193, a73\n" \
"v_accvgpr_read_b32 v194, a74\n" \
"v_accvgpr_read_b32 v195, a75\n" \
"v_mul_f32 v192, v192, v187\n" \
"v_mul_f32 v193, v193, v187\n" \
"v_mul_f32 v194, v194, v187\n" \
"v_mul_f32 v195, v195, v187\n" \
"v_accvgpr_write_b32 a72, v192\n" \
"v_accvgpr_write_b32 a73, v193\n" \
"v_accvgpr_write_b32 a74, v194\n" \
"v_accvgpr_write_b32 a75, v195\n" \
"v_accvgpr_read_b32 v192, a80\n" \
"v_accvgpr_read_b32 v193, a81\n" \
"v_accvgpr_read_b32 v194, a82\n" \
"v_accvgpr_read_b32 v195, a83\n" \
"v_mul_f32 v192, v192, v187\n" \
"v_mul_f32 v193, v193, v187\n" \
"v_mul_f32 v194, v194, v187\n" \
"v_mul_f32 v195, v195, v187\n" \
"v_accvgpr_write_b32 a80, v192\n" \
"v_accvgpr_write_b32 a81, v193\n" \
"v_accvgpr_write_b32 a82, v194\n" \
"v_accvgpr_write_b32 a83, v195\n" \
"v_accvgpr_read_b32 v192, a88\n" \
"v_accvgpr_read_b32 v193, a89\n" \
"v_accvgpr_read_b32 v194, a90\n" \
"v_accvgpr_read_b32 v195, a91\n" \
"v_mul_f32 v192, v192, v187\n" \
"v_mul_f32 v193, v193, v187\n" \
"v_mul_f32 v194, v194, v187\n" \
"v_mul_f32 v195, v195, v187\n" \
"v_accvgpr_write_b32 a88, v192\n" \
"v_accvgpr_write_b32 a89, v193\n" \
"v_accvgpr_write_b32 a90, v194\n" \
"v_accvgpr_write_b32 a91, v195\n" \
"v_accvgpr_read_b32 v192, a96\n" \
"v_accvgpr_read_b32 v193, a97\n" \
"v_accvgpr_read_b32 v194, a98\n" \
"v_accvgpr_read_b32 v195, a99\n" \
"v_mul_f32 v192, v192, v187\n" \
"v_mul_f32 v193, v193, v187\n" \
"v_mul_f32 v194, v194, v187\n" \
"v_mul_f32 v195, v195, v187\n" \
"v_accvgpr_write_b32 a96, v192\n" \
"v_accvgpr_write_b32 a97, v193\n" \
"v_accvgpr_write_b32 a98, v194\n" \
"v_accvgpr_write_b32 a99, v195\n" \
"v_accvgpr_read_b32 v192, a104\n" \
"v_accvgpr_read_b32 v193, a105\n" \
"v_accvgpr_read_b32 v194, a106\n" \
"v_accvgpr_read_b32 v195, a107\n" \
"v_mul_f32 v192, v192, v187\n" \
"v_mul_f32 v193, v193, v187\n" \
"v_mul_f32 v194, v194, v187\n" \
"v_mul_f32 v195, v195, v187\n" \
"v_accvgpr_write_b32 a104, v192\n" \
"v_accvgpr_write_b32 a105, v193\n" \
"v_accvgpr_write_b32 a106, v194\n" \
"v_accvgpr_write_b32 a107, v195\n" \
"v_accvgpr_read_b32 v192, a112\n" \
"v_accvgpr_read_b32 v193, a113\n" \
"v_accvgpr_read_b32 v194, a114\n" \
"v_accvgpr_read_b32 v195, a115\n" \
"v_mul_f32 v192, v192, v187\n" \
"v_mul_f32 v193, v193, v187\n" \
"v_mul_f32 v194, v194, v187\n" \
"v_mul_f32 v195, v195, v187\n" \
"v_accvgpr_write_b32 a112, v192\n" \
"v_accvgpr_write_b32 a113, v193\n" \
"v_accvgpr_write_b32 a114, v194\n" \
"v_accvgpr_write_b32 a115, v195\n" \
"v_accvgpr_read_b32 v192, a120\n" \
"v_accvgpr_read_b32 v193, a121\n" \
"v_accvgpr_read_b32 v194, a122\n" \
"v_accvgpr_read_b32 v195, a123\n" \
"v_mul_f32 v192, v192, v187\n" \
"v_mul_f32 v193, v193, v187\n" \
"v_mul_f32 v194, v194, v187\n" \
"v_mul_f32 v195, v195, v187\n" \
"v_accvgpr_write_b32 a120, v192\n" \
"v_accvgpr_write_b32 a121, v193\n" \
"v_accvgpr_write_b32 a122, v194\n" \
"v_accvgpr_write_b32 a123, v195\n" \
"v_max3_f32 v186, v4, v5, v6\n" \
"v_max3_f32 v184, v7, v12, v13\n" \
"v_max3_f32 v186, v186, v14, v15\n" \
"v_max3_f32 v184, v184, v20, v21\n" \
"v_max3_f32 v186, v186, v22, v23\n" \
"v_max3_f32 v184, v184, v28, v29\n" \
"v_max3_f32 v186, v186, v30, v31\n" \
"v_max_f32 v186, v186, v184\n" \
"v_mov_b32 v184, v186\n" \
"s_nop 1\n" \
"v_permlane16_swap_b32 v184, v186\n" \
"v_max_f32 v186, v186, v184\n" \
"v_mov_b32 v184, v186\n" \
"s_nop 1\n" \
"v_permlane32_swap_b32 v184, v186\n" \
"v_max_f32 v186, v186, v184\n" \
"v_max_f32 v186, 0, v186\n" \
"v_add_f32 v173, v173, v186\n" \
"v_exp_f32 v187, -v186\n" \
"v_sub_f32 v164, 0, v173\n" \
"v_sub_f32 v165, 0, v173\n" \
"v_sub_f32 v166, 0, v173\n" \
"v_sub_f32 v167, 0, v173\n" \
"v_mul_f32 v171, v171, v187\n" \
"v_mov_b32 v169, 0\n" \
"v_sub_f32 v188, v4, v186\n" \
"v_sub_f32 v189, v5, v186\n" \
"v_sub_f32 v190, v6, v186\n" \
"v_sub_f32 v191, v7, v186\n" \
"v_exp_f32 v188, v188\n" \
"v_exp_f32 v189, v189\n" \
"v_exp_f32 v190, v190\n" \
"v_exp_f32 v191, v191\n" \
"v_sub_f32 v36, v36, v186\n" \
"v_sub_f32 v37, v37, v186\n" \
"v_sub_f32 v38, v38, v186\n" \
"v_sub_f32 v39, v39, v186\n" \
"v_add_f32 v169, v169, v188\n" \
"v_add_f32 v169, v169, v189\n" \
"v_add_f32 v169, v169, v190\n" \
"v_add_f32 v169, v169, v191\n" \
"v_cvt_pk_bf16_f32 v72, v188, v189\n" \
"v_cvt_pk_bf16_f32 v73, v190, v191\n" \
"v_sub_f32 v188, v12, v186\n" \
"v_sub_f32 v189, v13, v186\n" \
"v_sub_f32 v190, v14, v186\n" \
"v_sub_f32 v191, v15, v186\n" \
"v_exp_f32 v188, v188\n" \
"v_exp_f32 v189, v189\n" \
"v_exp_f32 v190, v190\n" \
"v_exp_f32 v191, v191\n" \
"v_sub_f32 v44, v44, v186\n" \
"v_sub_f32 v45, v45, v186\n" \
"v_sub_f32 v46, v46, v186\n" \
"v_sub_f32 v47, v47, v186\n" \
"v_add_f32 v169, v169, v188\n" \
"v_add_f32 v169, v169, v189\n" \
"v_add_f32 v169, v169, v190\n" \
"v_add_f32 v169, v169, v191\n" \
"v_cvt_pk_bf16_f32 v74, v188, v189\n" \
"v_cvt_pk_bf16_f32 v75, v190, v191\n" \
"v_sub_f32 v188, v20, v186\n" \
"v_sub_f32 v189, v21, v186\n" \
"v_sub_f32 v190, v22, v186\n" \
"v_sub_f32 v191, v23, v186\n" \
"v_exp_f32 v188, v188\n" \
"v_exp_f32 v189, v189\n" \
"v_exp_f32 v190, v190\n" \
"v_exp_f32 v191, v191\n" \
"v_sub_f32 v52, v52, v186\n" \
"v_sub_f32 v53, v53, v186\n" \
"v_sub_f32 v54, v54, v186\n" \
"v_sub_f32 v55, v55, v186\n" \
"v_add_f32 v169, v169, v188\n" \
"v_add_f32 v169, v169, v189\n" \
"v_add_f32 v169, v169, v190\n" \
"v_add_f32 v169, v169, v191\n" \
"v_cvt_pk_bf16_f32 v76, v188, v189\n" \
"v_cvt_pk_bf16_f32 v77, v190, v191\n" \
"v_sub_f32 v188, v28, v186\n" \
"v_sub_f32 v189, v29, v186\n" \
"v_sub_f32 v190, v30, v186\n" \
"v_sub_f32 v191, v31, v186\n" \
"v_exp_f32 v188, v188\n" \
"v_exp_f32 v189, v189\n" \
"v_exp_f32 v190, v190\n" \
"v_exp_f32 v191, v191\n" \
"v_sub_f32 v60, v60, v186\n" \
"v_sub_f32 v61, v61, v186\n" \
"v_sub_f32 v62, v62, v186\n" \
"v_sub_f32 v63, v63, v186\n" \
"v_add_f32 v169, v169, v188\n" \
"v_add_f32 v169, v169, v189\n" \
"v_add_f32 v169, v169, v190\n" \
"v_add_f32 v169, v169, v191\n" \
"v_cvt_pk_bf16_f32 v78, v188, v189\n" \
"v_cvt_pk_bf16_f32 v79, v190, v191\n" \
"v_accvgpr_read_b32 v192, a4\n" \
"v_accvgpr_read_b32 v193, a5\n" \
"v_accvgpr_read_b32 v194, a6\n" \
"v_accvgpr_read_b32 v195, a7\n" \
"v_mul_f32 v192, v192, v187\n" \
"v_mul_f32 v193, v193, v187\n" \
"v_mul_f32 v194, v194, v187\n" \
"v_mul_f32 v195, v195, v187\n" \
"v_accvgpr_write_b32 a4, v192\n" \
"v_accvgpr_write_b32 a5, v193\n" \
"v_accvgpr_write_b32 a6, v194\n" \
"v_accvgpr_write_b32 a7, v195\n" \
"v_accvgpr_read_b32 v192, a12\n" \
"v_accvgpr_read_b32 v193, a13\n" \
"v_accvgpr_read_b32 v194, a14\n" \
"v_accvgpr_read_b32 v195, a15\n" \
"v_mul_f32 v192, v192, v187\n" \
"v_mul_f32 v193, v193, v187\n" \
"v_mul_f32 v194, v194, v187\n" \
"v_mul_f32 v195, v195, v187\n" \
"v_accvgpr_write_b32 a12, v192\n" \
"v_accvgpr_write_b32 a13, v193\n" \
"v_accvgpr_write_b32 a14, v194\n" \
"v_accvgpr_write_b32 a15, v195\n" \
"v_accvgpr_read_b32 v192, a20\n" \
"v_accvgpr_read_b32 v193, a21\n" \
"v_accvgpr_read_b32 v194, a22\n" \
"v_accvgpr_read_b32 v195, a23\n" \
"v_mul_f32 v192, v192, v187\n" \
"v_mul_f32 v193, v193, v187\n" \
"v_mul_f32 v194, v194, v187\n" \
"v_mul_f32 v195, v195, v187\n" \
"v_accvgpr_write_b32 a20, v192\n" \
"v_accvgpr_write_b32 a21, v193\n" \
"v_accvgpr_write_b32 a22, v194\n" \
"v_accvgpr_write_b32 a23, v195\n" \
"v_accvgpr_read_b32 v192, a28\n" \
"v_accvgpr_read_b32 v193, a29\n" \
"v_accvgpr_read_b32 v194, a30\n" \
"v_accvgpr_read_b32 v195, a31\n" \
"v_mul_f32 v192, v192, v187\n" \
"v_mul_f32 v193, v193, v187\n" \
"v_mul_f32 v194, v194, v187\n" \
"v_mul_f32 v195, v195, v187\n" \
"v_accvgpr_write_b32 a28, v192\n" \
"v_accvgpr_write_b32 a29, v193\n" \
"v_accvgpr_write_b32 a30, v194\n" \
"v_accvgpr_write_b32 a31, v195\n" \
"v_accvgpr_read_b32 v192, a36\n" \
"v_accvgpr_read_b32 v193, a37\n" \
"v_accvgpr_read_b32 v194, a38\n" \
"v_accvgpr_read_b32 v195, a39\n" \
"v_mul_f32 v192, v192, v187\n" \
"v_mul_f32 v193, v193, v187\n" \
"v_mul_f32 v194, v194, v187\n" \
"v_mul_f32 v195, v195, v187\n" \
"v_accvgpr_write_b32 a36, v192\n" \
"v_accvgpr_write_b32 a37, v193\n" \
"v_accvgpr_write_b32 a38, v194\n" \
"v_accvgpr_write_b32 a39, v195\n" \
"v_accvgpr_read_b32 v192, a44\n" \
"v_accvgpr_read_b32 v193, a45\n" \
"v_accvgpr_read_b32 v194, a46\n" \
"v_accvgpr_read_b32 v195, a47\n" \
"v_mul_f32 v192, v192, v187\n" \
"v_mul_f32 v193, v193, v187\n" \
"v_mul_f32 v194, v194, v187\n" \
"v_mul_f32 v195, v195, v187\n" \
"v_accvgpr_write_b32 a44, v192\n" \
"v_accvgpr_write_b32 a45, v193\n" \
"v_accvgpr_write_b32 a46, v194\n" \
"v_accvgpr_write_b32 a47, v195\n" \
"v_accvgpr_read_b32 v192, a52\n" \
"v_accvgpr_read_b32 v193, a53\n" \
"v_accvgpr_read_b32 v194, a54\n" \
"v_accvgpr_read_b32 v195, a55\n" \
"v_mul_f32 v192, v192, v187\n" \
"v_mul_f32 v193, v193, v187\n" \
"v_mul_f32 v194, v194, v187\n" \
"v_mul_f32 v195, v195, v187\n" \
"v_accvgpr_write_b32 a52, v192\n" \
"v_accvgpr_write_b32 a53, v193\n" \
"v_accvgpr_write_b32 a54, v194\n" \
"v_accvgpr_write_b32 a55, v195\n" \
"v_accvgpr_read_b32 v192, a60\n" \
"v_accvgpr_read_b32 v193, a61\n" \
"v_accvgpr_read_b32 v194, a62\n" \
"v_accvgpr_read_b32 v195, a63\n" \
"v_mul_f32 v192, v192, v187\n" \
"v_mul_f32 v193, v193, v187\n" \
"v_mul_f32 v194, v194, v187\n" \
"v_mul_f32 v195, v195, v187\n" \
"v_accvgpr_write_b32 a60, v192\n" \
"v_accvgpr_write_b32 a61, v193\n" \
"v_accvgpr_write_b32 a62, v194\n" \
"v_accvgpr_write_b32 a63, v195\n" \
"v_accvgpr_read_b32 v192, a68\n" \
"v_accvgpr_read_b32 v193, a69\n" \
"v_accvgpr_read_b32 v194, a70\n" \
"v_accvgpr_read_b32 v195, a71\n" \
"v_mul_f32 v192, v192, v187\n" \
"v_mul_f32 v193, v193, v187\n" \
"v_mul_f32 v194, v194, v187\n" \
"v_mul_f32 v195, v195, v187\n" \
"v_accvgpr_write_b32 a68, v192\n" \
"v_accvgpr_write_b32 a69, v193\n" \
"v_accvgpr_write_b32 a70, v194\n" \
"v_accvgpr_write_b32 a71, v195\n" \
"v_accvgpr_read_b32 v192, a76\n" \
"v_accvgpr_read_b32 v193, a77\n" \
"v_accvgpr_read_b32 v194, a78\n" \
"v_accvgpr_read_b32 v195, a79\n" \
"v_mul_f32 v192, v192, v187\n" \
"v_mul_f32 v193, v193, v187\n" \
"v_mul_f32 v194, v194, v187\n" \
"v_mul_f32 v195, v195, v187\n" \
"v_accvgpr_write_b32 a76, v192\n" \
"v_accvgpr_write_b32 a77, v193\n" \
"v_accvgpr_write_b32 a78, v194\n" \
"v_accvgpr_write_b32 a79, v195\n" \
"v_accvgpr_read_b32 v192, a84\n" \
"v_accvgpr_read_b32 v193, a85\n" \
"v_accvgpr_read_b32 v194, a86\n" \
"v_accvgpr_read_b32 v195, a87\n" \
"v_mul_f32 v192, v192, v187\n" \
"v_mul_f32 v193, v193, v187\n" \
"v_mul_f32 v194, v194, v187\n" \
"v_mul_f32 v195, v195, v187\n" \
"v_accvgpr_write_b32 a84, v192\n" \
"v_accvgpr_write_b32 a85, v193\n" \
"v_accvgpr_write_b32 a86, v194\n" \
"v_accvgpr_write_b32 a87, v195\n" \
"v_accvgpr_read_b32 v192, a92\n" \
"v_accvgpr_read_b32 v193, a93\n" \
"v_accvgpr_read_b32 v194, a94\n" \
"v_accvgpr_read_b32 v195, a95\n" \
"v_mul_f32 v192, v192, v187\n" \
"v_mul_f32 v193, v193, v187\n" \
"v_mul_f32 v194, v194, v187\n" \
"v_mul_f32 v195, v195, v187\n" \
"v_accvgpr_write_b32 a92, v192\n" \
"v_accvgpr_write_b32 a93, v193\n" \
"v_accvgpr_write_b32 a94, v194\n" \
"v_accvgpr_write_b32 a95, v195\n" \
"v_accvgpr_read_b32 v192, a100\n" \
"v_accvgpr_read_b32 v193, a101\n" \
"v_accvgpr_read_b32 v194, a102\n" \
"v_accvgpr_read_b32 v195, a103\n" \
"v_mul_f32 v192, v192, v187\n" \
"v_mul_f32 v193, v193, v187\n" \
"v_mul_f32 v194, v194, v187\n" \
"v_mul_f32 v195, v195, v187\n" \
"v_accvgpr_write_b32 a100, v192\n" \
"v_accvgpr_write_b32 a101, v193\n" \
"v_accvgpr_write_b32 a102, v194\n" \
"v_accvgpr_write_b32 a103, v195\n" \
"v_accvgpr_read_b32 v192, a108\n" \
"v_accvgpr_read_b32 v193, a109\n" \
"v_accvgpr_read_b32 v194, a110\n" \
"v_accvgpr_read_b32 v195, a111\n" \
"v_mul_f32 v192, v192, v187\n" \
"v_mul_f32 v193, v193, v187\n" \
"v_mul_f32 v194, v194, v187\n" \
"v_mul_f32 v195, v195, v187\n" \
"v_accvgpr_write_b32 a108, v192\n" \
"v_accvgpr_write_b32 a109, v193\n" \
"v_accvgpr_write_b32 a110, v194\n" \
"v_accvgpr_write_b32 a111, v195\n" \
"v_accvgpr_read_b32 v192, a116\n" \
"v_accvgpr_read_b32 v193, a117\n" \
"v_accvgpr_read_b32 v194, a118\n" \
"v_accvgpr_read_b32 v195, a119\n" \
"v_mul_f32 v192, v192, v187\n" \
"v_mul_f32 v193, v193, v187\n" \
"v_mul_f32 v194, v194, v187\n" \
"v_mul_f32 v195, v195, v187\n" \
"v_accvgpr_write_b32 a116, v192\n" \
"v_accvgpr_write_b32 a117, v193\n" \
"v_accvgpr_write_b32 a118, v194\n" \
"v_accvgpr_write_b32 a119, v195\n" \
"v_accvgpr_read_b32 v192, a124\n" \
"v_accvgpr_read_b32 v193, a125\n" \
"v_accvgpr_read_b32 v194, a126\n" \
"v_accvgpr_read_b32 v195, a127\n" \
"v_mul_f32 v192, v192, v187\n" \
"v_mul_f32 v193, v193, v187\n" \
"v_mul_f32 v194, v194, v187\n" \
"v_mul_f32 v195, v195, v187\n" \
"v_accvgpr_write_b32 a124, v192\n" \
"v_accvgpr_write_b32 a125, v193\n" \
"v_accvgpr_write_b32 a126, v194\n" \
"v_accvgpr_write_b32 a127, v195\n" \
"s_nop 1\n" \
"s_setpc_b64 s[92:93]\n" \
"Lrare1%=:\n" \
"s_nop 7\n" \
"v_max3_f32 v186, v32, v33, v34\n" \
"v_max3_f32 v184, v35, v40, v41\n" \
"v_max3_f32 v186, v186, v42, v43\n" \
"v_max3_f32 v184, v184, v48, v49\n" \
"v_max3_f32 v186, v186, v50, v51\n" \
"v_max3_f32 v184, v184, v56, v57\n" \
"v_max3_f32 v186, v186, v58, v59\n" \
"v_max_f32 v186, v186, v184\n" \
"v_mov_b32 v184, v186\n" \
"s_nop 1\n" \
"v_permlane16_swap_b32 v184, v186\n" \
"v_max_f32 v186, v186, v184\n" \
"v_mov_b32 v184, v186\n" \
"s_nop 1\n" \
"v_permlane32_swap_b32 v184, v186\n" \
"v_max_f32 v186, v186, v184\n" \
"v_max_f32 v186, 0, v186\n" \
"v_add_f32 v172, v172, v186\n" \
"v_exp_f32 v187, -v186\n" \
"v_sub_f32 v160, 0, v172\n" \
"v_sub_f32 v161, 0, v172\n" \
"v_sub_f32 v162, 0, v172\n" \
"v_sub_f32 v163, 0, v172\n" \
"v_mul_f32 v170, v170, v187\n" \
"v_mov_b32 v168, 0\n" \
"v_sub_f32 v188, v32, v186\n" \
"v_sub_f32 v189, v33, v186\n" \
"v_sub_f32 v190, v34, v186\n" \
"v_sub_f32 v191, v35, v186\n" \
"v_exp_f32 v188, v188\n" \
"v_exp_f32 v189, v189\n" \
"v_exp_f32 v190, v190\n" \
"v_exp_f32 v191, v191\n" \
"v_sub_f32 v0, v0, v186\n" \
"v_sub_f32 v1, v1, v186\n" \
"v_sub_f32 v2, v2, v186\n" \
"v_sub_f32 v3, v3, v186\n" \
"v_add_f32 v168, v168, v188\n" \
"v_add_f32 v168, v168, v189\n" \
"v_add_f32 v168, v168, v190\n" \
"v_add_f32 v168, v168, v191\n" \
"v_cvt_pk_bf16_f32 v80, v188, v189\n" \
"v_cvt_pk_bf16_f32 v81, v190, v191\n" \
"v_sub_f32 v188, v40, v186\n" \
"v_sub_f32 v189, v41, v186\n" \
"v_sub_f32 v190, v42, v186\n" \
"v_sub_f32 v191, v43, v186\n" \
"v_exp_f32 v188, v188\n" \
"v_exp_f32 v189, v189\n" \
"v_exp_f32 v190, v190\n" \
"v_exp_f32 v191, v191\n" \
"v_sub_f32 v8, v8, v186\n" \
"v_sub_f32 v9, v9, v186\n" \
"v_sub_f32 v10, v10, v186\n" \
"v_sub_f32 v11, v11, v186\n" \
"v_add_f32 v168, v168, v188\n" \
"v_add_f32 v168, v168, v189\n" \
"v_add_f32 v168, v168, v190\n" \
"v_add_f32 v168, v168, v191\n" \
"v_cvt_pk_bf16_f32 v82, v188, v189\n" \
"v_cvt_pk_bf16_f32 v83, v190, v191\n" \
"v_sub_f32 v188, v48, v186\n" \
"v_sub_f32 v189, v49, v186\n" \
"v_sub_f32 v190, v50, v186\n" \
"v_sub_f32 v191, v51, v186\n" \
"v_exp_f32 v188, v188\n" \
"v_exp_f32 v189, v189\n" \
"v_exp_f32 v190, v190\n" \
"v_exp_f32 v191, v191\n" \
"v_sub_f32 v16, v16, v186\n" \
"v_sub_f32 v17, v17, v186\n" \
"v_sub_f32 v18, v18, v186\n" \
"v_sub_f32 v19, v19, v186\n" \
"v_add_f32 v168, v168, v188\n" \
"v_add_f32 v168, v168, v189\n" \
"v_add_f32 v168, v168, v190\n" \
"v_add_f32 v168, v168, v191\n" \
"v_cvt_pk_bf16_f32 v84, v188, v189\n" \
"v_cvt_pk_bf16_f32 v85, v190, v191\n" \
"v_sub_f32 v188, v56, v186\n" \
"v_sub_f32 v189, v57, v186\n" \
"v_sub_f32 v190, v58, v186\n" \
"v_sub_f32 v191, v59, v186\n" \
"v_exp_f32 v188, v188\n" \
"v_exp_f32 v189, v189\n" \
"v_exp_f32 v190, v190\n" \
"v_exp_f32 v191, v191\n" \
"v_sub_f32 v24, v24, v186\n" \
"v_sub_f32 v25, v25, v186\n" \
"v_sub_f32 v26, v26, v186\n" \
"v_sub_f32 v27, v27, v186\n" \
"v_add_f32 v168, v168, v188\n" \
"v_add_f32 v168, v168, v189\n" \
"v_add_f32 v168, v168, v190\n" \
"v_add_f32 v168, v168, v191\n" \
"v_cvt_pk_bf16_f32 v86, v188, v189\n" \
"v_cvt_pk_bf16_f32 v87, v190, v191\n" \
"v_accvgpr_read_b32 v192, a0\n" \
"v_accvgpr_read_b32 v193, a1\n" \
"v_accvgpr_read_b32 v194, a2\n" \
"v_accvgpr_read_b32 v195, a3\n" \
"v_mul_f32 v192, v192, v187\n" \
"v_mul_f32 v193, v193, v187\n" \
"v_mul_f32 v194, v194, v187\n" \
"v_mul_f32 v195, v195, v187\n" \
"v_accvgpr_write_b32 a0, v192\n" \
"v_accvgpr_write_b32 a1, v193\n" \
"v_accvgpr_write_b32 a2, v194\n" \
"v_accvgpr_write_b32 a3, v195\n" \
"v_accvgpr_read_b32 v192, a8\n" \
"v_accvgpr_read_b32 v193, a9\n" \
"v_accvgpr_read_b32 v194, a10\n" \
"v_accvgpr_read_b32 v195, a11\n" \
"v_mul_f32 v192, v192, v187\n" \
"v_mul_f32 v193, v193, v187\n" \
"v_mul_f32 v194, v194, v187\n" \
"v_mul_f32 v195, v195, v187\n" \
"v_accvgpr_write_b32 a8, v192\n" \
"v_accvgpr_write_b32 a9, v193\n" \
"v_accvgpr_write_b32 a10, v194\n" \
"v_accvgpr_write_b32 a11, v195\n" \
"v_accvgpr_read_b32 v192, a16\n" \
"v_accvgpr_read_b32 v193, a17\n" \
"v_accvgpr_read_b32 v194, a18\n" \
"v_accvgpr_read_b32 v195, a19\n" \
"v_mul_f32 v192, v192, v187\n" \
"v_mul_f32 v193, v193, v187\n" \
"v_mul_f32 v194, v194, v187\n" \
"v_mul_f32 v195, v195, v187\n" \
"v_accvgpr_write_b32 a16, v192\n" \
"v_accvgpr_write_b32 a17, v193\n" \
"v_accvgpr_write_b32 a18, v194\n" \
"v_accvgpr_write_b32 a19, v195\n" \
"v_accvgpr_read_b32 v192, a24\n" \
"v_accvgpr_read_b32 v193, a25\n" \
"v_accvgpr_read_b32 v194, a26\n" \
"v_accvgpr_read_b32 v195, a27\n" \
"v_mul_f32 v192, v192, v187\n" \
"v_mul_f32 v193, v193, v187\n" \
"v_mul_f32 v194, v194, v187\n" \
"v_mul_f32 v195, v195, v187\n" \
"v_accvgpr_write_b32 a24, v192\n" \
"v_accvgpr_write_b32 a25, v193\n" \
"v_accvgpr_write_b32 a26, v194\n" \
"v_accvgpr_write_b32 a27, v195\n" \
"v_accvgpr_read_b32 v192, a32\n" \
"v_accvgpr_read_b32 v193, a33\n" \
"v_accvgpr_read_b32 v194, a34\n" \
"v_accvgpr_read_b32 v195, a35\n" \
"v_mul_f32 v192, v192, v187\n" \
"v_mul_f32 v193, v193, v187\n" \
"v_mul_f32 v194, v194, v187\n" \
"v_mul_f32 v195, v195, v187\n" \
"v_accvgpr_write_b32 a32, v192\n" \
"v_accvgpr_write_b32 a33, v193\n" \
"v_accvgpr_write_b32 a34, v194\n" \
"v_accvgpr_write_b32 a35, v195\n" \
"v_accvgpr_read_b32 v192, a40\n" \
"v_accvgpr_read_b32 v193, a41\n" \
"v_accvgpr_read_b32 v194, a42\n" \
"v_accvgpr_read_b32 v195, a43\n" \
"v_mul_f32 v192, v192, v187\n" \
"v_mul_f32 v193, v193, v187\n" \
"v_mul_f32 v194, v194, v187\n" \
"v_mul_f32 v195, v195, v187\n" \
"v_accvgpr_write_b32 a40, v192\n" \
"v_accvgpr_write_b32 a41, v193\n" \
"v_accvgpr_write_b32 a42, v194\n" \
"v_accvgpr_write_b32 a43, v195\n" \
"v_accvgpr_read_b32 v192, a48\n" \
"v_accvgpr_read_b32 v193, a49\n" \
"v_accvgpr_read_b32 v194, a50\n" \
"v_accvgpr_read_b32 v195, a51\n" \
"v_mul_f32 v192, v192, v187\n" \
"v_mul_f32 v193, v193, v187\n" \
"v_mul_f32 v194, v194, v187\n" \
"v_mul_f32 v195, v195, v187\n" \
"v_accvgpr_write_b32 a48, v192\n" \
"v_accvgpr_write_b32 a49, v193\n" \
"v_accvgpr_write_b32 a50, v194\n" \
"v_accvgpr_write_b32 a51, v195\n" \
"v_accvgpr_read_b32 v192, a56\n" \
"v_accvgpr_read_b32 v193, a57\n" \
"v_accvgpr_read_b32 v194, a58\n" \
"v_accvgpr_read_b32 v195, a59\n" \
"v_mul_f32 v192, v192, v187\n" \
"v_mul_f32 v193, v193, v187\n" \
"v_mul_f32 v194, v194, v187\n" \
"v_mul_f32 v195, v195, v187\n" \
"v_accvgpr_write_b32 a56, v192\n" \
"v_accvgpr_write_b32 a57, v193\n" \
"v_accvgpr_write_b32 a58, v194\n" \
"v_accvgpr_write_b32 a59, v195\n" \
"v_accvgpr_read_b32 v192, a64\n" \
"v_accvgpr_read_b32 v193, a65\n" \
"v_accvgpr_read_b32 v194, a66\n" \
"v_accvgpr_read_b32 v195, a67\n" \
"v_mul_f32 v192, v192, v187\n" \
"v_mul_f32 v193, v193, v187\n" \
"v_mul_f32 v194, v194, v187\n" \
"v_mul_f32 v195, v195, v187\n" \
"v_accvgpr_write_b32 a64, v192\n" \
"v_accvgpr_write_b32 a65, v193\n" \
"v_accvgpr_write_b32 a66, v194\n" \
"v_accvgpr_write_b32 a67, v195\n" \
"v_accvgpr_read_b32 v192, a72\n" \
"v_accvgpr_read_b32 v193, a73\n" \
"v_accvgpr_read_b32 v194, a74\n" \
"v_accvgpr_read_b32 v195, a75\n" \
"v_mul_f32 v192, v192, v187\n" \
"v_mul_f32 v193, v193, v187\n" \
"v_mul_f32 v194, v194, v187\n" \
"v_mul_f32 v195, v195, v187\n" \
"v_accvgpr_write_b32 a72, v192\n" \
"v_accvgpr_write_b32 a73, v193\n" \
"v_accvgpr_write_b32 a74, v194\n" \
"v_accvgpr_write_b32 a75, v195\n" \
"v_accvgpr_read_b32 v192, a80\n" \
"v_accvgpr_read_b32 v193, a81\n" \
"v_accvgpr_read_b32 v194, a82\n" \
"v_accvgpr_read_b32 v195, a83\n" \
"v_mul_f32 v192, v192, v187\n" \
"v_mul_f32 v193, v193, v187\n" \
"v_mul_f32 v194, v194, v187\n" \
"v_mul_f32 v195, v195, v187\n" \
"v_accvgpr_write_b32 a80, v192\n" \
"v_accvgpr_write_b32 a81, v193\n" \
"v_accvgpr_write_b32 a82, v194\n" \
"v_accvgpr_write_b32 a83, v195\n" \
"v_accvgpr_read_b32 v192, a88\n" \
"v_accvgpr_read_b32 v193, a89\n" \
"v_accvgpr_read_b32 v194, a90\n" \
"v_accvgpr_read_b32 v195, a91\n" \
"v_mul_f32 v192, v192, v187\n" \
"v_mul_f32 v193, v193, v187\n" \
"v_mul_f32 v194, v194, v187\n" \
"v_mul_f32 v195, v195, v187\n" \
"v_accvgpr_write_b32 a88, v192\n" \
"v_accvgpr_write_b32 a89, v193\n" \
"v_accvgpr_write_b32 a90, v194\n" \
"v_accvgpr_write_b32 a91, v195\n" \
"v_accvgpr_read_b32 v192, a96\n" \
"v_accvgpr_read_b32 v193, a97\n" \
"v_accvgpr_read_b32 v194, a98\n" \
"v_accvgpr_read_b32 v195, a99\n" \
"v_mul_f32 v192, v192, v187\n" \
"v_mul_f32 v193, v193, v187\n" \
"v_mul_f32 v194, v194, v187\n" \
"v_mul_f32 v195, v195, v187\n" \
"v_accvgpr_write_b32 a96, v192\n" \
"v_accvgpr_write_b32 a97, v193\n" \
"v_accvgpr_write_b32 a98, v194\n" \
"v_accvgpr_write_b32 a99, v195\n" \
"v_accvgpr_read_b32 v192, a104\n" \
"v_accvgpr_read_b32 v193, a105\n" \
"v_accvgpr_read_b32 v194, a106\n" \
"v_accvgpr_read_b32 v195, a107\n" \
"v_mul_f32 v192, v192, v187\n" \
"v_mul_f32 v193, v193, v187\n" \
"v_mul_f32 v194, v194, v187\n" \
"v_mul_f32 v195, v195, v187\n" \
"v_accvgpr_write_b32 a104, v192\n" \
"v_accvgpr_write_b32 a105, v193\n" \
"v_accvgpr_write_b32 a106, v194\n" \
"v_accvgpr_write_b32 a107, v195\n" \
"v_accvgpr_read_b32 v192, a112\n" \
"v_accvgpr_read_b32 v193, a113\n" \
"v_accvgpr_read_b32 v194, a114\n" \
"v_accvgpr_read_b32 v195, a115\n" \
"v_mul_f32 v192, v192, v187\n" \
"v_mul_f32 v193, v193, v187\n" \
"v_mul_f32 v194, v194, v187\n" \
"v_mul_f32 v195, v195, v187\n" \
"v_accvgpr_write_b32 a112, v192\n" \
"v_accvgpr_write_b32 a113, v193\n" \
"v_accvgpr_write_b32 a114, v194\n" \
"v_accvgpr_write_b32 a115, v195\n" \
"v_accvgpr_read_b32 v192, a120\n" \
"v_accvgpr_read_b32 v193, a121\n" \
"v_accvgpr_read_b32 v194, a122\n" \
"v_accvgpr_read_b32 v195, a123\n" \
"v_mul_f32 v192, v192, v187\n" \
"v_mul_f32 v193, v193, v187\n" \
"v_mul_f32 v194, v194, v187\n" \
"v_mul_f32 v195, v195, v187\n" \
"v_accvgpr_write_b32 a120, v192\n" \
"v_accvgpr_write_b32 a121, v193\n" \
"v_accvgpr_write_b32 a122, v194\n" \
"v_accvgpr_write_b32 a123, v195\n" \
"v_max3_f32 v186, v36, v37, v38\n" \
"v_max3_f32 v184, v39, v44, v45\n" \
"v_max3_f32 v186, v186, v46, v47\n" \
"v_max3_f32 v184, v184, v52, v53\n" \
"v_max3_f32 v186, v186, v54, v55\n" \
"v_max3_f32 v184, v184, v60, v61\n" \
"v_max3_f32 v186, v186, v62, v63\n" \
"v_max_f32 v186, v186, v184\n" \
"v_mov_b32 v184, v186\n" \
"s_nop 1\n" \
"v_permlane16_swap_b32 v184, v186\n" \
"v_max_f32 v186, v186, v184\n" \
"v_mov_b32 v184, v186\n" \
"s_nop 1\n" \
"v_permlane32_swap_b32 v184, v186\n" \
"v_max_f32 v186, v186, v184\n" \
"v_max_f32 v186, 0, v186\n" \
"v_add_f32 v173, v173, v186\n" \
"v_exp_f32 v187, -v186\n" \
"v_sub_f32 v164, 0, v173\n" \
"v_sub_f32 v165, 0, v173\n" \
"v_sub_f32 v166, 0, v173\n" \
"v_sub_f32 v167, 0, v173\n" \
"v_mul_f32 v171, v171, v187\n" \
"v_mov_b32 v169, 0\n" \
"v_sub_f32 v188, v36, v186\n" \
"v_sub_f32 v189, v37, v186\n" \
"v_sub_f32 v190, v38, v186\n" \
"v_sub_f32 v191, v39, v186\n" \
"v_exp_f32 v188, v188\n" \
"v_exp_f32 v189, v189\n" \
"v_exp_f32 v190, v190\n" \
"v_exp_f32 v191, v191\n" \
"v_sub_f32 v4, v4, v186\n" \
"v_sub_f32 v5, v5, v186\n" \
"v_sub_f32 v6, v6, v186\n" \
"v_sub_f32 v7, v7, v186\n" \
"v_add_f32 v169, v169, v188\n" \
"v_add_f32 v169, v169, v189\n" \
"v_add_f32 v169, v169, v190\n" \
"v_add_f32 v169, v169, v191\n" \
"v_cvt_pk_bf16_f32 v88, v188, v189\n" \
"v_cvt_pk_bf16_f32 v89, v190, v191\n" \
"v_sub_f32 v188, v44, v186\n" \
"v_sub_f32 v189, v45, v186\n" \
"v_sub_f32 v190, v46, v186\n" \
"v_sub_f32 v191, v47, v186\n" \
"v_exp_f32 v188, v188\n" \
"v_exp_f32 v189, v189\n" \
"v_exp_f32 v190, v190\n" \
"v_exp_f32 v191, v191\n" \
"v_sub_f32 v12, v12, v186\n" \
"v_sub_f32 v13, v13, v186\n" \
"v_sub_f32 v14, v14, v186\n" \
"v_sub_f32 v15, v15, v186\n" \
"v_add_f32 v169, v169, v188\n" \
"v_add_f32 v169, v169, v189\n" \
"v_add_f32 v169, v169, v190\n" \
"v_add_f32 v169, v169, v191\n" \
"v_cvt_pk_bf16_f32 v90, v188, v189\n" \
"v_cvt_pk_bf16_f32 v91, v190, v191\n" \
"v_sub_f32 v188, v52, v186\n" \
"v_sub_f32 v189, v53, v186\n" \
"v_sub_f32 v190, v54, v186\n" \
"v_sub_f32 v191, v55, v186\n" \
"v_exp_f32 v188, v188\n" \
"v_exp_f32 v189, v189\n" \
"v_exp_f32 v190, v190\n" \
"v_exp_f32 v191, v191\n" \
"v_sub_f32 v20, v20, v186\n" \
"v_sub_f32 v21, v21, v186\n" \
"v_sub_f32 v22, v22, v186\n" \
"v_sub_f32 v23, v23, v186\n" \
"v_add_f32 v169, v169, v188\n" \
"v_add_f32 v169, v169, v189\n" \
"v_add_f32 v169, v169, v190\n" \
"v_add_f32 v169, v169, v191\n" \
"v_cvt_pk_bf16_f32 v92, v188, v189\n" \
"v_cvt_pk_bf16_f32 v93, v190, v191\n" \
"v_sub_f32 v188, v60, v186\n" \
"v_sub_f32 v189, v61, v186\n" \
"v_sub_f32 v190, v62, v186\n" \
"v_sub_f32 v191, v63, v186\n" \
"v_exp_f32 v188, v188\n" \
"v_exp_f32 v189, v189\n" \
"v_exp_f32 v190, v190\n" \
"v_exp_f32 v191, v191\n" \
"v_sub_f32 v28, v28, v186\n" \
"v_sub_f32 v29, v29, v186\n" \
"v_sub_f32 v30, v30, v186\n" \
"v_sub_f32 v31, v31, v186\n" \
"v_add_f32 v169, v169, v188\n" \
"v_add_f32 v169, v169, v189\n" \
"v_add_f32 v169, v169, v190\n" \
"v_add_f32 v169, v169, v191\n" \
"v_cvt_pk_bf16_f32 v94, v188, v189\n" \
"v_cvt_pk_bf16_f32 v95, v190, v191\n" \
"v_accvgpr_read_b32 v192, a4\n" \
"v_accvgpr_read_b32 v193, a5\n" \
"v_accvgpr_read_b32 v194, a6\n" \
"v_accvgpr_read_b32 v195, a7\n" \
"v_mul_f32 v192, v192, v187\n" \
"v_mul_f32 v193, v193, v187\n" \
"v_mul_f32 v194, v194, v187\n" \
"v_mul_f32 v195, v195, v187\n" \
"v_accvgpr_write_b32 a4, v192\n" \
"v_accvgpr_write_b32 a5, v193\n" \
"v_accvgpr_write_b32 a6, v194\n" \
"v_accvgpr_write_b32 a7, v195\n" \
"v_accvgpr_read_b32 v192, a12\n" \
"v_accvgpr_read_b32 v193, a13\n" \
"v_accvgpr_read_b32 v194, a14\n" \
"v_accvgpr_read_b32 v195, a15\n" \
"v_mul_f32 v192, v192, v187\n" \
"v_mul_f32 v193, v193, v187\n" \
"v_mul_f32 v194, v194, v187\n" \
"v_mul_f32 v195, v195, v187\n" \
"v_accvgpr_write_b32 a12, v192\n" \
"v_accvgpr_write_b32 a13, v193\n" \
"v_accvgpr_write_b32 a14, v194\n" \
"v_accvgpr_write_b32 a15, v195\n" \
"v_accvgpr_read_b32 v192, a20\n" \
"v_accvgpr_read_b32 v193, a21\n" \
"v_accvgpr_read_b32 v194, a22\n" \
"v_accvgpr_read_b32 v195, a23\n" \
"v_mul_f32 v192, v192, v187\n" \
"v_mul_f32 v193, v193, v187\n" \
"v_mul_f32 v194, v194, v187\n" \
"v_mul_f32 v195, v195, v187\n" \
"v_accvgpr_write_b32 a20, v192\n" \
"v_accvgpr_write_b32 a21, v193\n" \
"v_accvgpr_write_b32 a22, v194\n" \
"v_accvgpr_write_b32 a23, v195\n" \
"v_accvgpr_read_b32 v192, a28\n" \
"v_accvgpr_read_b32 v193, a29\n" \
"v_accvgpr_read_b32 v194, a30\n" \
"v_accvgpr_read_b32 v195, a31\n" \
"v_mul_f32 v192, v192, v187\n" \
"v_mul_f32 v193, v193, v187\n" \
"v_mul_f32 v194, v194, v187\n" \
"v_mul_f32 v195, v195, v187\n" \
"v_accvgpr_write_b32 a28, v192\n" \
"v_accvgpr_write_b32 a29, v193\n" \
"v_accvgpr_write_b32 a30, v194\n" \
"v_accvgpr_write_b32 a31, v195\n" \
"v_accvgpr_read_b32 v192, a36\n" \
"v_accvgpr_read_b32 v193, a37\n" \
"v_accvgpr_read_b32 v194, a38\n" \
"v_accvgpr_read_b32 v195, a39\n" \
"v_mul_f32 v192, v192, v187\n" \
"v_mul_f32 v193, v193, v187\n" \
"v_mul_f32 v194, v194, v187\n" \
"v_mul_f32 v195, v195, v187\n" \
"v_accvgpr_write_b32 a36, v192\n" \
"v_accvgpr_write_b32 a37, v193\n" \
"v_accvgpr_write_b32 a38, v194\n" \
"v_accvgpr_write_b32 a39, v195\n" \
"v_accvgpr_read_b32 v192, a44\n" \
"v_accvgpr_read_b32 v193, a45\n" \
"v_accvgpr_read_b32 v194, a46\n" \
"v_accvgpr_read_b32 v195, a47\n" \
"v_mul_f32 v192, v192, v187\n" \
"v_mul_f32 v193, v193, v187\n" \
"v_mul_f32 v194, v194, v187\n" \
"v_mul_f32 v195, v195, v187\n" \
"v_accvgpr_write_b32 a44, v192\n" \
"v_accvgpr_write_b32 a45, v193\n" \
"v_accvgpr_write_b32 a46, v194\n" \
"v_accvgpr_write_b32 a47, v195\n" \
"v_accvgpr_read_b32 v192, a52\n" \
"v_accvgpr_read_b32 v193, a53\n" \
"v_accvgpr_read_b32 v194, a54\n" \
"v_accvgpr_read_b32 v195, a55\n" \
"v_mul_f32 v192, v192, v187\n" \
"v_mul_f32 v193, v193, v187\n" \
"v_mul_f32 v194, v194, v187\n" \
"v_mul_f32 v195, v195, v187\n" \
"v_accvgpr_write_b32 a52, v192\n" \
"v_accvgpr_write_b32 a53, v193\n" \
"v_accvgpr_write_b32 a54, v194\n" \
"v_accvgpr_write_b32 a55, v195\n" \
"v_accvgpr_read_b32 v192, a60\n" \
"v_accvgpr_read_b32 v193, a61\n" \
"v_accvgpr_read_b32 v194, a62\n" \
"v_accvgpr_read_b32 v195, a63\n" \
"v_mul_f32 v192, v192, v187\n" \
"v_mul_f32 v193, v193, v187\n" \
"v_mul_f32 v194, v194, v187\n" \
"v_mul_f32 v195, v195, v187\n" \
"v_accvgpr_write_b32 a60, v192\n" \
"v_accvgpr_write_b32 a61, v193\n" \
"v_accvgpr_write_b32 a62, v194\n" \
"v_accvgpr_write_b32 a63, v195\n" \
"v_accvgpr_read_b32 v192, a68\n" \
"v_accvgpr_read_b32 v193, a69\n" \
"v_accvgpr_read_b32 v194, a70\n" \
"v_accvgpr_read_b32 v195, a71\n" \
"v_mul_f32 v192, v192, v187\n" \
"v_mul_f32 v193, v193, v187\n" \
"v_mul_f32 v194, v194, v187\n" \
"v_mul_f32 v195, v195, v187\n" \
"v_accvgpr_write_b32 a68, v192\n" \
"v_accvgpr_write_b32 a69, v193\n" \
"v_accvgpr_write_b32 a70, v194\n" \
"v_accvgpr_write_b32 a71, v195\n" \
"v_accvgpr_read_b32 v192, a76\n" \
"v_accvgpr_read_b32 v193, a77\n" \
"v_accvgpr_read_b32 v194, a78\n" \
"v_accvgpr_read_b32 v195, a79\n" \
"v_mul_f32 v192, v192, v187\n" \
"v_mul_f32 v193, v193, v187\n" \
"v_mul_f32 v194, v194, v187\n" \
"v_mul_f32 v195, v195, v187\n" \
"v_accvgpr_write_b32 a76, v192\n" \
"v_accvgpr_write_b32 a77, v193\n" \
"v_accvgpr_write_b32 a78, v194\n" \
"v_accvgpr_write_b32 a79, v195\n" \
"v_accvgpr_read_b32 v192, a84\n" \
"v_accvgpr_read_b32 v193, a85\n" \
"v_accvgpr_read_b32 v194, a86\n" \
"v_accvgpr_read_b32 v195, a87\n" \
"v_mul_f32 v192, v192, v187\n" \
"v_mul_f32 v193, v193, v187\n" \
"v_mul_f32 v194, v194, v187\n" \
"v_mul_f32 v195, v195, v187\n" \
"v_accvgpr_write_b32 a84, v192\n" \
"v_accvgpr_write_b32 a85, v193\n" \
"v_accvgpr_write_b32 a86, v194\n" \
"v_accvgpr_write_b32 a87, v195\n" \
"v_accvgpr_read_b32 v192, a92\n" \
"v_accvgpr_read_b32 v193, a93\n" \
"v_accvgpr_read_b32 v194, a94\n" \
"v_accvgpr_read_b32 v195, a95\n" \
"v_mul_f32 v192, v192, v187\n" \
"v_mul_f32 v193, v193, v187\n" \
"v_mul_f32 v194, v194, v187\n" \
"v_mul_f32 v195, v195, v187\n" \
"v_accvgpr_write_b32 a92, v192\n" \
"v_accvgpr_write_b32 a93, v193\n" \
"v_accvgpr_write_b32 a94, v194\n" \
"v_accvgpr_write_b32 a95, v195\n" \
"v_accvgpr_read_b32 v192, a100\n" \
"v_accvgpr_read_b32 v193, a101\n" \
"v_accvgpr_read_b32 v194, a102\n" \
"v_accvgpr_read_b32 v195, a103\n" \
"v_mul_f32 v192, v192, v187\n" \
"v_mul_f32 v193, v193, v187\n" \
"v_mul_f32 v194, v194, v187\n" \
"v_mul_f32 v195, v195, v187\n" \
"v_accvgpr_write_b32 a100, v192\n" \
"v_accvgpr_write_b32 a101, v193\n" \
"v_accvgpr_write_b32 a102, v194\n" \
"v_accvgpr_write_b32 a103, v195\n" \
"v_accvgpr_read_b32 v192, a108\n" \
"v_accvgpr_read_b32 v193, a109\n" \
"v_accvgpr_read_b32 v194, a110\n" \
"v_accvgpr_read_b32 v195, a111\n" \
"v_mul_f32 v192, v192, v187\n" \
"v_mul_f32 v193, v193, v187\n" \
"v_mul_f32 v194, v194, v187\n" \
"v_mul_f32 v195, v195, v187\n" \
"v_accvgpr_write_b32 a108, v192\n" \
"v_accvgpr_write_b32 a109, v193\n" \
"v_accvgpr_write_b32 a110, v194\n" \
"v_accvgpr_write_b32 a111, v195\n" \
"v_accvgpr_read_b32 v192, a116\n" \
"v_accvgpr_read_b32 v193, a117\n" \
"v_accvgpr_read_b32 v194, a118\n" \
"v_accvgpr_read_b32 v195, a119\n" \
"v_mul_f32 v192, v192, v187\n" \
"v_mul_f32 v193, v193, v187\n" \
"v_mul_f32 v194, v194, v187\n" \
"v_mul_f32 v195, v195, v187\n" \
"v_accvgpr_write_b32 a116, v192\n" \
"v_accvgpr_write_b32 a117, v193\n" \
"v_accvgpr_write_b32 a118, v194\n" \
"v_accvgpr_write_b32 a119, v195\n" \
"v_accvgpr_read_b32 v192, a124\n" \
"v_accvgpr_read_b32 v193, a125\n" \
"v_accvgpr_read_b32 v194, a126\n" \
"v_accvgpr_read_b32 v195, a127\n" \
"v_mul_f32 v192, v192, v187\n" \
"v_mul_f32 v193, v193, v187\n" \
"v_mul_f32 v194, v194, v187\n" \
"v_mul_f32 v195, v195, v187\n" \
"v_accvgpr_write_b32 a124, v192\n" \
"v_accvgpr_write_b32 a125, v193\n" \
"v_accvgpr_write_b32 a126, v194\n" \
"v_accvgpr_write_b32 a127, v195\n" \
"s_nop 1\n" \
"s_setpc_b64 s[92:93]\n" \
"Lspec0%=:\n" \
"s_cmp_eq_u32 s49, 0\n" \
"s_cbranch_scc1 Lnsg0%=\n" \
"s_mov_b32 s49, 0\n" \
"s_cmp_lg_u32 s63, 0\n" \
"s_cbranch_scc1 Lsy0%=\n" \
"buffer_load_dword v181, off, %[rs], s62 sc1\n" \
"s_branch Lnsg0%=\n" \
"Lsy0%=:\n" \
"s_cmp_lg_u32 s36, 0\n" \
"s_cbranch_scc1 Lsp0%=\n" \
"s_mov_b64 s[96:97], exec\n" \
"s_mov_b64 exec, 1\n" \
"v_mov_b32 v184, %[tag]\n" \
"buffer_store_dword v184, off, %[rs], s62 sc1\n" \
"s_mov_b64 exec, s[96:97]\n" \
"Lsp0%=:\n" \
"Lnsg0%=:\n" \
"s_cmp_lt_i32 s47, 0\n" \
"s_cbranch_scc1 Lmr0%=\n" \
"s_nop 7\n" \
"v_mov_b32 v184, 0xff800000\n" \
"s_cmp_eq_i32 s47, 0\n" \
"s_cbranch_scc1 Lm00%=\n" \
"v_mov_b32 v0, v184\n" \
"v_mov_b32 v1, v184\n" \
"v_mov_b32 v2, v184\n" \
"v_mov_b32 v3, v184\n" \
"v_mov_b32 v8, v184\n" \
"v_mov_b32 v9, v184\n" \
"v_mov_b32 v10, v184\n" \
"v_mov_b32 v11, v184\n" \
"v_mov_b32 v16, v184\n" \
"v_mov_b32 v17, v184\n" \
"v_mov_b32 v18, v184\n" \
"v_mov_b32 v19, v184\n" \
"v_mov_b32 v24, v184\n" \
"v_mov_b32 v25, v184\n" \
"v_mov_b32 v26, v184\n" \
"v_mov_b32 v27, v184\n" \
"s_cmp_eq_u32 s36, 0\n" \
"s_cbranch_scc1 Lmw010%=\n" \
"s_cmp_eq_u32 s36, 1\n" \
"s_cbranch_scc1 Lmw011%=\n" \
"s_cmp_eq_u32 s36, 2\n" \
"s_cbranch_scc1 Lmw012%=\n" \
"v_cndmask_b32 v4, v4, v184, s[84:85]\n" \
"v_cndmask_b32 v5, v5, v184, s[86:87]\n" \
"v_cndmask_b32 v6, v6, v184, s[88:89]\n" \
"v_cndmask_b32 v7, v7, v184, s[90:91]\n" \
"v_mov_b32 v12, v184\n" \
"v_mov_b32 v13, v184\n" \
"v_mov_b32 v14, v184\n" \
"v_mov_b32 v15, v184\n" \
"v_mov_b32 v20, v184\n" \
"v_mov_b32 v21, v184\n" \
"v_mov_b32 v22, v184\n" \
"v_mov_b32 v23, v184\n" \
"v_mov_b32 v28, v184\n" \
"v_mov_b32 v29, v184\n" \
"v_mov_b32 v30, v184\n" \
"v_mov_b32 v31, v184\n" \
"s_setpc_b64 s[92:93]\n" \
"Lmw010%=:\n" \
"v_cndmask_b32 v28, v28, v184, s[84:85]\n" \
"v_cndmask_b32 v29, v29, v184, s[86:87]\n" \
"v_cndmask_b32 v30, v30, v184, s[88:89]\n" \
"v_cndmask_b32 v31, v31, v184, s[90:91]\n" \
"s_setpc_b64 s[92:93]\n" \
"Lmw011%=:\n" \
"v_cndmask_b32 v20, v20, v184, s[84:85]\n" \
"v_cndmask_b32 v21, v21, v184, s[86:87]\n" \
"v_cndmask_b32 v22, v22, v184, s[88:89]\n" \
"v_cndmask_b32 v23, v23, v184, s[90:91]\n" \
"v_mov_b32 v28, v184\n" \
"v_mov_b32 v29, v184\n" \
"v_mov_b32 v30, v184\n" \
"v_mov_b32 v31, v184\n" \
"s_setpc_b64 s[92:93]\n" \
"Lmw012%=:\n" \
"v_cndmask_b32 v12, v12, v184, s[84:85]\n" \
"v_cndmask_b32 v13, v13, v184, s[86:87]\n" \
"v_cndmask_b32 v14, v14, v184, s[88:89]\n" \
"v_cndmask_b32 v15, v15, v184, s[90:91]\n" \
"v_mov_b32 v20, v184\n" \
"v_mov_b32 v21, v184\n" \
"v_mov_b32 v22, v184\n" \
"v_mov_b32 v23, v184\n" \
"v_mov_b32 v28, v184\n" \
"v_mov_b32 v29, v184\n" \
"v_mov_b32 v30, v184\n" \
"v_mov_b32 v31, v184\n" \
"s_setpc_b64 s[92:93]\n" \
"Lm00%=:\n" \
"s_cmp_eq_u32 s36, 0\n" \
"s_cbranch_scc1 Lmw000%=\n" \
"s_cmp_eq_u32 s36, 1\n" \
"s_cbranch_scc1 Lmw001%=\n" \
"s_cmp_eq_u32 s36, 2\n" \
"s_cbranch_scc1 Lmw002%=\n" \
"v_cndmask_b32 v24, v24, v184, s[84:85]\n" \
"v_cndmask_b32 v25, v25, v184, s[86:87]\n" \
"v_cndmask_b32 v26, v26, v184, s[88:89]\n" \
"v_cndmask_b32 v27, v27, v184, s[90:91]\n" \
"s_setpc_b64 s[92:93]\n" \
"Lmw000%=:\n" \
"v_cndmask_b32 v0, v0, v184, s[84:85]\n" \
"v_cndmask_b32 v1, v1, v184, s[86:87]\n" \
"v_cndmask_b32 v2, v2, v184, s[88:89]\n" \
"v_cndmask_b32 v3, v3, v184, s[90:91]\n" \
"v_mov_b32 v8, v184\n" \
"v_mov_b32 v9, v184\n" \
"v_mov_b32 v10, v184\n" \
"v_mov_b32 v11, v184\n" \
"v_mov_b32 v16, v184\n" \
"v_mov_b32 v17, v184\n" \
"v_mov_b32 v18, v184\n" \
"v_mov_b32 v19, v184\n" \
"v_mov_b32 v24, v184\n" \
"v_mov_b32 v25, v184\n" \
"v_mov_b32 v26, v184\n" \
"v_mov_b32 v27, v184\n" \
"s_setpc_b64 s[92:93]\n" \
"Lmw001%=:\n" \
"v_cndmask_b32 v8, v8, v184, s[84:85]\n" \
"v_cndmask_b32 v9, v9, v184, s[86:87]\n" \
"v_cndmask_b32 v10, v10, v184, s[88:89]\n" \
"v_cndmask_b32 v11, v11, v184, s[90:91]\n" \
"v_mov_b32 v16, v184\n" \
"v_mov_b32 v17, v184\n" \
"v_mov_b32 v18, v184\n" \
"v_mov_b32 v19, v184\n" \
"v_mov_b32 v24, v184\n" \
"v_mov_b32 v25, v184\n" \
"v_mov_b32 v26, v184\n" \
"v_mov_b32 v27, v184\n" \
"s_setpc_b64 s[92:93]\n" \
"Lmw002%=:\n" \
"v_cndmask_b32 v16, v16, v184, s[84:85]\n" \
"v_cndmask_b32 v17, v17, v184, s[86:87]\n" \
"v_cndmask_b32 v18, v18, v184, s[88:89]\n" \
"v_cndmask_b32 v19, v19, v184, s[90:91]\n" \
"v_mov_b32 v24, v184\n" \
"v_mov_b32 v25, v184\n" \
"v_mov_b32 v26, v184\n" \
"v_mov_b32 v27, v184\n" \
"s_setpc_b64 s[92:93]\n" \
"Lmr0%=:\n" \
"s_setpc_b64 s[92:93]\n" \
"Lspec1%=:\n" \
"s_cmp_eq_u32 s49, 0\n" \
"s_cbranch_scc1 Lnsg1%=\n" \
"s_mov_b32 s49, 0\n" \
"s_cmp_lg_u32 s63, 0\n" \
"s_cbranch_scc1 Lsy1%=\n" \
"buffer_load_dword v181, off, %[rs], s62 sc1\n" \
"s_branch Lnsg1%=\n" \
"Lsy1%=:\n" \
"s_cmp_lg_u32 s36, 0\n" \
"s_cbranch_scc1 Lsp1%=\n" \
"s_mov_b64 s[96:97], exec\n" \
"s_mov_b64 exec, 1\n" \
"v_mov_b32 v184, %[tag]\n" \
"buffer_store_dword v184, off, %[rs], s62 sc1\n" \
"s_mov_b64 exec, s[96:97]\n" \
"Lsp1%=:\n" \
"Lnsg1%=:\n" \
"s_cmp_lt_i32 s47, 0\n" \
"s_cbranch_scc1 Lmr1%=\n" \
"s_nop 7\n" \
"v_mov_b32 v184, 0xff800000\n" \
"s_cmp_eq_i32 s47, 0\n" \
"s_cbranch_scc1 Lm01%=\n" \
"v_mov_b32 v32, v184\n" \
"v_mov_b32 v33, v184\n" \
"v_mov_b32 v34, v184\n" \
"v_mov_b32 v35, v184\n" \
"v_mov_b32 v40, v184\n" \
"v_mov_b32 v41, v184\n" \
"v_mov_b32 v42, v184\n" \
"v_mov_b32 v43, v184\n" \
"v_mov_b32 v48, v184\n" \
"v_mov_b32 v49, v184\n" \
"v_mov_b32 v50, v184\n" \
"v_mov_b32 v51, v184\n" \
"v_mov_b32 v56, v184\n" \
"v_mov_b32 v57, v184\n" \
"v_mov_b32 v58, v184\n" \
"v_mov_b32 v59, v184\n" \
"s_cmp_eq_u32 s36, 0\n" \
"s_cbranch_scc1 Lmw110%=\n" \
"s_cmp_eq_u32 s36, 1\n" \
"s_cbranch_scc1 Lmw111%=\n" \
"s_cmp_eq_u32 s36, 2\n" \
"s_cbranch_scc1 Lmw112%=\n" \
"v_cndmask_b32 v36, v36, v184, s[84:85]\n" \
"v_cndmask_b32 v37, v37, v184, s[86:87]\n" \
"v_cndmask_b32 v38, v38, v184, s[88:89]\n" \
"v_cndmask_b32 v39, v39, v184, s[90:91]\n" \
"v_mov_b32 v44, v184\n" \
"v_mov_b32 v45, v184\n" \
"v_mov_b32 v46, v184\n" \
"v_mov_b32 v47, v184\n" \
"v_mov_b32 v52, v184\n" \
"v_mov_b32 v53, v184\n" \
"v_mov_b32 v54, v184\n" \
"v_mov_b32 v55, v184\n" \
"v_mov_b32 v60, v184\n" \
"v_mov_b32 v61, v184\n" \
"v_mov_b32 v62, v184\n" \
"v_mov_b32 v63, v184\n" \
"s_setpc_b64 s[92:93]\n" \
"Lmw110%=:\n" \
"v_cndmask_b32 v60, v60, v184, s[84:85]\n" \
"v_cndmask_b32 v61, v61, v184, s[86:87]\n" \
"v_cndmask_b32 v62, v62, v184, s[88:89]\n" \
"v_cndmask_b32 v63, v63, v184, s[90:91]\n" \
"s_setpc_b64 s[92:93]\n" \
"Lmw111%=:\n" \
"v_cndmask_b32 v52, v52, v184, s[84:85]\n" \
"v_cndmask_b32 v53, v53, v184, s[86:87]\n" \
"v_cndmask_b32 v54, v54, v184, s[88:89]\n" \
"v_cndmask_b32 v55, v55, v184, s[90:91]\n" \
"v_mov_b32 v60, v184\n" \
"v_mov_b32 v61, v184\n" \
"v_mov_b32 v62, v184\n" \
"v_mov_b32 v63, v184\n" \
"s_setpc_b64 s[92:93]\n" \
"Lmw112%=:\n" \
"v_cndmask_b32 v44, v44, v184, s[84:85]\n" \
"v_cndmask_b32 v45, v45, v184, s[86:87]\n" \
"v_cndmask_b32 v46, v46, v184, s[88:89]\n" \
"v_cndmask_b32 v47, v47, v184, s[90:91]\n" \
"v_mov_b32 v52, v184\n" \
"v_mov_b32 v53, v184\n" \
"v_mov_b32 v54, v184\n" \
"v_mov_b32 v55, v184\n" \
"v_mov_b32 v60, v184\n" \
"v_mov_b32 v61, v184\n" \
"v_mov_b32 v62, v184\n" \
"v_mov_b32 v63, v184\n" \
"s_setpc_b64 s[92:93]\n" \
"Lm01%=:\n" \
"s_cmp_eq_u32 s36, 0\n" \
"s_cbranch_scc1 Lmw100%=\n" \
"s_cmp_eq_u32 s36, 1\n" \
"s_cbranch_scc1 Lmw101%=\n" \
"s_cmp_eq_u32 s36, 2\n" \
"s_cbranch_scc1 Lmw102%=\n" \
"v_cndmask_b32 v56, v56, v184, s[84:85]\n" \
"v_cndmask_b32 v57, v57, v184, s[86:87]\n" \
"v_cndmask_b32 v58, v58, v184, s[88:89]\n" \
"v_cndmask_b32 v59, v59, v184, s[90:91]\n" \
"s_setpc_b64 s[92:93]\n" \
"Lmw100%=:\n" \
"v_cndmask_b32 v32, v32, v184, s[84:85]\n" \
"v_cndmask_b32 v33, v33, v184, s[86:87]\n" \
"v_cndmask_b32 v34, v34, v184, s[88:89]\n" \
"v_cndmask_b32 v35, v35, v184, s[90:91]\n" \
"v_mov_b32 v40, v184\n" \
"v_mov_b32 v41, v184\n" \
"v_mov_b32 v42, v184\n" \
"v_mov_b32 v43, v184\n" \
"v_mov_b32 v48, v184\n" \
"v_mov_b32 v49, v184\n" \
"v_mov_b32 v50, v184\n" \
"v_mov_b32 v51, v184\n" \
"v_mov_b32 v56, v184\n" \
"v_mov_b32 v57, v184\n" \
"v_mov_b32 v58, v184\n" \
"v_mov_b32 v59, v184\n" \
"s_setpc_b64 s[92:93]\n" \
"Lmw101%=:\n" \
"v_cndmask_b32 v40, v40, v184, s[84:85]\n" \
"v_cndmask_b32 v41, v41, v184, s[86:87]\n" \
"v_cndmask_b32 v42, v42, v184, s[88:89]\n" \
"v_cndmask_b32 v43, v43, v184, s[90:91]\n" \
"v_mov_b32 v48, v184\n" \
"v_mov_b32 v49, v184\n" \
"v_mov_b32 v50, v184\n" \
"v_mov_b32 v51, v184\n" \
"v_mov_b32 v56, v184\n" \
"v_mov_b32 v57, v184\n" \
"v_mov_b32 v58, v184\n" \
"v_mov_b32 v59, v184\n" \
"s_setpc_b64 s[92:93]\n" \
"Lmw102%=:\n" \
"v_cndmask_b32 v48, v48, v184, s[84:85]\n" \
"v_cndmask_b32 v49, v49, v184, s[86:87]\n" \
"v_cndmask_b32 v50, v50, v184, s[88:89]\n" \
"v_cndmask_b32 v51, v51, v184, s[90:91]\n" \
"v_mov_b32 v56, v184\n" \
"v_mov_b32 v57, v184\n" \
"v_mov_b32 v58, v184\n" \
"v_mov_b32 v59, v184\n" \
"s_setpc_b64 s[92:93]\n" \
"Lmr1%=:\n" \
"s_setpc_b64 s[92:93]\n" \
"Ldr0%=:\n" \
"s_nop 7\n" \
"v_exp_f32 v176, v18\n" \
"v_exp_f32 v177, v19\n" \
"s_nop 0\n" \
"v_cvt_pk_bf16_f32 v69, v176, v177\n" \
"v_add_f32 v168, v168, v176\n" \
"v_add_f32 v168, v168, v177\n" \
"v_exp_f32 v174, v20\n" \
"v_exp_f32 v175, v21\n" \
"s_nop 0\n" \
"v_cvt_pk_bf16_f32 v76, v174, v175\n" \
"v_add_f32 v169, v169, v174\n" \
"v_add_f32 v169, v169, v175\n" \
"v_exp_f32 v176, v22\n" \
"v_exp_f32 v177, v23\n" \
"s_nop 0\n" \
"v_cvt_pk_bf16_f32 v77, v176, v177\n" \
"v_add_f32 v169, v169, v176\n" \
"v_add_f32 v169, v169, v177\n" \
"v_exp_f32 v174, v24\n" \
"v_exp_f32 v175, v25\n" \
"s_nop 0\n" \
"v_cvt_pk_bf16_f32 v70, v174, v175\n" \
"v_add_f32 v168, v168, v174\n" \
"v_add_f32 v168, v168, v175\n" \
"v_exp_f32 v176, v26\n" \
"v_exp_f32 v177, v27\n" \
"s_nop 0\n" \
"v_cvt_pk_bf16_f32 v71, v176, v177\n" \
"v_add_f32 v168, v168, v176\n" \
"v_add_f32 v168, v168, v177\n" \
"v_exp_f32 v174, v28\n" \
"v_exp_f32 v175, v29\n" \
"s_nop 0\n" \
"v_cvt_pk_bf16_f32 v78, v174, v175\n" \
"v_add_f32 v169, v169, v174\n" \
"v_add_f32 v169, v169, v175\n" \
"v_exp_f32 v176, v30\n" \
"v_exp_f32 v177, v31\n" \
"s_nop 0\n" \
"v_cvt_pk_bf16_f32 v79, v176, v177\n" \
"v_add_f32 v169, v169, v176\n" \
"v_add_f32 v169, v169, v177\n" \
"v_add_f32 v180, v168, v169\n" \
"v_cmp_nge_f32 vcc, s51, v180\n" \
"s_waitcnt vmcnt(8)\n" \
"v_cmp_ne_u32 s[96:97], %[tag], v181\n" \
"s_nop 1\n" \
"s_cmp_eq_u64 s[96:97], 0\n" \
"s_cselect_b32 s53, s61, 0x80000000\n" \
"s_cbranch_vccz Lf0%=\n" \
"s_call_b64 s[92:93], Lrare0%=\n" \
"Lf0%=:\n" \
"s_cmp_eq_u32 s63, 0\n" \
"s_cbranch_scc1 Ldf0%=\n" \
"ds_read_b128 v[128:131], v178 offset:8192\n" \
"buffer_load_dwordx4 v224, %[rs], s43 offen offset:0 sc1 lds\n" \
"v_mfma_f32_16x16x32_bf16 a[4:7], v[96:99], v[72:75], a[4:7]\n" \
"v_add_f32 v170, v170, v168\n" \
"v_add_f32 v171, v171, v169\n" \
"ds_read_b128 v[132:135], v178 offset:9216\n" \
"v_readlane_b32 s48, v238, s42\n" \
"v_mfma_f32_16x16x32_bf16 a[12:15], v[100:103], v[72:75], a[12:15]\n" \
"ds_read_b128 v[136:139], v178 offset:10240\n" \
"buffer_load_dwordx4 v224, %[rs], s43 offen offset:1024 sc1 lds\n" \
"v_mfma_f32_16x16x32_bf16 a[20:23], v[104:107], v[72:75], a[20:23]\n" \
"ds_read_b128 v[140:143], v178 offset:11264\n" \
"s_waitcnt lgkmcnt(4)\n" \
"v_mfma_f32_16x16x32_bf16 a[28:31], v[108:111], v[72:75], a[28:31]\n" \
"ds_read_b128 v[144:147], v178 offset:12288\n" \
"s_add_u32 s42, s42, 1\n" \
"v_mfma_f32_16x16x32_bf16 a[36:39], v[112:115], v[72:75], a[36:39]\n" \
"ds_read_b128 v[148:151], v178 offset:13312\n" \
"buffer_load_dwordx4 v224, %[rs], s43 offen offset:2048 sc1 lds\n" \
"v_mfma_f32_16x16x32_bf16 a[44:47], v[116:119], v[72:75], a[44:47]\n" \
"ds_read_b128 v[152:155], v178 offset:14336\n" \
"v_add_u32 v179, s48, v224\n" \
"v_mfma_f32_16x16x32_bf16 a[52:55], v[120:123], v[72:75], a[52:55]\n" \
"ds_read_b128 v[156:159], v178 offset:15360\n" \
"s_waitcnt lgkmcnt(4)\n" \
"v_mfma_f32_16x16x32_bf16 a[60:63], v[124:127], v[72:75], a[60:63]\n" \
"ds_read_b128 v[96:99], v178 offset:16384\n" \
"buffer_load_dwordx4 v224, %[rs], s43 offen offset:3072 sc1 lds\n" \
"v_mfma_f32_16x16x32_bf16 a[68:71], v[128:131], v[72:75], a[68:71]\n" \
"ds_read_b128 v[100:103], v178 offset:17408\n" \
"s_add_u32 m0, m0, 0x1000\n" \
"s_add_u32 s43, s43, 0x1000\n" \
"v_mfma_f32_16x16x32_bf16 a[76:79], v[132:135], v[72:75], a[76:79]\n" \
"ds_read_b128 v[104:107], v178 offset:18432\n" \
"v_mfma_f32_16x16x32_bf16 a[84:87], v[136:139], v[72:75], a[84:87]\n" \
"ds_read_b128 v[108:111], v178 offset:19456\n" \
"s_waitcnt lgkmcnt(4)\n" \
"v_mfma_f32_16x16x32_bf16 a[92:95], v[140:143], v[72:75], a[92:95]\n" \
"ds_read_b128 v[112:115], v178 offset:20480\n" \
"buffer_load_dwordx4 v224, %[rs], s43 offen offset:0 sc1 lds\n" \
"v_mfma_f32_16x16x32_bf16 a[100:103], v[144:147], v[72:75], a[100:103]\n" \
"ds_read_b128 v[116:119], v178 offset:21504\n" \
"v_mfma_f32_16x16x32_bf16 a[108:111], v[148:151], v[72:75], a[108:111]\n" \
"ds_read_b128 v[120:123], v178 offset:22528\n" \
"buffer_load_dwordx4 v224, %[rs], s43 offen offset:1024 sc1 lds\n" \
"v_mfma_f32_16x16x32_bf16 a[116:119], v[152:155], v[72:75], a[116:119]\n" \
"ds_read_b128 v[124:127], v178 offset:23552\n" \
"s_waitcnt lgkmcnt(4)\n" \
"v_mfma_f32_16x16x32_bf16 a[124:127], v[156:159], v[72:75], a[124:127]\n" \
"ds_read_b128 v[128:131], v178 offset:24576\n" \
"v_mfma_f32_16x16x32_bf16 a[4:7], v[96:99], v[76:79], a[4:7]\n" \
"ds_read_b128 v[132:135], v178 offset:25600\n" \
"buffer_load_dwordx4 v224, %[rs], s43 offen offset:2048 sc1 lds\n" \
"v_mfma_f32_16x16x32_bf16 a[12:15], v[100:103], v[76:79], a[12:15]\n" \
"ds_read_b128 v[136:139], v178 offset:26624\n" \
"v_mfma_f32_16x16x32_bf16 a[20:23], v[104:107], v[76:79], a[20:23]\n" \
"ds_read_b128 v[140:143], v178 offset:27648\n" \
"s_waitcnt lgkmcnt(4)\n" \
"v_mfma_f32_16x16x32_bf16 a[28:31], v[108:111], v[76:79], a[28:31]\n" \
"ds_read_b128 v[144:147], v178 offset:28672\n" \
"buffer_load_dwordx4 v224, %[rs], s43 offen offset:3072 sc1 lds\n" \
"v_mfma_f32_16x16x32_bf16 a[36:39], v[112:115], v[76:79], a[36:39]\n" \
"ds_read_b128 v[148:151], v178 offset:29696\n" \
"v_readlane_b32 s43, v239, s42\n" \
"v_mfma_f32_16x16x32_bf16 a[44:47], v[116:119], v[76:79], a[44:47]\n" \
"ds_read_b128 v[152:155], v178 offset:30720\n" \
"s_add_u32 m0, s44, s37\n" \
"v_mfma_f32_16x16x32_bf16 a[52:55], v[120:123], v[76:79], a[52:55]\n" \
"ds_read_b128 v[156:159], v178 offset:31744\n" \
"s_waitcnt lgkmcnt(4)\n" \
"v_mfma_f32_16x16x32_bf16 a[60:63], v[124:127], v[76:79], a[60:63]\n" \
"s_waitcnt vmcnt(16)\n" \
"s_barrier\n" \
"ds_read_b128 v[96:99], v179 offset:0\n" \
"v_mfma_f32_16x16x32_bf16 a[68:71], v[128:131], v[76:79], a[68:71]\n" \
"ds_read_b128 v[100:103], v179 offset:1024\n" \
"s_mov_b32 s44, s41\n" \
"v_mfma_f32_16x16x32_bf16 a[76:79], v[132:135], v[76:79], a[76:79]\n" \
"ds_read_b128 v[104:107], v179 offset:2048\n" \
"s_mov_b32 s41, s48\n" \
"v_mfma_f32_16x16x32_bf16 a[84:87], v[136:139], v[76:79], a[84:87]\n" \
"ds_read_b128 v[108:111], v179 offset:3072\n" \
"s_waitcnt lgkmcnt(4)\n" \
"v_mfma_f32_16x16x32_bf16 a[92:95], v[140:143], v[76:79], a[92:95]\n" \
"ds_read_b128 v[112:115], v179 offset:4096\n" \
"v_mfma_f32_16x16x32_bf16 a[100:103], v[144:147], v[76:79], a[100:103]\n" \
"ds_read_b128 v[116:119], v179 offset:5120\n" \
"v_mfma_f32_16x16x32_bf16 a[108:111], v[148:151], v[76:79], a[108:111]\n" \
"ds_read_b128 v[120:123], v179 offset:6144\n" \
"v_mfma_f32_16x16x32_bf16 a[116:119], v[152:155], v[76:79], a[116:119]\n" \
"ds_read_b128 v[124:127], v179 offset:7168\n" \
"s_waitcnt lgkmcnt(4)\n" \
"v_mfma_f32_16x16x32_bf16 a[124:127], v[156:159], v[76:79], a[124:127]\n" \
"v_mov_b32 v178, v179\n" \
"s_branch Ldone%=\n" \
"Ldf0%=:\n" \
"v_mfma_f32_16x16x32_bf16 a[0:3], v[96:99], v[64:67], a[0:3]\n" \
"ds_read_b128 v[128:131], v178 offset:8192\n" \
"buffer_load_dwordx4 v224, %[rs], s43 offen offset:0 sc1 lds\n" \
"v_mfma_f32_16x16x32_bf16 a[4:7], v[96:99], v[72:75], a[4:7]\n" \
"v_add_f32 v170, v170, v168\n" \
"v_add_f32 v171, v171, v169\n" \
"v_mfma_f32_16x16x32_bf16 a[8:11], v[100:103], v[64:67], a[8:11]\n" \
"ds_read_b128 v[132:135], v178 offset:9216\n" \
"v_readlane_b32 s48, v238, s42\n" \
"v_mfma_f32_16x16x32_bf16 a[12:15], v[100:103], v[72:75], a[12:15]\n" \
"s_add_u32 s83, s53, 16384\n" \
"v_mfma_f32_16x16x32_bf16 a[16:19], v[104:107], v[64:67], a[16:19]\n" \
"ds_read_b128 v[136:139], v178 offset:10240\n" \
"buffer_load_dwordx4 v224, %[rs], s43 offen offset:1024 sc1 lds\n" \
"v_mfma_f32_16x16x32_bf16 a[20:23], v[104:107], v[72:75], a[20:23]\n" \
"v_mfma_f32_16x16x32_bf16 a[24:27], v[108:111], v[64:67], a[24:27]\n" \
"ds_read_b128 v[140:143], v178 offset:11264\n" \
"s_waitcnt lgkmcnt(4)\n" \
"v_mfma_f32_16x16x32_bf16 a[28:31], v[108:111], v[72:75], a[28:31]\n" \
"buffer_load_dwordx4 v[216:219], v224, %[rs], s83 offen sc1\n" \
"v_mfma_f32_16x16x32_bf16 a[32:35], v[112:115], v[64:67], a[32:35]\n" \
"ds_read_b128 v[144:147], v178 offset:12288\n" \
"s_add_u32 s42, s42, 1\n" \
"v_mfma_f32_16x16x32_bf16 a[36:39], v[112:115], v[72:75], a[36:39]\n" \
"v_mfma_f32_16x16x32_bf16 a[40:43], v[116:119], v[64:67], a[40:43]\n" \
"ds_read_b128 v[148:151], v178 offset:13312\n" \
"buffer_load_dwordx4 v224, %[rs], s43 offen offset:2048 sc1 lds\n" \
"v_mfma_f32_16x16x32_bf16 a[44:47], v[116:119], v[72:75], a[44:47]\n" \
"v_mfma_f32_16x16x32_bf16 a[48:51], v[120:123], v[64:67], a[48:51]\n" \
"ds_read_b128 v[152:155], v178 offset:14336\n" \
"v_add_u32 v179, s48, v224\n" \
"v_mfma_f32_16x16x32_bf16 a[52:55], v[120:123], v[72:75], a[52:55]\n" \
"buffer_load_dwordx4 v[0:3], v224, %[rs], s53 offen offset:0 sc1\n" \
"v_mfma_f32_16x16x32_bf16 a[56:59], v[124:127], v[64:67], a[56:59]\n" \
"ds_read_b128 v[156:159], v178 offset:15360\n" \
"s_waitcnt lgkmcnt(4)\n" \
"v_mfma_f32_16x16x32_bf16 a[60:63], v[124:127], v[72:75], a[60:63]\n" \
"v_mfma_f32_16x16x32_bf16 a[64:67], v[128:131], v[64:67], a[64:67]\n" \
"ds_read_b128 v[96:99], v178 offset:16384\n" \
"buffer_load_dwordx4 v224, %[rs], s43 offen offset:3072 sc1 lds\n" \
"v_mfma_f32_16x16x32_bf16 a[68:71], v[128:131], v[72:75], a[68:71]\n" \
"v_mfma_f32_16x16x32_bf16 a[72:75], v[132:135], v[64:67], a[72:75]\n" \
"ds_read_b128 v[100:103], v178 offset:17408\n" \
"s_add_u32 m0, m0, 0x1000\n" \
"s_add_u32 s43, s43, 0x1000\n" \
"v_mfma_f32_16x16x32_bf16 a[76:79], v[132:135], v[72:75], a[76:79]\n" \
"v_mfma_f32_16x16x32_bf16 a[80:83], v[136:139], v[64:67], a[80:83]\n" \
"ds_read_b128 v[104:107], v178 offset:18432\n" \
"v_mfma_f32_16x16x32_bf16 a[84:87], v[136:139], v[72:75], a[84:87]\n" \
"buffer_load_dwordx4 v[4:7], v224, %[rs], s53 offen offset:1024 sc1\n" \
"v_mfma_f32_16x16x32_bf16 a[88:91], v[140:143], v[64:67], a[88:91]\n" \
"ds_read_b128 v[108:111], v178 offset:19456\n" \
"s_waitcnt lgkmcnt(4)\n" \
"v_mfma_f32_16x16x32_bf16 a[92:95], v[140:143], v[72:75], a[92:95]\n" \
"v_mfma_f32_16x16x32_bf16 a[96:99], v[144:147], v[64:67], a[96:99]\n" \
"ds_read_b128 v[112:115], v178 offset:20480\n" \
"buffer_load_dwordx4 v224, %[rs], s43 offen offset:0 sc1 lds\n" \
"v_mfma_f32_16x16x32_bf16 a[100:103], v[144:147], v[72:75], a[100:103]\n" \
"v_mfma_f32_16x16x32_bf16 a[104:107], v[148:151], v[64:67], a[104:107]\n" \
"ds_read_b128 v[116:119], v178 offset:21504\n" \
"v_mfma_f32_16x16x32_bf16 a[108:111], v[148:151], v[72:75], a[108:111]\n" \
"v_mfma_f32_16x16x32_bf16 a[112:115], v[152:155], v[64:67], a[112:115]\n" \
"ds_read_b128 v[120:123], v178 offset:22528\n" \
"buffer_load_dwordx4 v224, %[rs], s43 offen offset:1024 sc1 lds\n" \
"v_mfma_f32_16x16x32_bf16 a[116:119], v[152:155], v[72:75], a[116:119]\n" \
"v_mfma_f32_16x16x32_bf16 a[120:123], v[156:159], v[64:67], a[120:123]\n" \
"ds_read_b128 v[124:127], v178 offset:23552\n" \
"s_waitcnt lgkmcnt(4)\n" \
"v_mfma_f32_16x16x32_bf16 a[124:127], v[156:159], v[72:75], a[124:127]\n" \
"buffer_load_dwordx4 v[8:11], v224, %[rs], s53 offen offset:2048 sc1\n" \
"v_mfma_f32_16x16x32_bf16 a[0:3], v[96:99], v[68:71], a[0:3]\n" \
"ds_read_b128 v[128:131], v178 offset:24576\n" \
"v_mfma_f32_16x16x32_bf16 a[4:7], v[96:99], v[76:79], a[4:7]\n" \
"v_mfma_f32_16x16x32_bf16 a[8:11], v[100:103], v[68:71], a[8:11]\n" \
"ds_read_b128 v[132:135], v178 offset:25600\n" \
"buffer_load_dwordx4 v224, %[rs], s43 offen offset:2048 sc1 lds\n" \
"v_mfma_f32_16x16x32_bf16 a[12:15], v[100:103], v[76:79], a[12:15]\n" \
"v_mfma_f32_16x16x32_bf16 a[16:19], v[104:107], v[68:71], a[16:19]\n" \
"ds_read_b128 v[136:139], v178 offset:26624\n" \
"v_mfma_f32_16x16x32_bf16 a[20:23], v[104:107], v[76:79], a[20:23]\n" \
"buffer_load_dwordx4 v[12:15], v224, %[rs], s53 offen offset:3072 sc1\n" \
"v_mfma_f32_16x16x32_bf16 a[24:27], v[108:111], v[68:71], a[24:27]\n" \
"ds_read_b128 v[140:143], v178 offset:27648\n" \
"s_waitcnt lgkmcnt(4)\n" \
"v_mfma_f32_16x16x32_bf16 a[28:31], v[108:111], v[76:79], a[28:31]\n" \
"v_mfma_f32_16x16x32_bf16 a[32:35], v[112:115], v[68:71], a[32:35]\n" \
"ds_read_b128 v[144:147], v178 offset:28672\n" \
"buffer_load_dwordx4 v224, %[rs], s43 offen offset:3072 sc1 lds\n" \
"v_mfma_f32_16x16x32_bf16 a[36:39], v[112:115], v[76:79], a[36:39]\n" \
"v_mfma_f32_16x16x32_bf16 a[40:43], v[116:119], v[68:71], a[40:43]\n" \
"ds_read_b128 v[148:151], v178 offset:29696\n" \
"v_readlane_b32 s43, v239, s42\n" \
"v_mfma_f32_16x16x32_bf16 a[44:47], v[116:119], v[76:79], a[44:47]\n" \
"s_add_u32 s83, s53, 4096\n" \
"v_mfma_f32_16x16x32_bf16 a[48:51], v[120:123], v[68:71], a[48:51]\n" \
"ds_read_b128 v[152:155], v178 offset:30720\n" \
"s_add_u32 m0, s44, s37\n" \
"v_mfma_f32_16x16x32_bf16 a[52:55], v[120:123], v[76:79], a[52:55]\n" \
"buffer_load_dwordx4 v[16:19], v224, %[rs], s83 offen offset:0 sc1\n" \
"v_mfma_f32_16x16x32_bf16 a[56:59], v[124:127], v[68:71], a[56:59]\n" \
"ds_read_b128 v[156:159], v178 offset:31744\n" \
"s_waitcnt lgkmcnt(4)\n" \
"v_mfma_f32_16x16x32_bf16 a[60:63], v[124:127], v[76:79], a[60:63]\n" \
"s_waitcnt vmcnt(22)\n" \
"s_barrier\n" \
"v_mfma_f32_16x16x32_bf16 a[64:67], v[128:131], v[68:71], a[64:67]\n" \
"ds_read_b128 v[96:99], v179 offset:0\n" \
"v_mfma_f32_16x16x32_bf16 a[68:71], v[128:131], v[76:79], a[68:71]\n" \
"v_mfma_f32_16x16x32_bf16 a[72:75], v[132:135], v[68:71], a[72:75]\n" \
"ds_read_b128 v[100:103], v179 offset:1024\n" \
"s_mov_b32 s44, s41\n" \
"v_mfma_f32_16x16x32_bf16 a[76:79], v[132:135], v[76:79], a[76:79]\n" \
"buffer_load_dwordx4 v[20:23], v224, %[rs], s83 offen offset:1024 sc1\n" \
"v_mfma_f32_16x16x32_bf16 a[80:83], v[136:139], v[68:71], a[80:83]\n" \
"ds_read_b128 v[104:107], v179 offset:2048\n" \
"s_mov_b32 s41, s48\n" \
"v_mfma_f32_16x16x32_bf16 a[84:87], v[136:139], v[76:79], a[84:87]\n" \
"v_mfma_f32_16x16x32_bf16 a[88:91], v[140:143], v[68:71], a[88:91]\n" \
"ds_read_b128 v[108:111], v179 offset:3072\n" \
"s_waitcnt lgkmcnt(4)\n" \
"v_mfma_f32_16x16x32_bf16 a[92:95], v[140:143], v[76:79], a[92:95]\n" \
"v_mfma_f32_16x16x32_bf16 a[96:99], v[144:147], v[68:71], a[96:99]\n" \
"ds_read_b128 v[112:115], v179 offset:4096\n" \
"v_mfma_f32_16x16x32_bf16 a[100:103], v[144:147], v[76:79], a[100:103]\n" \
"buffer_load_dwordx4 v[24:27], v224, %[rs], s83 offen offset:2048 sc1\n" \
"v_mfma_f32_16x16x32_bf16 a[104:107], v[148:151], v[68:71], a[104:107]\n" \
"ds_read_b128 v[116:119], v179 offset:5120\n" \
"v_mfma_f32_16x16x32_bf16 a[108:111], v[148:151], v[76:79], a[108:111]\n" \
"v_mfma_f32_16x16x32_bf16 a[112:115], v[152:155], v[68:71], a[112:115]\n" \
"ds_read_b128 v[120:123], v179 offset:6144\n" \
"v_mfma_f32_16x16x32_bf16 a[116:119], v[152:155], v[76:79], a[116:119]\n" \
"v_mfma_f32_16x16x32_bf16 a[120:123], v[156:159], v[68:71], a[120:123]\n" \
"ds_read_b128 v[124:127], v179 offset:7168\n" \
"s_waitcnt lgkmcnt(4)\n" \
"buffer_load_dwordx4 v[28:31], v224, %[rs], s83 offen offset:3072 sc1\n" \
"v_mfma_f32_16x16x32_bf16 a[124:127], v[156:159], v[76:79], a[124:127]\n" \
"v_mov_b32 v178, v179\n" \
"s_branch Ldone%=\n" \
"Ldr1%=:\n" \
"s_nop 7\n" \
"v_exp_f32 v176, v50\n" \
"v_exp_f32 v177, v51\n" \
"s_nop 0\n" \
"v_cvt_pk_bf16_f32 v85, v176, v177\n" \
"v_add_f32 v168, v168, v176\n" \
"v_add_f32 v168, v168, v177\n" \
"v_exp_f32 v174, v52\n" \
"v_exp_f32 v175, v53\n" \
"s_nop 0\n" \
"v_cvt_pk_bf16_f32 v92, v174, v175\n" \
"v_add_f32 v169, v169, v174\n" \
"v_add_f32 v169, v169, v175\n" \
"v_exp_f32 v176, v54\n" \
"v_exp_f32 v177, v55\n" \
"s_nop 0\n" \
"v_cvt_pk_bf16_f32 v93, v176, v177\n" \
"v_add_f32 v169, v169, v176\n" \
"v_add_f32 v169, v169, v177\n" \
"v_exp_f32 v174, v56\n" \
"v_exp_f32 v175, v57\n" \
"s_nop 0\n" \
"v_cvt_pk_bf16_f32 v86, v174, v175\n" \
"v_add_f32 v168, v168, v174\n" \
"v_add_f32 v168, v168, v175\n" \
"v_exp_f32 v176, v58\n" \
"v_exp_f32 v177, v59\n" \
"s_nop 0\n" \
"v_cvt_pk_bf16_f32 v87, v176, v177\n" \
"v_add_f32 v168, v168, v176\n" \
"v_add_f32 v168, v168, v177\n" \
"v_exp_f32 v174, v60\n" \
"v_exp_f32 v175, v61\n" \
"s_nop 0\n" \
"v_cvt_pk_bf16_f32 v94, v174, v175\n" \
"v_add_f32 v169, v169, v174\n" \
"v_add_f32 v169, v169, v175\n" \
"v_exp_f32 v176, v62\n" \
"v_exp_f32 v177, v63\n" \
"s_nop 0\n" \
"v_cvt_pk_bf16_f32 v95, v176, v177\n" \
"v_add_f32 v169, v169, v176\n" \
"v_add_f32 v169, v169, v177\n" \
"v_add_f32 v180, v168, v169\n" \
"v_cmp_nge_f32 vcc, s51, v180\n" \
"s_waitcnt vmcnt(8)\n" \
"v_cmp_ne_u32 s[96:97], %[tag], v181\n" \
"s_nop 1\n" \
"s_cmp_eq_u64 s[96:97], 0\n" \
"s_cselect_b32 s53, s61, 0x80000000\n" \
"s_cbranch_vccz Lf1%=\n" \
"s_call_b64 s[92:93], Lrare1%=\n" \
"Lf1%=:\n" \
"s_cmp_eq_u32 s63, 0\n" \
"s_cbranch_scc1 Ldf1%=\n" \
"ds_read_b128 v[128:131], v178 offset:8192\n" \
"buffer_load_dwordx4 v224, %[rs], s43 offen offset:0 sc1 lds\n" \
"v_mfma_f32_16x16x32_bf16 a[4:7], v[96:99], v[88:91], a[4:7]\n" \
"v_add_f32 v170, v170, v168\n" \
"v_add_f32 v171, v171, v169\n" \
"ds_read_b128 v[132:135], v178 offset:9216\n" \
"v_readlane_b32 s48, v238, s42\n" \
"v_mfma_f32_16x16x32_bf16 a[12:15], v[100:103], v[88:91], a[12:15]\n" \
"ds_read_b128 v[136:139], v178 offset:10240\n" \
"buffer_load_dwordx4 v224, %[rs], s43 offen offset:1024 sc1 lds\n" \
"v_mfma_f32_16x16x32_bf16 a[20:23], v[104:107], v[88:91], a[20:23]\n" \
"ds_read_b128 v[140:143], v178 offset:11264\n" \
"s_waitcnt lgkmcnt(4)\n" \
"v_mfma_f32_16x16x32_bf16 a[28:31], v[108:111], v[88:91], a[28:31]\n" \
"ds_read_b128 v[144:147], v178 offset:12288\n" \
"s_add_u32 s42, s42, 1\n" \
"v_mfma_f32_16x16x32_bf16 a[36:39], v[112:115], v[88:91], a[36:39]\n" \
"ds_read_b128 v[148:151], v178 offset:13312\n" \
"buffer_load_dwordx4 v224, %[rs], s43 offen offset:2048 sc1 lds\n" \
"v_mfma_f32_16x16x32_bf16 a[44:47], v[116:119], v[88:91], a[44:47]\n" \
"ds_read_b128 v[152:155], v178 offset:14336\n" \
"v_add_u32 v179, s48, v224\n" \
"v_mfma_f32_16x16x32_bf16 a[52:55], v[120:123], v[88:91], a[52:55]\n" \
"ds_read_b128 v[156:159], v178 offset:15360\n" \
"s_waitcnt lgkmcnt(4)\n" \
"v_mfma_f32_16x16x32_bf16 a[60:63], v[124:127], v[88:91], a[60:63]\n" \
"ds_read_b128 v[96:99], v178 offset:16384\n" \
"buffer_load_dwordx4 v224, %[rs], s43 offen offset:3072 sc1 lds\n" \
"v_mfma_f32_16x16x32_bf16 a[68:71], v[128:131], v[88:91], a[68:71]\n" \
"ds_read_b128 v[100:103], v178 offset:17408\n" \
"s_add_u32 m0, m0, 0x1000\n" \
"s_add_u32 s43, s43, 0x1000\n" \
"v_mfma_f32_16x16x32_bf16 a[76:79], v[132:135], v[88:91], a[76:79]\n" \
"ds_read_b128 v[104:107], v178 offset:18432\n" \
"v_mfma_f32_16x16x32_bf16 a[84:87], v[136:139], v[88:91], a[84:87]\n" \
"ds_read_b128 v[108:111], v178 offset:19456\n" \
"s_waitcnt lgkmcnt(4)\n" \
"v_mfma_f32_16x16x32_bf16 a[92:95], v[140:143], v[88:91], a[92:95]\n" \
"ds_read_b128 v[112:115], v178 offset:20480\n" \
"buffer_load_dwordx4 v224, %[rs], s43 offen offset:0 sc1 lds\n" \
"v_mfma_f32_16x16x32_bf16 a[100:103], v[144:147], v[88:91], a[100:103]\n" \
"ds_read_b128 v[116:119], v178 offset:21504\n" \
"v_mfma_f32_16x16x32_bf16 a[108:111], v[148:151], v[88:91], a[108:111]\n" \
"ds_read_b128 v[120:123], v178 offset:22528\n" \
"buffer_load_dwordx4 v224, %[rs], s43 offen offset:1024 sc1 lds\n" \
"v_mfma_f32_16x16x32_bf16 a[116:119], v[152:155], v[88:91], a[116:119]\n" \
"ds_read_b128 v[124:127], v178 offset:23552\n" \
"s_waitcnt lgkmcnt(4)\n" \
"v_mfma_f32_16x16x32_bf16 a[124:127], v[156:159], v[88:91], a[124:127]\n" \
"ds_read_b128 v[128:131], v178 offset:24576\n" \
"v_mfma_f32_16x16x32_bf16 a[4:7], v[96:99], v[92:95], a[4:7]\n" \
"ds_read_b128 v[132:135], v178 offset:25600\n" \
"buffer_load_dwordx4 v224, %[rs], s43 offen offset:2048 sc1 lds\n" \
"v_mfma_f32_16x16x32_bf16 a[12:15], v[100:103], v[92:95], a[12:15]\n" \
"ds_read_b128 v[136:139], v178 offset:26624\n" \
"v_mfma_f32_16x16x32_bf16 a[20:23], v[104:107], v[92:95], a[20:23]\n" \
"ds_read_b128 v[140:143], v178 offset:27648\n" \
"s_waitcnt lgkmcnt(4)\n" \
"v_mfma_f32_16x16x32_bf16 a[28:31], v[108:111], v[92:95], a[28:31]\n" \
"ds_read_b128 v[144:147], v178 offset:28672\n" \
"buffer_load_dwordx4 v224, %[rs], s43 offen offset:3072 sc1 lds\n" \
"v_mfma_f32_16x16x32_bf16 a[36:39], v[112:115], v[92:95], a[36:39]\n" \
"ds_read_b128 v[148:151], v178 offset:29696\n" \
"v_readlane_b32 s43, v239, s42\n" \
"v_mfma_f32_16x16x32_bf16 a[44:47], v[116:119], v[92:95], a[44:47]\n" \
"ds_read_b128 v[152:155], v178 offset:30720\n" \
"s_add_u32 m0, s44, s37\n" \
"v_mfma_f32_16x16x32_bf16 a[52:55], v[120:123], v[92:95], a[52:55]\n" \
"ds_read_b128 v[156:159], v178 offset:31744\n" \
"s_waitcnt lgkmcnt(4)\n" \
"v_mfma_f32_16x16x32_bf16 a[60:63], v[124:127], v[92:95], a[60:63]\n" \
"s_waitcnt vmcnt(16)\n" \
"s_barrier\n" \
"ds_read_b128 v[96:99], v179 offset:0\n" \
"v_mfma_f32_16x16x32_bf16 a[68:71], v[128:131], v[92:95], a[68:71]\n" \
"ds_read_b128 v[100:103], v179 offset:1024\n" \
"s_mov_b32 s44, s41\n" \
"v_mfma_f32_16x16x32_bf16 a[76:79], v[132:135], v[92:95], a[76:79]\n" \
"ds_read_b128 v[104:107], v179 offset:2048\n" \
"s_mov_b32 s41, s48\n" \
"v_mfma_f32_16x16x32_bf16 a[84:87], v[136:139], v[92:95], a[84:87]\n" \
"ds_read_b128 v[108:111], v179 offset:3072\n" \
"s_waitcnt lgkmcnt(4)\n" \
"v_mfma_f32_16x16x32_bf16 a[92:95], v[140:143], v[92:95], a[92:95]\n" \
"ds_read_b128 v[112:115], v179 offset:4096\n" \
"v_mfma_f32_16x16x32_bf16 a[100:103], v[144:147], v[92:95], a[100:103]\n" \
"ds_read_b128 v[116:119], v179 offset:5120\n" \
"v_mfma_f32_16x16x32_bf16 a[108:111], v[148:151], v[92:95], a[108:111]\n" \
"ds_read_b128 v[120:123], v179 offset:6144\n" \
"v_mfma_f32_16x16x32_bf16 a[116:119], v[152:155], v[92:95], a[116:119]\n" \
"ds_read_b128 v[124:127], v179 offset:7168\n" \
"s_waitcnt lgkmcnt(4)\n" \
"v_mfma_f32_16x16x32_bf16 a[124:127], v[156:159], v[92:95], a[124:127]\n" \
"v_mov_b32 v178, v179\n" \
"s_branch Ldone%=\n" \
"Ldf1%=:\n" \
"v_mfma_f32_16x16x32_bf16 a[0:3], v[96:99], v[80:83], a[0:3]\n" \
"ds_read_b128 v[128:131], v178 offset:8192\n" \
"buffer_load_dwordx4 v224, %[rs], s43 offen offset:0 sc1 lds\n" \
"v_mfma_f32_16x16x32_bf16 a[4:7], v[96:99], v[88:91], a[4:7]\n" \
"v_add_f32 v170, v170, v168\n" \
"v_add_f32 v171, v171, v169\n" \
"v_mfma_f32_16x16x32_bf16 a[8:11], v[100:103], v[80:83], a[8:11]\n" \
"ds_read_b128 v[132:135], v178 offset:9216\n" \
"v_readlane_b32 s48, v238, s42\n" \
"v_mfma_f32_16x16x32_bf16 a[12:15], v[100:103], v[88:91], a[12:15]\n" \
"s_add_u32 s83, s53, 16384\n" \
"v_mfma_f32_16x16x32_bf16 a[16:19], v[104:107], v[80:83], a[16:19]\n" \
"ds_read_b128 v[136:139], v178 offset:10240\n" \
"buffer_load_dwordx4 v224, %[rs], s43 offen offset:1024 sc1 lds\n" \
"v_mfma_f32_16x16x32_bf16 a[20:23], v[104:107], v[88:91], a[20:23]\n" \
"v_mfma_f32_16x16x32_bf16 a[24:27], v[108:111], v[80:83], a[24:27]\n" \
"ds_read_b128 v[140:143], v178 offset:11264\n" \
"s_waitcnt lgkmcnt(4)\n" \
"v_mfma_f32_16x16x32_bf16 a[28:31], v[108:111], v[88:91], a[28:31]\n" \
"buffer_load_dwordx4 v[216:219], v224, %[rs], s83 offen sc1\n" \
"v_mfma_f32_16x16x32_bf16 a[32:35], v[112:115], v[80:83], a[32:35]\n" \
"ds_read_b128 v[144:147], v178 offset:12288\n" \
"s_add_u32 s42, s42, 1\n" \
"v_mfma_f32_16x16x32_bf16 a[36:39], v[112:115], v[88:91], a[36:39]\n" \
"v_mfma_f32_16x16x32_bf16 a[40:43], v[116:119], v[80:83], a[40:43]\n" \
"ds_read_b128 v[148:151], v178 offset:13312\n" \
"buffer_load_dwordx4 v224, %[rs], s43 offen offset:2048 sc1 lds\n" \
"v_mfma_f32_16x16x32_bf16 a[44:47], v[116:119], v[88:91], a[44:47]\n" \
"v_mfma_f32_16x16x32_bf16 a[48:51], v[120:123], v[80:83], a[48:51]\n" \
"ds_read_b128 v[152:155], v178 offset:14336\n" \
"v_add_u32 v179, s48, v224\n" \
"v_mfma_f32_16x16x32_bf16 a[52:55], v[120:123], v[88:91], a[52:55]\n" \
"buffer_load_dwordx4 v[0:3], v224, %[rs], s53 offen offset:0 sc1\n" \
"v_mfma_f32_16x16x32_bf16 a[56:59], v[124:127], v[80:83], a[56:59]\n" \
"ds_read_b128 v[156:159], v178 offset:15360\n" \
"s_waitcnt lgkmcnt(4)\n" \
"v_mfma_f32_16x16x32_bf16 a[60:63], v[124:127], v[88:91], a[60:63]\n" \
"v_mfma_f32_16x16x32_bf16 a[64:67], v[128:131], v[80:83], a[64:67]\n" \
"ds_read_b128 v[96:99], v178 offset:16384\n" \
"buffer_load_dwordx4 v224, %[rs], s43 offen offset:3072 sc1 lds\n" \
"v_mfma_f32_16x16x32_bf16 a[68:71], v[128:131], v[88:91], a[68:71]\n" \
"v_mfma_f32_16x16x32_bf16 a[72:75], v[132:135], v[80:83], a[72:75]\n" \
"ds_read_b128 v[100:103], v178 offset:17408\n" \
"s_add_u32 m0, m0, 0x1000\n" \
"s_add_u32 s43, s43, 0x1000\n" \
"v_mfma_f32_16x16x32_bf16 a[76:79], v[132:135], v[88:91], a[76:79]\n" \
"v_mfma_f32_16x16x32_bf16 a[80:83], v[136:139], v[80:83], a[80:83]\n" \
"ds_read_b128 v[104:107], v178 offset:18432\n" \
"v_mfma_f32_16x16x32_bf16 a[84:87], v[136:139], v[88:91], a[84:87]\n" \
"buffer_load_dwordx4 v[4:7], v224, %[rs], s53 offen offset:1024 sc1\n" \
"v_mfma_f32_16x16x32_bf16 a[88:91], v[140:143], v[80:83], a[88:91]\n" \
"ds_read_b128 v[108:111], v178 offset:19456\n" \
"s_waitcnt lgkmcnt(4)\n" \
"v_mfma_f32_16x16x32_bf16 a[92:95], v[140:143], v[88:91], a[92:95]\n" \
"v_mfma_f32_16x16x32_bf16 a[96:99], v[144:147], v[80:83], a[96:99]\n" \
"ds_read_b128 v[112:115], v178 offset:20480\n" \
"buffer_load_dwordx4 v224, %[rs], s43 offen offset:0 sc1 lds\n" \
"v_mfma_f32_16x16x32_bf16 a[100:103], v[144:147], v[88:91], a[100:103]\n" \
"v_mfma_f32_16x16x32_bf16 a[104:107], v[148:151], v[80:83], a[104:107]\n" \
"ds_read_b128 v[116:119], v178 offset:21504\n" \
"v_mfma_f32_16x16x32_bf16 a[108:111], v[148:151], v[88:91], a[108:111]\n" \
"v_mfma_f32_16x16x32_bf16 a[112:115], v[152:155], v[80:83], a[112:115]\n" \
"ds_read_b128 v[120:123], v178 offset:22528\n" \
"buffer_load_dwordx4 v224, %[rs], s43 offen offset:1024 sc1 lds\n" \
"v_mfma_f32_16x16x32_bf16 a[116:119], v[152:155], v[88:91], a[116:119]\n" \
"v_mfma_f32_16x16x32_bf16 a[120:123], v[156:159], v[80:83], a[120:123]\n" \
"ds_read_b128 v[124:127], v178 offset:23552\n" \
"s_waitcnt lgkmcnt(4)\n" \
"v_mfma_f32_16x16x32_bf16 a[124:127], v[156:159], v[88:91], a[124:127]\n" \
"buffer_load_dwordx4 v[8:11], v224, %[rs], s53 offen offset:2048 sc1\n" \
"v_mfma_f32_16x16x32_bf16 a[0:3], v[96:99], v[84:87], a[0:3]\n" \
"ds_read_b128 v[128:131], v178 offset:24576\n" \
"v_mfma_f32_16x16x32_bf16 a[4:7], v[96:99], v[92:95], a[4:7]\n" \
"v_mfma_f32_16x16x32_bf16 a[8:11], v[100:103], v[84:87], a[8:11]\n" \
"ds_read_b128 v[132:135], v178 offset:25600\n" \
"buffer_load_dwordx4 v224, %[rs], s43 offen offset:2048 sc1 lds\n" \
"v_mfma_f32_16x16x32_bf16 a[12:15], v[100:103], v[92:95], a[12:15]\n" \
"v_mfma_f32_16x16x32_bf16 a[16:19], v[104:107], v[84:87], a[16:19]\n" \
"ds_read_b128 v[136:139], v178 offset:26624\n" \
"v_mfma_f32_16x16x32_bf16 a[20:23], v[104:107], v[92:95], a[20:23]\n" \
"buffer_load_dwordx4 v[12:15], v224, %[rs], s53 offen offset:3072 sc1\n" \
"v_mfma_f32_16x16x32_bf16 a[24:27], v[108:111], v[84:87], a[24:27]\n" \
"ds_read_b128 v[140:143], v178 offset:27648\n" \
"s_waitcnt lgkmcnt(4)\n" \
"v_mfma_f32_16x16x32_bf16 a[28:31], v[108:111], v[92:95], a[28:31]\n" \
"v_mfma_f32_16x16x32_bf16 a[32:35], v[112:115], v[84:87], a[32:35]\n" \
"ds_read_b128 v[144:147], v178 offset:28672\n" \
"buffer_load_dwordx4 v224, %[rs], s43 offen offset:3072 sc1 lds\n" \
"v_mfma_f32_16x16x32_bf16 a[36:39], v[112:115], v[92:95], a[36:39]\n" \
"v_mfma_f32_16x16x32_bf16 a[40:43], v[116:119], v[84:87], a[40:43]\n" \
"ds_read_b128 v[148:151], v178 offset:29696\n" \
"v_readlane_b32 s43, v239, s42\n" \
"v_mfma_f32_16x16x32_bf16 a[44:47], v[116:119], v[92:95], a[44:47]\n" \
"s_add_u32 s83, s53, 4096\n" \
"v_mfma_f32_16x16x32_bf16 a[48:51], v[120:123], v[84:87], a[48:51]\n" \
"ds_read_b128 v[152:155], v178 offset:30720\n" \
"s_add_u32 m0, s44, s37\n" \
"v_mfma_f32_16x16x32_bf16 a[52:55], v[120:123], v[92:95], a[52:55]\n" \
"buffer_load_dwordx4 v[16:19], v224, %[rs], s83 offen offset:0 sc1\n" \
"v_mfma_f32_16x16x32_bf16 a[56:59], v[124:127], v[84:87], a[56:59]\n" \
"ds_read_b128 v[156:159], v178 offset:31744\n" \
"s_waitcnt lgkmcnt(4)\n" \
"v_mfma_f32_16x16x32_bf16 a[60:63], v[124:127], v[92:95], a[60:63]\n" \
"s_waitcnt vmcnt(22)\n" \
"s_barrier\n" \
"v_mfma_f32_16x16x32_bf16 a[64:67], v[128:131], v[84:87], a[64:67]\n" \
"ds_read_b128 v[96:99], v179 offset:0\n" \
"v_mfma_f32_16x16x32_bf16 a[68:71], v[128:131], v[92:95], a[68:71]\n" \
"v_mfma_f32_16x16x32_bf16 a[72:75], v[132:135], v[84:87], a[72:75]\n" \
"ds_read_b128 v[100:103], v179 offset:1024\n" \
"s_mov_b32 s44, s41\n" \
"v_mfma_f32_16x16x32_bf16 a[76:79], v[132:135], v[92:95], a[76:79]\n" \
"buffer_load_dwordx4 v[20:23], v224, %[rs], s83 offen offset:1024 sc1\n" \
"v_mfma_f32_16x16x32_bf16 a[80:83], v[136:139], v[84:87], a[80:83]\n" \
"ds_read_b128 v[104:107], v179 offset:2048\n" \
"s_mov_b32 s41, s48\n" \
"v_mfma_f32_16x16x32_bf16 a[84:87], v[136:139], v[92:95], a[84:87]\n" \
"v_mfma_f32_16x16x32_bf16 a[88:91], v[140:143], v[84:87], a[88:91]\n" \
"ds_read_b128 v[108:111], v179 offset:3072\n" \
"s_waitcnt lgkmcnt(4)\n" \
"v_mfma_f32_16x16x32_bf16 a[92:95], v[140:143], v[92:95], a[92:95]\n" \
"v_mfma_f32_16x16x32_bf16 a[96:99], v[144:147], v[84:87], a[96:99]\n" \
"ds_read_b128 v[112:115], v179 offset:4096\n" \
"v_mfma_f32_16x16x32_bf16 a[100:103], v[144:147], v[92:95], a[100:103]\n" \
"buffer_load_dwordx4 v[24:27], v224, %[rs], s83 offen offset:2048 sc1\n" \
"v_mfma_f32_16x16x32_bf16 a[104:107], v[148:151], v[84:87], a[104:107]\n" \
"ds_read_b128 v[116:119], v179 offset:5120\n" \
"v_mfma_f32_16x16x32_bf16 a[108:111], v[148:151], v[92:95], a[108:111]\n" \
"v_mfma_f32_16x16x32_bf16 a[112:115], v[152:155], v[84:87], a[112:115]\n" \
"ds_read_b128 v[120:123], v179 offset:6144\n" \
"v_mfma_f32_16x16x32_bf16 a[116:119], v[152:155], v[92:95], a[116:119]\n" \
"v_mfma_f32_16x16x32_bf16 a[120:123], v[156:159], v[84:87], a[120:123]\n" \
"ds_read_b128 v[124:127], v179 offset:7168\n" \
"s_waitcnt lgkmcnt(4)\n" \
"buffer_load_dwordx4 v[28:31], v224, %[rs], s83 offen offset:3072 sc1\n" \
"v_mfma_f32_16x16x32_bf16 a[124:127], v[156:159], v[92:95], a[124:127]\n" \
"v_mov_b32 v178, v179\n" \
"Ldone%=:\n" \
"v_mov_b32 v184, v170\n" \
"v_mov_b32 v185, v171\n" \
"s_nop 0\n" \
"v_permlane16_swap_b32 v184, v170\n" \
"v_permlane16_swap_b32 v185, v171\n" \
"v_add_f32 v170, v170, v184\n" \
"v_add_f32 v171, v171, v185\n" \
"v_mov_b32 v184, v170\n" \
"v_mov_b32 v185, v171\n" \
"s_nop 0\n" \
"v_permlane32_swap_b32 v184, v170\n" \
"v_permlane32_swap_b32 v185, v171\n" \
"v_add_f32 v170, v170, v184\n" \
"v_add_f32 v171, v171, v185\n" \
"s_cmp_lg_u32 s63, 0\n" \
"s_cbranch_scc1 Lnorm%=\n" \
"s_cmp_eq_u32 s53, s61\n" \
"s_cbranch_scc1 Lmg%=\n" \
"s_mov_b32 s81, 0\n" \
"Lpw_p%=:\n" \
"buffer_load_dword v184, off, %[rs], s62 sc1\n" \
"s_waitcnt vmcnt(0)\n" \
"v_cmp_ne_u32 s[96:97], %[tag], v184\n" \
"s_nop 1\n" \
"s_cmp_eq_u64 s[96:97], 0\n" \
"s_cbranch_scc1 Lpw_d%=\n" \
"s_sleep 4\n" \
"s_add_u32 s81, s81, 1\n" \
"s_cmp_lt_u32 s81, 0x20000\n" \
"s_cbranch_scc1 Lpw_p%=\n" \
"Lpw_d%=:\n" \
"s_add_u32 s83, s61, 16384\n" \
"buffer_load_dwordx4 v[216:219], v224, %[rs], s83 offen sc1\n" \
"s_add_u32 s83, s61, 0\n" \
"buffer_load_dwordx4 v[0:3], v224, %[rs], s83 offen offset:0 sc1\n" \
"buffer_load_dwordx4 v[4:7], v224, %[rs], s83 offen offset:1024 sc1\n" \
"buffer_load_dwordx4 v[8:11], v224, %[rs], s83 offen offset:2048 sc1\n" \
"buffer_load_dwordx4 v[12:15], v224, %[rs], s83 offen offset:3072 sc1\n" \
"s_add_u32 s83, s61, 4096\n" \
"buffer_load_dwordx4 v[16:19], v224, %[rs], s83 offen offset:0 sc1\n" \
"buffer_load_dwordx4 v[20:23], v224, %[rs], s83 offen offset:1024 sc1\n" \
"buffer_load_dwordx4 v[24:27], v224, %[rs], s83 offen offset:2048 sc1\n" \
"buffer_load_dwordx4 v[28:31], v224, %[rs], s83 offen offset:3072 sc1\n" \
"Lmg%=:\n" \
"s_add_u32 s83, s61, 8192\n" \
"buffer_load_dwordx4 v[32:35], v224, %[rs], s83 offen offset:0 sc1\n" \
"buffer_load_dwordx4 v[36:39], v224, %[rs], s83 offen offset:1024 sc1\n" \
"buffer_load_dwordx4 v[40:43], v224, %[rs], s83 offen offset:2048 sc1\n" \
"buffer_load_dwordx4 v[44:47], v224, %[rs], s83 offen offset:3072 sc1\n" \
"s_add_u32 s83, s61, 12288\n" \
"buffer_load_dwordx4 v[48:51], v224, %[rs], s83 offen offset:0 sc1\n" \
"buffer_load_dwordx4 v[52:55], v224, %[rs], s83 offen offset:1024 sc1\n" \
"buffer_load_dwordx4 v[56:59], v224, %[rs], s83 offen offset:2048 sc1\n" \
"buffer_load_dwordx4 v[60:63], v224, %[rs], s83 offen offset:3072 sc1\n" \
"s_waitcnt vmcnt(8)\n" \
"v_max_f32 v194, v172, v216\n" \
"v_sub_f32 v195, v172, v194\n" \
"v_sub_f32 v196, v216, v194\n" \
"v_exp_f32 v195, v195\n" \
"v_exp_f32 v196, v196\n" \
"s_nop 0\n" \
"v_mul_f32 v196, v196, v218\n" \
"v_fma_f32 v194, v195, v170, v196\n" \
"v_rcp_f32 v194, v194\n" \
"s_nop 0\n" \
"v_mul_f32 v186, v195, v194\n" \
"v_mul_f32 v190, v196, v194\n" \
"v_mul_f32 v187, v195, v194\n" \
"v_mul_f32 v191, v196, v194\n" \
"v_max_f32 v194, v173, v217\n" \
"v_sub_f32 v195, v173, v194\n" \
"v_sub_f32 v196, v217, v194\n" \
"v_exp_f32 v195, v195\n" \
"v_exp_f32 v196, v196\n" \
"s_nop 0\n" \
"v_mul_f32 v196, v196, v219\n" \
"v_fma_f32 v194, v195, v171, v196\n" \
"v_rcp_f32 v194, v194\n" \
"s_nop 0\n" \
"v_mul_f32 v188, v195, v194\n" \
"v_mul_f32 v192, v196, v194\n" \
"v_mul_f32 v189, v195, v194\n" \
"v_mul_f32 v193, v196, v194\n" \
"v_accvgpr_read_b32 v64, a0\n" \
"v_accvgpr_read_b32 v68, a8\n" \
"v_accvgpr_read_b32 v65, a1\n" \
"v_accvgpr_read_b32 v69, a9\n" \
"v_accvgpr_read_b32 v66, a2\n" \
"v_accvgpr_read_b32 v70, a10\n" \
"v_accvgpr_read_b32 v67, a3\n" \
"v_accvgpr_read_b32 v71, a11\n" \
"v_pk_mul_f32 v[64:65], v[64:65], v[186:187]\n" \
"v_pk_mul_f32 v[66:67], v[66:67], v[186:187]\n" \
"v_pk_mul_f32 v[68:69], v[68:69], v[186:187]\n" \
"v_pk_mul_f32 v[70:71], v[70:71], v[186:187]\n" \
"v_lshlrev_b32 v72, 16, v0\n" \
"v_and_b32 v73, 0xffff0000, v0\n" \
"v_lshlrev_b32 v74, 16, v1\n" \
"v_and_b32 v75, 0xffff0000, v1\n" \
"v_lshlrev_b32 v76, 16, v2\n" \
"v_and_b32 v77, 0xffff0000, v2\n" \
"v_lshlrev_b32 v78, 16, v3\n" \
"v_and_b32 v79, 0xffff0000, v3\n" \
"v_pk_fma_f32 v[64:65], v[72:73], v[190:191], v[64:65]\n" \
"v_pk_fma_f32 v[66:67], v[74:75], v[190:191], v[66:67]\n" \
"v_pk_fma_f32 v[68:69], v[76:77], v[190:191], v[68:69]\n" \
"v_pk_fma_f32 v[70:71], v[78:79], v[190:191], v[70:71]\n" \
"v_cvt_pk_bf16_f32 v0, v64, v65\n" \
"v_cvt_pk_bf16_f32 v1, v66, v67\n" \
"v_cvt_pk_bf16_f32 v2, v68, v69\n" \
"v_cvt_pk_bf16_f32 v3, v70, v71\n" \
"v_accvgpr_read_b32 v80, a16\n" \
"v_accvgpr_read_b32 v84, a24\n" \
"v_accvgpr_read_b32 v81, a17\n" \
"v_accvgpr_read_b32 v85, a25\n" \
"v_accvgpr_read_b32 v82, a18\n" \
"v_accvgpr_read_b32 v86, a26\n" \
"v_accvgpr_read_b32 v83, a19\n" \
"v_accvgpr_read_b32 v87, a27\n" \
"v_pk_mul_f32 v[80:81], v[80:81], v[186:187]\n" \
"v_pk_mul_f32 v[82:83], v[82:83], v[186:187]\n" \
"v_pk_mul_f32 v[84:85], v[84:85], v[186:187]\n" \
"v_pk_mul_f32 v[86:87], v[86:87], v[186:187]\n" \
"v_lshlrev_b32 v88, 16, v4\n" \
"v_and_b32 v89, 0xffff0000, v4\n" \
"v_lshlrev_b32 v90, 16, v5\n" \
"v_and_b32 v91, 0xffff0000, v5\n" \
"v_lshlrev_b32 v92, 16, v6\n" \
"v_and_b32 v93, 0xffff0000, v6\n" \
"v_lshlrev_b32 v94, 16, v7\n" \
"v_and_b32 v95, 0xffff0000, v7\n" \
"v_pk_fma_f32 v[80:81], v[88:89], v[190:191], v[80:81]\n" \
"v_pk_fma_f32 v[82:83], v[90:91], v[190:191], v[82:83]\n" \
"v_pk_fma_f32 v[84:85], v[92:93], v[190:191], v[84:85]\n" \
"v_pk_fma_f32 v[86:87], v[94:95], v[190:191], v[86:87]\n" \
"v_cvt_pk_bf16_f32 v4, v80, v81\n" \
"v_cvt_pk_bf16_f32 v5, v82, v83\n" \
"v_cvt_pk_bf16_f32 v6, v84, v85\n" \
"v_cvt_pk_bf16_f32 v7, v86, v87\n" \
"v_accvgpr_read_b32 v64, a32\n" \
"v_accvgpr_read_b32 v68, a40\n" \
"v_accvgpr_read_b32 v65, a33\n" \
"v_accvgpr_read_b32 v69, a41\n" \
"v_accvgpr_read_b32 v66, a34\n" \
"v_accvgpr_read_b32 v70, a42\n" \
"v_accvgpr_read_b32 v67, a35\n" \
"v_accvgpr_read_b32 v71, a43\n" \
"v_pk_mul_f32 v[64:65], v[64:65], v[186:187]\n" \
"v_pk_mul_f32 v[66:67], v[66:67], v[186:187]\n" \
"v_pk_mul_f32 v[68:69], v[68:69], v[186:187]\n" \
"v_pk_mul_f32 v[70:71], v[70:71], v[186:187]\n" \
"v_lshlrev_b32 v72, 16, v8\n" \
"v_and_b32 v73, 0xffff0000, v8\n" \
"v_lshlrev_b32 v74, 16, v9\n" \
"v_and_b32 v75, 0xffff0000, v9\n" \
"v_lshlrev_b32 v76, 16, v10\n" \
"v_and_b32 v77, 0xffff0000, v10\n" \
"v_lshlrev_b32 v78, 16, v11\n" \
"v_and_b32 v79, 0xffff0000, v11\n" \
"v_pk_fma_f32 v[64:65], v[72:73], v[190:191], v[64:65]\n" \
"v_pk_fma_f32 v[66:67], v[74:75], v[190:191], v[66:67]\n" \
"v_pk_fma_f32 v[68:69], v[76:77], v[190:191], v[68:69]\n" \
"v_pk_fma_f32 v[70:71], v[78:79], v[190:191], v[70:71]\n" \
"v_cvt_pk_bf16_f32 v8, v64, v65\n" \
"v_cvt_pk_bf16_f32 v9, v66, v67\n" \
"v_cvt_pk_bf16_f32 v10, v68, v69\n" \
"v_cvt_pk_bf16_f32 v11, v70, v71\n" \
"v_accvgpr_read_b32 v80, a48\n" \
"v_accvgpr_read_b32 v84, a56\n" \
"v_accvgpr_read_b32 v81, a49\n" \
"v_accvgpr_read_b32 v85, a57\n" \
"v_accvgpr_read_b32 v82, a50\n" \
"v_accvgpr_read_b32 v86, a58\n" \
"v_accvgpr_read_b32 v83, a51\n" \
"v_accvgpr_read_b32 v87, a59\n" \
"v_pk_mul_f32 v[80:81], v[80:81], v[186:187]\n" \
"v_pk_mul_f32 v[82:83], v[82:83], v[186:187]\n" \
"v_pk_mul_f32 v[84:85], v[84:85], v[186:187]\n" \
"v_pk_mul_f32 v[86:87], v[86:87], v[186:187]\n" \
"v_lshlrev_b32 v88, 16, v12\n" \
"v_and_b32 v89, 0xffff0000, v12\n" \
"v_lshlrev_b32 v90, 16, v13\n" \
"v_and_b32 v91, 0xffff0000, v13\n" \
"v_lshlrev_b32 v92, 16, v14\n" \
"v_and_b32 v93, 0xffff0000, v14\n" \
"v_lshlrev_b32 v94, 16, v15\n" \
"v_and_b32 v95, 0xffff0000, v15\n" \
"v_pk_fma_f32 v[80:81], v[88:89], v[190:191], v[80:81]\n" \
"v_pk_fma_f32 v[82:83], v[90:91], v[190:191], v[82:83]\n" \
"v_pk_fma_f32 v[84:85], v[92:93], v[190:191], v[84:85]\n" \
"v_pk_fma_f32 v[86:87], v[94:95], v[190:191], v[86:87]\n" \
"v_cvt_pk_bf16_f32 v12, v80, v81\n" \
"v_cvt_pk_bf16_f32 v13, v82, v83\n" \
"v_cvt_pk_bf16_f32 v14, v84, v85\n" \
"v_cvt_pk_bf16_f32 v15, v86, v87\n" \
"v_accvgpr_read_b32 v64, a64\n" \
"v_accvgpr_read_b32 v68, a72\n" \
"v_accvgpr_read_b32 v65, a65\n" \
"v_accvgpr_read_b32 v69, a73\n" \
"v_accvgpr_read_b32 v66, a66\n" \
"v_accvgpr_read_b32 v70, a74\n" \
"v_accvgpr_read_b32 v67, a67\n" \
"v_accvgpr_read_b32 v71, a75\n" \
"v_pk_mul_f32 v[64:65], v[64:65], v[186:187]\n" \
"v_pk_mul_f32 v[66:67], v[66:67], v[186:187]\n" \
"v_pk_mul_f32 v[68:69], v[68:69], v[186:187]\n" \
"v_pk_mul_f32 v[70:71], v[70:71], v[186:187]\n" \
"v_lshlrev_b32 v72, 16, v16\n" \
"v_and_b32 v73, 0xffff0000, v16\n" \
"v_lshlrev_b32 v74, 16, v17\n" \
"v_and_b32 v75, 0xffff0000, v17\n" \
"v_lshlrev_b32 v76, 16, v18\n" \
"v_and_b32 v77, 0xffff0000, v18\n" \
"v_lshlrev_b32 v78, 16, v19\n" \
"v_and_b32 v79, 0xffff0000, v19\n" \
"v_pk_fma_f32 v[64:65], v[72:73], v[190:191], v[64:65]\n" \
"v_pk_fma_f32 v[66:67], v[74:75], v[190:191], v[66:67]\n" \
"v_pk_fma_f32 v[68:69], v[76:77], v[190:191], v[68:69]\n" \
"v_pk_fma_f32 v[70:71], v[78:79], v[190:191], v[70:71]\n" \
"v_cvt_pk_bf16_f32 v16, v64, v65\n" \
"v_cvt_pk_bf16_f32 v17, v66, v67\n" \
"v_cvt_pk_bf16_f32 v18, v68, v69\n" \
"v_cvt_pk_bf16_f32 v19, v70, v71\n" \
"v_accvgpr_read_b32 v80, a80\n" \
"v_accvgpr_read_b32 v84, a88\n" \
"v_accvgpr_read_b32 v81, a81\n" \
"v_accvgpr_read_b32 v85, a89\n" \
"v_accvgpr_read_b32 v82, a82\n" \
"v_accvgpr_read_b32 v86, a90\n" \
"v_accvgpr_read_b32 v83, a83\n" \
"v_accvgpr_read_b32 v87, a91\n" \
"v_pk_mul_f32 v[80:81], v[80:81], v[186:187]\n" \
"v_pk_mul_f32 v[82:83], v[82:83], v[186:187]\n" \
"v_pk_mul_f32 v[84:85], v[84:85], v[186:187]\n" \
"v_pk_mul_f32 v[86:87], v[86:87], v[186:187]\n" \
"v_lshlrev_b32 v88, 16, v20\n" \
"v_and_b32 v89, 0xffff0000, v20\n" \
"v_lshlrev_b32 v90, 16, v21\n" \
"v_and_b32 v91, 0xffff0000, v21\n" \
"v_lshlrev_b32 v92, 16, v22\n" \
"v_and_b32 v93, 0xffff0000, v22\n" \
"v_lshlrev_b32 v94, 16, v23\n" \
"v_and_b32 v95, 0xffff0000, v23\n" \
"v_pk_fma_f32 v[80:81], v[88:89], v[190:191], v[80:81]\n" \
"v_pk_fma_f32 v[82:83], v[90:91], v[190:191], v[82:83]\n" \
"v_pk_fma_f32 v[84:85], v[92:93], v[190:191], v[84:85]\n" \
"v_pk_fma_f32 v[86:87], v[94:95], v[190:191], v[86:87]\n" \
"v_cvt_pk_bf16_f32 v20, v80, v81\n" \
"v_cvt_pk_bf16_f32 v21, v82, v83\n" \
"v_cvt_pk_bf16_f32 v22, v84, v85\n" \
"v_cvt_pk_bf16_f32 v23, v86, v87\n" \
"v_accvgpr_read_b32 v64, a96\n" \
"v_accvgpr_read_b32 v68, a104\n" \
"v_accvgpr_read_b32 v65, a97\n" \
"v_accvgpr_read_b32 v69, a105\n" \
"v_accvgpr_read_b32 v66, a98\n" \
"v_accvgpr_read_b32 v70, a106\n" \
"v_accvgpr_read_b32 v67, a99\n" \
"v_accvgpr_read_b32 v71, a107\n" \
"v_pk_mul_f32 v[64:65], v[64:65], v[186:187]\n" \
"v_pk_mul_f32 v[66:67], v[66:67], v[186:187]\n" \
"v_pk_mul_f32 v[68:69], v[68:69], v[186:187]\n" \
"v_pk_mul_f32 v[70:71], v[70:71], v[186:187]\n" \
"v_lshlrev_b32 v72, 16, v24\n" \
"v_and_b32 v73, 0xffff0000, v24\n" \
"v_lshlrev_b32 v74, 16, v25\n" \
"v_and_b32 v75, 0xffff0000, v25\n" \
"v_lshlrev_b32 v76, 16, v26\n" \
"v_and_b32 v77, 0xffff0000, v26\n" \
"v_lshlrev_b32 v78, 16, v27\n" \
"v_and_b32 v79, 0xffff0000, v27\n" \
"v_pk_fma_f32 v[64:65], v[72:73], v[190:191], v[64:65]\n" \
"v_pk_fma_f32 v[66:67], v[74:75], v[190:191], v[66:67]\n" \
"v_pk_fma_f32 v[68:69], v[76:77], v[190:191], v[68:69]\n" \
"v_pk_fma_f32 v[70:71], v[78:79], v[190:191], v[70:71]\n" \
"v_cvt_pk_bf16_f32 v24, v64, v65\n" \
"v_cvt_pk_bf16_f32 v25, v66, v67\n" \
"v_cvt_pk_bf16_f32 v26, v68, v69\n" \
"v_cvt_pk_bf16_f32 v27, v70, v71\n" \
"v_accvgpr_read_b32 v80, a112\n" \
"v_accvgpr_read_b32 v84, a120\n" \
"v_accvgpr_read_b32 v81, a113\n" \
"v_accvgpr_read_b32 v85, a121\n" \
"v_accvgpr_read_b32 v82, a114\n" \
"v_accvgpr_read_b32 v86, a122\n" \
"v_accvgpr_read_b32 v83, a115\n" \
"v_accvgpr_read_b32 v87, a123\n" \
"v_pk_mul_f32 v[80:81], v[80:81], v[186:187]\n" \
"v_pk_mul_f32 v[82:83], v[82:83], v[186:187]\n" \
"v_pk_mul_f32 v[84:85], v[84:85], v[186:187]\n" \
"v_pk_mul_f32 v[86:87], v[86:87], v[186:187]\n" \
"v_lshlrev_b32 v88, 16, v28\n" \
"v_and_b32 v89, 0xffff0000, v28\n" \
"v_lshlrev_b32 v90, 16, v29\n" \
"v_and_b32 v91, 0xffff0000, v29\n" \
"v_lshlrev_b32 v92, 16, v30\n" \
"v_and_b32 v93, 0xffff0000, v30\n" \
"v_lshlrev_b32 v94, 16, v31\n" \
"v_and_b32 v95, 0xffff0000, v31\n" \
"v_pk_fma_f32 v[80:81], v[88:89], v[190:191], v[80:81]\n" \
"v_pk_fma_f32 v[82:83], v[90:91], v[190:191], v[82:83]\n" \
"v_pk_fma_f32 v[84:85], v[92:93], v[190:191], v[84:85]\n" \
"v_pk_fma_f32 v[86:87], v[94:95], v[190:191], v[86:87]\n" \
"v_cvt_pk_bf16_f32 v28, v80, v81\n" \
"v_cvt_pk_bf16_f32 v29, v82, v83\n" \
"v_cvt_pk_bf16_f32 v30, v84, v85\n" \
"v_cvt_pk_bf16_f32 v31, v86, v87\n" \
"s_waitcnt vmcnt(7)\n" \
"v_accvgpr_read_b32 v64, a4\n" \
"v_accvgpr_read_b32 v68, a12\n" \
"v_accvgpr_read_b32 v65, a5\n" \
"v_accvgpr_read_b32 v69, a13\n" \
"v_accvgpr_read_b32 v66, a6\n" \
"v_accvgpr_read_b32 v70, a14\n" \
"v_accvgpr_read_b32 v67, a7\n" \
"v_accvgpr_read_b32 v71, a15\n" \
"v_pk_mul_f32 v[64:65], v[64:65], v[188:189]\n" \
"v_pk_mul_f32 v[66:67], v[66:67], v[188:189]\n" \
"v_pk_mul_f32 v[68:69], v[68:69], v[188:189]\n" \
"v_pk_mul_f32 v[70:71], v[70:71], v[188:189]\n" \
"v_lshlrev_b32 v72, 16, v32\n" \
"v_and_b32 v73, 0xffff0000, v32\n" \
"v_lshlrev_b32 v74, 16, v33\n" \
"v_and_b32 v75, 0xffff0000, v33\n" \
"v_lshlrev_b32 v76, 16, v34\n" \
"v_and_b32 v77, 0xffff0000, v34\n" \
"v_lshlrev_b32 v78, 16, v35\n" \
"v_and_b32 v79, 0xffff0000, v35\n" \
"v_pk_fma_f32 v[64:65], v[72:73], v[192:193], v[64:65]\n" \
"v_pk_fma_f32 v[66:67], v[74:75], v[192:193], v[66:67]\n" \
"v_pk_fma_f32 v[68:69], v[76:77], v[192:193], v[68:69]\n" \
"v_pk_fma_f32 v[70:71], v[78:79], v[192:193], v[70:71]\n" \
"v_cvt_pk_bf16_f32 v32, v64, v65\n" \
"v_cvt_pk_bf16_f32 v33, v66, v67\n" \
"v_cvt_pk_bf16_f32 v34, v68, v69\n" \
"v_cvt_pk_bf16_f32 v35, v70, v71\n" \
"s_waitcnt vmcnt(6)\n" \
"v_accvgpr_read_b32 v80, a20\n" \
"v_accvgpr_read_b32 v84, a28\n" \
"v_accvgpr_read_b32 v81, a21\n" \
"v_accvgpr_read_b32 v85, a29\n" \
"v_accvgpr_read_b32 v82, a22\n" \
"v_accvgpr_read_b32 v86, a30\n" \
"v_accvgpr_read_b32 v83, a23\n" \
"v_accvgpr_read_b32 v87, a31\n" \
"v_pk_mul_f32 v[80:81], v[80:81], v[188:189]\n" \
"v_pk_mul_f32 v[82:83], v[82:83], v[188:189]\n" \
"v_pk_mul_f32 v[84:85], v[84:85], v[188:189]\n" \
"v_pk_mul_f32 v[86:87], v[86:87], v[188:189]\n" \
"v_lshlrev_b32 v88, 16, v36\n" \
"v_and_b32 v89, 0xffff0000, v36\n" \
"v_lshlrev_b32 v90, 16, v37\n" \
"v_and_b32 v91, 0xffff0000, v37\n" \
"v_lshlrev_b32 v92, 16, v38\n" \
"v_and_b32 v93, 0xffff0000, v38\n" \
"v_lshlrev_b32 v94, 16, v39\n" \
"v_and_b32 v95, 0xffff0000, v39\n" \
"v_pk_fma_f32 v[80:81], v[88:89], v[192:193], v[80:81]\n" \
"v_pk_fma_f32 v[82:83], v[90:91], v[192:193], v[82:83]\n" \
"v_pk_fma_f32 v[84:85], v[92:93], v[192:193], v[84:85]\n" \
"v_pk_fma_f32 v[86:87], v[94:95], v[192:193], v[86:87]\n" \
"v_cvt_pk_bf16_f32 v36, v80, v81\n" \
"v_cvt_pk_bf16_f32 v37, v82, v83\n" \
"v_cvt_pk_bf16_f32 v38, v84, v85\n" \
"v_cvt_pk_bf16_f32 v39, v86, v87\n" \
"s_waitcnt vmcnt(5)\n" \
"v_accvgpr_read_b32 v64, a36\n" \
"v_accvgpr_read_b32 v68, a44\n" \
"v_accvgpr_read_b32 v65, a37\n" \
"v_accvgpr_read_b32 v69, a45\n" \
"v_accvgpr_read_b32 v66, a38\n" \
"v_accvgpr_read_b32 v70, a46\n" \
"v_accvgpr_read_b32 v67, a39\n" \
"v_accvgpr_read_b32 v71, a47\n" \
"v_pk_mul_f32 v[64:65], v[64:65], v[188:189]\n" \
"v_pk_mul_f32 v[66:67], v[66:67], v[188:189]\n" \
"v_pk_mul_f32 v[68:69], v[68:69], v[188:189]\n" \
"v_pk_mul_f32 v[70:71], v[70:71], v[188:189]\n" \
"v_lshlrev_b32 v72, 16, v40\n" \
"v_and_b32 v73, 0xffff0000, v40\n" \
"v_lshlrev_b32 v74, 16, v41\n" \
"v_and_b32 v75, 0xffff0000, v41\n" \
"v_lshlrev_b32 v76, 16, v42\n" \
"v_and_b32 v77, 0xffff0000, v42\n" \
"v_lshlrev_b32 v78, 16, v43\n" \
"v_and_b32 v79, 0xffff0000, v43\n" \
"v_pk_fma_f32 v[64:65], v[72:73], v[192:193], v[64:65]\n" \
"v_pk_fma_f32 v[66:67], v[74:75], v[192:193], v[66:67]\n" \
"v_pk_fma_f32 v[68:69], v[76:77], v[192:193], v[68:69]\n" \
"v_pk_fma_f32 v[70:71], v[78:79], v[192:193], v[70:71]\n" \
"v_cvt_pk_bf16_f32 v40, v64, v65\n" \
"v_cvt_pk_bf16_f32 v41, v66, v67\n" \
"v_cvt_pk_bf16_f32 v42, v68, v69\n" \
"v_cvt_pk_bf16_f32 v43, v70, v71\n" \
"s_waitcnt vmcnt(4)\n" \
"v_accvgpr_read_b32 v80, a52\n" \
"v_accvgpr_read_b32 v84, a60\n" \
"v_accvgpr_read_b32 v81, a53\n" \
"v_accvgpr_read_b32 v85, a61\n" \
"v_accvgpr_read_b32 v82, a54\n" \
"v_accvgpr_read_b32 v86, a62\n" \
"v_accvgpr_read_b32 v83, a55\n" \
"v_accvgpr_read_b32 v87, a63\n" \
"v_pk_mul_f32 v[80:81], v[80:81], v[188:189]\n" \
"v_pk_mul_f32 v[82:83], v[82:83], v[188:189]\n" \
"v_pk_mul_f32 v[84:85], v[84:85], v[188:189]\n" \
"v_pk_mul_f32 v[86:87], v[86:87], v[188:189]\n" \
"v_lshlrev_b32 v88, 16, v44\n" \
"v_and_b32 v89, 0xffff0000, v44\n" \
"v_lshlrev_b32 v90, 16, v45\n" \
"v_and_b32 v91, 0xffff0000, v45\n" \
"v_lshlrev_b32 v92, 16, v46\n" \
"v_and_b32 v93, 0xffff0000, v46\n" \
"v_lshlrev_b32 v94, 16, v47\n" \
"v_and_b32 v95, 0xffff0000, v47\n" \
"v_pk_fma_f32 v[80:81], v[88:89], v[192:193], v[80:81]\n" \
"v_pk_fma_f32 v[82:83], v[90:91], v[192:193], v[82:83]\n" \
"v_pk_fma_f32 v[84:85], v[92:93], v[192:193], v[84:85]\n" \
"v_pk_fma_f32 v[86:87], v[94:95], v[192:193], v[86:87]\n" \
"v_cvt_pk_bf16_f32 v44, v80, v81\n" \
"v_cvt_pk_bf16_f32 v45, v82, v83\n" \
"v_cvt_pk_bf16_f32 v46, v84, v85\n" \
"v_cvt_pk_bf16_f32 v47, v86, v87\n" \
"s_waitcnt vmcnt(3)\n" \
"v_accvgpr_read_b32 v64, a68\n" \
"v_accvgpr_read_b32 v68, a76\n" \
"v_accvgpr_read_b32 v65, a69\n" \
"v_accvgpr_read_b32 v69, a77\n" \
"v_accvgpr_read_b32 v66, a70\n" \
"v_accvgpr_read_b32 v70, a78\n" \
"v_accvgpr_read_b32 v67, a71\n" \
"v_accvgpr_read_b32 v71, a79\n" \
"v_pk_mul_f32 v[64:65], v[64:65], v[188:189]\n" \
"v_pk_mul_f32 v[66:67], v[66:67], v[188:189]\n" \
"v_pk_mul_f32 v[68:69], v[68:69], v[188:189]\n" \
"v_pk_mul_f32 v[70:71], v[70:71], v[188:189]\n" \
"v_lshlrev_b32 v72, 16, v48\n" \
"v_and_b32 v73, 0xffff0000, v48\n" \
"v_lshlrev_b32 v74, 16, v49\n" \
"v_and_b32 v75, 0xffff0000, v49\n" \
"v_lshlrev_b32 v76, 16, v50\n" \
"v_and_b32 v77, 0xffff0000, v50\n" \
"v_lshlrev_b32 v78, 16, v51\n" \
"v_and_b32 v79, 0xffff0000, v51\n" \
"v_pk_fma_f32 v[64:65], v[72:73], v[192:193], v[64:65]\n" \
"v_pk_fma_f32 v[66:67], v[74:75], v[192:193], v[66:67]\n" \
"v_pk_fma_f32 v[68:69], v[76:77], v[192:193], v[68:69]\n" \
"v_pk_fma_f32 v[70:71], v[78:79], v[192:193], v[70:71]\n" \
"v_cvt_pk_bf16_f32 v48, v64, v65\n" \
"v_cvt_pk_bf16_f32 v49, v66, v67\n" \
"v_cvt_pk_bf16_f32 v50, v68, v69\n" \
"v_cvt_pk_bf16_f32 v51, v70, v71\n" \
"s_waitcnt vmcnt(2)\n" \
"v_accvgpr_read_b32 v80, a84\n" \
"v_accvgpr_read_b32 v84, a92\n" \
"v_accvgpr_read_b32 v81, a85\n" \
"v_accvgpr_read_b32 v85, a93\n" \
"v_accvgpr_read_b32 v82, a86\n" \
"v_accvgpr_read_b32 v86, a94\n" \
"v_accvgpr_read_b32 v83, a87\n" \
"v_accvgpr_read_b32 v87, a95\n" \
"v_pk_mul_f32 v[80:81], v[80:81], v[188:189]\n" \
"v_pk_mul_f32 v[82:83], v[82:83], v[188:189]\n" \
"v_pk_mul_f32 v[84:85], v[84:85], v[188:189]\n" \
"v_pk_mul_f32 v[86:87], v[86:87], v[188:189]\n" \
"v_lshlrev_b32 v88, 16, v52\n" \
"v_and_b32 v89, 0xffff0000, v52\n" \
"v_lshlrev_b32 v90, 16, v53\n" \
"v_and_b32 v91, 0xffff0000, v53\n" \
"v_lshlrev_b32 v92, 16, v54\n" \
"v_and_b32 v93, 0xffff0000, v54\n" \
"v_lshlrev_b32 v94, 16, v55\n" \
"v_and_b32 v95, 0xffff0000, v55\n" \
"v_pk_fma_f32 v[80:81], v[88:89], v[192:193], v[80:81]\n" \
"v_pk_fma_f32 v[82:83], v[90:91], v[192:193], v[82:83]\n" \
"v_pk_fma_f32 v[84:85], v[92:93], v[192:193], v[84:85]\n" \
"v_pk_fma_f32 v[86:87], v[94:95], v[192:193], v[86:87]\n" \
"v_cvt_pk_bf16_f32 v52, v80, v81\n" \
"v_cvt_pk_bf16_f32 v53, v82, v83\n" \
"v_cvt_pk_bf16_f32 v54, v84, v85\n" \
"v_cvt_pk_bf16_f32 v55, v86, v87\n" \
"s_waitcnt vmcnt(1)\n" \
"v_accvgpr_read_b32 v64, a100\n" \
"v_accvgpr_read_b32 v68, a108\n" \
"v_accvgpr_read_b32 v65, a101\n" \
"v_accvgpr_read_b32 v69, a109\n" \
"v_accvgpr_read_b32 v66, a102\n" \
"v_accvgpr_read_b32 v70, a110\n" \
"v_accvgpr_read_b32 v67, a103\n" \
"v_accvgpr_read_b32 v71, a111\n" \
"v_pk_mul_f32 v[64:65], v[64:65], v[188:189]\n" \
"v_pk_mul_f32 v[66:67], v[66:67], v[188:189]\n" \
"v_pk_mul_f32 v[68:69], v[68:69], v[188:189]\n" \
"v_pk_mul_f32 v[70:71], v[70:71], v[188:189]\n" \
"v_lshlrev_b32 v72, 16, v56\n" \
"v_and_b32 v73, 0xffff0000, v56\n" \
"v_lshlrev_b32 v74, 16, v57\n" \
"v_and_b32 v75, 0xffff0000, v57\n" \
"v_lshlrev_b32 v76, 16, v58\n" \
"v_and_b32 v77, 0xffff0000, v58\n" \
"v_lshlrev_b32 v78, 16, v59\n" \
"v_and_b32 v79, 0xffff0000, v59\n" \
"v_pk_fma_f32 v[64:65], v[72:73], v[192:193], v[64:65]\n" \
"v_pk_fma_f32 v[66:67], v[74:75], v[192:193], v[66:67]\n" \
"v_pk_fma_f32 v[68:69], v[76:77], v[192:193], v[68:69]\n" \
"v_pk_fma_f32 v[70:71], v[78:79], v[192:193], v[70:71]\n" \
"v_cvt_pk_bf16_f32 v56, v64, v65\n" \
"v_cvt_pk_bf16_f32 v57, v66, v67\n" \
"v_cvt_pk_bf16_f32 v58, v68, v69\n" \
"v_cvt_pk_bf16_f32 v59, v70, v71\n" \
"s_waitcnt vmcnt(0)\n" \
"v_accvgpr_read_b32 v80, a116\n" \
"v_accvgpr_read_b32 v84, a124\n" \
"v_accvgpr_read_b32 v81, a117\n" \
"v_accvgpr_read_b32 v85, a125\n" \
"v_accvgpr_read_b32 v82, a118\n" \
"v_accvgpr_read_b32 v86, a126\n" \
"v_accvgpr_read_b32 v83, a119\n" \
"v_accvgpr_read_b32 v87, a127\n" \
"v_pk_mul_f32 v[80:81], v[80:81], v[188:189]\n" \
"v_pk_mul_f32 v[82:83], v[82:83], v[188:189]\n" \
"v_pk_mul_f32 v[84:85], v[84:85], v[188:189]\n" \
"v_pk_mul_f32 v[86:87], v[86:87], v[188:189]\n" \
"v_lshlrev_b32 v88, 16, v60\n" \
"v_and_b32 v89, 0xffff0000, v60\n" \
"v_lshlrev_b32 v90, 16, v61\n" \
"v_and_b32 v91, 0xffff0000, v61\n" \
"v_lshlrev_b32 v92, 16, v62\n" \
"v_and_b32 v93, 0xffff0000, v62\n" \
"v_lshlrev_b32 v94, 16, v63\n" \
"v_and_b32 v95, 0xffff0000, v63\n" \
"v_pk_fma_f32 v[80:81], v[88:89], v[192:193], v[80:81]\n" \
"v_pk_fma_f32 v[82:83], v[90:91], v[192:193], v[82:83]\n" \
"v_pk_fma_f32 v[84:85], v[92:93], v[192:193], v[84:85]\n" \
"v_pk_fma_f32 v[86:87], v[94:95], v[192:193], v[86:87]\n" \
"v_cvt_pk_bf16_f32 v60, v80, v81\n" \
"v_cvt_pk_bf16_f32 v61, v82, v83\n" \
"v_cvt_pk_bf16_f32 v62, v84, v85\n" \
"v_cvt_pk_bf16_f32 v63, v86, v87\n" \
"s_branch Lop%=\n" \
"Lnorm%=:\n" \
"v_max_f32 v194, 0x00800000, v170\n" \
"v_rcp_f32 v186, v194\n" \
"v_max_f32 v194, 0x00800000, v171\n" \
"v_rcp_f32 v188, v194\n" \
"v_mov_b32 v187, v186\n" \
"v_mov_b32 v189, v188\n" \
"s_cmp_eq_u32 s50, 0\n" \
"s_cbranch_scc1 Lpub%=\n" \
"v_accvgpr_read_b32 v64, a0\n" \
"v_accvgpr_read_b32 v68, a8\n" \
"v_accvgpr_read_b32 v65, a1\n" \
"v_accvgpr_read_b32 v69, a9\n" \
"v_accvgpr_read_b32 v66, a2\n" \
"v_accvgpr_read_b32 v70, a10\n" \
"v_accvgpr_read_b32 v67, a3\n" \
"v_accvgpr_read_b32 v71, a11\n" \
"v_pk_mul_f32 v[64:65], v[64:65], v[186:187]\n" \
"v_pk_mul_f32 v[66:67], v[66:67], v[186:187]\n" \
"v_pk_mul_f32 v[68:69], v[68:69], v[186:187]\n" \
"v_pk_mul_f32 v[70:71], v[70:71], v[186:187]\n" \
"v_cvt_pk_bf16_f32 v0, v64, v65\n" \
"v_cvt_pk_bf16_f32 v1, v66, v67\n" \
"v_cvt_pk_bf16_f32 v2, v68, v69\n" \
"v_cvt_pk_bf16_f32 v3, v70, v71\n" \
"v_accvgpr_read_b32 v72, a16\n" \
"v_accvgpr_read_b32 v76, a24\n" \
"v_accvgpr_read_b32 v73, a17\n" \
"v_accvgpr_read_b32 v77, a25\n" \
"v_accvgpr_read_b32 v74, a18\n" \
"v_accvgpr_read_b32 v78, a26\n" \
"v_accvgpr_read_b32 v75, a19\n" \
"v_accvgpr_read_b32 v79, a27\n" \
"v_pk_mul_f32 v[72:73], v[72:73], v[186:187]\n" \
"v_pk_mul_f32 v[74:75], v[74:75], v[186:187]\n" \
"v_pk_mul_f32 v[76:77], v[76:77], v[186:187]\n" \
"v_pk_mul_f32 v[78:79], v[78:79], v[186:187]\n" \
"v_cvt_pk_bf16_f32 v4, v72, v73\n" \
"v_cvt_pk_bf16_f32 v5, v74, v75\n" \
"v_cvt_pk_bf16_f32 v6, v76, v77\n" \
"v_cvt_pk_bf16_f32 v7, v78, v79\n" \
"v_accvgpr_read_b32 v64, a32\n" \
"v_accvgpr_read_b32 v68, a40\n" \
"v_accvgpr_read_b32 v65, a33\n" \
"v_accvgpr_read_b32 v69, a41\n" \
"v_accvgpr_read_b32 v66, a34\n" \
"v_accvgpr_read_b32 v70, a42\n" \
"v_accvgpr_read_b32 v67, a35\n" \
"v_accvgpr_read_b32 v71, a43\n" \
"v_pk_mul_f32 v[64:65], v[64:65], v[186:187]\n" \
"v_pk_mul_f32 v[66:67], v[66:67], v[186:187]\n" \
"v_pk_mul_f32 v[68:69], v[68:69], v[186:187]\n" \
"v_pk_mul_f32 v[70:71], v[70:71], v[186:187]\n" \
"v_cvt_pk_bf16_f32 v8, v64, v65\n" \
"v_cvt_pk_bf16_f32 v9, v66, v67\n" \
"v_cvt_pk_bf16_f32 v10, v68, v69\n" \
"v_cvt_pk_bf16_f32 v11, v70, v71\n" \
"v_accvgpr_read_b32 v72, a48\n" \
"v_accvgpr_read_b32 v76, a56\n" \
"v_accvgpr_read_b32 v73, a49\n" \
"v_accvgpr_read_b32 v77, a57\n" \
"v_accvgpr_read_b32 v74, a50\n" \
"v_accvgpr_read_b32 v78, a58\n" \
"v_accvgpr_read_b32 v75, a51\n" \
"v_accvgpr_read_b32 v79, a59\n" \
"v_pk_mul_f32 v[72:73], v[72:73], v[186:187]\n" \
"v_pk_mul_f32 v[74:75], v[74:75], v[186:187]\n" \
"v_pk_mul_f32 v[76:77], v[76:77], v[186:187]\n" \
"v_pk_mul_f32 v[78:79], v[78:79], v[186:187]\n" \
"v_cvt_pk_bf16_f32 v12, v72, v73\n" \
"v_cvt_pk_bf16_f32 v13, v74, v75\n" \
"v_cvt_pk_bf16_f32 v14, v76, v77\n" \
"v_cvt_pk_bf16_f32 v15, v78, v79\n" \
"v_accvgpr_read_b32 v64, a64\n" \
"v_accvgpr_read_b32 v68, a72\n" \
"v_accvgpr_read_b32 v65, a65\n" \
"v_accvgpr_read_b32 v69, a73\n" \
"v_accvgpr_read_b32 v66, a66\n" \
"v_accvgpr_read_b32 v70, a74\n" \
"v_accvgpr_read_b32 v67, a67\n" \
"v_accvgpr_read_b32 v71, a75\n" \
"v_pk_mul_f32 v[64:65], v[64:65], v[186:187]\n" \
"v_pk_mul_f32 v[66:67], v[66:67], v[186:187]\n" \
"v_pk_mul_f32 v[68:69], v[68:69], v[186:187]\n" \
"v_pk_mul_f32 v[70:71], v[70:71], v[186:187]\n" \
"v_cvt_pk_bf16_f32 v16, v64, v65\n" \
"v_cvt_pk_bf16_f32 v17, v66, v67\n" \
"v_cvt_pk_bf16_f32 v18, v68, v69\n" \
"v_cvt_pk_bf16_f32 v19, v70, v71\n" \
"v_accvgpr_read_b32 v72, a80\n" \
"v_accvgpr_read_b32 v76, a88\n" \
"v_accvgpr_read_b32 v73, a81\n" \
"v_accvgpr_read_b32 v77, a89\n" \
"v_accvgpr_read_b32 v74, a82\n" \
"v_accvgpr_read_b32 v78, a90\n" \
"v_accvgpr_read_b32 v75, a83\n" \
"v_accvgpr_read_b32 v79, a91\n" \
"v_pk_mul_f32 v[72:73], v[72:73], v[186:187]\n" \
"v_pk_mul_f32 v[74:75], v[74:75], v[186:187]\n" \
"v_pk_mul_f32 v[76:77], v[76:77], v[186:187]\n" \
"v_pk_mul_f32 v[78:79], v[78:79], v[186:187]\n" \
"v_cvt_pk_bf16_f32 v20, v72, v73\n" \
"v_cvt_pk_bf16_f32 v21, v74, v75\n" \
"v_cvt_pk_bf16_f32 v22, v76, v77\n" \
"v_cvt_pk_bf16_f32 v23, v78, v79\n" \
"v_accvgpr_read_b32 v64, a96\n" \
"v_accvgpr_read_b32 v68, a104\n" \
"v_accvgpr_read_b32 v65, a97\n" \
"v_accvgpr_read_b32 v69, a105\n" \
"v_accvgpr_read_b32 v66, a98\n" \
"v_accvgpr_read_b32 v70, a106\n" \
"v_accvgpr_read_b32 v67, a99\n" \
"v_accvgpr_read_b32 v71, a107\n" \
"v_pk_mul_f32 v[64:65], v[64:65], v[186:187]\n" \
"v_pk_mul_f32 v[66:67], v[66:67], v[186:187]\n" \
"v_pk_mul_f32 v[68:69], v[68:69], v[186:187]\n" \
"v_pk_mul_f32 v[70:71], v[70:71], v[186:187]\n" \
"v_cvt_pk_bf16_f32 v24, v64, v65\n" \
"v_cvt_pk_bf16_f32 v25, v66, v67\n" \
"v_cvt_pk_bf16_f32 v26, v68, v69\n" \
"v_cvt_pk_bf16_f32 v27, v70, v71\n" \
"v_accvgpr_read_b32 v72, a112\n" \
"v_accvgpr_read_b32 v76, a120\n" \
"v_accvgpr_read_b32 v73, a113\n" \
"v_accvgpr_read_b32 v77, a121\n" \
"v_accvgpr_read_b32 v74, a114\n" \
"v_accvgpr_read_b32 v78, a122\n" \
"v_accvgpr_read_b32 v75, a115\n" \
"v_accvgpr_read_b32 v79, a123\n" \
"v_pk_mul_f32 v[72:73], v[72:73], v[186:187]\n" \
"v_pk_mul_f32 v[74:75], v[74:75], v[186:187]\n" \
"v_pk_mul_f32 v[76:77], v[76:77], v[186:187]\n" \
"v_pk_mul_f32 v[78:79], v[78:79], v[186:187]\n" \
"v_cvt_pk_bf16_f32 v28, v72, v73\n" \
"v_cvt_pk_bf16_f32 v29, v74, v75\n" \
"v_cvt_pk_bf16_f32 v30, v76, v77\n" \
"v_cvt_pk_bf16_f32 v31, v78, v79\n" \
"v_accvgpr_read_b32 v64, a4\n" \
"v_accvgpr_read_b32 v68, a12\n" \
"v_accvgpr_read_b32 v65, a5\n" \
"v_accvgpr_read_b32 v69, a13\n" \
"v_accvgpr_read_b32 v66, a6\n" \
"v_accvgpr_read_b32 v70, a14\n" \
"v_accvgpr_read_b32 v67, a7\n" \
"v_accvgpr_read_b32 v71, a15\n" \
"v_pk_mul_f32 v[64:65], v[64:65], v[188:189]\n" \
"v_pk_mul_f32 v[66:67], v[66:67], v[188:189]\n" \
"v_pk_mul_f32 v[68:69], v[68:69], v[188:189]\n" \
"v_pk_mul_f32 v[70:71], v[70:71], v[188:189]\n" \
"v_cvt_pk_bf16_f32 v32, v64, v65\n" \
"v_cvt_pk_bf16_f32 v33, v66, v67\n" \
"v_cvt_pk_bf16_f32 v34, v68, v69\n" \
"v_cvt_pk_bf16_f32 v35, v70, v71\n" \
"v_accvgpr_read_b32 v72, a20\n" \
"v_accvgpr_read_b32 v76, a28\n" \
"v_accvgpr_read_b32 v73, a21\n" \
"v_accvgpr_read_b32 v77, a29\n" \
"v_accvgpr_read_b32 v74, a22\n" \
"v_accvgpr_read_b32 v78, a30\n" \
"v_accvgpr_read_b32 v75, a23\n" \
"v_accvgpr_read_b32 v79, a31\n" \
"v_pk_mul_f32 v[72:73], v[72:73], v[188:189]\n" \
"v_pk_mul_f32 v[74:75], v[74:75], v[188:189]\n" \
"v_pk_mul_f32 v[76:77], v[76:77], v[188:189]\n" \
"v_pk_mul_f32 v[78:79], v[78:79], v[188:189]\n" \
"v_cvt_pk_bf16_f32 v36, v72, v73\n" \
"v_cvt_pk_bf16_f32 v37, v74, v75\n" \
"v_cvt_pk_bf16_f32 v38, v76, v77\n" \
"v_cvt_pk_bf16_f32 v39, v78, v79\n" \
"v_accvgpr_read_b32 v64, a36\n" \
"v_accvgpr_read_b32 v68, a44\n" \
"v_accvgpr_read_b32 v65, a37\n" \
"v_accvgpr_read_b32 v69, a45\n" \
"v_accvgpr_read_b32 v66, a38\n" \
"v_accvgpr_read_b32 v70, a46\n" \
"v_accvgpr_read_b32 v67, a39\n" \
"v_accvgpr_read_b32 v71, a47\n" \
"v_pk_mul_f32 v[64:65], v[64:65], v[188:189]\n" \
"v_pk_mul_f32 v[66:67], v[66:67], v[188:189]\n" \
"v_pk_mul_f32 v[68:69], v[68:69], v[188:189]\n" \
"v_pk_mul_f32 v[70:71], v[70:71], v[188:189]\n" \
"v_cvt_pk_bf16_f32 v40, v64, v65\n" \
"v_cvt_pk_bf16_f32 v41, v66, v67\n" \
"v_cvt_pk_bf16_f32 v42, v68, v69\n" \
"v_cvt_pk_bf16_f32 v43, v70, v71\n" \
"v_accvgpr_read_b32 v72, a52\n" \
"v_accvgpr_read_b32 v76, a60\n" \
"v_accvgpr_read_b32 v73, a53\n" \
"v_accvgpr_read_b32 v77, a61\n" \
"v_accvgpr_read_b32 v74, a54\n" \
"v_accvgpr_read_b32 v78, a62\n" \
"v_accvgpr_read_b32 v75, a55\n" \
"v_accvgpr_read_b32 v79, a63\n" \
"v_pk_mul_f32 v[72:73], v[72:73], v[188:189]\n" \
"v_pk_mul_f32 v[74:75], v[74:75], v[188:189]\n" \
"v_pk_mul_f32 v[76:77], v[76:77], v[188:189]\n" \
"v_pk_mul_f32 v[78:79], v[78:79], v[188:189]\n" \
"v_cvt_pk_bf16_f32 v44, v72, v73\n" \
"v_cvt_pk_bf16_f32 v45, v74, v75\n" \
"v_cvt_pk_bf16_f32 v46, v76, v77\n" \
"v_cvt_pk_bf16_f32 v47, v78, v79\n" \
"v_accvgpr_read_b32 v64, a68\n" \
"v_accvgpr_read_b32 v68, a76\n" \
"v_accvgpr_read_b32 v65, a69\n" \
"v_accvgpr_read_b32 v69, a77\n" \
"v_accvgpr_read_b32 v66, a70\n" \
"v_accvgpr_read_b32 v70, a78\n" \
"v_accvgpr_read_b32 v67, a71\n" \
"v_accvgpr_read_b32 v71, a79\n" \
"v_pk_mul_f32 v[64:65], v[64:65], v[188:189]\n" \
"v_pk_mul_f32 v[66:67], v[66:67], v[188:189]\n" \
"v_pk_mul_f32 v[68:69], v[68:69], v[188:189]\n" \
"v_pk_mul_f32 v[70:71], v[70:71], v[188:189]\n" \
"v_cvt_pk_bf16_f32 v48, v64, v65\n" \
"v_cvt_pk_bf16_f32 v49, v66, v67\n" \
"v_cvt_pk_bf16_f32 v50, v68, v69\n" \
"v_cvt_pk_bf16_f32 v51, v70, v71\n" \
"v_accvgpr_read_b32 v72, a84\n" \
"v_accvgpr_read_b32 v76, a92\n" \
"v_accvgpr_read_b32 v73, a85\n" \
"v_accvgpr_read_b32 v77, a93\n" \
"v_accvgpr_read_b32 v74, a86\n" \
"v_accvgpr_read_b32 v78, a94\n" \
"v_accvgpr_read_b32 v75, a87\n" \
"v_accvgpr_read_b32 v79, a95\n" \
"v_pk_mul_f32 v[72:73], v[72:73], v[188:189]\n" \
"v_pk_mul_f32 v[74:75], v[74:75], v[188:189]\n" \
"v_pk_mul_f32 v[76:77], v[76:77], v[188:189]\n" \
"v_pk_mul_f32 v[78:79], v[78:79], v[188:189]\n" \
"v_cvt_pk_bf16_f32 v52, v72, v73\n" \
"v_cvt_pk_bf16_f32 v53, v74, v75\n" \
"v_cvt_pk_bf16_f32 v54, v76, v77\n" \
"v_cvt_pk_bf16_f32 v55, v78, v79\n" \
"v_accvgpr_read_b32 v64, a100\n" \
"v_accvgpr_read_b32 v68, a108\n" \
"v_accvgpr_read_b32 v65, a101\n" \
"v_accvgpr_read_b32 v69, a109\n" \
"v_accvgpr_read_b32 v66, a102\n" \
"v_accvgpr_read_b32 v70, a110\n" \
"v_accvgpr_read_b32 v67, a103\n" \
"v_accvgpr_read_b32 v71, a111\n" \
"v_pk_mul_f32 v[64:65], v[64:65], v[188:189]\n" \
"v_pk_mul_f32 v[66:67], v[66:67], v[188:189]\n" \
"v_pk_mul_f32 v[68:69], v[68:69], v[188:189]\n" \
"v_pk_mul_f32 v[70:71], v[70:71], v[188:189]\n" \
"v_cvt_pk_bf16_f32 v56, v64, v65\n" \
"v_cvt_pk_bf16_f32 v57, v66, v67\n" \
"v_cvt_pk_bf16_f32 v58, v68, v69\n" \
"v_cvt_pk_bf16_f32 v59, v70, v71\n" \
"v_accvgpr_read_b32 v72, a116\n" \
"v_accvgpr_read_b32 v76, a124\n" \
"v_accvgpr_read_b32 v73, a117\n" \
"v_accvgpr_read_b32 v77, a125\n" \
"v_accvgpr_read_b32 v74, a118\n" \
"v_accvgpr_read_b32 v78, a126\n" \
"v_accvgpr_read_b32 v75, a119\n" \
"v_accvgpr_read_b32 v79, a127\n" \
"v_pk_mul_f32 v[72:73], v[72:73], v[188:189]\n" \
"v_pk_mul_f32 v[74:75], v[74:75], v[188:189]\n" \
"v_pk_mul_f32 v[76:77], v[76:77], v[188:189]\n" \
"v_pk_mul_f32 v[78:79], v[78:79], v[188:189]\n" \
"v_cvt_pk_bf16_f32 v60, v72, v73\n" \
"v_cvt_pk_bf16_f32 v61, v74, v75\n" \
"v_cvt_pk_bf16_f32 v62, v76, v77\n" \
"v_cvt_pk_bf16_f32 v63, v78, v79\n" \
"s_branch Lop%=\n" \
"Lpub%=:\n" \
"v_accvgpr_read_b32 v64, a0\n" \
"v_accvgpr_read_b32 v68, a8\n" \
"v_accvgpr_read_b32 v65, a1\n" \
"v_accvgpr_read_b32 v69, a9\n" \
"v_accvgpr_read_b32 v66, a2\n" \
"v_accvgpr_read_b32 v70, a10\n" \
"v_accvgpr_read_b32 v67, a3\n" \
"v_accvgpr_read_b32 v71, a11\n" \
"v_pk_mul_f32 v[64:65], v[64:65], v[186:187]\n" \
"v_pk_mul_f32 v[66:67], v[66:67], v[186:187]\n" \
"v_pk_mul_f32 v[68:69], v[68:69], v[186:187]\n" \
"v_pk_mul_f32 v[70:71], v[70:71], v[186:187]\n" \
"v_cvt_pk_bf16_f32 v0, v64, v65\n" \
"v_cvt_pk_bf16_f32 v1, v66, v67\n" \
"v_cvt_pk_bf16_f32 v2, v68, v69\n" \
"v_cvt_pk_bf16_f32 v3, v70, v71\n" \
"s_add_u32 s83, s61, 0\n" \
"buffer_store_dwordx4 v[0:3], v224, %[rs], s83 offen offset:0 sc1\n" \
"v_accvgpr_read_b32 v72, a16\n" \
"v_accvgpr_read_b32 v76, a24\n" \
"v_accvgpr_read_b32 v73, a17\n" \
"v_accvgpr_read_b32 v77, a25\n" \
"v_accvgpr_read_b32 v74, a18\n" \
"v_accvgpr_read_b32 v78, a26\n" \
"v_accvgpr_read_b32 v75, a19\n" \
"v_accvgpr_read_b32 v79, a27\n" \
"v_pk_mul_f32 v[72:73], v[72:73], v[186:187]\n" \
"v_pk_mul_f32 v[74:75], v[74:75], v[186:187]\n" \
"v_pk_mul_f32 v[76:77], v[76:77], v[186:187]\n" \
"v_pk_mul_f32 v[78:79], v[78:79], v[186:187]\n" \
"v_cvt_pk_bf16_f32 v4, v72, v73\n" \
"v_cvt_pk_bf16_f32 v5, v74, v75\n" \
"v_cvt_pk_bf16_f32 v6, v76, v77\n" \
"v_cvt_pk_bf16_f32 v7, v78, v79\n" \
"buffer_store_dwordx4 v[4:7], v224, %[rs], s83 offen offset:1024 sc1\n" \
"v_accvgpr_read_b32 v64, a32\n" \
"v_accvgpr_read_b32 v68, a40\n" \
"v_accvgpr_read_b32 v65, a33\n" \
"v_accvgpr_read_b32 v69, a41\n" \
"v_accvgpr_read_b32 v66, a34\n" \
"v_accvgpr_read_b32 v70, a42\n" \
"v_accvgpr_read_b32 v67, a35\n" \
"v_accvgpr_read_b32 v71, a43\n" \
"v_pk_mul_f32 v[64:65], v[64:65], v[186:187]\n" \
"v_pk_mul_f32 v[66:67], v[66:67], v[186:187]\n" \
"v_pk_mul_f32 v[68:69], v[68:69], v[186:187]\n" \
"v_pk_mul_f32 v[70:71], v[70:71], v[186:187]\n" \
"v_cvt_pk_bf16_f32 v8, v64, v65\n" \
"v_cvt_pk_bf16_f32 v9, v66, v67\n" \
"v_cvt_pk_bf16_f32 v10, v68, v69\n" \
"v_cvt_pk_bf16_f32 v11, v70, v71\n" \
"buffer_store_dwordx4 v[8:11], v224, %[rs], s83 offen offset:2048 sc1\n" \
"v_accvgpr_read_b32 v72, a48\n" \
"v_accvgpr_read_b32 v76, a56\n" \
"v_accvgpr_read_b32 v73, a49\n" \
"v_accvgpr_read_b32 v77, a57\n" \
"v_accvgpr_read_b32 v74, a50\n" \
"v_accvgpr_read_b32 v78, a58\n" \
"v_accvgpr_read_b32 v75, a51\n" \
"v_accvgpr_read_b32 v79, a59\n" \
"v_pk_mul_f32 v[72:73], v[72:73], v[186:187]\n" \
"v_pk_mul_f32 v[74:75], v[74:75], v[186:187]\n" \
"v_pk_mul_f32 v[76:77], v[76:77], v[186:187]\n" \
"v_pk_mul_f32 v[78:79], v[78:79], v[186:187]\n" \
"v_cvt_pk_bf16_f32 v12, v72, v73\n" \
"v_cvt_pk_bf16_f32 v13, v74, v75\n" \
"v_cvt_pk_bf16_f32 v14, v76, v77\n" \
"v_cvt_pk_bf16_f32 v15, v78, v79\n" \
"buffer_store_dwordx4 v[12:15], v224, %[rs], s83 offen offset:3072 sc1\n" \
"v_accvgpr_read_b32 v64, a64\n" \
"v_accvgpr_read_b32 v68, a72\n" \
"v_accvgpr_read_b32 v65, a65\n" \
"v_accvgpr_read_b32 v69, a73\n" \
"v_accvgpr_read_b32 v66, a66\n" \
"v_accvgpr_read_b32 v70, a74\n" \
"v_accvgpr_read_b32 v67, a67\n" \
"v_accvgpr_read_b32 v71, a75\n" \
"v_pk_mul_f32 v[64:65], v[64:65], v[186:187]\n" \
"v_pk_mul_f32 v[66:67], v[66:67], v[186:187]\n" \
"v_pk_mul_f32 v[68:69], v[68:69], v[186:187]\n" \
"v_pk_mul_f32 v[70:71], v[70:71], v[186:187]\n" \
"v_cvt_pk_bf16_f32 v16, v64, v65\n" \
"v_cvt_pk_bf16_f32 v17, v66, v67\n" \
"v_cvt_pk_bf16_f32 v18, v68, v69\n" \
"v_cvt_pk_bf16_f32 v19, v70, v71\n" \
"s_add_u32 s83, s61, 4096\n" \
"buffer_store_dwordx4 v[16:19], v224, %[rs], s83 offen offset:0 sc1\n" \
"v_accvgpr_read_b32 v72, a80\n" \
"v_accvgpr_read_b32 v76, a88\n" \
"v_accvgpr_read_b32 v73, a81\n" \
"v_accvgpr_read_b32 v77, a89\n" \
"v_accvgpr_read_b32 v74, a82\n" \
"v_accvgpr_read_b32 v78, a90\n" \
"v_accvgpr_read_b32 v75, a83\n" \
"v_accvgpr_read_b32 v79, a91\n" \
"v_pk_mul_f32 v[72:73], v[72:73], v[186:187]\n" \
"v_pk_mul_f32 v[74:75], v[74:75], v[186:187]\n" \
"v_pk_mul_f32 v[76:77], v[76:77], v[186:187]\n" \
"v_pk_mul_f32 v[78:79], v[78:79], v[186:187]\n" \
"v_cvt_pk_bf16_f32 v20, v72, v73\n" \
"v_cvt_pk_bf16_f32 v21, v74, v75\n" \
"v_cvt_pk_bf16_f32 v22, v76, v77\n" \
"v_cvt_pk_bf16_f32 v23, v78, v79\n" \
"buffer_store_dwordx4 v[20:23], v224, %[rs], s83 offen offset:1024 sc1\n" \
"v_accvgpr_read_b32 v64, a96\n" \
"v_accvgpr_read_b32 v68, a104\n" \
"v_accvgpr_read_b32 v65, a97\n" \
"v_accvgpr_read_b32 v69, a105\n" \
"v_accvgpr_read_b32 v66, a98\n" \
"v_accvgpr_read_b32 v70, a106\n" \
"v_accvgpr_read_b32 v67, a99\n" \
"v_accvgpr_read_b32 v71, a107\n" \
"v_pk_mul_f32 v[64:65], v[64:65], v[186:187]\n" \
"v_pk_mul_f32 v[66:67], v[66:67], v[186:187]\n" \
"v_pk_mul_f32 v[68:69], v[68:69], v[186:187]\n" \
"v_pk_mul_f32 v[70:71], v[70:71], v[186:187]\n" \
"v_cvt_pk_bf16_f32 v24, v64, v65\n" \
"v_cvt_pk_bf16_f32 v25, v66, v67\n" \
"v_cvt_pk_bf16_f32 v26, v68, v69\n" \
"v_cvt_pk_bf16_f32 v27, v70, v71\n" \
"buffer_store_dwordx4 v[24:27], v224, %[rs], s83 offen offset:2048 sc1\n" \
"v_accvgpr_read_b32 v72, a112\n" \
"v_accvgpr_read_b32 v76, a120\n" \
"v_accvgpr_read_b32 v73, a113\n" \
"v_accvgpr_read_b32 v77, a121\n" \
"v_accvgpr_read_b32 v74, a114\n" \
"v_accvgpr_read_b32 v78, a122\n" \
"v_accvgpr_read_b32 v75, a115\n" \
"v_accvgpr_read_b32 v79, a123\n" \
"v_pk_mul_f32 v[72:73], v[72:73], v[186:187]\n" \
"v_pk_mul_f32 v[74:75], v[74:75], v[186:187]\n" \
"v_pk_mul_f32 v[76:77], v[76:77], v[186:187]\n" \
"v_pk_mul_f32 v[78:79], v[78:79], v[186:187]\n" \
"v_cvt_pk_bf16_f32 v28, v72, v73\n" \
"v_cvt_pk_bf16_f32 v29, v74, v75\n" \
"v_cvt_pk_bf16_f32 v30, v76, v77\n" \
"v_cvt_pk_bf16_f32 v31, v78, v79\n" \
"buffer_store_dwordx4 v[28:31], v224, %[rs], s83 offen offset:3072 sc1\n" \
"v_accvgpr_read_b32 v64, a4\n" \
"v_accvgpr_read_b32 v68, a12\n" \
"v_accvgpr_read_b32 v65, a5\n" \
"v_accvgpr_read_b32 v69, a13\n" \
"v_accvgpr_read_b32 v66, a6\n" \
"v_accvgpr_read_b32 v70, a14\n" \
"v_accvgpr_read_b32 v67, a7\n" \
"v_accvgpr_read_b32 v71, a15\n" \
"v_pk_mul_f32 v[64:65], v[64:65], v[188:189]\n" \
"v_pk_mul_f32 v[66:67], v[66:67], v[188:189]\n" \
"v_pk_mul_f32 v[68:69], v[68:69], v[188:189]\n" \
"v_pk_mul_f32 v[70:71], v[70:71], v[188:189]\n" \
"v_cvt_pk_bf16_f32 v32, v64, v65\n" \
"v_cvt_pk_bf16_f32 v33, v66, v67\n" \
"v_cvt_pk_bf16_f32 v34, v68, v69\n" \
"v_cvt_pk_bf16_f32 v35, v70, v71\n" \
"s_add_u32 s83, s61, 8192\n" \
"buffer_store_dwordx4 v[32:35], v224, %[rs], s83 offen offset:0 sc1\n" \
"v_accvgpr_read_b32 v72, a20\n" \
"v_accvgpr_read_b32 v76, a28\n" \
"v_accvgpr_read_b32 v73, a21\n" \
"v_accvgpr_read_b32 v77, a29\n" \
"v_accvgpr_read_b32 v74, a22\n" \
"v_accvgpr_read_b32 v78, a30\n" \
"v_accvgpr_read_b32 v75, a23\n" \
"v_accvgpr_read_b32 v79, a31\n" \
"v_pk_mul_f32 v[72:73], v[72:73], v[188:189]\n" \
"v_pk_mul_f32 v[74:75], v[74:75], v[188:189]\n" \
"v_pk_mul_f32 v[76:77], v[76:77], v[188:189]\n" \
"v_pk_mul_f32 v[78:79], v[78:79], v[188:189]\n" \
"v_cvt_pk_bf16_f32 v36, v72, v73\n" \
"v_cvt_pk_bf16_f32 v37, v74, v75\n" \
"v_cvt_pk_bf16_f32 v38, v76, v77\n" \
"v_cvt_pk_bf16_f32 v39, v78, v79\n" \
"buffer_store_dwordx4 v[36:39], v224, %[rs], s83 offen offset:1024 sc1\n" \
"v_accvgpr_read_b32 v64, a36\n" \
"v_accvgpr_read_b32 v68, a44\n" \
"v_accvgpr_read_b32 v65, a37\n" \
"v_accvgpr_read_b32 v69, a45\n" \
"v_accvgpr_read_b32 v66, a38\n" \
"v_accvgpr_read_b32 v70, a46\n" \
"v_accvgpr_read_b32 v67, a39\n" \
"v_accvgpr_read_b32 v71, a47\n" \
"v_pk_mul_f32 v[64:65], v[64:65], v[188:189]\n" \
"v_pk_mul_f32 v[66:67], v[66:67], v[188:189]\n" \
"v_pk_mul_f32 v[68:69], v[68:69], v[188:189]\n" \
"v_pk_mul_f32 v[70:71], v[70:71], v[188:189]\n" \
"v_cvt_pk_bf16_f32 v40, v64, v65\n" \
"v_cvt_pk_bf16_f32 v41, v66, v67\n" \
"v_cvt_pk_bf16_f32 v42, v68, v69\n" \
"v_cvt_pk_bf16_f32 v43, v70, v71\n" \
"buffer_store_dwordx4 v[40:43], v224, %[rs], s83 offen offset:2048 sc1\n" \
"v_accvgpr_read_b32 v72, a52\n" \
"v_accvgpr_read_b32 v76, a60\n" \
"v_accvgpr_read_b32 v73, a53\n" \
"v_accvgpr_read_b32 v77, a61\n" \
"v_accvgpr_read_b32 v74, a54\n" \
"v_accvgpr_read_b32 v78, a62\n" \
"v_accvgpr_read_b32 v75, a55\n" \
"v_accvgpr_read_b32 v79, a63\n" \
"v_pk_mul_f32 v[72:73], v[72:73], v[188:189]\n" \
"v_pk_mul_f32 v[74:75], v[74:75], v[188:189]\n" \
"v_pk_mul_f32 v[76:77], v[76:77], v[188:189]\n" \
"v_pk_mul_f32 v[78:79], v[78:79], v[188:189]\n" \
"v_cvt_pk_bf16_f32 v44, v72, v73\n" \
"v_cvt_pk_bf16_f32 v45, v74, v75\n" \
"v_cvt_pk_bf16_f32 v46, v76, v77\n" \
"v_cvt_pk_bf16_f32 v47, v78, v79\n" \
"buffer_store_dwordx4 v[44:47], v224, %[rs], s83 offen offset:3072 sc1\n" \
"v_accvgpr_read_b32 v64, a68\n" \
"v_accvgpr_read_b32 v68, a76\n" \
"v_accvgpr_read_b32 v65, a69\n" \
"v_accvgpr_read_b32 v69, a77\n" \
"v_accvgpr_read_b32 v66, a70\n" \
"v_accvgpr_read_b32 v70, a78\n" \
"v_accvgpr_read_b32 v67, a71\n" \
"v_accvgpr_read_b32 v71, a79\n" \
"v_pk_mul_f32 v[64:65], v[64:65], v[188:189]\n" \
"v_pk_mul_f32 v[66:67], v[66:67], v[188:189]\n" \
"v_pk_mul_f32 v[68:69], v[68:69], v[188:189]\n" \
"v_pk_mul_f32 v[70:71], v[70:71], v[188:189]\n" \
"v_cvt_pk_bf16_f32 v48, v64, v65\n" \
"v_cvt_pk_bf16_f32 v49, v66, v67\n" \
"v_cvt_pk_bf16_f32 v50, v68, v69\n" \
"v_cvt_pk_bf16_f32 v51, v70, v71\n" \
"s_add_u32 s83, s61, 12288\n" \
"buffer_store_dwordx4 v[48:51], v224, %[rs], s83 offen offset:0 sc1\n" \
"v_accvgpr_read_b32 v72, a84\n" \
"v_accvgpr_read_b32 v76, a92\n" \
"v_accvgpr_read_b32 v73, a85\n" \
"v_accvgpr_read_b32 v77, a93\n" \
"v_accvgpr_read_b32 v74, a86\n" \
"v_accvgpr_read_b32 v78, a94\n" \
"v_accvgpr_read_b32 v75, a87\n" \
"v_accvgpr_read_b32 v79, a95\n" \
"v_pk_mul_f32 v[72:73], v[72:73], v[188:189]\n" \
"v_pk_mul_f32 v[74:75], v[74:75], v[188:189]\n" \
"v_pk_mul_f32 v[76:77], v[76:77], v[188:189]\n" \
"v_pk_mul_f32 v[78:79], v[78:79], v[188:189]\n" \
"v_cvt_pk_bf16_f32 v52, v72, v73\n" \
"v_cvt_pk_bf16_f32 v53, v74, v75\n" \
"v_cvt_pk_bf16_f32 v54, v76, v77\n" \
"v_cvt_pk_bf16_f32 v55, v78, v79\n" \
"buffer_store_dwordx4 v[52:55], v224, %[rs], s83 offen offset:1024 sc1\n" \
"v_accvgpr_read_b32 v64, a100\n" \
"v_accvgpr_read_b32 v68, a108\n" \
"v_accvgpr_read_b32 v65, a101\n" \
"v_accvgpr_read_b32 v69, a109\n" \
"v_accvgpr_read_b32 v66, a102\n" \
"v_accvgpr_read_b32 v70, a110\n" \
"v_accvgpr_read_b32 v67, a103\n" \
"v_accvgpr_read_b32 v71, a111\n" \
"v_pk_mul_f32 v[64:65], v[64:65], v[188:189]\n" \
"v_pk_mul_f32 v[66:67], v[66:67], v[188:189]\n" \
"v_pk_mul_f32 v[68:69], v[68:69], v[188:189]\n" \
"v_pk_mul_f32 v[70:71], v[70:71], v[188:189]\n" \
"v_cvt_pk_bf16_f32 v56, v64, v65\n" \
"v_cvt_pk_bf16_f32 v57, v66, v67\n" \
"v_cvt_pk_bf16_f32 v58, v68, v69\n" \
"v_cvt_pk_bf16_f32 v59, v70, v71\n" \
"buffer_store_dwordx4 v[56:59], v224, %[rs], s83 offen offset:2048 sc1\n" \
"v_accvgpr_read_b32 v72, a116\n" \
"v_accvgpr_read_b32 v76, a124\n" \
"v_accvgpr_read_b32 v73, a117\n" \
"v_accvgpr_read_b32 v77, a125\n" \
"v_accvgpr_read_b32 v74, a118\n" \
"v_accvgpr_read_b32 v78, a126\n" \
"v_accvgpr_read_b32 v75, a119\n" \
"v_accvgpr_read_b32 v79, a127\n" \
"v_pk_mul_f32 v[72:73], v[72:73], v[188:189]\n" \
"v_pk_mul_f32 v[74:75], v[74:75], v[188:189]\n" \
"v_pk_mul_f32 v[76:77], v[76:77], v[188:189]\n" \
"v_pk_mul_f32 v[78:79], v[78:79], v[188:189]\n" \
"v_cvt_pk_bf16_f32 v60, v72, v73\n" \
"v_cvt_pk_bf16_f32 v61, v74, v75\n" \
"v_cvt_pk_bf16_f32 v62, v76, v77\n" \
"v_cvt_pk_bf16_f32 v63, v78, v79\n" \
"buffer_store_dwordx4 v[60:63], v224, %[rs], s83 offen offset:3072 sc1\n" \
"v_mov_b32 v216, v172\n" \
"v_mov_b32 v217, v173\n" \
"v_mov_b32 v218, v170\n" \
"v_mov_b32 v219, v171\n" \
"s_add_u32 s83, s61, 16384\n" \
"buffer_store_dwordx4 v[216:219], v224, %[rs], s83 offen sc1\n" \
"v_accvgpr_mov_b32 a128, a192\n" \
"v_accvgpr_mov_b32 a129, a193\n" \
"v_accvgpr_mov_b32 a130, a194\n" \
"v_accvgpr_mov_b32 a131, a195\n" \
"v_accvgpr_mov_b32 a132, a196\n" \
"v_accvgpr_mov_b32 a133, a197\n" \
"v_accvgpr_mov_b32 a134, a198\n" \
"v_accvgpr_mov_b32 a135, a199\n" \
"v_accvgpr_mov_b32 a136, a200\n" \
"v_accvgpr_mov_b32 a137, a201\n" \
"v_accvgpr_mov_b32 a138, a202\n" \
"v_accvgpr_mov_b32 a139, a203\n" \
"v_accvgpr_mov_b32 a140, a204\n" \
"v_accvgpr_mov_b32 a141, a205\n" \
"v_accvgpr_mov_b32 a142, a206\n" \
"v_accvgpr_mov_b32 a143, a207\n" \
"v_accvgpr_mov_b32 a144, a208\n" \
"v_accvgpr_mov_b32 a145, a209\n" \
"v_accvgpr_mov_b32 a146, a210\n" \
"v_accvgpr_mov_b32 a147, a211\n" \
"v_accvgpr_mov_b32 a148, a212\n" \
"v_accvgpr_mov_b32 a149, a213\n" \
"v_accvgpr_mov_b32 a150, a214\n" \
"v_accvgpr_mov_b32 a151, a215\n" \
"v_accvgpr_mov_b32 a152, a216\n" \
"v_accvgpr_mov_b32 a153, a217\n" \
"v_accvgpr_mov_b32 a154, a218\n" \
"v_accvgpr_mov_b32 a155, a219\n" \
"v_accvgpr_mov_b32 a156, a220\n" \
"v_accvgpr_mov_b32 a157, a221\n" \
"v_accvgpr_mov_b32 a158, a222\n" \
"v_accvgpr_mov_b32 a159, a223\n" \
"v_accvgpr_mov_b32 a160, a224\n" \
"v_accvgpr_mov_b32 a161, a225\n" \
"v_accvgpr_mov_b32 a162, a226\n" \
"v_accvgpr_mov_b32 a163, a227\n" \
"v_accvgpr_mov_b32 a164, a228\n" \
"v_accvgpr_mov_b32 a165, a229\n" \
"v_accvgpr_mov_b32 a166, a230\n" \
"v_accvgpr_mov_b32 a167, a231\n" \
"v_accvgpr_mov_b32 a168, a232\n" \
"v_accvgpr_mov_b32 a169, a233\n" \
"v_accvgpr_mov_b32 a170, a234\n" \
"v_accvgpr_mov_b32 a171, a235\n" \
"v_accvgpr_mov_b32 a172, a236\n" \
"v_accvgpr_mov_b32 a173, a237\n" \
"v_accvgpr_mov_b32 a174, a238\n" \
"v_accvgpr_mov_b32 a175, a239\n" \
"v_accvgpr_mov_b32 a176, a240\n" \
"v_accvgpr_mov_b32 a177, a241\n" \
"v_accvgpr_mov_b32 a178, a242\n" \
"v_accvgpr_mov_b32 a179, a243\n" \
"v_accvgpr_mov_b32 a180, a244\n" \
"v_accvgpr_mov_b32 a181, a245\n" \
"v_accvgpr_mov_b32 a182, a246\n" \
"v_accvgpr_mov_b32 a183, a247\n" \
"v_accvgpr_mov_b32 a184, a248\n" \
"v_accvgpr_mov_b32 a185, a249\n" \
"v_accvgpr_mov_b32 a186, a250\n" \
"v_accvgpr_mov_b32 a187, a251\n" \
"v_accvgpr_mov_b32 a188, a252\n" \
"v_accvgpr_mov_b32 a189, a253\n" \
"v_accvgpr_mov_b32 a190, a254\n" \
"v_accvgpr_mov_b32 a191, a255\n" \
"s_mov_b32 s50, 1\n" \
"s_mov_b32 s49, 1\n" \
"s_mov_b32 s46, s65\n" \
"s_mov_b32 s47, s67\n" \
"v_mov_b32 v160, 0\n" \
"v_mov_b32 v161, 0\n" \
"v_mov_b32 v162, 0\n" \
"v_mov_b32 v163, 0\n" \
"v_mov_b32 v164, 0\n" \
"v_mov_b32 v165, 0\n" \
"v_mov_b32 v166, 0\n" \
"v_mov_b32 v167, 0\n" \
"v_mov_b32 v170, 0\n" \
"v_mov_b32 v171, 0\n" \
"v_mov_b32 v181, 0\n" \
"s_mov_b32 s45, 1\n" \
"s_sub_u32 s52, s46, 1\n" \
"v_mfma_f32_16x16x32_bf16 v[0:3], v[96:99], a[128:131], v[160:163]\n" \
"ds_read_b128 v[128:131], v178 offset:8192\n" \
"buffer_load_dwordx4 v224, %[rs], s43 offen offset:0 sc1 lds\n" \
"v_accvgpr_write_b32 a0, 0\n" \
"v_mfma_f32_16x16x32_bf16 v[4:7], v[96:99], a[160:163], v[164:167]\n" \
"v_accvgpr_write_b32 a1, 0\n" \
"v_accvgpr_write_b32 a2, 0\n" \
"v_accvgpr_write_b32 a3, 0\n" \
"v_mfma_f32_16x16x32_bf16 v[8:11], v[100:103], a[128:131], v[160:163]\n" \
"ds_read_b128 v[132:135], v178 offset:9216\n" \
"v_readlane_b32 s48, v238, s42\n" \
"v_accvgpr_write_b32 a4, 0\n" \
"v_mfma_f32_16x16x32_bf16 v[12:15], v[100:103], a[160:163], v[164:167]\n" \
"v_accvgpr_write_b32 a5, 0\n" \
"v_accvgpr_write_b32 a6, 0\n" \
"v_accvgpr_write_b32 a7, 0\n" \
"v_mfma_f32_16x16x32_bf16 v[0:3], v[104:107], a[132:135], v[0:3]\n" \
"ds_read_b128 v[136:139], v178 offset:10240\n" \
"buffer_load_dwordx4 v224, %[rs], s43 offen offset:1024 sc1 lds\n" \
"v_accvgpr_write_b32 a8, 0\n" \
"v_mfma_f32_16x16x32_bf16 v[4:7], v[104:107], a[164:167], v[4:7]\n" \
"v_accvgpr_write_b32 a9, 0\n" \
"v_accvgpr_write_b32 a10, 0\n" \
"v_accvgpr_write_b32 a11, 0\n" \
"v_mfma_f32_16x16x32_bf16 v[8:11], v[108:111], a[132:135], v[8:11]\n" \
"ds_read_b128 v[140:143], v178 offset:11264\n" \
"s_waitcnt lgkmcnt(4)\n" \
"v_accvgpr_write_b32 a12, 0\n" \
"v_mfma_f32_16x16x32_bf16 v[12:15], v[108:111], a[164:167], v[12:15]\n" \
"v_accvgpr_write_b32 a13, 0\n" \
"v_accvgpr_write_b32 a14, 0\n" \
"v_accvgpr_write_b32 a15, 0\n" \
"v_mfma_f32_16x16x32_bf16 v[0:3], v[112:115], a[136:139], v[0:3]\n" \
"ds_read_b128 v[144:147], v178 offset:12288\n" \
"s_add_u32 s42, s42, 1\n" \
"v_accvgpr_write_b32 a16, 0\n" \
"v_mfma_f32_16x16x32_bf16 v[4:7], v[112:115], a[168:171], v[4:7]\n" \
"v_accvgpr_write_b32 a17, 0\n" \
"v_accvgpr_write_b32 a18, 0\n" \
"v_accvgpr_write_b32 a19, 0\n" \
"v_mfma_f32_16x16x32_bf16 v[8:11], v[116:119], a[136:139], v[8:11]\n" \
"ds_read_b128 v[148:151], v178 offset:13312\n" \
"buffer_load_dwordx4 v224, %[rs], s43 offen offset:2048 sc1 lds\n" \
"v_accvgpr_write_b32 a20, 0\n" \
"v_mfma_f32_16x16x32_bf16 v[12:15], v[116:119], a[168:171], v[12:15]\n" \
"v_accvgpr_write_b32 a21, 0\n" \
"v_accvgpr_write_b32 a22, 0\n" \
"v_accvgpr_write_b32 a23, 0\n" \
"v_mfma_f32_16x16x32_bf16 v[0:3], v[120:123], a[140:143], v[0:3]\n" \
"ds_read_b128 v[152:155], v178 offset:14336\n" \
"v_add_u32 v179, s48, v224\n" \
"v_accvgpr_write_b32 a24, 0\n" \
"v_mfma_f32_16x16x32_bf16 v[4:7], v[120:123], a[172:175], v[4:7]\n" \
"v_accvgpr_write_b32 a25, 0\n" \
"v_accvgpr_write_b32 a26, 0\n" \
"v_accvgpr_write_b32 a27, 0\n" \
"v_mfma_f32_16x16x32_bf16 v[8:11], v[124:127], a[140:143], v[8:11]\n" \
"ds_read_b128 v[156:159], v178 offset:15360\n" \
"s_waitcnt lgkmcnt(4)\n" \
"v_accvgpr_write_b32 a28, 0\n" \
"v_mfma_f32_16x16x32_bf16 v[12:15], v[124:127], a[172:175], v[12:15]\n" \
"v_accvgpr_write_b32 a29, 0\n" \
"v_accvgpr_write_b32 a30, 0\n" \
"v_accvgpr_write_b32 a31, 0\n" \
"v_mfma_f32_16x16x32_bf16 v[0:3], v[128:131], a[144:147], v[0:3]\n" \
"ds_read_b128 v[96:99], v178 offset:16384\n" \
"buffer_load_dwordx4 v224, %[rs], s43 offen offset:3072 sc1 lds\n" \
"v_accvgpr_write_b32 a32, 0\n" \
"v_mfma_f32_16x16x32_bf16 v[4:7], v[128:131], a[176:179], v[4:7]\n" \
"v_accvgpr_write_b32 a33, 0\n" \
"v_accvgpr_write_b32 a34, 0\n" \
"v_accvgpr_write_b32 a35, 0\n" \
"v_mfma_f32_16x16x32_bf16 v[8:11], v[132:135], a[144:147], v[8:11]\n" \
"ds_read_b128 v[100:103], v178 offset:17408\n" \
"s_add_u32 m0, m0, 0x1000\n" \
"s_add_u32 s43, s43, 0x1000\n" \
"v_mfma_f32_16x16x32_bf16 v[12:15], v[132:135], a[176:179], v[12:15]\n" \
"v_accvgpr_write_b32 a36, 0\n" \
"v_accvgpr_write_b32 a37, 0\n" \
"v_accvgpr_write_b32 a38, 0\n" \
"v_mfma_f32_16x16x32_bf16 v[0:3], v[136:139], a[148:151], v[0:3]\n" \
"ds_read_b128 v[104:107], v178 offset:18432\n" \
"v_accvgpr_write_b32 a39, 0\n" \
"v_accvgpr_write_b32 a40, 0\n" \
"v_mfma_f32_16x16x32_bf16 v[4:7], v[136:139], a[180:183], v[4:7]\n" \
"v_accvgpr_write_b32 a41, 0\n" \
"v_accvgpr_write_b32 a42, 0\n" \
"v_accvgpr_write_b32 a43, 0\n" \
"v_mfma_f32_16x16x32_bf16 v[8:11], v[140:143], a[148:151], v[8:11]\n" \
"ds_read_b128 v[108:111], v178 offset:19456\n" \
"s_waitcnt lgkmcnt(4)\n" \
"v_accvgpr_write_b32 a44, 0\n" \
"v_mfma_f32_16x16x32_bf16 v[12:15], v[140:143], a[180:183], v[12:15]\n" \
"v_accvgpr_write_b32 a45, 0\n" \
"v_accvgpr_write_b32 a46, 0\n" \
"v_accvgpr_write_b32 a47, 0\n" \
"v_mfma_f32_16x16x32_bf16 v[0:3], v[144:147], a[152:155], v[0:3]\n" \
"ds_read_b128 v[112:115], v178 offset:20480\n" \
"buffer_load_dwordx4 v224, %[rs], s43 offen offset:0 sc1 lds\n" \
"v_accvgpr_write_b32 a48, 0\n" \
"v_mfma_f32_16x16x32_bf16 v[4:7], v[144:147], a[184:187], v[4:7]\n" \
"v_accvgpr_write_b32 a49, 0\n" \
"v_accvgpr_write_b32 a50, 0\n" \
"v_accvgpr_write_b32 a51, 0\n" \
"v_mfma_f32_16x16x32_bf16 v[8:11], v[148:151], a[152:155], v[8:11]\n" \
"ds_read_b128 v[116:119], v178 offset:21504\n" \
"v_accvgpr_write_b32 a52, 0\n" \
"v_accvgpr_write_b32 a53, 0\n" \
"v_mfma_f32_16x16x32_bf16 v[12:15], v[148:151], a[184:187], v[12:15]\n" \
"v_accvgpr_write_b32 a54, 0\n" \
"v_accvgpr_write_b32 a55, 0\n" \
"v_accvgpr_write_b32 a56, 0\n" \
"v_mfma_f32_16x16x32_bf16 v[0:3], v[152:155], a[156:159], v[0:3]\n" \
"ds_read_b128 v[120:123], v178 offset:22528\n" \
"buffer_load_dwordx4 v224, %[rs], s43 offen offset:1024 sc1 lds\n" \
"v_accvgpr_write_b32 a57, 0\n" \
"v_mfma_f32_16x16x32_bf16 v[4:7], v[152:155], a[188:191], v[4:7]\n" \
"v_accvgpr_write_b32 a58, 0\n" \
"v_accvgpr_write_b32 a59, 0\n" \
"v_accvgpr_write_b32 a60, 0\n" \
"v_mfma_f32_16x16x32_bf16 v[8:11], v[156:159], a[156:159], v[8:11]\n" \
"ds_read_b128 v[124:127], v178 offset:23552\n" \
"s_waitcnt lgkmcnt(4)\n" \
"v_accvgpr_write_b32 a61, 0\n" \
"v_mfma_f32_16x16x32_bf16 v[12:15], v[156:159], a[188:191], v[12:15]\n" \
"v_accvgpr_write_b32 a62, 0\n" \
"v_accvgpr_write_b32 a63, 0\n" \
"v_accvgpr_write_b32 a64, 0\n" \
"v_mfma_f32_16x16x32_bf16 v[16:19], v[96:99], a[128:131], v[160:163]\n" \
"ds_read_b128 v[128:131], v178 offset:24576\n" \
"v_accvgpr_write_b32 a65, 0\n" \
"v_accvgpr_write_b32 a66, 0\n" \
"v_mfma_f32_16x16x32_bf16 v[20:23], v[96:99], a[160:163], v[164:167]\n" \
"v_accvgpr_write_b32 a67, 0\n" \
"v_accvgpr_write_b32 a68, 0\n" \
"v_accvgpr_write_b32 a69, 0\n" \
"v_mfma_f32_16x16x32_bf16 v[24:27], v[100:103], a[128:131], v[160:163]\n" \
"ds_read_b128 v[132:135], v178 offset:25600\n" \
"buffer_load_dwordx4 v224, %[rs], s43 offen offset:2048 sc1 lds\n" \
"v_accvgpr_write_b32 a70, 0\n" \
"v_mfma_f32_16x16x32_bf16 v[28:31], v[100:103], a[160:163], v[164:167]\n" \
"v_accvgpr_write_b32 a71, 0\n" \
"v_accvgpr_write_b32 a72, 0\n" \
"v_accvgpr_write_b32 a73, 0\n" \
"v_mfma_f32_16x16x32_bf16 v[16:19], v[104:107], a[132:135], v[16:19]\n" \
"ds_read_b128 v[136:139], v178 offset:26624\n" \
"v_accvgpr_write_b32 a74, 0\n" \
"v_accvgpr_write_b32 a75, 0\n" \
"v_mfma_f32_16x16x32_bf16 v[20:23], v[104:107], a[164:167], v[20:23]\n" \
"v_accvgpr_write_b32 a76, 0\n" \
"v_accvgpr_write_b32 a77, 0\n" \
"v_accvgpr_write_b32 a78, 0\n" \
"v_mfma_f32_16x16x32_bf16 v[24:27], v[108:111], a[132:135], v[24:27]\n" \
"ds_read_b128 v[140:143], v178 offset:27648\n" \
"s_waitcnt lgkmcnt(4)\n" \
"v_accvgpr_write_b32 a79, 0\n" \
"v_mfma_f32_16x16x32_bf16 v[28:31], v[108:111], a[164:167], v[28:31]\n" \
"v_accvgpr_write_b32 a80, 0\n" \
"v_accvgpr_write_b32 a81, 0\n" \
"v_accvgpr_write_b32 a82, 0\n" \
"v_mfma_f32_16x16x32_bf16 v[16:19], v[112:115], a[136:139], v[16:19]\n" \
"ds_read_b128 v[144:147], v178 offset:28672\n" \
"buffer_load_dwordx4 v224, %[rs], s43 offen offset:3072 sc1 lds\n" \
"v_accvgpr_write_b32 a83, 0\n" \
"v_mfma_f32_16x16x32_bf16 v[20:23], v[112:115], a[168:171], v[20:23]\n" \
"v_accvgpr_write_b32 a84, 0\n" \
"v_accvgpr_write_b32 a85, 0\n" \
"v_accvgpr_write_b32 a86, 0\n" \
"v_mfma_f32_16x16x32_bf16 v[24:27], v[116:119], a[136:139], v[24:27]\n" \
"ds_read_b128 v[148:151], v178 offset:29696\n" \
"v_readlane_b32 s43, v239, s42\n" \
"v_accvgpr_write_b32 a87, 0\n" \
"v_mfma_f32_16x16x32_bf16 v[28:31], v[116:119], a[168:171], v[28:31]\n" \
"v_accvgpr_write_b32 a88, 0\n" \
"v_accvgpr_write_b32 a89, 0\n" \
"v_accvgpr_write_b32 a90, 0\n" \
"v_mfma_f32_16x16x32_bf16 v[16:19], v[120:123], a[140:143], v[16:19]\n" \
"ds_read_b128 v[152:155], v178 offset:30720\n" \
"s_add_u32 m0, s44, s37\n" \
"v_accvgpr_write_b32 a91, 0\n" \
"v_mfma_f32_16x16x32_bf16 v[20:23], v[120:123], a[172:175], v[20:23]\n" \
"v_accvgpr_write_b32 a92, 0\n" \
"v_accvgpr_write_b32 a93, 0\n" \
"v_accvgpr_write_b32 a94, 0\n" \
"v_mfma_f32_16x16x32_bf16 v[24:27], v[124:127], a[140:143], v[24:27]\n" \
"ds_read_b128 v[156:159], v178 offset:31744\n" \
"s_waitcnt lgkmcnt(4)\n" \
"v_accvgpr_write_b32 a95, 0\n" \
"v_mfma_f32_16x16x32_bf16 v[28:31], v[124:127], a[172:175], v[28:31]\n" \
"v_accvgpr_write_b32 a96, 0\n" \
"v_accvgpr_write_b32 a97, 0\n" \
"v_accvgpr_write_b32 a98, 0\n" \
"s_waitcnt vmcnt(8)\n" \
"s_barrier\n" \
"v_mfma_f32_16x16x32_bf16 v[16:19], v[128:131], a[144:147], v[16:19]\n" \
"ds_read_b128 v[96:99], v179 offset:0\n" \
"v_accvgpr_write_b32 a99, 0\n" \
"v_accvgpr_write_b32 a100, 0\n" \
"v_mfma_f32_16x16x32_bf16 v[20:23], v[128:131], a[176:179], v[20:23]\n" \
"v_accvgpr_write_b32 a101, 0\n" \
"v_accvgpr_write_b32 a102, 0\n" \
"v_accvgpr_write_b32 a103, 0\n" \
"v_mfma_f32_16x16x32_bf16 v[24:27], v[132:135], a[144:147], v[24:27]\n" \
"ds_read_b128 v[100:103], v179 offset:1024\n" \
"s_mov_b32 s44, s41\n" \
"v_accvgpr_write_b32 a104, 0\n" \
"v_mfma_f32_16x16x32_bf16 v[28:31], v[132:135], a[176:179], v[28:31]\n" \
"v_accvgpr_write_b32 a105, 0\n" \
"v_accvgpr_write_b32 a106, 0\n" \
"v_accvgpr_write_b32 a107, 0\n" \
"v_mfma_f32_16x16x32_bf16 v[16:19], v[136:139], a[148:151], v[16:19]\n" \
"ds_read_b128 v[104:107], v179 offset:2048\n" \
"s_mov_b32 s41, s48\n" \
"v_accvgpr_write_b32 a108, 0\n" \
"v_mfma_f32_16x16x32_bf16 v[20:23], v[136:139], a[180:183], v[20:23]\n" \
"v_accvgpr_write_b32 a109, 0\n" \
"v_accvgpr_write_b32 a110, 0\n" \
"v_accvgpr_write_b32 a111, 0\n" \
"v_mfma_f32_16x16x32_bf16 v[24:27], v[140:143], a[148:151], v[24:27]\n" \
"ds_read_b128 v[108:111], v179 offset:3072\n" \
"s_waitcnt lgkmcnt(4)\n" \
"v_accvgpr_write_b32 a112, 0\n" \
"v_mfma_f32_16x16x32_bf16 v[28:31], v[140:143], a[180:183], v[28:31]\n" \
"v_accvgpr_write_b32 a113, 0\n" \
"v_accvgpr_write_b32 a114, 0\n" \
"v_accvgpr_write_b32 a115, 0\n" \
"v_mfma_f32_16x16x32_bf16 v[16:19], v[144:147], a[152:155], v[16:19]\n" \
"ds_read_b128 v[112:115], v179 offset:4096\n" \
"v_accvgpr_write_b32 a116, 0\n" \
"v_accvgpr_write_b32 a117, 0\n" \
"v_mfma_f32_16x16x32_bf16 v[20:23], v[144:147], a[184:187], v[20:23]\n" \
"v_accvgpr_write_b32 a118, 0\n" \
"v_accvgpr_write_b32 a119, 0\n" \
"v_accvgpr_write_b32 a120, 0\n" \
"v_mfma_f32_16x16x32_bf16 v[24:27], v[148:151], a[152:155], v[24:27]\n" \
"ds_read_b128 v[116:119], v179 offset:5120\n" \
"v_accvgpr_write_b32 a121, 0\n" \
"v_accvgpr_write_b32 a122, 0\n" \
"v_mfma_f32_16x16x32_bf16 v[28:31], v[148:151], a[184:187], v[28:31]\n" \
"v_accvgpr_write_b32 a123, 0\n" \
"v_accvgpr_write_b32 a124, 0\n" \
"v_accvgpr_write_b32 a125, 0\n" \
"v_mfma_f32_16x16x32_bf16 v[16:19], v[152:155], a[156:159], v[16:19]\n" \
"ds_read_b128 v[120:123], v179 offset:6144\n" \
"v_accvgpr_write_b32 a126, 0\n" \
"v_accvgpr_write_b32 a127, 0\n" \
"v_mfma_f32_16x16x32_bf16 v[20:23], v[152:155], a[188:191], v[20:23]\n" \
"v_mfma_f32_16x16x32_bf16 v[24:27], v[156:159], a[156:159], v[24:27]\n" \
"ds_read_b128 v[124:127], v179 offset:7168\n" \
"s_waitcnt lgkmcnt(4)\n" \
"v_mfma_f32_16x16x32_bf16 v[28:31], v[156:159], a[188:191], v[28:31]\n" \
"v_mov_b32 v178, v179\n" \
"s_branch Litem%=\n" \
"Lop%=:\n" \
"s_nop 1\n" \
"s_lshl_b32 s80, s40, 8\n" \
"s_sub_u32 s84, s28, s80\n" \
"s_subb_u32 s85, s29, 0\n" \
"s_mov_b32 s86, 1605632\n" \
"s_mov_b32 s87, 0x20000\n" \
"v_mfma_f32_16x16x32_bf16 v[64:67], v[96:99], v[0:3], 0\n" \
"ds_read_b128 v[128:131], v178 offset:8192\n" \
"buffer_load_dwordx4 v224, %[rs], s43 offen offset:0 sc1 lds\n" \
"v_mfma_f32_16x16x32_bf16 v[68:71], v[96:99], v[32:35], 0\n" \
"v_mfma_f32_16x16x32_bf16 v[72:75], v[100:103], v[0:3], 0\n" \
"ds_read_b128 v[132:135], v178 offset:9216\n" \
"v_readlane_b32 s48, v238, s42\n" \
"v_mfma_f32_16x16x32_bf16 v[76:79], v[100:103], v[32:35], 0\n" \
"v_mfma_f32_16x16x32_bf16 v[80:83], v[104:107], v[0:3], 0\n" \
"ds_read_b128 v[136:139], v178 offset:10240\n" \
"buffer_load_dwordx4 v224, %[rs], s43 offen offset:1024 sc1 lds\n" \
"v_mfma_f32_16x16x32_bf16 v[84:87], v[104:107], v[32:35], 0\n" \
"v_mfma_f32_16x16x32_bf16 v[88:91], v[108:111], v[0:3], 0\n" \
"ds_read_b128 v[140:143], v178 offset:11264\n" \
"s_waitcnt lgkmcnt(4)\n" \
"v_mfma_f32_16x16x32_bf16 v[92:95], v[108:111], v[32:35], 0\n" \
"v_mfma_f32_16x16x32_bf16 v[64:67], v[112:115], v[4:7], v[64:67]\n" \
"ds_read_b128 v[144:147], v178 offset:12288\n" \
"s_add_u32 s42, s42, 1\n" \
"v_mfma_f32_16x16x32_bf16 v[68:71], v[112:115], v[36:39], v[68:71]\n" \
"v_mfma_f32_16x16x32_bf16 v[72:75], v[116:119], v[4:7], v[72:75]\n" \
"ds_read_b128 v[148:151], v178 offset:13312\n" \
"buffer_load_dwordx4 v224, %[rs], s43 offen offset:2048 sc1 lds\n" \
"v_mfma_f32_16x16x32_bf16 v[76:79], v[116:119], v[36:39], v[76:79]\n" \
"v_mfma_f32_16x16x32_bf16 v[80:83], v[120:123], v[4:7], v[80:83]\n" \
"ds_read_b128 v[152:155], v178 offset:14336\n" \
"v_add_u32 v179, s48, v224\n" \
"v_mfma_f32_16x16x32_bf16 v[84:87], v[120:123], v[36:39], v[84:87]\n" \
"v_mfma_f32_16x16x32_bf16 v[88:91], v[124:127], v[4:7], v[88:91]\n" \
"ds_read_b128 v[156:159], v178 offset:15360\n" \
"s_waitcnt lgkmcnt(4)\n" \
"v_mfma_f32_16x16x32_bf16 v[92:95], v[124:127], v[36:39], v[92:95]\n" \
"v_mfma_f32_16x16x32_bf16 v[64:67], v[128:131], v[8:11], v[64:67]\n" \
"ds_read_b128 v[96:99], v178 offset:16384\n" \
"buffer_load_dwordx4 v224, %[rs], s43 offen offset:3072 sc1 lds\n" \
"v_mfma_f32_16x16x32_bf16 v[68:71], v[128:131], v[40:43], v[68:71]\n" \
"v_mfma_f32_16x16x32_bf16 v[72:75], v[132:135], v[8:11], v[72:75]\n" \
"ds_read_b128 v[100:103], v178 offset:17408\n" \
"s_add_u32 m0, m0, 0x1000\n" \
"s_add_u32 s43, s43, 0x1000\n" \
"v_mfma_f32_16x16x32_bf16 v[76:79], v[132:135], v[40:43], v[76:79]\n" \
"v_mfma_f32_16x16x32_bf16 v[80:83], v[136:139], v[8:11], v[80:83]\n" \
"ds_read_b128 v[104:107], v178 offset:18432\n" \
"v_mfma_f32_16x16x32_bf16 v[84:87], v[136:139], v[40:43], v[84:87]\n" \
"v_mfma_f32_16x16x32_bf16 v[88:91], v[140:143], v[8:11], v[88:91]\n" \
"ds_read_b128 v[108:111], v178 offset:19456\n" \
"s_waitcnt lgkmcnt(4)\n" \
"v_mfma_f32_16x16x32_bf16 v[92:95], v[140:143], v[40:43], v[92:95]\n" \
"v_mfma_f32_16x16x32_bf16 v[64:67], v[144:147], v[12:15], v[64:67]\n" \
"ds_read_b128 v[112:115], v178 offset:20480\n" \
"buffer_load_dwordx4 v224, %[rs], s43 offen offset:0 sc1 lds\n" \
"v_mfma_f32_16x16x32_bf16 v[68:71], v[144:147], v[44:47], v[68:71]\n" \
"v_mfma_f32_16x16x32_bf16 v[72:75], v[148:151], v[12:15], v[72:75]\n" \
"ds_read_b128 v[116:119], v178 offset:21504\n" \
"v_mfma_f32_16x16x32_bf16 v[76:79], v[148:151], v[44:47], v[76:79]\n" \
"v_mfma_f32_16x16x32_bf16 v[80:83], v[152:155], v[12:15], v[80:83]\n" \
"ds_read_b128 v[120:123], v178 offset:22528\n" \
"buffer_load_dwordx4 v224, %[rs], s43 offen offset:1024 sc1 lds\n" \
"v_mfma_f32_16x16x32_bf16 v[84:87], v[152:155], v[44:47], v[84:87]\n" \
"v_mfma_f32_16x16x32_bf16 v[88:91], v[156:159], v[12:15], v[88:91]\n" \
"ds_read_b128 v[124:127], v178 offset:23552\n" \
"s_waitcnt lgkmcnt(4)\n" \
"v_mfma_f32_16x16x32_bf16 v[92:95], v[156:159], v[44:47], v[92:95]\n" \
"v_mfma_f32_16x16x32_bf16 v[64:67], v[96:99], v[16:19], v[64:67]\n" \
"ds_read_b128 v[128:131], v178 offset:24576\n" \
"v_mfma_f32_16x16x32_bf16 v[68:71], v[96:99], v[48:51], v[68:71]\n" \
"v_mfma_f32_16x16x32_bf16 v[72:75], v[100:103], v[16:19], v[72:75]\n" \
"ds_read_b128 v[132:135], v178 offset:25600\n" \
"buffer_load_dwordx4 v224, %[rs], s43 offen offset:2048 sc1 lds\n" \
"v_mfma_f32_16x16x32_bf16 v[76:79], v[100:103], v[48:51], v[76:79]\n" \
"v_mfma_f32_16x16x32_bf16 v[80:83], v[104:107], v[16:19], v[80:83]\n" \
"ds_read_b128 v[136:139], v178 offset:26624\n" \
"v_mfma_f32_16x16x32_bf16 v[84:87], v[104:107], v[48:51], v[84:87]\n" \
"v_mfma_f32_16x16x32_bf16 v[88:91], v[108:111], v[16:19], v[88:91]\n" \
"ds_read_b128 v[140:143], v178 offset:27648\n" \
"s_waitcnt lgkmcnt(4)\n" \
"v_mfma_f32_16x16x32_bf16 v[92:95], v[108:111], v[48:51], v[92:95]\n" \
"v_mfma_f32_16x16x32_bf16 v[64:67], v[112:115], v[20:23], v[64:67]\n" \
"ds_read_b128 v[144:147], v178 offset:28672\n" \
"buffer_load_dwordx4 v224, %[rs], s43 offen offset:3072 sc1 lds\n" \
"v_mfma_f32_16x16x32_bf16 v[68:71], v[112:115], v[52:55], v[68:71]\n" \
"v_mfma_f32_16x16x32_bf16 v[72:75], v[116:119], v[20:23], v[72:75]\n" \
"ds_read_b128 v[148:151], v178 offset:29696\n" \
"v_readlane_b32 s43, v239, s42\n" \
"v_mfma_f32_16x16x32_bf16 v[76:79], v[116:119], v[52:55], v[76:79]\n" \
"v_mfma_f32_16x16x32_bf16 v[80:83], v[120:123], v[20:23], v[80:83]\n" \
"ds_read_b128 v[152:155], v178 offset:30720\n" \
"s_add_u32 m0, s44, s37\n" \
"v_mfma_f32_16x16x32_bf16 v[84:87], v[120:123], v[52:55], v[84:87]\n" \
"v_mfma_f32_16x16x32_bf16 v[88:91], v[124:127], v[20:23], v[88:91]\n" \
"ds_read_b128 v[156:159], v178 offset:31744\n" \
"s_waitcnt lgkmcnt(4)\n" \
"v_mfma_f32_16x16x32_bf16 v[92:95], v[124:127], v[52:55], v[92:95]\n" \
"s_waitcnt vmcnt(16)\n" \
"s_barrier\n" \
"v_mfma_f32_16x16x32_bf16 v[64:67], v[128:131], v[24:27], v[64:67]\n" \
"ds_read_b128 v[96:99], v179 offset:0\n" \
"v_mfma_f32_16x16x32_bf16 v[68:71], v[128:131], v[56:59], v[68:71]\n" \
"v_mfma_f32_16x16x32_bf16 v[72:75], v[132:135], v[24:27], v[72:75]\n" \
"ds_read_b128 v[100:103], v179 offset:1024\n" \
"s_mov_b32 s44, s41\n" \
"v_mfma_f32_16x16x32_bf16 v[76:79], v[132:135], v[56:59], v[76:79]\n" \
"v_mfma_f32_16x16x32_bf16 v[80:83], v[136:139], v[24:27], v[80:83]\n" \
"ds_read_b128 v[104:107], v179 offset:2048\n" \
"s_mov_b32 s41, s48\n" \
"v_mfma_f32_16x16x32_bf16 v[84:87], v[136:139], v[56:59], v[84:87]\n" \
"v_mfma_f32_16x16x32_bf16 v[88:91], v[140:143], v[24:27], v[88:91]\n" \
"ds_read_b128 v[108:111], v179 offset:3072\n" \
"s_waitcnt lgkmcnt(4)\n" \
"v_mfma_f32_16x16x32_bf16 v[92:95], v[140:143], v[56:59], v[92:95]\n" \
"v_mfma_f32_16x16x32_bf16 v[64:67], v[144:147], v[28:31], v[64:67]\n" \
"ds_read_b128 v[112:115], v179 offset:4096\n" \
"v_mfma_f32_16x16x32_bf16 v[68:71], v[144:147], v[60:63], v[68:71]\n" \
"v_mfma_f32_16x16x32_bf16 v[72:75], v[148:151], v[28:31], v[72:75]\n" \
"ds_read_b128 v[116:119], v179 offset:5120\n" \
"v_mfma_f32_16x16x32_bf16 v[76:79], v[148:151], v[60:63], v[76:79]\n" \
"v_mfma_f32_16x16x32_bf16 v[80:83], v[152:155], v[28:31], v[80:83]\n" \
"ds_read_b128 v[120:123], v179 offset:6144\n" \
"v_mfma_f32_16x16x32_bf16 v[84:87], v[152:155], v[60:63], v[84:87]\n" \
"v_mfma_f32_16x16x32_bf16 v[88:91], v[156:159], v[28:31], v[88:91]\n" \
"ds_read_b128 v[124:127], v179 offset:7168\n" \
"s_waitcnt lgkmcnt(4)\n" \
"v_mfma_f32_16x16x32_bf16 v[92:95], v[156:159], v[60:63], v[92:95]\n" \
"v_mov_b32 v178, v179\n" \
"v_mfma_f32_16x16x32_bf16 v[184:187], v[96:99], v[0:3], 0\n" \
"ds_read_b128 v[128:131], v178 offset:8192\n" \
"buffer_load_dwordx4 v224, %[rs], s43 offen offset:0 sc1 lds\n" \
"v_mfma_f32_16x16x32_bf16 v[188:191], v[96:99], v[32:35], 0\n" \
"v_mfma_f32_16x16x32_bf16 v[192:195], v[100:103], v[0:3], 0\n" \
"ds_read_b128 v[132:135], v178 offset:9216\n" \
"v_readlane_b32 s48, v238, s42\n" \
"v_mfma_f32_16x16x32_bf16 v[196:199], v[100:103], v[32:35], 0\n" \
"v_mfma_f32_16x16x32_bf16 v[200:203], v[104:107], v[0:3], 0\n" \
"ds_read_b128 v[136:139], v178 offset:10240\n" \
"buffer_load_dwordx4 v224, %[rs], s43 offen offset:1024 sc1 lds\n" \
"v_mfma_f32_16x16x32_bf16 v[204:207], v[104:107], v[32:35], 0\n" \
"v_cvt_pk_bf16_f32 v216, v64, v65\n" \
"v_cvt_pk_bf16_f32 v217, v66, v67\n" \
"v_mfma_f32_16x16x32_bf16 v[208:211], v[108:111], v[0:3], 0\n" \
"ds_read_b128 v[140:143], v178 offset:11264\n" \
"s_waitcnt lgkmcnt(4)\n" \
"v_mfma_f32_16x16x32_bf16 v[212:215], v[108:111], v[32:35], 0\n" \
"v_cvt_pk_bf16_f32 v218, v72, v73\n" \
"v_cvt_pk_bf16_f32 v219, v74, v75\n" \
"v_mfma_f32_16x16x32_bf16 v[184:187], v[112:115], v[4:7], v[184:187]\n" \
"ds_read_b128 v[144:147], v178 offset:12288\n" \
"s_add_u32 s42, s42, 1\n" \
"v_mfma_f32_16x16x32_bf16 v[188:191], v[112:115], v[36:39], v[188:191]\n" \
"s_add_u32 s83, s69, 0\n" \
"buffer_store_dwordx4 v[216:219], v224, %[rs], s83 offen sc1\n" \
"v_mfma_f32_16x16x32_bf16 v[192:195], v[116:119], v[4:7], v[192:195]\n" \
"ds_read_b128 v[148:151], v178 offset:13312\n" \
"buffer_load_dwordx4 v224, %[rs], s43 offen offset:2048 sc1 lds\n" \
"v_mfma_f32_16x16x32_bf16 v[196:199], v[116:119], v[36:39], v[196:199]\n" \
"v_cvt_pk_bf16_f32 v220, v68, v69\n" \
"v_cvt_pk_bf16_f32 v221, v70, v71\n" \
"v_mfma_f32_16x16x32_bf16 v[200:203], v[120:123], v[4:7], v[200:203]\n" \
"ds_read_b128 v[152:155], v178 offset:14336\n" \
"v_add_u32 v179, s48, v224\n" \
"v_mfma_f32_16x16x32_bf16 v[204:207], v[120:123], v[36:39], v[204:207]\n" \
"v_cvt_pk_bf16_f32 v222, v76, v77\n" \
"v_cvt_pk_bf16_f32 v223, v78, v79\n" \
"v_mfma_f32_16x16x32_bf16 v[208:211], v[124:127], v[4:7], v[208:211]\n" \
"ds_read_b128 v[156:159], v178 offset:15360\n" \
"s_waitcnt lgkmcnt(4)\n" \
"v_mfma_f32_16x16x32_bf16 v[212:215], v[124:127], v[36:39], v[212:215]\n" \
"s_add_u32 s83, s71, 0\n" \
"buffer_store_dwordx4 v[220:223], v224, %[rs], s83 offen sc1\n" \
"v_mfma_f32_16x16x32_bf16 v[184:187], v[128:131], v[8:11], v[184:187]\n" \
"ds_read_b128 v[96:99], v178 offset:16384\n" \
"buffer_load_dwordx4 v224, %[rs], s43 offen offset:3072 sc1 lds\n" \
"v_mfma_f32_16x16x32_bf16 v[188:191], v[128:131], v[40:43], v[188:191]\n" \
"v_cvt_pk_bf16_f32 v216, v80, v81\n" \
"v_cvt_pk_bf16_f32 v217, v82, v83\n" \
"v_mfma_f32_16x16x32_bf16 v[192:195], v[132:135], v[8:11], v[192:195]\n" \
"ds_read_b128 v[100:103], v178 offset:17408\n" \
"s_add_u32 m0, m0, 0x1000\n" \
"s_add_u32 s43, s43, 0x1000\n" \
"v_mfma_f32_16x16x32_bf16 v[196:199], v[132:135], v[40:43], v[196:199]\n" \
"v_cvt_pk_bf16_f32 v218, v88, v89\n" \
"v_cvt_pk_bf16_f32 v219, v90, v91\n" \
"v_mfma_f32_16x16x32_bf16 v[200:203], v[136:139], v[8:11], v[200:203]\n" \
"ds_read_b128 v[104:107], v178 offset:18432\n" \
"s_add_u32 s83, s69, 1024\n" \
"buffer_store_dwordx4 v[216:219], v224, %[rs], s83 offen sc1\n" \
"v_mfma_f32_16x16x32_bf16 v[204:207], v[136:139], v[40:43], v[204:207]\n" \
"v_cvt_pk_bf16_f32 v220, v84, v85\n" \
"v_cvt_pk_bf16_f32 v221, v86, v87\n" \
"v_mfma_f32_16x16x32_bf16 v[208:211], v[140:143], v[8:11], v[208:211]\n" \
"ds_read_b128 v[108:111], v178 offset:19456\n" \
"s_waitcnt lgkmcnt(4)\n" \
"v_mfma_f32_16x16x32_bf16 v[212:215], v[140:143], v[40:43], v[212:215]\n" \
"v_cvt_pk_bf16_f32 v222, v92, v93\n" \
"v_cvt_pk_bf16_f32 v223, v94, v95\n" \
"v_mfma_f32_16x16x32_bf16 v[184:187], v[144:147], v[12:15], v[184:187]\n" \
"ds_read_b128 v[112:115], v178 offset:20480\n" \
"buffer_load_dwordx4 v224, %[rs], s43 offen offset:0 sc1 lds\n" \
"v_mfma_f32_16x16x32_bf16 v[188:191], v[144:147], v[44:47], v[188:191]\n" \
"v_mfma_f32_16x16x32_bf16 v[192:195], v[148:151], v[12:15], v[192:195]\n" \
"ds_read_b128 v[116:119], v178 offset:21504\n" \
"v_mfma_f32_16x16x32_bf16 v[196:199], v[148:151], v[44:47], v[196:199]\n" \
"v_mfma_f32_16x16x32_bf16 v[200:203], v[152:155], v[12:15], v[200:203]\n" \
"ds_read_b128 v[120:123], v178 offset:22528\n" \
"buffer_load_dwordx4 v224, %[rs], s43 offen offset:1024 sc1 lds\n" \
"v_mfma_f32_16x16x32_bf16 v[204:207], v[152:155], v[44:47], v[204:207]\n" \
"v_mfma_f32_16x16x32_bf16 v[208:211], v[156:159], v[12:15], v[208:211]\n" \
"ds_read_b128 v[124:127], v178 offset:23552\n" \
"s_waitcnt lgkmcnt(4)\n" \
"v_mfma_f32_16x16x32_bf16 v[212:215], v[156:159], v[44:47], v[212:215]\n" \
"v_mfma_f32_16x16x32_bf16 v[184:187], v[96:99], v[16:19], v[184:187]\n" \
"ds_read_b128 v[128:131], v178 offset:24576\n" \
"s_add_u32 s83, s71, 1024\n" \
"buffer_store_dwordx4 v[220:223], v224, %[rs], s83 offen sc1\n" \
"v_mfma_f32_16x16x32_bf16 v[188:191], v[96:99], v[48:51], v[188:191]\n" \
"v_mfma_f32_16x16x32_bf16 v[192:195], v[100:103], v[16:19], v[192:195]\n" \
"ds_read_b128 v[132:135], v178 offset:25600\n" \
"buffer_load_dwordx4 v224, %[rs], s43 offen offset:2048 sc1 lds\n" \
"v_mfma_f32_16x16x32_bf16 v[196:199], v[100:103], v[48:51], v[196:199]\n" \
"v_mfma_f32_16x16x32_bf16 v[200:203], v[104:107], v[16:19], v[200:203]\n" \
"ds_read_b128 v[136:139], v178 offset:26624\n" \
"v_mfma_f32_16x16x32_bf16 v[204:207], v[104:107], v[48:51], v[204:207]\n" \
"v_mfma_f32_16x16x32_bf16 v[208:211], v[108:111], v[16:19], v[208:211]\n" \
"ds_read_b128 v[140:143], v178 offset:27648\n" \
"s_waitcnt lgkmcnt(4)\n" \
"v_mfma_f32_16x16x32_bf16 v[212:215], v[108:111], v[48:51], v[212:215]\n" \
"v_mfma_f32_16x16x32_bf16 v[184:187], v[112:115], v[20:23], v[184:187]\n" \
"ds_read_b128 v[144:147], v178 offset:28672\n" \
"buffer_load_dwordx4 v224, %[rs], s43 offen offset:3072 sc1 lds\n" \
"v_mfma_f32_16x16x32_bf16 v[188:191], v[112:115], v[52:55], v[188:191]\n" \
"v_mfma_f32_16x16x32_bf16 v[192:195], v[116:119], v[20:23], v[192:195]\n" \
"ds_read_b128 v[148:151], v178 offset:29696\n" \
"v_readlane_b32 s43, v239, s42\n" \
"v_mfma_f32_16x16x32_bf16 v[196:199], v[116:119], v[52:55], v[196:199]\n" \
"v_mfma_f32_16x16x32_bf16 v[200:203], v[120:123], v[20:23], v[200:203]\n" \
"ds_read_b128 v[152:155], v178 offset:30720\n" \
"s_add_u32 m0, s44, s37\n" \
"v_mfma_f32_16x16x32_bf16 v[204:207], v[120:123], v[52:55], v[204:207]\n" \
"v_mfma_f32_16x16x32_bf16 v[208:211], v[124:127], v[20:23], v[208:211]\n" \
"ds_read_b128 v[156:159], v178 offset:31744\n" \
"s_waitcnt lgkmcnt(4)\n" \
"v_mfma_f32_16x16x32_bf16 v[212:215], v[124:127], v[52:55], v[212:215]\n" \
"s_waitcnt vmcnt(20)\n" \
"s_barrier\n" \
"v_mfma_f32_16x16x32_bf16 v[184:187], v[128:131], v[24:27], v[184:187]\n" \
"ds_read_b128 v[96:99], v179 offset:0\n" \
"v_mfma_f32_16x16x32_bf16 v[188:191], v[128:131], v[56:59], v[188:191]\n" \
"v_mfma_f32_16x16x32_bf16 v[192:195], v[132:135], v[24:27], v[192:195]\n" \
"ds_read_b128 v[100:103], v179 offset:1024\n" \
"s_mov_b32 s44, s41\n" \
"v_mfma_f32_16x16x32_bf16 v[196:199], v[132:135], v[56:59], v[196:199]\n" \
"v_mfma_f32_16x16x32_bf16 v[200:203], v[136:139], v[24:27], v[200:203]\n" \
"ds_read_b128 v[104:107], v179 offset:2048\n" \
"s_mov_b32 s41, s48\n" \
"v_mfma_f32_16x16x32_bf16 v[204:207], v[136:139], v[56:59], v[204:207]\n" \
"v_mfma_f32_16x16x32_bf16 v[208:211], v[140:143], v[24:27], v[208:211]\n" \
"ds_read_b128 v[108:111], v179 offset:3072\n" \
"s_waitcnt lgkmcnt(4)\n" \
"v_mfma_f32_16x16x32_bf16 v[212:215], v[140:143], v[56:59], v[212:215]\n" \
"v_mfma_f32_16x16x32_bf16 v[184:187], v[144:147], v[28:31], v[184:187]\n" \
"ds_read_b128 v[112:115], v179 offset:4096\n" \
"v_mfma_f32_16x16x32_bf16 v[188:191], v[144:147], v[60:63], v[188:191]\n" \
"v_mfma_f32_16x16x32_bf16 v[192:195], v[148:151], v[28:31], v[192:195]\n" \
"ds_read_b128 v[116:119], v179 offset:5120\n" \
"v_mfma_f32_16x16x32_bf16 v[196:199], v[148:151], v[60:63], v[196:199]\n" \
"v_mfma_f32_16x16x32_bf16 v[200:203], v[152:155], v[28:31], v[200:203]\n" \
"ds_read_b128 v[120:123], v179 offset:6144\n" \
"v_mfma_f32_16x16x32_bf16 v[204:207], v[152:155], v[60:63], v[204:207]\n" \
"v_mfma_f32_16x16x32_bf16 v[208:211], v[156:159], v[28:31], v[208:211]\n" \
"ds_read_b128 v[124:127], v179 offset:7168\n" \
"s_waitcnt lgkmcnt(4)\n" \
"v_mfma_f32_16x16x32_bf16 v[212:215], v[156:159], v[60:63], v[212:215]\n" \
"v_mov_b32 v178, v179\n" \
"v_mfma_f32_16x16x32_bf16 v[64:67], v[96:99], v[0:3], 0\n" \
"ds_read_b128 v[128:131], v178 offset:8192\n" \
"buffer_load_dwordx4 v224, %[rs], s43 offen offset:0 sc1 lds\n" \
"v_mfma_f32_16x16x32_bf16 v[68:71], v[96:99], v[32:35], 0\n" \
"v_mfma_f32_16x16x32_bf16 v[72:75], v[100:103], v[0:3], 0\n" \
"ds_read_b128 v[132:135], v178 offset:9216\n" \
"v_readlane_b32 s48, v238, s42\n" \
"v_mfma_f32_16x16x32_bf16 v[76:79], v[100:103], v[32:35], 0\n" \
"v_mfma_f32_16x16x32_bf16 v[80:83], v[104:107], v[0:3], 0\n" \
"ds_read_b128 v[136:139], v178 offset:10240\n" \
"buffer_load_dwordx4 v224, %[rs], s43 offen offset:1024 sc1 lds\n" \
"v_mfma_f32_16x16x32_bf16 v[84:87], v[104:107], v[32:35], 0\n" \
"v_cvt_pk_bf16_f32 v216, v184, v185\n" \
"v_cvt_pk_bf16_f32 v217, v186, v187\n" \
"v_mfma_f32_16x16x32_bf16 v[88:91], v[108:111], v[0:3], 0\n" \
"ds_read_b128 v[140:143], v178 offset:11264\n" \
"s_waitcnt lgkmcnt(4)\n" \
"v_mfma_f32_16x16x32_bf16 v[92:95], v[108:111], v[32:35], 0\n" \
"v_cvt_pk_bf16_f32 v218, v192, v193\n" \
"v_cvt_pk_bf16_f32 v219, v194, v195\n" \
"v_mfma_f32_16x16x32_bf16 v[64:67], v[112:115], v[4:7], v[64:67]\n" \
"ds_read_b128 v[144:147], v178 offset:12288\n" \
"s_add_u32 s42, s42, 1\n" \
"v_mfma_f32_16x16x32_bf16 v[68:71], v[112:115], v[36:39], v[68:71]\n" \
"s_add_u32 s83, s69, 2048\n" \
"buffer_store_dwordx4 v[216:219], v224, %[rs], s83 offen sc1\n" \
"v_mfma_f32_16x16x32_bf16 v[72:75], v[116:119], v[4:7], v[72:75]\n" \
"ds_read_b128 v[148:151], v178 offset:13312\n" \
"buffer_load_dwordx4 v224, %[rs], s43 offen offset:2048 sc1 lds\n" \
"v_mfma_f32_16x16x32_bf16 v[76:79], v[116:119], v[36:39], v[76:79]\n" \
"v_cvt_pk_bf16_f32 v220, v188, v189\n" \
"v_cvt_pk_bf16_f32 v221, v190, v191\n" \
"v_mfma_f32_16x16x32_bf16 v[80:83], v[120:123], v[4:7], v[80:83]\n" \
"ds_read_b128 v[152:155], v178 offset:14336\n" \
"v_add_u32 v179, s48, v224\n" \
"v_mfma_f32_16x16x32_bf16 v[84:87], v[120:123], v[36:39], v[84:87]\n" \
"v_cvt_pk_bf16_f32 v222, v196, v197\n" \
"v_cvt_pk_bf16_f32 v223, v198, v199\n" \
"v_mfma_f32_16x16x32_bf16 v[88:91], v[124:127], v[4:7], v[88:91]\n" \
"ds_read_b128 v[156:159], v178 offset:15360\n" \
"s_waitcnt lgkmcnt(4)\n" \
"v_mfma_f32_16x16x32_bf16 v[92:95], v[124:127], v[36:39], v[92:95]\n" \
"s_add_u32 s83, s71, 2048\n" \
"buffer_store_dwordx4 v[220:223], v224, %[rs], s83 offen sc1\n" \
"v_mfma_f32_16x16x32_bf16 v[64:67], v[128:131], v[8:11], v[64:67]\n" \
"ds_read_b128 v[96:99], v178 offset:16384\n" \
"buffer_load_dwordx4 v224, %[rs], s43 offen offset:3072 sc1 lds\n" \
"v_mfma_f32_16x16x32_bf16 v[68:71], v[128:131], v[40:43], v[68:71]\n" \
"v_cvt_pk_bf16_f32 v216, v200, v201\n" \
"v_cvt_pk_bf16_f32 v217, v202, v203\n" \
"v_mfma_f32_16x16x32_bf16 v[72:75], v[132:135], v[8:11], v[72:75]\n" \
"ds_read_b128 v[100:103], v178 offset:17408\n" \
"s_add_u32 m0, m0, 0x1000\n" \
"s_add_u32 s43, s43, 0x1000\n" \
"v_mfma_f32_16x16x32_bf16 v[76:79], v[132:135], v[40:43], v[76:79]\n" \
"v_cvt_pk_bf16_f32 v218, v208, v209\n" \
"v_cvt_pk_bf16_f32 v219, v210, v211\n" \
"v_mfma_f32_16x16x32_bf16 v[80:83], v[136:139], v[8:11], v[80:83]\n" \
"ds_read_b128 v[104:107], v178 offset:18432\n" \
"s_add_u32 s83, s69, 3072\n" \
"buffer_store_dwordx4 v[216:219], v224, %[rs], s83 offen sc1\n" \
"v_mfma_f32_16x16x32_bf16 v[84:87], v[136:139], v[40:43], v[84:87]\n" \
"v_cvt_pk_bf16_f32 v220, v204, v205\n" \
"v_cvt_pk_bf16_f32 v221, v206, v207\n" \
"v_mfma_f32_16x16x32_bf16 v[88:91], v[140:143], v[8:11], v[88:91]\n" \
"ds_read_b128 v[108:111], v178 offset:19456\n" \
"s_waitcnt lgkmcnt(4)\n" \
"v_mfma_f32_16x16x32_bf16 v[92:95], v[140:143], v[40:43], v[92:95]\n" \
"v_cvt_pk_bf16_f32 v222, v212, v213\n" \
"v_cvt_pk_bf16_f32 v223, v214, v215\n" \
"v_mfma_f32_16x16x32_bf16 v[64:67], v[144:147], v[12:15], v[64:67]\n" \
"ds_read_b128 v[112:115], v178 offset:20480\n" \
"buffer_load_dwordx4 v224, %[rs], s43 offen offset:0 sc1 lds\n" \
"v_mfma_f32_16x16x32_bf16 v[68:71], v[144:147], v[44:47], v[68:71]\n" \
"v_mfma_f32_16x16x32_bf16 v[72:75], v[148:151], v[12:15], v[72:75]\n" \
"ds_read_b128 v[116:119], v178 offset:21504\n" \
"v_mfma_f32_16x16x32_bf16 v[76:79], v[148:151], v[44:47], v[76:79]\n" \
"v_mfma_f32_16x16x32_bf16 v[80:83], v[152:155], v[12:15], v[80:83]\n" \
"ds_read_b128 v[120:123], v178 offset:22528\n" \
"buffer_load_dwordx4 v224, %[rs], s43 offen offset:1024 sc1 lds\n" \
"v_mfma_f32_16x16x32_bf16 v[84:87], v[152:155], v[44:47], v[84:87]\n" \
"v_mfma_f32_16x16x32_bf16 v[88:91], v[156:159], v[12:15], v[88:91]\n" \
"ds_read_b128 v[124:127], v178 offset:23552\n" \
"s_waitcnt lgkmcnt(4)\n" \
"v_mfma_f32_16x16x32_bf16 v[92:95], v[156:159], v[44:47], v[92:95]\n" \
"v_mfma_f32_16x16x32_bf16 v[64:67], v[96:99], v[16:19], v[64:67]\n" \
"ds_read_b128 v[128:131], v178 offset:24576\n" \
"s_add_u32 s83, s71, 3072\n" \
"buffer_store_dwordx4 v[220:223], v224, %[rs], s83 offen sc1\n" \
"v_mfma_f32_16x16x32_bf16 v[68:71], v[96:99], v[48:51], v[68:71]\n" \
"v_mfma_f32_16x16x32_bf16 v[72:75], v[100:103], v[16:19], v[72:75]\n" \
"ds_read_b128 v[132:135], v178 offset:25600\n" \
"buffer_load_dwordx4 v224, %[rs], s43 offen offset:2048 sc1 lds\n" \
"v_mfma_f32_16x16x32_bf16 v[76:79], v[100:103], v[48:51], v[76:79]\n" \
"v_mfma_f32_16x16x32_bf16 v[80:83], v[104:107], v[16:19], v[80:83]\n" \
"ds_read_b128 v[136:139], v178 offset:26624\n" \
"v_mfma_f32_16x16x32_bf16 v[84:87], v[104:107], v[48:51], v[84:87]\n" \
"v_mfma_f32_16x16x32_bf16 v[88:91], v[108:111], v[16:19], v[88:91]\n" \
"ds_read_b128 v[140:143], v178 offset:27648\n" \
"s_waitcnt lgkmcnt(4)\n" \
"v_mfma_f32_16x16x32_bf16 v[92:95], v[108:111], v[48:51], v[92:95]\n" \
"v_mfma_f32_16x16x32_bf16 v[64:67], v[112:115], v[20:23], v[64:67]\n" \
"ds_read_b128 v[144:147], v178 offset:28672\n" \
"buffer_load_dwordx4 v224, %[rs], s43 offen offset:3072 sc1 lds\n" \
"v_mfma_f32_16x16x32_bf16 v[68:71], v[112:115], v[52:55], v[68:71]\n" \
"v_mfma_f32_16x16x32_bf16 v[72:75], v[116:119], v[20:23], v[72:75]\n" \
"ds_read_b128 v[148:151], v178 offset:29696\n" \
"v_readlane_b32 s43, v239, s42\n" \
"v_mfma_f32_16x16x32_bf16 v[76:79], v[116:119], v[52:55], v[76:79]\n" \
"v_mfma_f32_16x16x32_bf16 v[80:83], v[120:123], v[20:23], v[80:83]\n" \
"ds_read_b128 v[152:155], v178 offset:30720\n" \
"s_add_u32 m0, s44, s37\n" \
"v_mfma_f32_16x16x32_bf16 v[84:87], v[120:123], v[52:55], v[84:87]\n" \
"v_mfma_f32_16x16x32_bf16 v[88:91], v[124:127], v[20:23], v[88:91]\n" \
"ds_read_b128 v[156:159], v178 offset:31744\n" \
"s_waitcnt lgkmcnt(4)\n" \
"v_mfma_f32_16x16x32_bf16 v[92:95], v[124:127], v[52:55], v[92:95]\n" \
"s_waitcnt vmcnt(24)\n" \
"s_barrier\n" \
"v_mfma_f32_16x16x32_bf16 v[64:67], v[128:131], v[24:27], v[64:67]\n" \
"ds_read_b128 v[96:99], v179 offset:0\n" \
"v_mfma_f32_16x16x32_bf16 v[68:71], v[128:131], v[56:59], v[68:71]\n" \
"v_mfma_f32_16x16x32_bf16 v[72:75], v[132:135], v[24:27], v[72:75]\n" \
"ds_read_b128 v[100:103], v179 offset:1024\n" \
"s_mov_b32 s44, s41\n" \
"v_mfma_f32_16x16x32_bf16 v[76:79], v[132:135], v[56:59], v[76:79]\n" \
"v_mfma_f32_16x16x32_bf16 v[80:83], v[136:139], v[24:27], v[80:83]\n" \
"ds_read_b128 v[104:107], v179 offset:2048\n" \
"s_mov_b32 s41, s48\n" \
"v_mfma_f32_16x16x32_bf16 v[84:87], v[136:139], v[56:59], v[84:87]\n" \
"v_mfma_f32_16x16x32_bf16 v[88:91], v[140:143], v[24:27], v[88:91]\n" \
"ds_read_b128 v[108:111], v179 offset:3072\n" \
"s_waitcnt lgkmcnt(4)\n" \
"v_mfma_f32_16x16x32_bf16 v[92:95], v[140:143], v[56:59], v[92:95]\n" \
"v_mfma_f32_16x16x32_bf16 v[64:67], v[144:147], v[28:31], v[64:67]\n" \
"ds_read_b128 v[112:115], v179 offset:4096\n" \
"v_mfma_f32_16x16x32_bf16 v[68:71], v[144:147], v[60:63], v[68:71]\n" \
"v_mfma_f32_16x16x32_bf16 v[72:75], v[148:151], v[28:31], v[72:75]\n" \
"ds_read_b128 v[116:119], v179 offset:5120\n" \
"v_mfma_f32_16x16x32_bf16 v[76:79], v[148:151], v[60:63], v[76:79]\n" \
"v_mfma_f32_16x16x32_bf16 v[80:83], v[152:155], v[28:31], v[80:83]\n" \
"ds_read_b128 v[120:123], v179 offset:6144\n" \
"v_mfma_f32_16x16x32_bf16 v[84:87], v[152:155], v[60:63], v[84:87]\n" \
"v_mfma_f32_16x16x32_bf16 v[88:91], v[156:159], v[28:31], v[88:91]\n" \
"ds_read_b128 v[124:127], v179 offset:7168\n" \
"s_waitcnt lgkmcnt(4)\n" \
"v_mfma_f32_16x16x32_bf16 v[92:95], v[156:159], v[60:63], v[92:95]\n" \
"v_mov_b32 v178, v179\n" \
"v_mfma_f32_16x16x32_bf16 v[184:187], v[96:99], v[0:3], 0\n" \
"ds_read_b128 v[128:131], v178 offset:8192\n" \
"buffer_load_dwordx4 v224, %[rs], s43 offen offset:0 sc1 lds\n" \
"v_mfma_f32_16x16x32_bf16 v[188:191], v[96:99], v[32:35], 0\n" \
"v_mfma_f32_16x16x32_bf16 v[192:195], v[100:103], v[0:3], 0\n" \
"ds_read_b128 v[132:135], v178 offset:9216\n" \
"v_readlane_b32 s48, v238, s42\n" \
"v_mfma_f32_16x16x32_bf16 v[196:199], v[100:103], v[32:35], 0\n" \
"v_mfma_f32_16x16x32_bf16 v[200:203], v[104:107], v[0:3], 0\n" \
"ds_read_b128 v[136:139], v178 offset:10240\n" \
"buffer_load_dwordx4 v224, %[rs], s43 offen offset:1024 sc1 lds\n" \
"v_mfma_f32_16x16x32_bf16 v[204:207], v[104:107], v[32:35], 0\n" \
"v_cvt_pk_bf16_f32 v216, v64, v65\n" \
"v_cvt_pk_bf16_f32 v217, v66, v67\n" \
"v_mfma_f32_16x16x32_bf16 v[208:211], v[108:111], v[0:3], 0\n" \
"ds_read_b128 v[140:143], v178 offset:11264\n" \
"s_waitcnt lgkmcnt(4)\n" \
"v_mfma_f32_16x16x32_bf16 v[212:215], v[108:111], v[32:35], 0\n" \
"v_cvt_pk_bf16_f32 v218, v72, v73\n" \
"v_cvt_pk_bf16_f32 v219, v74, v75\n" \
"v_mfma_f32_16x16x32_bf16 v[184:187], v[112:115], v[4:7], v[184:187]\n" \
"ds_read_b128 v[144:147], v178 offset:12288\n" \
"s_add_u32 s42, s42, 1\n" \
"v_mfma_f32_16x16x32_bf16 v[188:191], v[112:115], v[36:39], v[188:191]\n" \
"s_add_u32 s83, s69, 4096\n" \
"buffer_store_dwordx4 v[216:219], v224, %[rs], s83 offen sc1\n" \
"v_mfma_f32_16x16x32_bf16 v[192:195], v[116:119], v[4:7], v[192:195]\n" \
"ds_read_b128 v[148:151], v178 offset:13312\n" \
"buffer_load_dwordx4 v224, %[rs], s43 offen offset:2048 sc1 lds\n" \
"v_mfma_f32_16x16x32_bf16 v[196:199], v[116:119], v[36:39], v[196:199]\n" \
"v_cvt_pk_bf16_f32 v220, v68, v69\n" \
"v_cvt_pk_bf16_f32 v221, v70, v71\n" \
"v_mfma_f32_16x16x32_bf16 v[200:203], v[120:123], v[4:7], v[200:203]\n" \
"ds_read_b128 v[152:155], v178 offset:14336\n" \
"v_add_u32 v179, s48, v224\n" \
"v_mfma_f32_16x16x32_bf16 v[204:207], v[120:123], v[36:39], v[204:207]\n" \
"v_cvt_pk_bf16_f32 v222, v76, v77\n" \
"v_cvt_pk_bf16_f32 v223, v78, v79\n" \
"v_mfma_f32_16x16x32_bf16 v[208:211], v[124:127], v[4:7], v[208:211]\n" \
"ds_read_b128 v[156:159], v178 offset:15360\n" \
"s_waitcnt lgkmcnt(4)\n" \
"v_mfma_f32_16x16x32_bf16 v[212:215], v[124:127], v[36:39], v[212:215]\n" \
"s_add_u32 s83, s71, 4096\n" \
"buffer_store_dwordx4 v[220:223], v224, %[rs], s83 offen sc1\n" \
"v_mfma_f32_16x16x32_bf16 v[184:187], v[128:131], v[8:11], v[184:187]\n" \
"ds_read_b128 v[96:99], v178 offset:16384\n" \
"buffer_load_dwordx4 v224, %[rs], s43 offen offset:3072 sc1 lds\n" \
"v_mfma_f32_16x16x32_bf16 v[188:191], v[128:131], v[40:43], v[188:191]\n" \
"v_cvt_pk_bf16_f32 v216, v80, v81\n" \
"v_cvt_pk_bf16_f32 v217, v82, v83\n" \
"v_mfma_f32_16x16x32_bf16 v[192:195], v[132:135], v[8:11], v[192:195]\n" \
"ds_read_b128 v[100:103], v178 offset:17408\n" \
"s_add_u32 m0, m0, 0x1000\n" \
"s_add_u32 s43, s43, 0x1000\n" \
"v_mfma_f32_16x16x32_bf16 v[196:199], v[132:135], v[40:43], v[196:199]\n" \
"v_cvt_pk_bf16_f32 v218, v88, v89\n" \
"v_cvt_pk_bf16_f32 v219, v90, v91\n" \
"v_mfma_f32_16x16x32_bf16 v[200:203], v[136:139], v[8:11], v[200:203]\n" \
"ds_read_b128 v[104:107], v178 offset:18432\n" \
"s_add_u32 s83, s69, 5120\n" \
"buffer_store_dwordx4 v[216:219], v224, %[rs], s83 offen sc1\n" \
"v_mfma_f32_16x16x32_bf16 v[204:207], v[136:139], v[40:43], v[204:207]\n" \
"v_cvt_pk_bf16_f32 v220, v84, v85\n" \
"v_cvt_pk_bf16_f32 v221, v86, v87\n" \
"v_mfma_f32_16x16x32_bf16 v[208:211], v[140:143], v[8:11], v[208:211]\n" \
"ds_read_b128 v[108:111], v178 offset:19456\n" \
"s_waitcnt lgkmcnt(4)\n" \
"v_mfma_f32_16x16x32_bf16 v[212:215], v[140:143], v[40:43], v[212:215]\n" \
"v_cvt_pk_bf16_f32 v222, v92, v93\n" \
"v_cvt_pk_bf16_f32 v223, v94, v95\n" \
"v_mfma_f32_16x16x32_bf16 v[184:187], v[144:147], v[12:15], v[184:187]\n" \
"ds_read_b128 v[112:115], v178 offset:20480\n" \
"buffer_load_dwordx4 v224, %[rs], s43 offen offset:0 sc1 lds\n" \
"v_mfma_f32_16x16x32_bf16 v[188:191], v[144:147], v[44:47], v[188:191]\n" \
"v_mfma_f32_16x16x32_bf16 v[192:195], v[148:151], v[12:15], v[192:195]\n" \
"ds_read_b128 v[116:119], v178 offset:21504\n" \
"v_mfma_f32_16x16x32_bf16 v[196:199], v[148:151], v[44:47], v[196:199]\n" \
"v_mfma_f32_16x16x32_bf16 v[200:203], v[152:155], v[12:15], v[200:203]\n" \
"ds_read_b128 v[120:123], v178 offset:22528\n" \
"buffer_load_dwordx4 v224, %[rs], s43 offen offset:1024 sc1 lds\n" \
"v_mfma_f32_16x16x32_bf16 v[204:207], v[152:155], v[44:47], v[204:207]\n" \
"v_mfma_f32_16x16x32_bf16 v[208:211], v[156:159], v[12:15], v[208:211]\n" \
"ds_read_b128 v[124:127], v178 offset:23552\n" \
"s_waitcnt lgkmcnt(4)\n" \
"v_mfma_f32_16x16x32_bf16 v[212:215], v[156:159], v[44:47], v[212:215]\n" \
"v_mfma_f32_16x16x32_bf16 v[184:187], v[96:99], v[16:19], v[184:187]\n" \
"ds_read_b128 v[128:131], v178 offset:24576\n" \
"s_add_u32 s83, s71, 5120\n" \
"buffer_store_dwordx4 v[220:223], v224, %[rs], s83 offen sc1\n" \
"v_mfma_f32_16x16x32_bf16 v[188:191], v[96:99], v[48:51], v[188:191]\n" \
"v_mfma_f32_16x16x32_bf16 v[192:195], v[100:103], v[16:19], v[192:195]\n" \
"ds_read_b128 v[132:135], v178 offset:25600\n" \
"buffer_load_dwordx4 v224, %[rs], s43 offen offset:2048 sc1 lds\n" \
"v_mfma_f32_16x16x32_bf16 v[196:199], v[100:103], v[48:51], v[196:199]\n" \
"v_mfma_f32_16x16x32_bf16 v[200:203], v[104:107], v[16:19], v[200:203]\n" \
"ds_read_b128 v[136:139], v178 offset:26624\n" \
"v_mfma_f32_16x16x32_bf16 v[204:207], v[104:107], v[48:51], v[204:207]\n" \
"v_mfma_f32_16x16x32_bf16 v[208:211], v[108:111], v[16:19], v[208:211]\n" \
"ds_read_b128 v[140:143], v178 offset:27648\n" \
"s_waitcnt lgkmcnt(4)\n" \
"v_mfma_f32_16x16x32_bf16 v[212:215], v[108:111], v[48:51], v[212:215]\n" \
"v_mfma_f32_16x16x32_bf16 v[184:187], v[112:115], v[20:23], v[184:187]\n" \
"ds_read_b128 v[144:147], v178 offset:28672\n" \
"buffer_load_dwordx4 v224, %[rs], s43 offen offset:3072 sc1 lds\n" \
"v_mfma_f32_16x16x32_bf16 v[188:191], v[112:115], v[52:55], v[188:191]\n" \
"v_mfma_f32_16x16x32_bf16 v[192:195], v[116:119], v[20:23], v[192:195]\n" \
"ds_read_b128 v[148:151], v178 offset:29696\n" \
"v_readlane_b32 s43, v239, s42\n" \
"v_mfma_f32_16x16x32_bf16 v[196:199], v[116:119], v[52:55], v[196:199]\n" \
"v_mfma_f32_16x16x32_bf16 v[200:203], v[120:123], v[20:23], v[200:203]\n" \
"ds_read_b128 v[152:155], v178 offset:30720\n" \
"s_add_u32 m0, s44, s37\n" \
"v_mfma_f32_16x16x32_bf16 v[204:207], v[120:123], v[52:55], v[204:207]\n" \
"v_mfma_f32_16x16x32_bf16 v[208:211], v[124:127], v[20:23], v[208:211]\n" \
"ds_read_b128 v[156:159], v178 offset:31744\n" \
"s_waitcnt lgkmcnt(4)\n" \
"v_mfma_f32_16x16x32_bf16 v[212:215], v[124:127], v[52:55], v[212:215]\n" \
"s_waitcnt vmcnt(24)\n" \
"s_barrier\n" \
"v_mfma_f32_16x16x32_bf16 v[184:187], v[128:131], v[24:27], v[184:187]\n" \
"ds_read_b128 v[96:99], v179 offset:0\n" \
"v_mfma_f32_16x16x32_bf16 v[188:191], v[128:131], v[56:59], v[188:191]\n" \
"v_mfma_f32_16x16x32_bf16 v[192:195], v[132:135], v[24:27], v[192:195]\n" \
"ds_read_b128 v[100:103], v179 offset:1024\n" \
"s_mov_b32 s44, s41\n" \
"v_mfma_f32_16x16x32_bf16 v[196:199], v[132:135], v[56:59], v[196:199]\n" \
"v_mfma_f32_16x16x32_bf16 v[200:203], v[136:139], v[24:27], v[200:203]\n" \
"ds_read_b128 v[104:107], v179 offset:2048\n" \
"s_mov_b32 s41, s48\n" \
"v_mfma_f32_16x16x32_bf16 v[204:207], v[136:139], v[56:59], v[204:207]\n" \
"v_mfma_f32_16x16x32_bf16 v[208:211], v[140:143], v[24:27], v[208:211]\n" \
"ds_read_b128 v[108:111], v179 offset:3072\n" \
"s_waitcnt lgkmcnt(4)\n" \
"v_mfma_f32_16x16x32_bf16 v[212:215], v[140:143], v[56:59], v[212:215]\n" \
"v_mfma_f32_16x16x32_bf16 v[184:187], v[144:147], v[28:31], v[184:187]\n" \
"ds_read_b128 v[112:115], v179 offset:4096\n" \
"v_mfma_f32_16x16x32_bf16 v[188:191], v[144:147], v[60:63], v[188:191]\n" \
"v_mfma_f32_16x16x32_bf16 v[192:195], v[148:151], v[28:31], v[192:195]\n" \
"ds_read_b128 v[116:119], v179 offset:5120\n" \
"v_mfma_f32_16x16x32_bf16 v[196:199], v[148:151], v[60:63], v[196:199]\n" \
"v_mfma_f32_16x16x32_bf16 v[200:203], v[152:155], v[28:31], v[200:203]\n" \
"ds_read_b128 v[120:123], v179 offset:6144\n" \
"v_mfma_f32_16x16x32_bf16 v[204:207], v[152:155], v[60:63], v[204:207]\n" \
"v_mfma_f32_16x16x32_bf16 v[208:211], v[156:159], v[28:31], v[208:211]\n" \
"ds_read_b128 v[124:127], v179 offset:7168\n" \
"s_waitcnt lgkmcnt(4)\n" \
"v_mfma_f32_16x16x32_bf16 v[212:215], v[156:159], v[60:63], v[212:215]\n" \
"v_mov_b32 v178, v179\n" \
"v_mfma_f32_16x16x32_bf16 v[64:67], v[96:99], v[0:3], 0\n" \
"ds_read_b128 v[128:131], v178 offset:8192\n" \
"v_mfma_f32_16x16x32_bf16 v[68:71], v[96:99], v[32:35], 0\n" \
"v_mfma_f32_16x16x32_bf16 v[72:75], v[100:103], v[0:3], 0\n" \
"ds_read_b128 v[132:135], v178 offset:9216\n" \
"v_readlane_b32 s48, v238, s42\n" \
"v_mfma_f32_16x16x32_bf16 v[76:79], v[100:103], v[32:35], 0\n" \
"v_mfma_f32_16x16x32_bf16 v[80:83], v[104:107], v[0:3], 0\n" \
"ds_read_b128 v[136:139], v178 offset:10240\n" \
"v_mfma_f32_16x16x32_bf16 v[84:87], v[104:107], v[32:35], 0\n" \
"v_cvt_pk_bf16_f32 v216, v184, v185\n" \
"v_cvt_pk_bf16_f32 v217, v186, v187\n" \
"v_mfma_f32_16x16x32_bf16 v[88:91], v[108:111], v[0:3], 0\n" \
"ds_read_b128 v[140:143], v178 offset:11264\n" \
"s_waitcnt lgkmcnt(4)\n" \
"v_mfma_f32_16x16x32_bf16 v[92:95], v[108:111], v[32:35], 0\n" \
"v_cvt_pk_bf16_f32 v218, v192, v193\n" \
"v_cvt_pk_bf16_f32 v219, v194, v195\n" \
"v_mfma_f32_16x16x32_bf16 v[64:67], v[112:115], v[4:7], v[64:67]\n" \
"ds_read_b128 v[144:147], v178 offset:12288\n" \
"s_add_u32 s42, s42, 1\n" \
"v_mfma_f32_16x16x32_bf16 v[68:71], v[112:115], v[36:39], v[68:71]\n" \
"s_add_u32 s83, s69, 6144\n" \
"buffer_store_dwordx4 v[216:219], v224, %[rs], s83 offen sc1\n" \
"v_mfma_f32_16x16x32_bf16 v[72:75], v[116:119], v[4:7], v[72:75]\n" \
"ds_read_b128 v[148:151], v178 offset:13312\n" \
"v_cvt_pk_bf16_f32 v220, v188, v189\n" \
"v_mfma_f32_16x16x32_bf16 v[76:79], v[116:119], v[36:39], v[76:79]\n" \
"v_cvt_pk_bf16_f32 v221, v190, v191\n" \
"v_cvt_pk_bf16_f32 v222, v196, v197\n" \
"v_mfma_f32_16x16x32_bf16 v[80:83], v[120:123], v[4:7], v[80:83]\n" \
"ds_read_b128 v[152:155], v178 offset:14336\n" \
"v_add_u32 v179, s48, v224\n" \
"v_mfma_f32_16x16x32_bf16 v[84:87], v[120:123], v[36:39], v[84:87]\n" \
"v_cvt_pk_bf16_f32 v223, v198, v199\n" \
"s_add_u32 s83, s71, 6144\n" \
"buffer_store_dwordx4 v[220:223], v224, %[rs], s83 offen sc1\n" \
"v_mfma_f32_16x16x32_bf16 v[88:91], v[124:127], v[4:7], v[88:91]\n" \
"ds_read_b128 v[156:159], v178 offset:15360\n" \
"s_waitcnt lgkmcnt(4)\n" \
"v_mfma_f32_16x16x32_bf16 v[92:95], v[124:127], v[36:39], v[92:95]\n" \
"v_cvt_pk_bf16_f32 v216, v200, v201\n" \
"v_cvt_pk_bf16_f32 v217, v202, v203\n" \
"v_mfma_f32_16x16x32_bf16 v[64:67], v[128:131], v[8:11], v[64:67]\n" \
"ds_read_b128 v[96:99], v178 offset:16384\n" \
"v_cvt_pk_bf16_f32 v218, v208, v209\n" \
"v_mfma_f32_16x16x32_bf16 v[68:71], v[128:131], v[40:43], v[68:71]\n" \
"v_cvt_pk_bf16_f32 v219, v210, v211\n" \
"s_add_u32 s83, s69, 7168\n" \
"buffer_store_dwordx4 v[216:219], v224, %[rs], s83 offen sc1\n" \
"v_mfma_f32_16x16x32_bf16 v[72:75], v[132:135], v[8:11], v[72:75]\n" \
"ds_read_b128 v[100:103], v178 offset:17408\n" \
"v_cvt_pk_bf16_f32 v220, v204, v205\n" \
"v_mfma_f32_16x16x32_bf16 v[76:79], v[132:135], v[40:43], v[76:79]\n" \
"v_cvt_pk_bf16_f32 v221, v206, v207\n" \
"v_cvt_pk_bf16_f32 v222, v212, v213\n" \
"v_mfma_f32_16x16x32_bf16 v[80:83], v[136:139], v[8:11], v[80:83]\n" \
"ds_read_b128 v[104:107], v178 offset:18432\n" \
"v_cvt_pk_bf16_f32 v223, v214, v215\n" \
"v_mfma_f32_16x16x32_bf16 v[84:87], v[136:139], v[40:43], v[84:87]\n" \
"s_add_u32 s83, s71, 7168\n" \
"buffer_store_dwordx4 v[220:223], v224, %[rs], s83 offen sc1\n" \
"v_mfma_f32_16x16x32_bf16 v[88:91], v[140:143], v[8:11], v[88:91]\n" \
"ds_read_b128 v[108:111], v178 offset:19456\n" \
"s_waitcnt lgkmcnt(4)\n" \
"v_mfma_f32_16x16x32_bf16 v[92:95], v[140:143], v[40:43], v[92:95]\n" \
"v_mfma_f32_16x16x32_bf16 v[64:67], v[144:147], v[12:15], v[64:67]\n" \
"ds_read_b128 v[112:115], v178 offset:20480\n" \
"v_mfma_f32_16x16x32_bf16 v[68:71], v[144:147], v[44:47], v[68:71]\n" \
"v_mfma_f32_16x16x32_bf16 v[72:75], v[148:151], v[12:15], v[72:75]\n" \
"ds_read_b128 v[116:119], v178 offset:21504\n" \
"v_mfma_f32_16x16x32_bf16 v[76:79], v[148:151], v[44:47], v[76:79]\n" \
"v_mfma_f32_16x16x32_bf16 v[80:83], v[152:155], v[12:15], v[80:83]\n" \
"ds_read_b128 v[120:123], v178 offset:22528\n" \
"v_mfma_f32_16x16x32_bf16 v[84:87], v[152:155], v[44:47], v[84:87]\n" \
"v_mfma_f32_16x16x32_bf16 v[88:91], v[156:159], v[12:15], v[88:91]\n" \
"ds_read_b128 v[124:127], v178 offset:23552\n" \
"s_waitcnt lgkmcnt(4)\n" \
"v_mfma_f32_16x16x32_bf16 v[92:95], v[156:159], v[44:47], v[92:95]\n" \
"v_mfma_f32_16x16x32_bf16 v[64:67], v[96:99], v[16:19], v[64:67]\n" \
"ds_read_b128 v[128:131], v178 offset:24576\n" \
"v_mfma_f32_16x16x32_bf16 v[68:71], v[96:99], v[48:51], v[68:71]\n" \
"v_mfma_f32_16x16x32_bf16 v[72:75], v[100:103], v[16:19], v[72:75]\n" \
"ds_read_b128 v[132:135], v178 offset:25600\n" \
"v_mfma_f32_16x16x32_bf16 v[76:79], v[100:103], v[48:51], v[76:79]\n" \
"v_mfma_f32_16x16x32_bf16 v[80:83], v[104:107], v[16:19], v[80:83]\n" \
"ds_read_b128 v[136:139], v178 offset:26624\n" \
"v_mfma_f32_16x16x32_bf16 v[84:87], v[104:107], v[48:51], v[84:87]\n" \
"v_mfma_f32_16x16x32_bf16 v[88:91], v[108:111], v[16:19], v[88:91]\n" \
"ds_read_b128 v[140:143], v178 offset:27648\n" \
"s_waitcnt lgkmcnt(4)\n" \
"v_mfma_f32_16x16x32_bf16 v[92:95], v[108:111], v[48:51], v[92:95]\n" \
"v_mfma_f32_16x16x32_bf16 v[64:67], v[112:115], v[20:23], v[64:67]\n" \
"ds_read_b128 v[144:147], v178 offset:28672\n" \
"v_mfma_f32_16x16x32_bf16 v[68:71], v[112:115], v[52:55], v[68:71]\n" \
"v_mfma_f32_16x16x32_bf16 v[72:75], v[116:119], v[20:23], v[72:75]\n" \
"ds_read_b128 v[148:151], v178 offset:29696\n" \
"v_mfma_f32_16x16x32_bf16 v[76:79], v[116:119], v[52:55], v[76:79]\n" \
"v_mfma_f32_16x16x32_bf16 v[80:83], v[120:123], v[20:23], v[80:83]\n" \
"ds_read_b128 v[152:155], v178 offset:30720\n" \
"v_mfma_f32_16x16x32_bf16 v[84:87], v[120:123], v[52:55], v[84:87]\n" \
"v_mfma_f32_16x16x32_bf16 v[88:91], v[124:127], v[20:23], v[88:91]\n" \
"ds_read_b128 v[156:159], v178 offset:31744\n" \
"s_waitcnt lgkmcnt(4)\n" \
"v_mfma_f32_16x16x32_bf16 v[92:95], v[124:127], v[52:55], v[92:95]\n" \
"s_waitcnt vmcnt(16)\n" \
"s_barrier\n" \
"v_mfma_f32_16x16x32_bf16 v[64:67], v[128:131], v[24:27], v[64:67]\n" \
"ds_read_b128 v[96:99], v179 offset:0\n" \
"v_mfma_f32_16x16x32_bf16 v[68:71], v[128:131], v[56:59], v[68:71]\n" \
"v_mfma_f32_16x16x32_bf16 v[72:75], v[132:135], v[24:27], v[72:75]\n" \
"ds_read_b128 v[100:103], v179 offset:1024\n" \
"s_mov_b32 s44, s41\n" \
"v_mfma_f32_16x16x32_bf16 v[76:79], v[132:135], v[56:59], v[76:79]\n" \
"v_mfma_f32_16x16x32_bf16 v[80:83], v[136:139], v[24:27], v[80:83]\n" \
"ds_read_b128 v[104:107], v179 offset:2048\n" \
"s_mov_b32 s41, s48\n" \
"v_mfma_f32_16x16x32_bf16 v[84:87], v[136:139], v[56:59], v[84:87]\n" \
"v_mfma_f32_16x16x32_bf16 v[88:91], v[140:143], v[24:27], v[88:91]\n" \
"ds_read_b128 v[108:111], v179 offset:3072\n" \
"s_waitcnt lgkmcnt(4)\n" \
"v_mfma_f32_16x16x32_bf16 v[92:95], v[140:143], v[56:59], v[92:95]\n" \
"v_mfma_f32_16x16x32_bf16 v[64:67], v[144:147], v[28:31], v[64:67]\n" \
"ds_read_b128 v[112:115], v179 offset:4096\n" \
"v_mfma_f32_16x16x32_bf16 v[68:71], v[144:147], v[60:63], v[68:71]\n" \
"v_mfma_f32_16x16x32_bf16 v[72:75], v[148:151], v[28:31], v[72:75]\n" \
"ds_read_b128 v[116:119], v179 offset:5120\n" \
"v_mfma_f32_16x16x32_bf16 v[76:79], v[148:151], v[60:63], v[76:79]\n" \
"v_mfma_f32_16x16x32_bf16 v[80:83], v[152:155], v[28:31], v[80:83]\n" \
"ds_read_b128 v[120:123], v179 offset:6144\n" \
"v_mfma_f32_16x16x32_bf16 v[84:87], v[152:155], v[60:63], v[84:87]\n" \
"v_mfma_f32_16x16x32_bf16 v[88:91], v[156:159], v[28:31], v[88:91]\n" \
"ds_read_b128 v[124:127], v179 offset:7168\n" \
"s_waitcnt lgkmcnt(4)\n" \
"v_mfma_f32_16x16x32_bf16 v[92:95], v[156:159], v[60:63], v[92:95]\n" \
"v_mov_b32 v178, v179\n" \
"v_mfma_f32_16x16x32_bf16 v[184:187], v[96:99], v[0:3], 0\n" \
"ds_read_b128 v[128:131], v178 offset:8192\n" \
"v_mfma_f32_16x16x32_bf16 v[188:191], v[96:99], v[32:35], 0\n" \
"v_mfma_f32_16x16x32_bf16 v[192:195], v[100:103], v[0:3], 0\n" \
"ds_read_b128 v[132:135], v178 offset:9216\n" \
"v_readlane_b32 s48, v238, s42\n" \
"v_mfma_f32_16x16x32_bf16 v[196:199], v[100:103], v[32:35], 0\n" \
"v_mfma_f32_16x16x32_bf16 v[200:203], v[104:107], v[0:3], 0\n" \
"ds_read_b128 v[136:139], v178 offset:10240\n" \
"v_mfma_f32_16x16x32_bf16 v[204:207], v[104:107], v[32:35], 0\n" \
"v_cvt_pk_bf16_f32 v216, v64, v65\n" \
"v_cvt_pk_bf16_f32 v217, v66, v67\n" \
"v_mfma_f32_16x16x32_bf16 v[208:211], v[108:111], v[0:3], 0\n" \
"ds_read_b128 v[140:143], v178 offset:11264\n" \
"s_waitcnt lgkmcnt(4)\n" \
"v_mfma_f32_16x16x32_bf16 v[212:215], v[108:111], v[32:35], 0\n" \
"v_cvt_pk_bf16_f32 v218, v72, v73\n" \
"v_cvt_pk_bf16_f32 v219, v74, v75\n" \
"v_mfma_f32_16x16x32_bf16 v[184:187], v[112:115], v[4:7], v[184:187]\n" \
"ds_read_b128 v[144:147], v178 offset:12288\n" \
"s_add_u32 s42, s42, 1\n" \
"v_mfma_f32_16x16x32_bf16 v[188:191], v[112:115], v[36:39], v[188:191]\n" \
"s_add_u32 s83, s69, 8192\n" \
"buffer_store_dwordx4 v[216:219], v224, %[rs], s83 offen sc1\n" \
"v_mfma_f32_16x16x32_bf16 v[192:195], v[116:119], v[4:7], v[192:195]\n" \
"ds_read_b128 v[148:151], v178 offset:13312\n" \
"v_cvt_pk_bf16_f32 v220, v68, v69\n" \
"v_mfma_f32_16x16x32_bf16 v[196:199], v[116:119], v[36:39], v[196:199]\n" \
"v_cvt_pk_bf16_f32 v221, v70, v71\n" \
"v_cvt_pk_bf16_f32 v222, v76, v77\n" \
"v_mfma_f32_16x16x32_bf16 v[200:203], v[120:123], v[4:7], v[200:203]\n" \
"ds_read_b128 v[152:155], v178 offset:14336\n" \
"v_add_u32 v179, s48, v224\n" \
"v_mfma_f32_16x16x32_bf16 v[204:207], v[120:123], v[36:39], v[204:207]\n" \
"v_cvt_pk_bf16_f32 v223, v78, v79\n" \
"s_add_u32 s83, s71, 8192\n" \
"buffer_store_dwordx4 v[220:223], v224, %[rs], s83 offen sc1\n" \
"v_mfma_f32_16x16x32_bf16 v[208:211], v[124:127], v[4:7], v[208:211]\n" \
"ds_read_b128 v[156:159], v178 offset:15360\n" \
"s_waitcnt lgkmcnt(4)\n" \
"v_mfma_f32_16x16x32_bf16 v[212:215], v[124:127], v[36:39], v[212:215]\n" \
"v_cvt_pk_bf16_f32 v216, v80, v81\n" \
"v_cvt_pk_bf16_f32 v217, v82, v83\n" \
"v_mfma_f32_16x16x32_bf16 v[184:187], v[128:131], v[8:11], v[184:187]\n" \
"ds_read_b128 v[96:99], v178 offset:16384\n" \
"v_cvt_pk_bf16_f32 v218, v88, v89\n" \
"v_mfma_f32_16x16x32_bf16 v[188:191], v[128:131], v[40:43], v[188:191]\n" \
"v_cvt_pk_bf16_f32 v219, v90, v91\n" \
"s_add_u32 s83, s69, 9216\n" \
"buffer_store_dwordx4 v[216:219], v224, %[rs], s83 offen sc1\n" \
"v_mfma_f32_16x16x32_bf16 v[192:195], v[132:135], v[8:11], v[192:195]\n" \
"ds_read_b128 v[100:103], v178 offset:17408\n" \
"v_cvt_pk_bf16_f32 v220, v84, v85\n" \
"v_mfma_f32_16x16x32_bf16 v[196:199], v[132:135], v[40:43], v[196:199]\n" \
"v_cvt_pk_bf16_f32 v221, v86, v87\n" \
"v_cvt_pk_bf16_f32 v222, v92, v93\n" \
"v_mfma_f32_16x16x32_bf16 v[200:203], v[136:139], v[8:11], v[200:203]\n" \
"ds_read_b128 v[104:107], v178 offset:18432\n" \
"v_cvt_pk_bf16_f32 v223, v94, v95\n" \
"v_mfma_f32_16x16x32_bf16 v[204:207], v[136:139], v[40:43], v[204:207]\n" \
"s_add_u32 s83, s71, 9216\n" \
"buffer_store_dwordx4 v[220:223], v224, %[rs], s83 offen sc1\n" \
"v_mfma_f32_16x16x32_bf16 v[208:211], v[140:143], v[8:11], v[208:211]\n" \
"ds_read_b128 v[108:111], v178 offset:19456\n" \
"s_waitcnt lgkmcnt(4)\n" \
"v_mfma_f32_16x16x32_bf16 v[212:215], v[140:143], v[40:43], v[212:215]\n" \
"v_mfma_f32_16x16x32_bf16 v[184:187], v[144:147], v[12:15], v[184:187]\n" \
"ds_read_b128 v[112:115], v178 offset:20480\n" \
"v_mfma_f32_16x16x32_bf16 v[188:191], v[144:147], v[44:47], v[188:191]\n" \
"v_mfma_f32_16x16x32_bf16 v[192:195], v[148:151], v[12:15], v[192:195]\n" \
"ds_read_b128 v[116:119], v178 offset:21504\n" \
"v_mfma_f32_16x16x32_bf16 v[196:199], v[148:151], v[44:47], v[196:199]\n" \
"v_mfma_f32_16x16x32_bf16 v[200:203], v[152:155], v[12:15], v[200:203]\n" \
"ds_read_b128 v[120:123], v178 offset:22528\n" \
"v_mfma_f32_16x16x32_bf16 v[204:207], v[152:155], v[44:47], v[204:207]\n" \
"v_mfma_f32_16x16x32_bf16 v[208:211], v[156:159], v[12:15], v[208:211]\n" \
"ds_read_b128 v[124:127], v178 offset:23552\n" \
"s_waitcnt lgkmcnt(4)\n" \
"v_mfma_f32_16x16x32_bf16 v[212:215], v[156:159], v[44:47], v[212:215]\n" \
"v_mfma_f32_16x16x32_bf16 v[184:187], v[96:99], v[16:19], v[184:187]\n" \
"ds_read_b128 v[128:131], v178 offset:24576\n" \
"v_mfma_f32_16x16x32_bf16 v[188:191], v[96:99], v[48:51], v[188:191]\n" \
"v_mfma_f32_16x16x32_bf16 v[192:195], v[100:103], v[16:19], v[192:195]\n" \
"ds_read_b128 v[132:135], v178 offset:25600\n" \
"v_mfma_f32_16x16x32_bf16 v[196:199], v[100:103], v[48:51], v[196:199]\n" \
"v_mfma_f32_16x16x32_bf16 v[200:203], v[104:107], v[16:19], v[200:203]\n" \
"ds_read_b128 v[136:139], v178 offset:26624\n" \
"v_mfma_f32_16x16x32_bf16 v[204:207], v[104:107], v[48:51], v[204:207]\n" \
"v_mfma_f32_16x16x32_bf16 v[208:211], v[108:111], v[16:19], v[208:211]\n" \
"ds_read_b128 v[140:143], v178 offset:27648\n" \
"s_waitcnt lgkmcnt(4)\n" \
"v_mfma_f32_16x16x32_bf16 v[212:215], v[108:111], v[48:51], v[212:215]\n" \
"v_mfma_f32_16x16x32_bf16 v[184:187], v[112:115], v[20:23], v[184:187]\n" \
"ds_read_b128 v[144:147], v178 offset:28672\n" \
"v_mfma_f32_16x16x32_bf16 v[188:191], v[112:115], v[52:55], v[188:191]\n" \
"v_mfma_f32_16x16x32_bf16 v[192:195], v[116:119], v[20:23], v[192:195]\n" \
"ds_read_b128 v[148:151], v178 offset:29696\n" \
"v_mfma_f32_16x16x32_bf16 v[196:199], v[116:119], v[52:55], v[196:199]\n" \
"v_mfma_f32_16x16x32_bf16 v[200:203], v[120:123], v[20:23], v[200:203]\n" \
"ds_read_b128 v[152:155], v178 offset:30720\n" \
"v_mfma_f32_16x16x32_bf16 v[204:207], v[120:123], v[52:55], v[204:207]\n" \
"v_mfma_f32_16x16x32_bf16 v[208:211], v[124:127], v[20:23], v[208:211]\n" \
"ds_read_b128 v[156:159], v178 offset:31744\n" \
"s_waitcnt lgkmcnt(4)\n" \
"v_mfma_f32_16x16x32_bf16 v[212:215], v[124:127], v[52:55], v[212:215]\n" \
"s_waitcnt vmcnt(8)\n" \
"s_barrier\n" \
"v_mfma_f32_16x16x32_bf16 v[184:187], v[128:131], v[24:27], v[184:187]\n" \
"ds_read_b128 v[96:99], v179 offset:0\n" \
"v_mfma_f32_16x16x32_bf16 v[188:191], v[128:131], v[56:59], v[188:191]\n" \
"v_mfma_f32_16x16x32_bf16 v[192:195], v[132:135], v[24:27], v[192:195]\n" \
"ds_read_b128 v[100:103], v179 offset:1024\n" \
"s_mov_b32 s44, s41\n" \
"v_mfma_f32_16x16x32_bf16 v[196:199], v[132:135], v[56:59], v[196:199]\n" \
"v_mfma_f32_16x16x32_bf16 v[200:203], v[136:139], v[24:27], v[200:203]\n" \
"ds_read_b128 v[104:107], v179 offset:2048\n" \
"s_mov_b32 s41, s48\n" \
"v_mfma_f32_16x16x32_bf16 v[204:207], v[136:139], v[56:59], v[204:207]\n" \
"v_mfma_f32_16x16x32_bf16 v[208:211], v[140:143], v[24:27], v[208:211]\n" \
"ds_read_b128 v[108:111], v179 offset:3072\n" \
"s_waitcnt lgkmcnt(4)\n" \
"v_mfma_f32_16x16x32_bf16 v[212:215], v[140:143], v[56:59], v[212:215]\n" \
"v_mfma_f32_16x16x32_bf16 v[184:187], v[144:147], v[28:31], v[184:187]\n" \
"ds_read_b128 v[112:115], v179 offset:4096\n" \
"v_mfma_f32_16x16x32_bf16 v[188:191], v[144:147], v[60:63], v[188:191]\n" \
"v_mfma_f32_16x16x32_bf16 v[192:195], v[148:151], v[28:31], v[192:195]\n" \
"ds_read_b128 v[116:119], v179 offset:5120\n" \
"v_mfma_f32_16x16x32_bf16 v[196:199], v[148:151], v[60:63], v[196:199]\n" \
"v_mfma_f32_16x16x32_bf16 v[200:203], v[152:155], v[28:31], v[200:203]\n" \
"ds_read_b128 v[120:123], v179 offset:6144\n" \
"v_mfma_f32_16x16x32_bf16 v[204:207], v[152:155], v[60:63], v[204:207]\n" \
"v_mfma_f32_16x16x32_bf16 v[208:211], v[156:159], v[28:31], v[208:211]\n" \
"ds_read_b128 v[124:127], v179 offset:7168\n" \
"s_waitcnt lgkmcnt(4)\n" \
"v_mfma_f32_16x16x32_bf16 v[212:215], v[156:159], v[60:63], v[212:215]\n" \
"v_mov_b32 v178, v179\n" \
"v_mfma_f32_16x16x32_bf16 v[64:67], v[96:99], v[0:3], 0\n" \
"ds_read_b128 v[128:131], v178 offset:8192\n" \
"v_mfma_f32_16x16x32_bf16 v[68:71], v[96:99], v[32:35], 0\n" \
"v_mfma_f32_16x16x32_bf16 v[72:75], v[100:103], v[0:3], 0\n" \
"ds_read_b128 v[132:135], v178 offset:9216\n" \
"v_readlane_b32 s48, v238, s42\n" \
"v_mfma_f32_16x16x32_bf16 v[76:79], v[100:103], v[32:35], 0\n" \
"v_mfma_f32_16x16x32_bf16 v[80:83], v[104:107], v[0:3], 0\n" \
"ds_read_b128 v[136:139], v178 offset:10240\n" \
"v_mfma_f32_16x16x32_bf16 v[84:87], v[104:107], v[32:35], 0\n" \
"v_cvt_pk_bf16_f32 v216, v184, v185\n" \
"v_cvt_pk_bf16_f32 v217, v186, v187\n" \
"v_mfma_f32_16x16x32_bf16 v[88:91], v[108:111], v[0:3], 0\n" \
"ds_read_b128 v[140:143], v178 offset:11264\n" \
"s_waitcnt lgkmcnt(4)\n" \
"v_mfma_f32_16x16x32_bf16 v[92:95], v[108:111], v[32:35], 0\n" \
"v_cvt_pk_bf16_f32 v218, v192, v193\n" \
"v_cvt_pk_bf16_f32 v219, v194, v195\n" \
"v_mfma_f32_16x16x32_bf16 v[64:67], v[112:115], v[4:7], v[64:67]\n" \
"ds_read_b128 v[144:147], v178 offset:12288\n" \
"s_add_u32 s42, s42, 1\n" \
"v_mfma_f32_16x16x32_bf16 v[68:71], v[112:115], v[36:39], v[68:71]\n" \
"s_add_u32 s83, s69, 10240\n" \
"buffer_store_dwordx4 v[216:219], v224, %[rs], s83 offen sc1\n" \
"v_mfma_f32_16x16x32_bf16 v[72:75], v[116:119], v[4:7], v[72:75]\n" \
"ds_read_b128 v[148:151], v178 offset:13312\n" \
"v_cvt_pk_bf16_f32 v220, v188, v189\n" \
"v_mfma_f32_16x16x32_bf16 v[76:79], v[116:119], v[36:39], v[76:79]\n" \
"v_cvt_pk_bf16_f32 v221, v190, v191\n" \
"v_cvt_pk_bf16_f32 v222, v196, v197\n" \
"v_mfma_f32_16x16x32_bf16 v[80:83], v[120:123], v[4:7], v[80:83]\n" \
"ds_read_b128 v[152:155], v178 offset:14336\n" \
"v_add_u32 v179, s48, v224\n" \
"v_mfma_f32_16x16x32_bf16 v[84:87], v[120:123], v[36:39], v[84:87]\n" \
"v_cvt_pk_bf16_f32 v223, v198, v199\n" \
"s_add_u32 s83, s71, 10240\n" \
"buffer_store_dwordx4 v[220:223], v224, %[rs], s83 offen sc1\n" \
"v_mfma_f32_16x16x32_bf16 v[88:91], v[124:127], v[4:7], v[88:91]\n" \
"ds_read_b128 v[156:159], v178 offset:15360\n" \
"s_waitcnt lgkmcnt(4)\n" \
"v_mfma_f32_16x16x32_bf16 v[92:95], v[124:127], v[36:39], v[92:95]\n" \
"v_cvt_pk_bf16_f32 v216, v200, v201\n" \
"v_cvt_pk_bf16_f32 v217, v202, v203\n" \
"v_mfma_f32_16x16x32_bf16 v[64:67], v[128:131], v[8:11], v[64:67]\n" \
"ds_read_b128 v[96:99], v178 offset:16384\n" \
"v_cvt_pk_bf16_f32 v218, v208, v209\n" \
"v_mfma_f32_16x16x32_bf16 v[68:71], v[128:131], v[40:43], v[68:71]\n" \
"v_cvt_pk_bf16_f32 v219, v210, v211\n" \
"s_add_u32 s83, s69, 11264\n" \
"buffer_store_dwordx4 v[216:219], v224, %[rs], s83 offen sc1\n" \
"v_mfma_f32_16x16x32_bf16 v[72:75], v[132:135], v[8:11], v[72:75]\n" \
"ds_read_b128 v[100:103], v178 offset:17408\n" \
"v_cvt_pk_bf16_f32 v220, v204, v205\n" \
"v_mfma_f32_16x16x32_bf16 v[76:79], v[132:135], v[40:43], v[76:79]\n" \
"v_cvt_pk_bf16_f32 v221, v206, v207\n" \
"v_cvt_pk_bf16_f32 v222, v212, v213\n" \
"v_mfma_f32_16x16x32_bf16 v[80:83], v[136:139], v[8:11], v[80:83]\n" \
"ds_read_b128 v[104:107], v178 offset:18432\n" \
"v_cvt_pk_bf16_f32 v223, v214, v215\n" \
"v_mfma_f32_16x16x32_bf16 v[84:87], v[136:139], v[40:43], v[84:87]\n" \
"s_add_u32 s83, s71, 11264\n" \
"buffer_store_dwordx4 v[220:223], v224, %[rs], s83 offen sc1\n" \
"v_mfma_f32_16x16x32_bf16 v[88:91], v[140:143], v[8:11], v[88:91]\n" \
"ds_read_b128 v[108:111], v178 offset:19456\n" \
"s_waitcnt lgkmcnt(4)\n" \
"v_mfma_f32_16x16x32_bf16 v[92:95], v[140:143], v[40:43], v[92:95]\n" \
"v_mfma_f32_16x16x32_bf16 v[64:67], v[144:147], v[12:15], v[64:67]\n" \
"ds_read_b128 v[112:115], v178 offset:20480\n" \
"v_mfma_f32_16x16x32_bf16 v[68:71], v[144:147], v[44:47], v[68:71]\n" \
"v_mfma_f32_16x16x32_bf16 v[72:75], v[148:151], v[12:15], v[72:75]\n" \
"ds_read_b128 v[116:119], v178 offset:21504\n" \
"v_mfma_f32_16x16x32_bf16 v[76:79], v[148:151], v[44:47], v[76:79]\n" \
"v_mfma_f32_16x16x32_bf16 v[80:83], v[152:155], v[12:15], v[80:83]\n" \
"ds_read_b128 v[120:123], v178 offset:22528\n" \
"v_mfma_f32_16x16x32_bf16 v[84:87], v[152:155], v[44:47], v[84:87]\n" \
"v_mfma_f32_16x16x32_bf16 v[88:91], v[156:159], v[12:15], v[88:91]\n" \
"ds_read_b128 v[124:127], v178 offset:23552\n" \
"s_waitcnt lgkmcnt(4)\n" \
"v_mfma_f32_16x16x32_bf16 v[92:95], v[156:159], v[44:47], v[92:95]\n" \
"v_mfma_f32_16x16x32_bf16 v[64:67], v[96:99], v[16:19], v[64:67]\n" \
"ds_read_b128 v[128:131], v178 offset:24576\n" \
"v_mfma_f32_16x16x32_bf16 v[68:71], v[96:99], v[48:51], v[68:71]\n" \
"v_mfma_f32_16x16x32_bf16 v[72:75], v[100:103], v[16:19], v[72:75]\n" \
"ds_read_b128 v[132:135], v178 offset:25600\n" \
"v_mfma_f32_16x16x32_bf16 v[76:79], v[100:103], v[48:51], v[76:79]\n" \
"v_mfma_f32_16x16x32_bf16 v[80:83], v[104:107], v[16:19], v[80:83]\n" \
"ds_read_b128 v[136:139], v178 offset:26624\n" \
"v_mfma_f32_16x16x32_bf16 v[84:87], v[104:107], v[48:51], v[84:87]\n" \
"v_mfma_f32_16x16x32_bf16 v[88:91], v[108:111], v[16:19], v[88:91]\n" \
"ds_read_b128 v[140:143], v178 offset:27648\n" \
"s_waitcnt lgkmcnt(4)\n" \
"v_mfma_f32_16x16x32_bf16 v[92:95], v[108:111], v[48:51], v[92:95]\n" \
"v_mfma_f32_16x16x32_bf16 v[64:67], v[112:115], v[20:23], v[64:67]\n" \
"ds_read_b128 v[144:147], v178 offset:28672\n" \
"v_mfma_f32_16x16x32_bf16 v[68:71], v[112:115], v[52:55], v[68:71]\n" \
"v_mfma_f32_16x16x32_bf16 v[72:75], v[116:119], v[20:23], v[72:75]\n" \
"ds_read_b128 v[148:151], v178 offset:29696\n" \
"v_mfma_f32_16x16x32_bf16 v[76:79], v[116:119], v[52:55], v[76:79]\n" \
"v_mfma_f32_16x16x32_bf16 v[80:83], v[120:123], v[20:23], v[80:83]\n" \
"ds_read_b128 v[152:155], v178 offset:30720\n" \
"v_mfma_f32_16x16x32_bf16 v[84:87], v[120:123], v[52:55], v[84:87]\n" \
"v_mfma_f32_16x16x32_bf16 v[88:91], v[124:127], v[20:23], v[88:91]\n" \
"ds_read_b128 v[156:159], v178 offset:31744\n" \
"s_waitcnt lgkmcnt(4)\n" \
"v_mfma_f32_16x16x32_bf16 v[92:95], v[124:127], v[52:55], v[92:95]\n" \
"s_waitcnt vmcnt(63)\n" \
"s_barrier\n" \
"v_mfma_f32_16x16x32_bf16 v[64:67], v[128:131], v[24:27], v[64:67]\n" \
"ds_read_b128 v[96:99], v179 offset:0\n" \
"v_mfma_f32_16x16x32_bf16 v[68:71], v[128:131], v[56:59], v[68:71]\n" \
"v_mfma_f32_16x16x32_bf16 v[72:75], v[132:135], v[24:27], v[72:75]\n" \
"ds_read_b128 v[100:103], v179 offset:1024\n" \
"s_mov_b32 s44, s41\n" \
"v_mfma_f32_16x16x32_bf16 v[76:79], v[132:135], v[56:59], v[76:79]\n" \
"v_mfma_f32_16x16x32_bf16 v[80:83], v[136:139], v[24:27], v[80:83]\n" \
"ds_read_b128 v[104:107], v179 offset:2048\n" \
"s_mov_b32 s41, s48\n" \
"v_mfma_f32_16x16x32_bf16 v[84:87], v[136:139], v[56:59], v[84:87]\n" \
"v_mfma_f32_16x16x32_bf16 v[88:91], v[140:143], v[24:27], v[88:91]\n" \
"ds_read_b128 v[108:111], v179 offset:3072\n" \
"s_waitcnt lgkmcnt(4)\n" \
"v_mfma_f32_16x16x32_bf16 v[92:95], v[140:143], v[56:59], v[92:95]\n" \
"v_mfma_f32_16x16x32_bf16 v[64:67], v[144:147], v[28:31], v[64:67]\n" \
"ds_read_b128 v[112:115], v179 offset:4096\n" \
"v_mfma_f32_16x16x32_bf16 v[68:71], v[144:147], v[60:63], v[68:71]\n" \
"v_mfma_f32_16x16x32_bf16 v[72:75], v[148:151], v[28:31], v[72:75]\n" \
"ds_read_b128 v[116:119], v179 offset:5120\n" \
"v_mfma_f32_16x16x32_bf16 v[76:79], v[148:151], v[60:63], v[76:79]\n" \
"v_mfma_f32_16x16x32_bf16 v[80:83], v[152:155], v[28:31], v[80:83]\n" \
"ds_read_b128 v[120:123], v179 offset:6144\n" \
"v_mfma_f32_16x16x32_bf16 v[84:87], v[152:155], v[60:63], v[84:87]\n" \
"v_mfma_f32_16x16x32_bf16 v[88:91], v[156:159], v[28:31], v[88:91]\n" \
"ds_read_b128 v[124:127], v179 offset:7168\n" \
"s_waitcnt lgkmcnt(4)\n" \
"v_mfma_f32_16x16x32_bf16 v[92:95], v[156:159], v[60:63], v[92:95]\n" \
"v_mov_b32 v178, v179\n" \
"s_nop 7\n" \
"v_cvt_pk_bf16_f32 v216, v64, v65\n" \
"v_cvt_pk_bf16_f32 v217, v66, v67\n" \
"v_cvt_pk_bf16_f32 v218, v72, v73\n" \
"v_cvt_pk_bf16_f32 v219, v74, v75\n" \
"s_add_u32 s83, s69, 12288\n" \
"buffer_store_dwordx4 v[216:219], v224, %[rs], s83 offen sc1\n" \
"v_cvt_pk_bf16_f32 v220, v68, v69\n" \
"v_cvt_pk_bf16_f32 v221, v70, v71\n" \
"v_cvt_pk_bf16_f32 v222, v76, v77\n" \
"v_cvt_pk_bf16_f32 v223, v78, v79\n" \
"s_add_u32 s83, s71, 12288\n" \
"buffer_store_dwordx4 v[220:223], v224, %[rs], s83 offen sc1\n" \
"v_cvt_pk_bf16_f32 v216, v80, v81\n" \
"v_cvt_pk_bf16_f32 v217, v82, v83\n" \
"v_cvt_pk_bf16_f32 v218, v88, v89\n" \
"v_cvt_pk_bf16_f32 v219, v90, v91\n" \
"s_add_u32 s83, s69, 13312\n" \
"buffer_store_dwordx4 v[216:219], v224, %[rs], s83 offen sc1\n" \
"v_cvt_pk_bf16_f32 v220, v84, v85\n" \
"v_cvt_pk_bf16_f32 v221, v86, v87\n" \
"v_cvt_pk_bf16_f32 v222, v92, v93\n" \
"v_cvt_pk_bf16_f32 v223, v94, v95\n" \
"s_add_u32 s83, s71, 13312\n" \
"buffer_store_dwordx4 v[220:223], v224, %[rs], s83 offen sc1\n" \
"s_waitcnt vmcnt(0) lgkmcnt(0)\n" \
"s_barrier\n" \
"s_lshl_b32 s59, s18, 8\n" \
"s_add_u32 s59, s59, 40960\n" \
"s_lshl_b32 s80, s16, 2\n" \
"s_add_u32 s59, s59, s80\n" \
"s_cmp_lg_u32 s36, 0\n" \
"s_cbranch_scc1 Lts1%=\n" \
"s_mov_b64 s[96:97], exec\n" \
"s_mov_b64 exec, 1\n" \
"v_mov_b32 v238, %[tag]\n" \
"buffer_store_dword v238, off, %[rs], s59 sc1\n" \
"s_mov_b64 exec, s[96:97]\n" \
"Lts1%=:\n" \
"v_xor_b32 v160, 128, v237\n" \
"s_mul_i32 s83, s36, 401408\n" \
"s_mul_i32 s82, s36, 28672\n" \
"s_mov_b32 s43, s83\n" \
"s_mov_b32 s53, s82\n" \
"s_add_u32 m0, s53, 0\n" \
"s_add_u32 s94, s43, 0\n" \
"buffer_load_dwordx4 v237, s[28:31], s94 offen offset:0 lds\n" \
"s_add_u32 s94, s43, 13312\n" \
"buffer_load_dwordx4 v160, s[28:31], s94 offen offset:1024 lds\n" \
"s_add_u32 s94, s43, 26624\n" \
"buffer_load_dwordx4 v237, s[28:31], s94 offen offset:2048 lds\n" \
"s_add_u32 s94, s43, 39936\n" \
"buffer_load_dwordx4 v160, s[28:31], s94 offen offset:3072 lds\n" \
"s_add_u32 m0, s53, 4096\n" \
"s_add_u32 s94, s43, 57344\n" \
"buffer_load_dwordx4 v237, s[28:31], s94 offen offset:0 lds\n" \
"s_add_u32 s94, s43, 70656\n" \
"buffer_load_dwordx4 v160, s[28:31], s94 offen offset:1024 lds\n" \
"s_add_u32 s94, s43, 83968\n" \
"buffer_load_dwordx4 v237, s[28:31], s94 offen offset:2048 lds\n" \
"s_add_u32 s94, s43, 97280\n" \
"buffer_load_dwordx4 v160, s[28:31], s94 offen offset:3072 lds\n" \
"s_add_u32 m0, s53, 8192\n" \
"s_add_u32 s94, s43, 114688\n" \
"buffer_load_dwordx4 v237, s[28:31], s94 offen offset:0 lds\n" \
"s_add_u32 s94, s43, 128000\n" \
"buffer_load_dwordx4 v160, s[28:31], s94 offen offset:1024 lds\n" \
"s_add_u32 s94, s43, 141312\n" \
"buffer_load_dwordx4 v237, s[28:31], s94 offen offset:2048 lds\n" \
"s_add_u32 s94, s43, 154624\n" \
"buffer_load_dwordx4 v160, s[28:31], s94 offen offset:3072 lds\n" \
"s_add_u32 m0, s53, 12288\n" \
"s_add_u32 s94, s43, 172032\n" \
"buffer_load_dwordx4 v237, s[28:31], s94 offen offset:0 lds\n" \
"s_add_u32 s94, s43, 185344\n" \
"buffer_load_dwordx4 v160, s[28:31], s94 offen offset:1024 lds\n" \
"s_add_u32 s94, s43, 198656\n" \
"buffer_load_dwordx4 v237, s[28:31], s94 offen offset:2048 lds\n" \
"s_add_u32 s94, s43, 211968\n" \
"buffer_load_dwordx4 v160, s[28:31], s94 offen offset:3072 lds\n" \
"s_add_u32 m0, s53, 16384\n" \
"s_add_u32 s94, s43, 229376\n" \
"buffer_load_dwordx4 v237, s[28:31], s94 offen offset:0 lds\n" \
"s_add_u32 s94, s43, 242688\n" \
"buffer_load_dwordx4 v160, s[28:31], s94 offen offset:1024 lds\n" \
"s_add_u32 s94, s43, 256000\n" \
"buffer_load_dwordx4 v237, s[28:31], s94 offen offset:2048 lds\n" \
"s_add_u32 s94, s43, 269312\n" \
"buffer_load_dwordx4 v160, s[28:31], s94 offen offset:3072 lds\n" \
"s_add_u32 m0, s53, 20480\n" \
"s_add_u32 s94, s43, 286720\n" \
"buffer_load_dwordx4 v237, s[28:31], s94 offen offset:0 lds\n" \
"s_add_u32 s94, s43, 300032\n" \
"buffer_load_dwordx4 v160, s[28:31], s94 offen offset:1024 lds\n" \
"s_add_u32 s94, s43, 313344\n" \
"buffer_load_dwordx4 v237, s[28:31], s94 offen offset:2048 lds\n" \
"s_add_u32 s94, s43, 326656\n" \
"buffer_load_dwordx4 v160, s[28:31], s94 offen offset:3072 lds\n" \
"s_add_u32 m0, s53, 24576\n" \
"s_add_u32 s94, s43, 344064\n" \
"buffer_load_dwordx4 v237, s[28:31], s94 offen offset:0 lds\n" \
"s_add_u32 s94, s43, 357376\n" \
"buffer_load_dwordx4 v160, s[28:31], s94 offen offset:1024 lds\n" \
"s_add_u32 s94, s43, 370688\n" \
"buffer_load_dwordx4 v237, s[28:31], s94 offen offset:2048 lds\n" \
"s_add_u32 s94, s43, 384000\n" \
"buffer_load_dwordx4 v160, s[28:31], s94 offen offset:3072 lds\n" \
"s_lshl_b32 s80, s39, 8\n" \
"s_add_u32 s60, s80, 40960\n" \
"s_add_u32 s61, s80, 45056\n" \
"s_add_u32 s62, s80, 49152\n" \
"s_lshr_b32 s80, s40, 1\n" \
"s_lshl_b32 s81, s39, 3\n" \
"s_add_u32 s80, s80, s81\n" \
"s_and_b32 s81, s40, 1\n" \
"s_mul_i32 s81, s81, 7\n" \
"s_add_u32 s81, s81, s36\n" \
"s_mul_i32 s82, s80, 14336\n" \
"s_lshl_b32 s83, s81, 10\n" \
"s_add_u32 s83, s83, s82\n" \
"s_add_u32 s63, s83, 58720256\n" \
"s_add_u32 s65, s83, 88080384\n" \
"s_lshl_b32 s69, s81, 6\n" \
"s_add_u32 s69, s69, s82\n" \
"s_lshl_b32 s67, s80, 11\n" \
"s_lshl_b32 s81, s81, 3\n" \
"s_add_u32 s67, s67, s81\n" \
"s_add_u32 s67, s67, 90177536\n" \
"s_add_u32 s64, s63, 4096\n" \
"s_add_u32 s66, s65, 4096\n" \
"s_add_u32 s70, s69, 256\n" \
"s_add_u32 s68, s67, 32\n" \
"s_cmp_lt_u32 s36, 3\n" \
"s_cselect_b32 s64, s64, 0x80000000\n" \
"s_cselect_b32 s66, s66, 0x80000000\n" \
"s_cselect_b32 s70, s70, 0x80000000\n" \
"s_cselect_b32 s68, s68, 0x80000000\n" \
"s_lshl_b32 s80, s39, 2\n" \
"s_add_u32 s80, s80, s36\n" \
"s_mul_i32 s71, s80, 28672\n" \
"s_add_u32 s71, s71, 88080384\n" \
"s_mul_i32 s72, s80, 4096\n" \
"s_add_u32 s72, s72, 90177536\n" \
"s_mul_i32 s73, s80, 114688\n" \
"s_add_u32 s73, s73, 91226112\n" \
"s_lshl_b32 s81, s40, 12\n" \
"s_add_u32 s73, s73, s81\n" \
"s_cmp_lt_u32 s40, 14\n" \
"s_cselect_b32 s74, 1, 0\n" \
"s_cmp_ge_u32 s40, 7\n" \
"s_cselect_b32 s80, 7, 0\n" \
"s_cselect_b32 s81, 4, 0\n" \
"s_sub_u32 s80, s40, s80\n" \
"s_lshl_b32 s82, s39, 3\n" \
"s_add_u32 s81, s81, s82\n" \
"s_and_b32 s82, s36, 1\n" \
"s_lshl_b32 s78, s82, 13\n" \
"s_mul_i32 s79, s82, 57344\n" \
"s_lshl_b32 s82, s82, 1\n" \
"s_add_u32 s81, s81, s82\n" \
"s_lshr_b32 s82, s36, 1\n" \
"s_mul_i32 s75, s81, 57344\n" \
"s_mul_i32 s83, s82, 28672\n" \
"s_add_u32 s75, s75, s83\n" \
"s_add_u32 s75, s75, 91226112\n" \
"s_lshl_b32 s83, s82, 14\n" \
"s_add_u32 s78, s78, s83\n" \
"s_mul_i32 s83, s82, 802816\n" \
"s_add_u32 s79, s79, s83\n" \
"s_add_u32 s81, s81, s82\n" \
"s_mul_i32 s81, s81, 14336\n" \
"s_lshl_b32 s82, s80, 11\n" \
"s_add_u32 s76, s81, s82\n" \
"s_add_u32 s76, s76, 88080384\n" \
"s_lshl_b32 s80, s80, 7\n" \
"s_add_u32 s77, s81, s80\n" \
"s_add_u32 s79, s79, s80\n" \
"s_mov_b32 s54, 0\n" \
"s_mov_b32 s55, 32768\n" \
"s_mov_b32 s56, 65536\n" \
"s_mov_b32 s57, 98304\n" \
"s_mov_b32 s58, 131072\n" \
"v_lshrrev_b32 v226, 8, v224\n" \
"v_bfe_u32 v236, v224, 4, 4\n" \
"v_mul_u32_u24 v225, 0x380, v236\n" \
"v_lshl_add_u32 v225, v226, 3, v225\n" \
"v_lshrrev_b32 v227, 2, v236\n" \
"v_lshl_add_u32 v227, v226, 2, v227\n" \
"buffer_load_dwordx2 v[128:129], v225, s[24:27], s69 offen\n" \
"buffer_load_dwordx2 v[130:131], v225, s[24:27], s69 offen offset:32\n" \
"buffer_load_dwordx2 v[132:133], v225, s[24:27], s70 offen\n" \
"buffer_load_dwordx2 v[134:135], v225, s[24:27], s70 offen offset:32\n" \
"s_mov_b32 s81, 0\n" \
"Ltw1_p%=:\n" \
"buffer_load_dword v243, v240, %[rs], s60 offen sc1\n" \
"s_waitcnt vmcnt(0)\n" \
"v_cmp_ne_u32 s[96:97], %[tag], v243\n" \
"s_nop 1\n" \
"s_cmp_eq_u64 s[96:97], 0\n" \
"s_cbranch_scc1 Ltw1_d%=\n" \
"s_sleep 4\n" \
"s_add_u32 s81, s81, 1\n" \
"s_cmp_lt_u32 s81, 0x20000\n" \
"s_cbranch_scc1 Ltw1_p%=\n" \
"Ltw1_d%=:\n" \
"buffer_load_dwordx4 v[0:3], v224, %[rs], s63 offen sc1\n" \
"s_add_u32 s63, s63, 1835008\n" \
"buffer_load_dwordx4 v[4:7], v224, %[rs], s63 offen sc1\n" \
"s_add_u32 s63, s63, 1835008\n" \
"buffer_load_dwordx4 v[8:11], v224, %[rs], s63 offen sc1\n" \
"s_add_u32 s63, s63, 1835008\n" \
"buffer_load_dwordx4 v[12:15], v224, %[rs], s63 offen sc1\n" \
"s_add_u32 s63, s63, 1835008\n" \
"buffer_load_dwordx4 v[16:19], v224, %[rs], s63 offen sc1\n" \
"s_add_u32 s63, s63, 1835008\n" \
"buffer_load_dwordx4 v[20:23], v224, %[rs], s63 offen sc1\n" \
"s_add_u32 s63, s63, 1835008\n" \
"buffer_load_dwordx4 v[24:27], v224, %[rs], s63 offen sc1\n" \
"s_add_u32 s63, s63, 1835008\n" \
"buffer_load_dwordx4 v[28:31], v224, %[rs], s63 offen sc1\n" \
"s_add_u32 s63, s63, 1835008\n" \
"buffer_load_dwordx4 v[32:35], v224, %[rs], s63 offen sc1\n" \
"s_add_u32 s63, s63, 1835008\n" \
"buffer_load_dwordx4 v[36:39], v224, %[rs], s63 offen sc1\n" \
"s_add_u32 s63, s63, 1835008\n" \
"buffer_load_dwordx4 v[40:43], v224, %[rs], s63 offen sc1\n" \
"s_add_u32 s63, s63, 1835008\n" \
"buffer_load_dwordx4 v[44:47], v224, %[rs], s63 offen sc1\n" \
"s_add_u32 s63, s63, 1835008\n" \
"buffer_load_dwordx4 v[48:51], v224, %[rs], s63 offen sc1\n" \
"s_add_u32 s63, s63, 1835008\n" \
"buffer_load_dwordx4 v[52:55], v224, %[rs], s63 offen sc1\n" \
"s_add_u32 s63, s63, 1835008\n" \
"buffer_load_dwordx4 v[56:59], v224, %[rs], s63 offen sc1\n" \
"s_add_u32 s63, s63, 1835008\n" \
"buffer_load_dwordx4 v[60:63], v224, %[rs], s63 offen sc1\n" \
"buffer_load_dwordx4 v[64:67], v224, %[rs], s64 offen sc1\n" \
"s_add_u32 s64, s64, 1835008\n" \
"buffer_load_dwordx4 v[68:71], v224, %[rs], s64 offen sc1\n" \
"s_add_u32 s64, s64, 1835008\n" \
"buffer_load_dwordx4 v[72:75], v224, %[rs], s64 offen sc1\n" \
"s_add_u32 s64, s64, 1835008\n" \
"buffer_load_dwordx4 v[76:79], v224, %[rs], s64 offen sc1\n" \
"s_add_u32 s64, s64, 1835008\n" \
"buffer_load_dwordx4 v[80:83], v224, %[rs], s64 offen sc1\n" \
"s_add_u32 s64, s64, 1835008\n" \
"buffer_load_dwordx4 v[84:87], v224, %[rs], s64 offen sc1\n" \
"s_add_u32 s64, s64, 1835008\n" \
"buffer_load_dwordx4 v[88:91], v224, %[rs], s64 offen sc1\n" \
"s_add_u32 s64, s64, 1835008\n" \
"buffer_load_dwordx4 v[92:95], v224, %[rs], s64 offen sc1\n" \
"s_add_u32 s64, s64, 1835008\n" \
"buffer_load_dwordx4 v[96:99], v224, %[rs], s64 offen sc1\n" \
"s_add_u32 s64, s64, 1835008\n" \
"buffer_load_dwordx4 v[100:103], v224, %[rs], s64 offen sc1\n" \
"s_add_u32 s64, s64, 1835008\n" \
"buffer_load_dwordx4 v[104:107], v224, %[rs], s64 offen sc1\n" \
"s_add_u32 s64, s64, 1835008\n" \
"buffer_load_dwordx4 v[108:111], v224, %[rs], s64 offen sc1\n" \
"s_add_u32 s64, s64, 1835008\n" \
"buffer_load_dwordx4 v[112:115], v224, %[rs], s64 offen sc1\n" \
"s_add_u32 s64, s64, 1835008\n" \
"buffer_load_dwordx4 v[116:119], v224, %[rs], s64 offen sc1\n" \
"s_add_u32 s64, s64, 1835008\n" \
"buffer_load_dwordx4 v[120:123], v224, %[rs], s64 offen sc1\n" \
"s_add_u32 s64, s64, 1835008\n" \
"buffer_load_dwordx4 v[124:127], v224, %[rs], s64 offen sc1\n" \
"s_waitcnt vmcnt(30)\n" \
"v_lshlrev_b32 v136, 16, v0\n" \
"v_and_b32 v137, 0xffff0000, v0\n" \
"v_lshlrev_b32 v138, 16, v1\n" \
"v_and_b32 v139, 0xffff0000, v1\n" \
"v_lshlrev_b32 v140, 16, v2\n" \
"v_and_b32 v141, 0xffff0000, v2\n" \
"v_lshlrev_b32 v142, 16, v3\n" \
"v_and_b32 v143, 0xffff0000, v3\n" \
"v_lshlrev_b32 v144, 16, v4\n" \
"v_and_b32 v145, 0xffff0000, v4\n" \
"v_lshlrev_b32 v146, 16, v5\n" \
"v_and_b32 v147, 0xffff0000, v5\n" \
"v_lshlrev_b32 v148, 16, v6\n" \
"v_and_b32 v149, 0xffff0000, v6\n" \
"v_lshlrev_b32 v150, 16, v7\n" \
"v_and_b32 v151, 0xffff0000, v7\n" \
"v_pk_add_f32 v[136:137], v[136:137], v[144:145]\n" \
"v_pk_add_f32 v[138:139], v[138:139], v[146:147]\n" \
"v_pk_add_f32 v[140:141], v[140:141], v[148:149]\n" \
"v_pk_add_f32 v[142:143], v[142:143], v[150:151]\n" \
"s_waitcnt vmcnt(28)\n" \
"v_lshlrev_b32 v144, 16, v8\n" \
"v_and_b32 v145, 0xffff0000, v8\n" \
"v_lshlrev_b32 v146, 16, v9\n" \
"v_and_b32 v147, 0xffff0000, v9\n" \
"v_lshlrev_b32 v148, 16, v10\n" \
"v_and_b32 v149, 0xffff0000, v10\n" \
"v_lshlrev_b32 v150, 16, v11\n" \
"v_and_b32 v151, 0xffff0000, v11\n" \
"v_pk_add_f32 v[136:137], v[136:137], v[144:145]\n" \
"v_pk_add_f32 v[138:139], v[138:139], v[146:147]\n" \
"v_pk_add_f32 v[140:141], v[140:141], v[148:149]\n" \
"v_pk_add_f32 v[142:143], v[142:143], v[150:151]\n" \
"v_lshlrev_b32 v144, 16, v12\n" \
"v_and_b32 v145, 0xffff0000, v12\n" \
"v_lshlrev_b32 v146, 16, v13\n" \
"v_and_b32 v147, 0xffff0000, v13\n" \
"v_lshlrev_b32 v148, 16, v14\n" \
"v_and_b32 v149, 0xffff0000, v14\n" \
"v_lshlrev_b32 v150, 16, v15\n" \
"v_and_b32 v151, 0xffff0000, v15\n" \
"v_pk_add_f32 v[136:137], v[136:137], v[144:145]\n" \
"v_pk_add_f32 v[138:139], v[138:139], v[146:147]\n" \
"v_pk_add_f32 v[140:141], v[140:141], v[148:149]\n" \
"v_pk_add_f32 v[142:143], v[142:143], v[150:151]\n" \
"s_waitcnt vmcnt(26)\n" \
"v_lshlrev_b32 v144, 16, v16\n" \
"v_and_b32 v145, 0xffff0000, v16\n" \
"v_lshlrev_b32 v146, 16, v17\n" \
"v_and_b32 v147, 0xffff0000, v17\n" \
"v_lshlrev_b32 v148, 16, v18\n" \
"v_and_b32 v149, 0xffff0000, v18\n" \
"v_lshlrev_b32 v150, 16, v19\n" \
"v_and_b32 v151, 0xffff0000, v19\n" \
"v_pk_add_f32 v[136:137], v[136:137], v[144:145]\n" \
"v_pk_add_f32 v[138:139], v[138:139], v[146:147]\n" \
"v_pk_add_f32 v[140:141], v[140:141], v[148:149]\n" \
"v_pk_add_f32 v[142:143], v[142:143], v[150:151]\n" \
"v_lshlrev_b32 v144, 16, v20\n" \
"v_and_b32 v145, 0xffff0000, v20\n" \
"v_lshlrev_b32 v146, 16, v21\n" \
"v_and_b32 v147, 0xffff0000, v21\n" \
"v_lshlrev_b32 v148, 16, v22\n" \
"v_and_b32 v149, 0xffff0000, v22\n" \
"v_lshlrev_b32 v150, 16, v23\n" \
"v_and_b32 v151, 0xffff0000, v23\n" \
"v_pk_add_f32 v[136:137], v[136:137], v[144:145]\n" \
"v_pk_add_f32 v[138:139], v[138:139], v[146:147]\n" \
"v_pk_add_f32 v[140:141], v[140:141], v[148:149]\n" \
"v_pk_add_f32 v[142:143], v[142:143], v[150:151]\n" \
"s_waitcnt vmcnt(24)\n" \
"v_lshlrev_b32 v144, 16, v24\n" \
"v_and_b32 v145, 0xffff0000, v24\n" \
"v_lshlrev_b32 v146, 16, v25\n" \
"v_and_b32 v147, 0xffff0000, v25\n" \
"v_lshlrev_b32 v148, 16, v26\n" \
"v_and_b32 v149, 0xffff0000, v26\n" \
"v_lshlrev_b32 v150, 16, v27\n" \
"v_and_b32 v151, 0xffff0000, v27\n" \
"v_pk_add_f32 v[136:137], v[136:137], v[144:145]\n" \
"v_pk_add_f32 v[138:139], v[138:139], v[146:147]\n" \
"v_pk_add_f32 v[140:141], v[140:141], v[148:149]\n" \
"v_pk_add_f32 v[142:143], v[142:143], v[150:151]\n" \
"v_lshlrev_b32 v144, 16, v28\n" \
"v_and_b32 v145, 0xffff0000, v28\n" \
"v_lshlrev_b32 v146, 16, v29\n" \
"v_and_b32 v147, 0xffff0000, v29\n" \
"v_lshlrev_b32 v148, 16, v30\n" \
"v_and_b32 v149, 0xffff0000, v30\n" \
"v_lshlrev_b32 v150, 16, v31\n" \
"v_and_b32 v151, 0xffff0000, v31\n" \
"v_pk_add_f32 v[136:137], v[136:137], v[144:145]\n" \
"v_pk_add_f32 v[138:139], v[138:139], v[146:147]\n" \
"v_pk_add_f32 v[140:141], v[140:141], v[148:149]\n" \
"v_pk_add_f32 v[142:143], v[142:143], v[150:151]\n" \
"s_waitcnt vmcnt(22)\n" \
"v_lshlrev_b32 v144, 16, v32\n" \
"v_and_b32 v145, 0xffff0000, v32\n" \
"v_lshlrev_b32 v146, 16, v33\n" \
"v_and_b32 v147, 0xffff0000, v33\n" \
"v_lshlrev_b32 v148, 16, v34\n" \
"v_and_b32 v149, 0xffff0000, v34\n" \
"v_lshlrev_b32 v150, 16, v35\n" \
"v_and_b32 v151, 0xffff0000, v35\n" \
"v_pk_add_f32 v[136:137], v[136:137], v[144:145]\n" \
"v_pk_add_f32 v[138:139], v[138:139], v[146:147]\n" \
"v_pk_add_f32 v[140:141], v[140:141], v[148:149]\n" \
"v_pk_add_f32 v[142:143], v[142:143], v[150:151]\n" \
"v_lshlrev_b32 v144, 16, v36\n" \
"v_and_b32 v145, 0xffff0000, v36\n" \
"v_lshlrev_b32 v146, 16, v37\n" \
"v_and_b32 v147, 0xffff0000, v37\n" \
"v_lshlrev_b32 v148, 16, v38\n" \
"v_and_b32 v149, 0xffff0000, v38\n" \
"v_lshlrev_b32 v150, 16, v39\n" \
"v_and_b32 v151, 0xffff0000, v39\n" \
"v_pk_add_f32 v[136:137], v[136:137], v[144:145]\n" \
"v_pk_add_f32 v[138:139], v[138:139], v[146:147]\n" \
"v_pk_add_f32 v[140:141], v[140:141], v[148:149]\n" \
"v_pk_add_f32 v[142:143], v[142:143], v[150:151]\n" \
"s_waitcnt vmcnt(20)\n" \
"v_lshlrev_b32 v144, 16, v40\n" \
"v_and_b32 v145, 0xffff0000, v40\n" \
"v_lshlrev_b32 v146, 16, v41\n" \
"v_and_b32 v147, 0xffff0000, v41\n" \
"v_lshlrev_b32 v148, 16, v42\n" \
"v_and_b32 v149, 0xffff0000, v42\n" \
"v_lshlrev_b32 v150, 16, v43\n" \
"v_and_b32 v151, 0xffff0000, v43\n" \
"v_pk_add_f32 v[136:137], v[136:137], v[144:145]\n" \
"v_pk_add_f32 v[138:139], v[138:139], v[146:147]\n" \
"v_pk_add_f32 v[140:141], v[140:141], v[148:149]\n" \
"v_pk_add_f32 v[142:143], v[142:143], v[150:151]\n" \
"v_lshlrev_b32 v144, 16, v44\n" \
"v_and_b32 v145, 0xffff0000, v44\n" \
"v_lshlrev_b32 v146, 16, v45\n" \
"v_and_b32 v147, 0xffff0000, v45\n" \
"v_lshlrev_b32 v148, 16, v46\n" \
"v_and_b32 v149, 0xffff0000, v46\n" \
"v_lshlrev_b32 v150, 16, v47\n" \
"v_and_b32 v151, 0xffff0000, v47\n" \
"v_pk_add_f32 v[136:137], v[136:137], v[144:145]\n" \
"v_pk_add_f32 v[138:139], v[138:139], v[146:147]\n" \
"v_pk_add_f32 v[140:141], v[140:141], v[148:149]\n" \
"v_pk_add_f32 v[142:143], v[142:143], v[150:151]\n" \
"s_waitcnt vmcnt(18)\n" \
"v_lshlrev_b32 v144, 16, v48\n" \
"v_and_b32 v145, 0xffff0000, v48\n" \
"v_lshlrev_b32 v146, 16, v49\n" \
"v_and_b32 v147, 0xffff0000, v49\n" \
"v_lshlrev_b32 v148, 16, v50\n" \
"v_and_b32 v149, 0xffff0000, v50\n" \
"v_lshlrev_b32 v150, 16, v51\n" \
"v_and_b32 v151, 0xffff0000, v51\n" \
"v_pk_add_f32 v[136:137], v[136:137], v[144:145]\n" \
"v_pk_add_f32 v[138:139], v[138:139], v[146:147]\n" \
"v_pk_add_f32 v[140:141], v[140:141], v[148:149]\n" \
"v_pk_add_f32 v[142:143], v[142:143], v[150:151]\n" \
"v_lshlrev_b32 v144, 16, v52\n" \
"v_and_b32 v145, 0xffff0000, v52\n" \
"v_lshlrev_b32 v146, 16, v53\n" \
"v_and_b32 v147, 0xffff0000, v53\n" \
"v_lshlrev_b32 v148, 16, v54\n" \
"v_and_b32 v149, 0xffff0000, v54\n" \
"v_lshlrev_b32 v150, 16, v55\n" \
"v_and_b32 v151, 0xffff0000, v55\n" \
"v_pk_add_f32 v[136:137], v[136:137], v[144:145]\n" \
"v_pk_add_f32 v[138:139], v[138:139], v[146:147]\n" \
"v_pk_add_f32 v[140:141], v[140:141], v[148:149]\n" \
"v_pk_add_f32 v[142:143], v[142:143], v[150:151]\n" \
"s_waitcnt vmcnt(16)\n" \
"v_lshlrev_b32 v144, 16, v56\n" \
"v_and_b32 v145, 0xffff0000, v56\n" \
"v_lshlrev_b32 v146, 16, v57\n" \
"v_and_b32 v147, 0xffff0000, v57\n" \
"v_lshlrev_b32 v148, 16, v58\n" \
"v_and_b32 v149, 0xffff0000, v58\n" \
"v_lshlrev_b32 v150, 16, v59\n" \
"v_and_b32 v151, 0xffff0000, v59\n" \
"v_pk_add_f32 v[136:137], v[136:137], v[144:145]\n" \
"v_pk_add_f32 v[138:139], v[138:139], v[146:147]\n" \
"v_pk_add_f32 v[140:141], v[140:141], v[148:149]\n" \
"v_pk_add_f32 v[142:143], v[142:143], v[150:151]\n" \
"v_lshlrev_b32 v144, 16, v60\n" \
"v_and_b32 v145, 0xffff0000, v60\n" \
"v_lshlrev_b32 v146, 16, v61\n" \
"v_and_b32 v147, 0xffff0000, v61\n" \
"v_lshlrev_b32 v148, 16, v62\n" \
"v_and_b32 v149, 0xffff0000, v62\n" \
"v_lshlrev_b32 v150, 16, v63\n" \
"v_and_b32 v151, 0xffff0000, v63\n" \
"v_pk_add_f32 v[136:137], v[136:137], v[144:145]\n" \
"v_pk_add_f32 v[138:139], v[138:139], v[146:147]\n" \
"v_pk_add_f32 v[140:141], v[140:141], v[148:149]\n" \
"v_pk_add_f32 v[142:143], v[142:143], v[150:151]\n" \
"v_cvt_pk_bf16_f32 v152, v136, v137\n" \
"v_cvt_pk_bf16_f32 v153, v138, v139\n" \
"v_cvt_pk_bf16_f32 v154, v140, v141\n" \
"v_cvt_pk_bf16_f32 v155, v142, v143\n" \
"v_lshlrev_b32 v136, 16, v152\n" \
"v_and_b32 v137, 0xffff0000, v152\n" \
"v_lshlrev_b32 v144, 16, v128\n" \
"v_and_b32 v145, 0xffff0000, v128\n" \
"v_lshlrev_b32 v138, 16, v153\n" \
"v_and_b32 v139, 0xffff0000, v153\n" \
"v_lshlrev_b32 v146, 16, v129\n" \
"v_and_b32 v147, 0xffff0000, v129\n" \
"v_lshlrev_b32 v140, 16, v154\n" \
"v_and_b32 v141, 0xffff0000, v154\n" \
"v_lshlrev_b32 v148, 16, v130\n" \
"v_and_b32 v149, 0xffff0000, v130\n" \
"v_lshlrev_b32 v142, 16, v155\n" \
"v_and_b32 v143, 0xffff0000, v155\n" \
"v_lshlrev_b32 v150, 16, v131\n" \
"v_and_b32 v151, 0xffff0000, v131\n" \
"v_pk_add_f32 v[136:137], v[136:137], v[144:145]\n" \
"v_pk_add_f32 v[138:139], v[138:139], v[146:147]\n" \
"v_pk_add_f32 v[140:141], v[140:141], v[148:149]\n" \
"v_pk_add_f32 v[142:143], v[142:143], v[150:151]\n" \
"v_cvt_pk_bf16_f32 v152, v136, v137\n" \
"v_cvt_pk_bf16_f32 v153, v138, v139\n" \
"v_cvt_pk_bf16_f32 v154, v140, v141\n" \
"v_cvt_pk_bf16_f32 v155, v142, v143\n" \
"buffer_store_dwordx4 v[152:155], v224, %[rs], s65 offen sc1\n" \
"v_lshlrev_b32 v136, 16, v152\n" \
"v_and_b32 v137, 0xffff0000, v152\n" \
"v_lshlrev_b32 v138, 16, v153\n" \
"v_and_b32 v139, 0xffff0000, v153\n" \
"v_lshlrev_b32 v140, 16, v154\n" \
"v_and_b32 v141, 0xffff0000, v154\n" \
"v_lshlrev_b32 v142, 16, v155\n" \
"v_and_b32 v143, 0xffff0000, v155\n" \
"v_add_f32 v160, v136, v137\n" \
"v_add_f32 v160, v160, v138\n" \
"v_add_f32 v160, v160, v139\n" \
"v_add_f32 v160, v160, v140\n" \
"v_add_f32 v160, v160, v141\n" \
"v_add_f32 v160, v160, v142\n" \
"v_add_f32 v160, v160, v143\n" \
"v_mov_b32 v236, v160\n" \
"s_nop 1\n" \
"v_permlane16_swap_b32 v236, v160\n" \
"v_add_f32 v160, v160, v236\n" \
"v_mov_b32 v236, v160\n" \
"s_nop 1\n" \
"v_permlane32_swap_b32 v236, v160\n" \
"v_add_f32 v160, v160, v236\n" \
"v_mul_f32 v160, 0x3d000000, v160\n" \
"v_sub_f32 v136, v136, v160\n" \
"v_sub_f32 v137, v137, v160\n" \
"v_sub_f32 v138, v138, v160\n" \
"v_sub_f32 v139, v139, v160\n" \
"v_sub_f32 v140, v140, v160\n" \
"v_sub_f32 v141, v141, v160\n" \
"v_sub_f32 v142, v142, v160\n" \
"v_sub_f32 v143, v143, v160\n" \
"v_mul_f32 v161, v136, v136\n" \
"v_fmac_f32 v161, v137, v137\n" \
"v_fmac_f32 v161, v138, v138\n" \
"v_fmac_f32 v161, v139, v139\n" \
"v_fmac_f32 v161, v140, v140\n" \
"v_fmac_f32 v161, v141, v141\n" \
"v_fmac_f32 v161, v142, v142\n" \
"v_fmac_f32 v161, v143, v143\n" \
"v_mov_b32 v236, v161\n" \
"s_nop 1\n" \
"v_permlane16_swap_b32 v236, v161\n" \
"v_add_f32 v161, v161, v236\n" \
"v_mov_b32 v236, v161\n" \
"s_nop 1\n" \
"v_permlane32_swap_b32 v236, v161\n" \
"v_add_f32 v161, v161, v236\n" \
"v_lshlrev_b32 v237, 3, v224\n" \
"v_bfrev_b32 v238, 1\n" \
"v_cmp_gt_u32 vcc, 0x100, v224\n" \
"s_nop 1\n" \
"v_cndmask_b32 v237, v238, v237, vcc\n" \
"buffer_store_dwordx2 v[160:161], v237, %[rs], s67 offen sc1\n" \
"s_waitcnt vmcnt(16)\n" \
"v_lshlrev_b32 v136, 16, v64\n" \
"v_and_b32 v137, 0xffff0000, v64\n" \
"v_lshlrev_b32 v138, 16, v65\n" \
"v_and_b32 v139, 0xffff0000, v65\n" \
"v_lshlrev_b32 v140, 16, v66\n" \
"v_and_b32 v141, 0xffff0000, v66\n" \
"v_lshlrev_b32 v142, 16, v67\n" \
"v_and_b32 v143, 0xffff0000, v67\n" \
"v_lshlrev_b32 v144, 16, v68\n" \
"v_and_b32 v145, 0xffff0000, v68\n" \
"v_lshlrev_b32 v146, 16, v69\n" \
"v_and_b32 v147, 0xffff0000, v69\n" \
"v_lshlrev_b32 v148, 16, v70\n" \
"v_and_b32 v149, 0xffff0000, v70\n" \
"v_lshlrev_b32 v150, 16, v71\n" \
"v_and_b32 v151, 0xffff0000, v71\n" \
"v_pk_add_f32 v[136:137], v[136:137], v[144:145]\n" \
"v_pk_add_f32 v[138:139], v[138:139], v[146:147]\n" \
"v_pk_add_f32 v[140:141], v[140:141], v[148:149]\n" \
"v_pk_add_f32 v[142:143], v[142:143], v[150:151]\n" \
"s_waitcnt vmcnt(14)\n" \
"v_lshlrev_b32 v144, 16, v72\n" \
"v_and_b32 v145, 0xffff0000, v72\n" \
"v_lshlrev_b32 v146, 16, v73\n" \
"v_and_b32 v147, 0xffff0000, v73\n" \
"v_lshlrev_b32 v148, 16, v74\n" \
"v_and_b32 v149, 0xffff0000, v74\n" \
"v_lshlrev_b32 v150, 16, v75\n" \
"v_and_b32 v151, 0xffff0000, v75\n" \
"v_pk_add_f32 v[136:137], v[136:137], v[144:145]\n" \
"v_pk_add_f32 v[138:139], v[138:139], v[146:147]\n" \
"v_pk_add_f32 v[140:141], v[140:141], v[148:149]\n" \
"v_pk_add_f32 v[142:143], v[142:143], v[150:151]\n" \
"v_lshlrev_b32 v144, 16, v76\n" \
"v_and_b32 v145, 0xffff0000, v76\n" \
"v_lshlrev_b32 v146, 16, v77\n" \
"v_and_b32 v147, 0xffff0000, v77\n" \
"v_lshlrev_b32 v148, 16, v78\n" \
"v_and_b32 v149, 0xffff0000, v78\n" \
"v_lshlrev_b32 v150, 16, v79\n" \
"v_and_b32 v151, 0xffff0000, v79\n" \
"v_pk_add_f32 v[136:137], v[136:137], v[144:145]\n" \
"v_pk_add_f32 v[138:139], v[138:139], v[146:147]\n" \
"v_pk_add_f32 v[140:141], v[140:141], v[148:149]\n" \
"v_pk_add_f32 v[142:143], v[142:143], v[150:151]\n" \
"s_waitcnt vmcnt(12)\n" \
"v_lshlrev_b32 v144, 16, v80\n" \
"v_and_b32 v145, 0xffff0000, v80\n" \
"v_lshlrev_b32 v146, 16, v81\n" \
"v_and_b32 v147, 0xffff0000, v81\n" \
"v_lshlrev_b32 v148, 16, v82\n" \
"v_and_b32 v149, 0xffff0000, v82\n" \
"v_lshlrev_b32 v150, 16, v83\n" \
"v_and_b32 v151, 0xffff0000, v83\n" \
"v_pk_add_f32 v[136:137], v[136:137], v[144:145]\n" \
"v_pk_add_f32 v[138:139], v[138:139], v[146:147]\n" \
"v_pk_add_f32 v[140:141], v[140:141], v[148:149]\n" \
"v_pk_add_f32 v[142:143], v[142:143], v[150:151]\n" \
"v_lshlrev_b32 v144, 16, v84\n" \
"v_and_b32 v145, 0xffff0000, v84\n" \
"v_lshlrev_b32 v146, 16, v85\n" \
"v_and_b32 v147, 0xffff0000, v85\n" \
"v_lshlrev_b32 v148, 16, v86\n" \
"v_and_b32 v149, 0xffff0000, v86\n" \
"v_lshlrev_b32 v150, 16, v87\n" \
"v_and_b32 v151, 0xffff0000, v87\n" \
"v_pk_add_f32 v[136:137], v[136:137], v[144:145]\n" \
"v_pk_add_f32 v[138:139], v[138:139], v[146:147]\n" \
"v_pk_add_f32 v[140:141], v[140:141], v[148:149]\n" \
"v_pk_add_f32 v[142:143], v[142:143], v[150:151]\n" \
"s_waitcnt vmcnt(10)\n" \
"v_lshlrev_b32 v144, 16, v88\n" \
"v_and_b32 v145, 0xffff0000, v88\n" \
"v_lshlrev_b32 v146, 16, v89\n" \
"v_and_b32 v147, 0xffff0000, v89\n" \
"v_lshlrev_b32 v148, 16, v90\n" \
"v_and_b32 v149, 0xffff0000, v90\n" \
"v_lshlrev_b32 v150, 16, v91\n" \
"v_and_b32 v151, 0xffff0000, v91\n" \
"v_pk_add_f32 v[136:137], v[136:137], v[144:145]\n" \
"v_pk_add_f32 v[138:139], v[138:139], v[146:147]\n" \
"v_pk_add_f32 v[140:141], v[140:141], v[148:149]\n" \
"v_pk_add_f32 v[142:143], v[142:143], v[150:151]\n" \
"v_lshlrev_b32 v144, 16, v92\n" \
"v_and_b32 v145, 0xffff0000, v92\n" \
"v_lshlrev_b32 v146, 16, v93\n" \
"v_and_b32 v147, 0xffff0000, v93\n" \
"v_lshlrev_b32 v148, 16, v94\n" \
"v_and_b32 v149, 0xffff0000, v94\n" \
"v_lshlrev_b32 v150, 16, v95\n" \
"v_and_b32 v151, 0xffff0000, v95\n" \
"v_pk_add_f32 v[136:137], v[136:137], v[144:145]\n" \
"v_pk_add_f32 v[138:139], v[138:139], v[146:147]\n" \
"v_pk_add_f32 v[140:141], v[140:141], v[148:149]\n" \
"v_pk_add_f32 v[142:143], v[142:143], v[150:151]\n" \
"s_waitcnt vmcnt(8)\n" \
"v_lshlrev_b32 v144, 16, v96\n" \
"v_and_b32 v145, 0xffff0000, v96\n" \
"v_lshlrev_b32 v146, 16, v97\n" \
"v_and_b32 v147, 0xffff0000, v97\n" \
"v_lshlrev_b32 v148, 16, v98\n" \
"v_and_b32 v149, 0xffff0000, v98\n" \
"v_lshlrev_b32 v150, 16, v99\n" \
"v_and_b32 v151, 0xffff0000, v99\n" \
"v_pk_add_f32 v[136:137], v[136:137], v[144:145]\n" \
"v_pk_add_f32 v[138:139], v[138:139], v[146:147]\n" \
"v_pk_add_f32 v[140:141], v[140:141], v[148:149]\n" \
"v_pk_add_f32 v[142:143], v[142:143], v[150:151]\n" \
"v_lshlrev_b32 v144, 16, v100\n" \
"v_and_b32 v145, 0xffff0000, v100\n" \
"v_lshlrev_b32 v146, 16, v101\n" \
"v_and_b32 v147, 0xffff0000, v101\n" \
"v_lshlrev_b32 v148, 16, v102\n" \
"v_and_b32 v149, 0xffff0000, v102\n" \
"v_lshlrev_b32 v150, 16, v103\n" \
"v_and_b32 v151, 0xffff0000, v103\n" \
"v_pk_add_f32 v[136:137], v[136:137], v[144:145]\n" \
"v_pk_add_f32 v[138:139], v[138:139], v[146:147]\n" \
"v_pk_add_f32 v[140:141], v[140:141], v[148:149]\n" \
"v_pk_add_f32 v[142:143], v[142:143], v[150:151]\n" \
"s_waitcnt vmcnt(6)\n" \
"v_lshlrev_b32 v144, 16, v104\n" \
"v_and_b32 v145, 0xffff0000, v104\n" \
"v_lshlrev_b32 v146, 16, v105\n" \
"v_and_b32 v147, 0xffff0000, v105\n" \
"v_lshlrev_b32 v148, 16, v106\n" \
"v_and_b32 v149, 0xffff0000, v106\n" \
"v_lshlrev_b32 v150, 16, v107\n" \
"v_and_b32 v151, 0xffff0000, v107\n" \
"v_pk_add_f32 v[136:137], v[136:137], v[144:145]\n" \
"v_pk_add_f32 v[138:139], v[138:139], v[146:147]\n" \
"v_pk_add_f32 v[140:141], v[140:141], v[148:149]\n" \
"v_pk_add_f32 v[142:143], v[142:143], v[150:151]\n" \
"v_lshlrev_b32 v144, 16, v108\n" \
"v_and_b32 v145, 0xffff0000, v108\n" \
"v_lshlrev_b32 v146, 16, v109\n" \
"v_and_b32 v147, 0xffff0000, v109\n" \
"v_lshlrev_b32 v148, 16, v110\n" \
"v_and_b32 v149, 0xffff0000, v110\n" \
"v_lshlrev_b32 v150, 16, v111\n" \
"v_and_b32 v151, 0xffff0000, v111\n" \
"v_pk_add_f32 v[136:137], v[136:137], v[144:145]\n" \
"v_pk_add_f32 v[138:139], v[138:139], v[146:147]\n" \
"v_pk_add_f32 v[140:141], v[140:141], v[148:149]\n" \
"v_pk_add_f32 v[142:143], v[142:143], v[150:151]\n" \
"s_waitcnt vmcnt(4)\n" \
"v_lshlrev_b32 v144, 16, v112\n" \
"v_and_b32 v145, 0xffff0000, v112\n" \
"v_lshlrev_b32 v146, 16, v113\n" \
"v_and_b32 v147, 0xffff0000, v113\n" \
"v_lshlrev_b32 v148, 16, v114\n" \
"v_and_b32 v149, 0xffff0000, v114\n" \
"v_lshlrev_b32 v150, 16, v115\n" \
"v_and_b32 v151, 0xffff0000, v115\n" \
"v_pk_add_f32 v[136:137], v[136:137], v[144:145]\n" \
"v_pk_add_f32 v[138:139], v[138:139], v[146:147]\n" \
"v_pk_add_f32 v[140:141], v[140:141], v[148:149]\n" \
"v_pk_add_f32 v[142:143], v[142:143], v[150:151]\n" \
"v_lshlrev_b32 v144, 16, v116\n" \
"v_and_b32 v145, 0xffff0000, v116\n" \
"v_lshlrev_b32 v146, 16, v117\n" \
"v_and_b32 v147, 0xffff0000, v117\n" \
"v_lshlrev_b32 v148, 16, v118\n" \
"v_and_b32 v149, 0xffff0000, v118\n" \
"v_lshlrev_b32 v150, 16, v119\n" \
"v_and_b32 v151, 0xffff0000, v119\n" \
"v_pk_add_f32 v[136:137], v[136:137], v[144:145]\n" \
"v_pk_add_f32 v[138:139], v[138:139], v[146:147]\n" \
"v_pk_add_f32 v[140:141], v[140:141], v[148:149]\n" \
"v_pk_add_f32 v[142:143], v[142:143], v[150:151]\n" \
"s_waitcnt vmcnt(2)\n" \
"v_lshlrev_b32 v144, 16, v120\n" \
"v_and_b32 v145, 0xffff0000, v120\n" \
"v_lshlrev_b32 v146, 16, v121\n" \
"v_and_b32 v147, 0xffff0000, v121\n" \
"v_lshlrev_b32 v148, 16, v122\n" \
"v_and_b32 v149, 0xffff0000, v122\n" \
"v_lshlrev_b32 v150, 16, v123\n" \
"v_and_b32 v151, 0xffff0000, v123\n" \
"v_pk_add_f32 v[136:137], v[136:137], v[144:145]\n" \
"v_pk_add_f32 v[138:139], v[138:139], v[146:147]\n" \
"v_pk_add_f32 v[140:141], v[140:141], v[148:149]\n" \
"v_pk_add_f32 v[142:143], v[142:143], v[150:151]\n" \
"v_lshlrev_b32 v144, 16, v124\n" \
"v_and_b32 v145, 0xffff0000, v124\n" \
"v_lshlrev_b32 v146, 16, v125\n" \
"v_and_b32 v147, 0xffff0000, v125\n" \
"v_lshlrev_b32 v148, 16, v126\n" \
"v_and_b32 v149, 0xffff0000, v126\n" \
"v_lshlrev_b32 v150, 16, v127\n" \
"v_and_b32 v151, 0xffff0000, v127\n" \
"v_pk_add_f32 v[136:137], v[136:137], v[144:145]\n" \
"v_pk_add_f32 v[138:139], v[138:139], v[146:147]\n" \
"v_pk_add_f32 v[140:141], v[140:141], v[148:149]\n" \
"v_pk_add_f32 v[142:143], v[142:143], v[150:151]\n" \
"v_cvt_pk_bf16_f32 v156, v136, v137\n" \
"v_cvt_pk_bf16_f32 v157, v138, v139\n" \
"v_cvt_pk_bf16_f32 v158, v140, v141\n" \
"v_cvt_pk_bf16_f32 v159, v142, v143\n" \
"v_lshlrev_b32 v136, 16, v156\n" \
"v_and_b32 v137, 0xffff0000, v156\n" \
"v_lshlrev_b32 v144, 16, v132\n" \
"v_and_b32 v145, 0xffff0000, v132\n" \
"v_lshlrev_b32 v138, 16, v157\n" \
"v_and_b32 v139, 0xffff0000, v157\n" \
"v_lshlrev_b32 v146, 16, v133\n" \
"v_and_b32 v147, 0xffff0000, v133\n" \
"v_lshlrev_b32 v140, 16, v158\n" \
"v_and_b32 v141, 0xffff0000, v158\n" \
"v_lshlrev_b32 v148, 16, v134\n" \
"v_and_b32 v149, 0xffff0000, v134\n" \
"v_lshlrev_b32 v142, 16, v159\n" \
"v_and_b32 v143, 0xffff0000, v159\n" \
"v_lshlrev_b32 v150, 16, v135\n" \
"v_and_b32 v151, 0xffff0000, v135\n" \
"v_pk_add_f32 v[136:137], v[136:137], v[144:145]\n" \
"v_pk_add_f32 v[138:139], v[138:139], v[146:147]\n" \
"v_pk_add_f32 v[140:141], v[140:141], v[148:149]\n" \
"v_pk_add_f32 v[142:143], v[142:143], v[150:151]\n" \
"v_cvt_pk_bf16_f32 v156, v136, v137\n" \
"v_cvt_pk_bf16_f32 v157, v138, v139\n" \
"v_cvt_pk_bf16_f32 v158, v140, v141\n" \
"v_cvt_pk_bf16_f32 v159, v142, v143\n" \
"buffer_store_dwordx4 v[156:159], v224, %[rs], s66 offen sc1\n" \
"v_lshlrev_b32 v136, 16, v156\n" \
"v_and_b32 v137, 0xffff0000, v156\n" \
"v_lshlrev_b32 v138, 16, v157\n" \
"v_and_b32 v139, 0xffff0000, v157\n" \
"v_lshlrev_b32 v140, 16, v158\n" \
"v_and_b32 v141, 0xffff0000, v158\n" \
"v_lshlrev_b32 v142, 16, v159\n" \
"v_and_b32 v143, 0xffff0000, v159\n" \
"v_add_f32 v162, v136, v137\n" \
"v_add_f32 v162, v162, v138\n" \
"v_add_f32 v162, v162, v139\n" \
"v_add_f32 v162, v162, v140\n" \
"v_add_f32 v162, v162, v141\n" \
"v_add_f32 v162, v162, v142\n" \
"v_add_f32 v162, v162, v143\n" \
"v_mov_b32 v236, v162\n" \
"s_nop 1\n" \
"v_permlane16_swap_b32 v236, v162\n" \
"v_add_f32 v162, v162, v236\n" \
"v_mov_b32 v236, v162\n" \
"s_nop 1\n" \
"v_permlane32_swap_b32 v236, v162\n" \
"v_add_f32 v162, v162, v236\n" \
"v_mul_f32 v162, 0x3d000000, v162\n" \
"v_sub_f32 v136, v136, v162\n" \
"v_sub_f32 v137, v137, v162\n" \
"v_sub_f32 v138, v138, v162\n" \
"v_sub_f32 v139, v139, v162\n" \
"v_sub_f32 v140, v140, v162\n" \
"v_sub_f32 v141, v141, v162\n" \
"v_sub_f32 v142, v142, v162\n" \
"v_sub_f32 v143, v143, v162\n" \
"v_mul_f32 v163, v136, v136\n" \
"v_fmac_f32 v163, v137, v137\n" \
"v_fmac_f32 v163, v138, v138\n" \
"v_fmac_f32 v163, v139, v139\n" \
"v_fmac_f32 v163, v140, v140\n" \
"v_fmac_f32 v163, v141, v141\n" \
"v_fmac_f32 v163, v142, v142\n" \
"v_fmac_f32 v163, v143, v143\n" \
"v_mov_b32 v236, v163\n" \
"s_nop 1\n" \
"v_permlane16_swap_b32 v236, v163\n" \
"v_add_f32 v163, v163, v236\n" \
"v_mov_b32 v236, v163\n" \
"s_nop 1\n" \
"v_permlane32_swap_b32 v236, v163\n" \
"v_add_f32 v163, v163, v236\n" \
"buffer_store_dwordx2 v[162:163], v237, %[rs], s68 offen sc1\n" \
"s_waitcnt vmcnt(0)\n" \
"s_barrier\n" \
"s_lshl_b32 s82, s40, 2\n" \
"s_add_u32 s80, s82, s61\n" \
"s_cmp_lg_u32 s36, 0\n" \
"s_cbranch_scc1 Lts2%=\n" \
"s_mov_b64 s[96:97], exec\n" \
"s_mov_b64 exec, 1\n" \
"v_mov_b32 v236, %[tag]\n" \
"buffer_store_dword v236, off, %[rs], s80 sc1\n" \
"s_mov_b64 exec, s[96:97]\n" \
"Lts2%=:\n" \
"s_cmp_eq_u32 s74, 0\n" \
"s_cbranch_scc1 Lend%=\n" \
"v_lshrrev_b32 v236, 7, v224\n" \
"v_mul_u32_u24 v242, 0x380, v236\n" \
"v_bfe_u32 v236, v224, 4, 3\n" \
"v_lshlrev_b32 v237, 1, v226\n" \
"v_xor_b32 v236, v236, v237\n" \
"v_lshl_add_u32 v242, v236, 4, v242\n" \
"s_add_u32 s80, s58, s78\n" \
"s_add_u32 m0, s80, 0\n" \
"s_add_u32 s94, s79, 0\n" \
"buffer_load_dwordx4 v242, s[32:35], s94 offen offset:0 lds\n" \
"s_add_u32 s94, s79, 6144\n" \
"buffer_load_dwordx4 v242, s[32:35], s94 offen offset:1024 lds\n" \
"s_add_u32 s94, s79, 12288\n" \
"buffer_load_dwordx4 v242, s[32:35], s94 offen offset:2048 lds\n" \
"s_add_u32 s94, s79, 18432\n" \
"buffer_load_dwordx4 v242, s[32:35], s94 offen offset:3072 lds\n" \
"s_add_u32 s80, s58, s78\n" \
"s_add_u32 m0, s80, 4096\n" \
"s_add_u32 s94, s79, 28672\n" \
"buffer_load_dwordx4 v242, s[32:35], s94 offen offset:0 lds\n" \
"s_add_u32 s94, s79, 34816\n" \
"buffer_load_dwordx4 v242, s[32:35], s94 offen offset:1024 lds\n" \
"s_add_u32 s94, s79, 40960\n" \
"buffer_load_dwordx4 v242, s[32:35], s94 offen offset:2048 lds\n" \
"s_add_u32 s94, s79, 47104\n" \
"buffer_load_dwordx4 v242, s[32:35], s94 offen offset:3072 lds\n" \
"v_and_b32 v236, 7, v227\n" \
"v_lshlrev_b32 v236, 5, v236\n" \
"v_bfe_u32 v237, v224, 4, 2\n" \
"v_lshlrev_b32 v237, 3, v237\n" \
"v_lshl_add_u32 v237, v227, 8, v237\n" \
"v_xor_b32 v228, 0, v236\n" \
"v_xor_b32 v229, 32, v236\n" \
"v_xor_b32 v230, 64, v236\n" \
"v_xor_b32 v231, 96, v236\n" \
"v_xor_b32 v232, 128, v236\n" \
"v_xor_b32 v233, 160, v236\n" \
"v_xor_b32 v234, 192, v236\n" \
"v_xor_b32 v235, 224, v236\n" \
"v_add3_u32 v228, v228, v237, s54\n" \
"v_add3_u32 v229, v229, v237, s54\n" \
"v_add3_u32 v230, v230, v237, s54\n" \
"v_add3_u32 v231, v231, v237, s54\n" \
"v_add3_u32 v232, v232, v237, s54\n" \
"v_add3_u32 v233, v233, v237, s54\n" \
"v_add3_u32 v234, v234, v237, s54\n" \
"v_add3_u32 v235, v235, v237, s54\n" \
"s_lshl_b32 s80, s36, 6\n" \
"v_xor_b32 v164, s80, v236\n" \
"v_add_u32 v164, v164, v237\n" \
"v_xor_b32 v165, 32, v164\n" \
"v_mov_b32 v168, 0x3f803f80\n" \
"v_mov_b32 v169, 0x3f803f80\n" \
"v_mov_b32 v170, 0x3f803f80\n" \
"v_mov_b32 v171, 0x3f803f80\n" \
"v_add_u32 v166, s54, v164\n" \
"v_add_u32 v167, s54, v165\n" \
"ds_read_b64_tr_b16 v[0:1], v166 offset:0\n" \
"ds_read_b64_tr_b16 v[2:3], v166 offset:4096\n" \
"ds_read_b64_tr_b16 v[4:5], v167 offset:0\n" \
"ds_read_b64_tr_b16 v[6:7], v167 offset:4096\n" \
"ds_read_b64_tr_b16 v[8:9], v166 offset:8192\n" \
"ds_read_b64_tr_b16 v[10:11], v166 offset:12288\n" \
"ds_read_b64_tr_b16 v[12:13], v167 offset:8192\n" \
"ds_read_b64_tr_b16 v[14:15], v167 offset:12288\n" \
"ds_read_b64_tr_b16 v[16:17], v166 offset:16384\n" \
"ds_read_b64_tr_b16 v[18:19], v166 offset:20480\n" \
"ds_read_b64_tr_b16 v[20:21], v167 offset:16384\n" \
"ds_read_b64_tr_b16 v[22:23], v167 offset:20480\n" \
"ds_read_b64_tr_b16 v[24:25], v166 offset:24576\n" \
"ds_read_b64_tr_b16 v[26:27], v166 offset:28672\n" \
"ds_read_b64_tr_b16 v[28:29], v167 offset:24576\n" \
"ds_read_b64_tr_b16 v[30:31], v167 offset:28672\n" \
"s_waitcnt lgkmcnt(0)\n" \
"v_mfma_f32_16x16x32_bf16 v[172:175], v[0:3], v[168:171], 0\n" \
"v_mfma_f32_16x16x32_bf16 v[176:179], v[4:7], v[168:171], 0\n" \
"v_mfma_f32_16x16x32_bf16 v[172:175], v[8:11], v[168:171], v[172:175]\n" \
"v_mfma_f32_16x16x32_bf16 v[176:179], v[12:15], v[168:171], v[176:179]\n" \
"v_mfma_f32_16x16x32_bf16 v[172:175], v[16:19], v[168:171], v[172:175]\n" \
"v_mfma_f32_16x16x32_bf16 v[176:179], v[20:23], v[168:171], v[176:179]\n" \
"v_mfma_f32_16x16x32_bf16 v[172:175], v[24:27], v[168:171], v[172:175]\n" \
"v_mfma_f32_16x16x32_bf16 v[176:179], v[28:31], v[168:171], v[176:179]\n" \
"v_add_u32 v166, s55, v164\n" \
"v_add_u32 v167, s55, v165\n" \
"ds_read_b64_tr_b16 v[32:33], v166 offset:0\n" \
"ds_read_b64_tr_b16 v[34:35], v166 offset:4096\n" \
"ds_read_b64_tr_b16 v[36:37], v167 offset:0\n" \
"ds_read_b64_tr_b16 v[38:39], v167 offset:4096\n" \
"ds_read_b64_tr_b16 v[40:41], v166 offset:8192\n" \
"ds_read_b64_tr_b16 v[42:43], v166 offset:12288\n" \
"ds_read_b64_tr_b16 v[44:45], v167 offset:8192\n" \
"ds_read_b64_tr_b16 v[46:47], v167 offset:12288\n" \
"ds_read_b64_tr_b16 v[48:49], v166 offset:16384\n" \
"ds_read_b64_tr_b16 v[50:51], v166 offset:20480\n" \
"ds_read_b64_tr_b16 v[52:53], v167 offset:16384\n" \
"ds_read_b64_tr_b16 v[54:55], v167 offset:20480\n" \
"ds_read_b64_tr_b16 v[56:57], v166 offset:24576\n" \
"ds_read_b64_tr_b16 v[58:59], v166 offset:28672\n" \
"ds_read_b64_tr_b16 v[60:61], v167 offset:24576\n" \
"ds_read_b64_tr_b16 v[62:63], v167 offset:28672\n" \
"s_waitcnt lgkmcnt(0)\n" \
"v_mfma_f32_16x16x32_bf16 v[172:175], v[32:35], v[168:171], v[172:175]\n" \
"v_mfma_f32_16x16x32_bf16 v[176:179], v[36:39], v[168:171], v[176:179]\n" \
"v_mfma_f32_16x16x32_bf16 v[172:175], v[40:43], v[168:171], v[172:175]\n" \
"v_mfma_f32_16x16x32_bf16 v[176:179], v[44:47], v[168:171], v[176:179]\n" \
"v_mfma_f32_16x16x32_bf16 v[172:175], v[48:51], v[168:171], v[172:175]\n" \
"v_mfma_f32_16x16x32_bf16 v[176:179], v[52:55], v[168:171], v[176:179]\n" \
"v_mfma_f32_16x16x32_bf16 v[172:175], v[56:59], v[168:171], v[172:175]\n" \
"v_mfma_f32_16x16x32_bf16 v[176:179], v[60:63], v[168:171], v[176:179]\n" \
"v_add_u32 v166, s56, v164\n" \
"v_add_u32 v167, s56, v165\n" \
"ds_read_b64_tr_b16 v[0:1], v166 offset:0\n" \
"ds_read_b64_tr_b16 v[2:3], v166 offset:4096\n" \
"ds_read_b64_tr_b16 v[4:5], v167 offset:0\n" \
"ds_read_b64_tr_b16 v[6:7], v167 offset:4096\n" \
"ds_read_b64_tr_b16 v[8:9], v166 offset:8192\n" \
"ds_read_b64_tr_b16 v[10:11], v166 offset:12288\n" \
"ds_read_b64_tr_b16 v[12:13], v167 offset:8192\n" \
"ds_read_b64_tr_b16 v[14:15], v167 offset:12288\n" \
"ds_read_b64_tr_b16 v[16:17], v166 offset:16384\n" \
"ds_read_b64_tr_b16 v[18:19], v166 offset:20480\n" \
"ds_read_b64_tr_b16 v[20:21], v167 offset:16384\n" \
"ds_read_b64_tr_b16 v[22:23], v167 offset:20480\n" \
"ds_read_b64_tr_b16 v[24:25], v166 offset:24576\n" \
"ds_read_b64_tr_b16 v[26:27], v166 offset:28672\n" \
"ds_read_b64_tr_b16 v[28:29], v167 offset:24576\n" \
"ds_read_b64_tr_b16 v[30:31], v167 offset:28672\n" \
"s_waitcnt lgkmcnt(0)\n" \
"v_mfma_f32_16x16x32_bf16 v[172:175], v[0:3], v[168:171], v[172:175]\n" \
"v_mfma_f32_16x16x32_bf16 v[176:179], v[4:7], v[168:171], v[176:179]\n" \
"v_mfma_f32_16x16x32_bf16 v[172:175], v[8:11], v[168:171], v[172:175]\n" \
"v_mfma_f32_16x16x32_bf16 v[176:179], v[12:15], v[168:171], v[176:179]\n" \
"v_mfma_f32_16x16x32_bf16 v[172:175], v[16:19], v[168:171], v[172:175]\n" \
"v_mfma_f32_16x16x32_bf16 v[176:179], v[20:23], v[168:171], v[176:179]\n" \
"v_mfma_f32_16x16x32_bf16 v[172:175], v[24:27], v[168:171], v[172:175]\n" \
"v_mfma_f32_16x16x32_bf16 v[176:179], v[28:31], v[168:171], v[176:179]\n" \
"v_add_u32 v166, s57, v164\n" \
"v_add_u32 v167, s57, v165\n" \
"ds_read_b64_tr_b16 v[32:33], v166 offset:0\n" \
"ds_read_b64_tr_b16 v[34:35], v166 offset:4096\n" \
"ds_read_b64_tr_b16 v[36:37], v167 offset:0\n" \
"ds_read_b64_tr_b16 v[38:39], v167 offset:4096\n" \
"ds_read_b64_tr_b16 v[40:41], v166 offset:8192\n" \
"ds_read_b64_tr_b16 v[42:43], v166 offset:12288\n" \
"ds_read_b64_tr_b16 v[44:45], v167 offset:8192\n" \
"ds_read_b64_tr_b16 v[46:47], v167 offset:12288\n" \
"s_waitcnt lgkmcnt(0)\n" \
"v_mfma_f32_16x16x32_bf16 v[172:175], v[32:35], v[168:171], v[172:175]\n" \
"v_mfma_f32_16x16x32_bf16 v[176:179], v[36:39], v[168:171], v[176:179]\n" \
"v_mfma_f32_16x16x32_bf16 v[172:175], v[40:43], v[168:171], v[172:175]\n" \
"v_mfma_f32_16x16x32_bf16 v[176:179], v[44:47], v[168:171], v[176:179]\n" \
"s_lshl_b32 s80, s36, 7\n" \
"s_add_u32 s80, s80, 114688\n" \
"v_lshlrev_b32 v236, 4, v226\n" \
"v_add_u32 v236, s80, v236\n" \
"s_nop 2\n" \
"ds_write_b128 v236, v[172:175]\n" \
"ds_write_b128 v236, v[176:179] offset:64\n" \
"s_mov_b32 s81, 0\n" \
"Ltw2_p%=:\n" \
"buffer_load_dword v243, v240, %[rs], s61 offen sc1\n" \
"s_waitcnt vmcnt(0)\n" \
"v_cmp_ne_u32 s[96:97], %[tag], v243\n" \
"s_nop 1\n" \
"s_cmp_eq_u64 s[96:97], 0\n" \
"s_cbranch_scc1 Ltw2_d%=\n" \
"s_sleep 4\n" \
"s_add_u32 s81, s81, 1\n" \
"s_cmp_lt_u32 s81, 0x20000\n" \
"s_cbranch_scc1 Ltw2_p%=\n" \
"Ltw2_d%=:\n" \
"s_add_u32 s83, s71, 0\n" \
"buffer_load_dwordx4 v[112:115], v224, %[rs], s83 offen sc1\n" \
"s_add_u32 s83, s71, 14336\n" \
"buffer_load_dwordx4 v[116:119], v224, %[rs], s83 offen sc1\n" \
"s_add_u32 s83, s71, 1024\n" \
"buffer_load_dwordx4 v[120:123], v224, %[rs], s83 offen sc1\n" \
"s_add_u32 s83, s71, 15360\n" \
"buffer_load_dwordx4 v[124:127], v224, %[rs], s83 offen sc1\n" \
"s_add_u32 s83, s71, 2048\n" \
"buffer_load_dwordx4 v[128:131], v224, %[rs], s83 offen sc1\n" \
"s_add_u32 s83, s71, 16384\n" \
"buffer_load_dwordx4 v[132:135], v224, %[rs], s83 offen sc1\n" \
"s_add_u32 s83, s71, 3072\n" \
"buffer_load_dwordx4 v[136:139], v224, %[rs], s83 offen sc1\n" \
"s_add_u32 s83, s71, 17408\n" \
"buffer_load_dwordx4 v[140:143], v224, %[rs], s83 offen sc1\n" \
"ds_read_b64_tr_b16 v[64:65], v228 offset:0\n" \
"ds_read_b64_tr_b16 v[66:67], v228 offset:4096\n" \
"ds_read_b64_tr_b16 v[68:69], v229 offset:0\n" \
"ds_read_b64_tr_b16 v[70:71], v229 offset:4096\n" \
"ds_read_b64_tr_b16 v[72:73], v230 offset:0\n" \
"ds_read_b64_tr_b16 v[74:75], v230 offset:4096\n" \
"ds_read_b64_tr_b16 v[76:77], v231 offset:0\n" \
"ds_read_b64_tr_b16 v[78:79], v231 offset:4096\n" \
"ds_read_b64_tr_b16 v[80:81], v232 offset:0\n" \
"ds_read_b64_tr_b16 v[82:83], v232 offset:4096\n" \
"ds_read_b64_tr_b16 v[84:85], v233 offset:0\n" \
"ds_read_b64_tr_b16 v[86:87], v233 offset:4096\n" \
"v_bfe_u32 v236, v224, 4, 4\n" \
"v_lshlrev_b32 v236, 7, v236\n" \
"v_lshl_add_u32 v236, v226, 5, v236\n" \
"s_waitcnt lgkmcnt(8)\n" \
"s_waitcnt vmcnt(6)\n" \
"v_mfma_f32_16x16x32_bf16 v[0:3], v[64:67], v[112:115], 0\n" \
"ds_read_b64_tr_b16 v[88:89], v234 offset:0\n" \
"s_add_u32 s83, s71, 4096\n" \
"v_mfma_f32_16x16x32_bf16 v[4:7], v[64:67], v[116:119], 0\n" \
"ds_read_b64_tr_b16 v[90:91], v234 offset:4096\n" \
"v_mfma_f32_16x16x32_bf16 v[8:11], v[68:71], v[112:115], 0\n" \
"ds_read_b64_tr_b16 v[92:93], v235 offset:0\n" \
"buffer_load_dwordx4 v[144:147], v224, %[rs], s83 offen sc1\n" \
"v_mfma_f32_16x16x32_bf16 v[12:15], v[68:71], v[116:119], 0\n" \
"ds_read_b64_tr_b16 v[94:95], v235 offset:4096\n" \
"s_waitcnt lgkmcnt(8)\n" \
"v_mfma_f32_16x16x32_bf16 v[16:19], v[72:75], v[112:115], 0\n" \
"ds_read_b64_tr_b16 v[64:65], v228 offset:8192\n" \
"s_add_u32 s83, s71, 18432\n" \
"v_mfma_f32_16x16x32_bf16 v[20:23], v[72:75], v[116:119], 0\n" \
"ds_read_b64_tr_b16 v[66:67], v228 offset:12288\n" \
"v_mfma_f32_16x16x32_bf16 v[24:27], v[76:79], v[112:115], 0\n" \
"ds_read_b64_tr_b16 v[68:69], v229 offset:8192\n" \
"buffer_load_dwordx4 v[148:151], v224, %[rs], s83 offen sc1\n" \
"v_mfma_f32_16x16x32_bf16 v[28:31], v[76:79], v[116:119], 0\n" \
"ds_read_b64_tr_b16 v[70:71], v229 offset:12288\n" \
"s_waitcnt lgkmcnt(8)\n" \
"v_mfma_f32_16x16x32_bf16 v[32:35], v[80:83], v[112:115], 0\n" \
"ds_read_b64_tr_b16 v[72:73], v230 offset:8192\n" \
"s_add_u32 s83, s71, 5120\n" \
"v_mfma_f32_16x16x32_bf16 v[36:39], v[80:83], v[116:119], 0\n" \
"ds_read_b64_tr_b16 v[74:75], v230 offset:12288\n" \
"v_mfma_f32_16x16x32_bf16 v[40:43], v[84:87], v[112:115], 0\n" \
"ds_read_b64_tr_b16 v[76:77], v231 offset:8192\n" \
"buffer_load_dwordx4 v[152:155], v224, %[rs], s83 offen sc1\n" \
"v_mfma_f32_16x16x32_bf16 v[44:47], v[84:87], v[116:119], 0\n" \
"ds_read_b64_tr_b16 v[78:79], v231 offset:12288\n" \
"s_waitcnt lgkmcnt(8)\n" \
"v_mfma_f32_16x16x32_bf16 v[48:51], v[88:91], v[112:115], 0\n" \
"ds_read_b64_tr_b16 v[80:81], v232 offset:8192\n" \
"s_add_u32 s83, s71, 19456\n" \
"v_mfma_f32_16x16x32_bf16 v[52:55], v[88:91], v[116:119], 0\n" \
"ds_read_b64_tr_b16 v[82:83], v232 offset:12288\n" \
"v_mfma_f32_16x16x32_bf16 v[56:59], v[92:95], v[112:115], 0\n" \
"ds_read_b64_tr_b16 v[84:85], v233 offset:8192\n" \
"buffer_load_dwordx4 v[156:159], v224, %[rs], s83 offen sc1\n" \
"v_mfma_f32_16x16x32_bf16 v[60:63], v[92:95], v[116:119], 0\n" \
"ds_read_b64_tr_b16 v[86:87], v233 offset:12288\n" \
"s_waitcnt lgkmcnt(8)\n" \
"s_waitcnt vmcnt(8)\n" \
"v_mfma_f32_16x16x32_bf16 v[0:3], v[64:67], v[120:123], v[0:3]\n" \
"ds_read_b64_tr_b16 v[88:89], v234 offset:8192\n" \
"s_add_u32 s83, s71, 6144\n" \
"v_mfma_f32_16x16x32_bf16 v[4:7], v[64:67], v[124:127], v[4:7]\n" \
"ds_read_b64_tr_b16 v[90:91], v234 offset:12288\n" \
"v_mfma_f32_16x16x32_bf16 v[8:11], v[68:71], v[120:123], v[8:11]\n" \
"ds_read_b64_tr_b16 v[92:93], v235 offset:8192\n" \
"buffer_load_dwordx4 v[160:163], v224, %[rs], s83 offen sc1\n" \
"v_mfma_f32_16x16x32_bf16 v[12:15], v[68:71], v[124:127], v[12:15]\n" \
"ds_read_b64_tr_b16 v[94:95], v235 offset:12288\n" \
"s_waitcnt lgkmcnt(8)\n" \
"v_mfma_f32_16x16x32_bf16 v[16:19], v[72:75], v[120:123], v[16:19]\n" \
"ds_read_b64_tr_b16 v[64:65], v228 offset:16384\n" \
"s_add_u32 s83, s71, 20480\n" \
"v_mfma_f32_16x16x32_bf16 v[20:23], v[72:75], v[124:127], v[20:23]\n" \
"ds_read_b64_tr_b16 v[66:67], v228 offset:20480\n" \
"v_mfma_f32_16x16x32_bf16 v[24:27], v[76:79], v[120:123], v[24:27]\n" \
"ds_read_b64_tr_b16 v[68:69], v229 offset:16384\n" \
"buffer_load_dwordx4 v[164:167], v224, %[rs], s83 offen sc1\n" \
"v_mfma_f32_16x16x32_bf16 v[28:31], v[76:79], v[124:127], v[28:31]\n" \
"ds_read_b64_tr_b16 v[70:71], v229 offset:20480\n" \
"s_waitcnt lgkmcnt(8)\n" \
"v_mfma_f32_16x16x32_bf16 v[32:35], v[80:83], v[120:123], v[32:35]\n" \
"ds_read_b64_tr_b16 v[72:73], v230 offset:16384\n" \
"s_add_u32 s83, s71, 7168\n" \
"v_mfma_f32_16x16x32_bf16 v[36:39], v[80:83], v[124:127], v[36:39]\n" \
"ds_read_b64_tr_b16 v[74:75], v230 offset:20480\n" \
"v_mfma_f32_16x16x32_bf16 v[40:43], v[84:87], v[120:123], v[40:43]\n" \
"ds_read_b64_tr_b16 v[76:77], v231 offset:16384\n" \
"buffer_load_dwordx4 v[168:171], v224, %[rs], s83 offen sc1\n" \
"v_mfma_f32_16x16x32_bf16 v[44:47], v[84:87], v[124:127], v[44:47]\n" \
"ds_read_b64_tr_b16 v[78:79], v231 offset:20480\n" \
"s_waitcnt lgkmcnt(8)\n" \
"v_mfma_f32_16x16x32_bf16 v[48:51], v[88:91], v[120:123], v[48:51]\n" \
"ds_read_b64_tr_b16 v[80:81], v232 offset:16384\n" \
"s_add_u32 s83, s71, 21504\n" \
"v_mfma_f32_16x16x32_bf16 v[52:55], v[88:91], v[124:127], v[52:55]\n" \
"ds_read_b64_tr_b16 v[82:83], v232 offset:20480\n" \
"v_mfma_f32_16x16x32_bf16 v[56:59], v[92:95], v[120:123], v[56:59]\n" \
"ds_read_b64_tr_b16 v[84:85], v233 offset:16384\n" \
"buffer_load_dwordx4 v[172:175], v224, %[rs], s83 offen sc1\n" \
"v_mfma_f32_16x16x32_bf16 v[60:63], v[92:95], v[124:127], v[60:63]\n" \
"ds_read_b64_tr_b16 v[86:87], v233 offset:20480\n" \
"s_waitcnt lgkmcnt(8)\n" \
"s_waitcnt vmcnt(10)\n" \
"v_mfma_f32_16x16x32_bf16 v[0:3], v[64:67], v[128:131], v[0:3]\n" \
"ds_read_b64_tr_b16 v[88:89], v234 offset:16384\n" \
"s_add_u32 s83, s71, 8192\n" \
"v_mfma_f32_16x16x32_bf16 v[4:7], v[64:67], v[132:135], v[4:7]\n" \
"ds_read_b64_tr_b16 v[90:91], v234 offset:20480\n" \
"v_mfma_f32_16x16x32_bf16 v[8:11], v[68:71], v[128:131], v[8:11]\n" \
"ds_read_b64_tr_b16 v[92:93], v235 offset:16384\n" \
"buffer_load_dwordx4 v[176:179], v224, %[rs], s83 offen sc1\n" \
"v_mfma_f32_16x16x32_bf16 v[12:15], v[68:71], v[132:135], v[12:15]\n" \
"ds_read_b64_tr_b16 v[94:95], v235 offset:20480\n" \
"s_waitcnt lgkmcnt(8)\n" \
"v_mfma_f32_16x16x32_bf16 v[16:19], v[72:75], v[128:131], v[16:19]\n" \
"ds_read_b64_tr_b16 v[64:65], v228 offset:24576\n" \
"s_add_u32 s83, s71, 22528\n" \
"v_mfma_f32_16x16x32_bf16 v[20:23], v[72:75], v[132:135], v[20:23]\n" \
"ds_read_b64_tr_b16 v[66:67], v228 offset:28672\n" \
"v_mfma_f32_16x16x32_bf16 v[24:27], v[76:79], v[128:131], v[24:27]\n" \
"ds_read_b64_tr_b16 v[68:69], v229 offset:24576\n" \
"buffer_load_dwordx4 v[180:183], v224, %[rs], s83 offen sc1\n" \
"v_mfma_f32_16x16x32_bf16 v[28:31], v[76:79], v[132:135], v[28:31]\n" \
"ds_read_b64_tr_b16 v[70:71], v229 offset:28672\n" \
"s_waitcnt lgkmcnt(8)\n" \
"v_mfma_f32_16x16x32_bf16 v[32:35], v[80:83], v[128:131], v[32:35]\n" \
"ds_read_b64_tr_b16 v[72:73], v230 offset:24576\n" \
"s_add_u32 s83, s71, 9216\n" \
"v_mfma_f32_16x16x32_bf16 v[36:39], v[80:83], v[132:135], v[36:39]\n" \
"ds_read_b64_tr_b16 v[74:75], v230 offset:28672\n" \
"v_mfma_f32_16x16x32_bf16 v[40:43], v[84:87], v[128:131], v[40:43]\n" \
"ds_read_b64_tr_b16 v[76:77], v231 offset:24576\n" \
"buffer_load_dwordx4 v[184:187], v224, %[rs], s83 offen sc1\n" \
"v_mfma_f32_16x16x32_bf16 v[44:47], v[84:87], v[132:135], v[44:47]\n" \
"ds_read_b64_tr_b16 v[78:79], v231 offset:28672\n" \
"s_waitcnt lgkmcnt(8)\n" \
"v_mfma_f32_16x16x32_bf16 v[48:51], v[88:91], v[128:131], v[48:51]\n" \
"ds_read_b64_tr_b16 v[80:81], v232 offset:24576\n" \
"s_add_u32 s83, s71, 23552\n" \
"v_mfma_f32_16x16x32_bf16 v[52:55], v[88:91], v[132:135], v[52:55]\n" \
"ds_read_b64_tr_b16 v[82:83], v232 offset:28672\n" \
"v_mfma_f32_16x16x32_bf16 v[56:59], v[92:95], v[128:131], v[56:59]\n" \
"ds_read_b64_tr_b16 v[84:85], v233 offset:24576\n" \
"buffer_load_dwordx4 v[188:191], v224, %[rs], s83 offen sc1\n" \
"v_mfma_f32_16x16x32_bf16 v[60:63], v[92:95], v[132:135], v[60:63]\n" \
"ds_read_b64_tr_b16 v[86:87], v233 offset:28672\n" \
"s_waitcnt lgkmcnt(8)\n" \
"s_waitcnt vmcnt(12)\n" \
"v_mfma_f32_16x16x32_bf16 v[0:3], v[64:67], v[136:139], v[0:3]\n" \
"ds_read_b64_tr_b16 v[88:89], v234 offset:24576\n" \
"s_add_u32 s83, s71, 10240\n" \
"v_mfma_f32_16x16x32_bf16 v[4:7], v[64:67], v[140:143], v[4:7]\n" \
"ds_read_b64_tr_b16 v[90:91], v234 offset:28672\n" \
"v_mfma_f32_16x16x32_bf16 v[8:11], v[68:71], v[136:139], v[8:11]\n" \
"ds_read_b64_tr_b16 v[92:93], v235 offset:24576\n" \
"buffer_load_dwordx4 v[192:195], v224, %[rs], s83 offen sc1\n" \
"v_mfma_f32_16x16x32_bf16 v[12:15], v[68:71], v[140:143], v[12:15]\n" \
"ds_read_b64_tr_b16 v[94:95], v235 offset:28672\n" \
"s_waitcnt lgkmcnt(8)\n" \
"v_mfma_f32_16x16x32_bf16 v[16:19], v[72:75], v[136:139], v[16:19]\n" \
"s_sub_u32 s82, s55, s54\n" \
"v_add_u32 v228, s82, v228\n" \
"ds_read_b64_tr_b16 v[64:65], v228 offset:0\n" \
"s_add_u32 s83, s71, 24576\n" \
"v_mfma_f32_16x16x32_bf16 v[20:23], v[72:75], v[140:143], v[20:23]\n" \
"ds_read_b64_tr_b16 v[66:67], v228 offset:4096\n" \
"v_mfma_f32_16x16x32_bf16 v[24:27], v[76:79], v[136:139], v[24:27]\n" \
"v_add_u32 v229, s82, v229\n" \
"ds_read_b64_tr_b16 v[68:69], v229 offset:0\n" \
"buffer_load_dwordx4 v[196:199], v224, %[rs], s83 offen sc1\n" \
"v_mfma_f32_16x16x32_bf16 v[28:31], v[76:79], v[140:143], v[28:31]\n" \
"ds_read_b64_tr_b16 v[70:71], v229 offset:4096\n" \
"s_waitcnt lgkmcnt(8)\n" \
"v_mfma_f32_16x16x32_bf16 v[32:35], v[80:83], v[136:139], v[32:35]\n" \
"v_add_u32 v230, s82, v230\n" \
"ds_read_b64_tr_b16 v[72:73], v230 offset:0\n" \
"s_add_u32 s83, s71, 11264\n" \
"v_mfma_f32_16x16x32_bf16 v[36:39], v[80:83], v[140:143], v[36:39]\n" \
"ds_read_b64_tr_b16 v[74:75], v230 offset:4096\n" \
"v_mfma_f32_16x16x32_bf16 v[40:43], v[84:87], v[136:139], v[40:43]\n" \
"v_add_u32 v231, s82, v231\n" \
"ds_read_b64_tr_b16 v[76:77], v231 offset:0\n" \
"buffer_load_dwordx4 v[200:203], v224, %[rs], s83 offen sc1\n" \
"v_mfma_f32_16x16x32_bf16 v[44:47], v[84:87], v[140:143], v[44:47]\n" \
"ds_read_b64_tr_b16 v[78:79], v231 offset:4096\n" \
"s_waitcnt lgkmcnt(8)\n" \
"v_mfma_f32_16x16x32_bf16 v[48:51], v[88:91], v[136:139], v[48:51]\n" \
"v_add_u32 v232, s82, v232\n" \
"ds_read_b64_tr_b16 v[80:81], v232 offset:0\n" \
"s_add_u32 s83, s71, 25600\n" \
"v_mfma_f32_16x16x32_bf16 v[52:55], v[88:91], v[140:143], v[52:55]\n" \
"ds_read_b64_tr_b16 v[82:83], v232 offset:4096\n" \
"v_mfma_f32_16x16x32_bf16 v[56:59], v[92:95], v[136:139], v[56:59]\n" \
"v_add_u32 v233, s82, v233\n" \
"ds_read_b64_tr_b16 v[84:85], v233 offset:0\n" \
"buffer_load_dwordx4 v[204:207], v224, %[rs], s83 offen sc1\n" \
"v_mfma_f32_16x16x32_bf16 v[60:63], v[92:95], v[140:143], v[60:63]\n" \
"ds_read_b64_tr_b16 v[86:87], v233 offset:4096\n" \
"s_waitcnt lgkmcnt(8)\n" \
"s_waitcnt vmcnt(14)\n" \
"v_mfma_f32_16x16x32_bf16 v[0:3], v[64:67], v[144:147], v[0:3]\n" \
"v_add_u32 v234, s82, v234\n" \
"ds_read_b64_tr_b16 v[88:89], v234 offset:0\n" \
"s_add_u32 s83, s71, 12288\n" \
"v_mfma_f32_16x16x32_bf16 v[4:7], v[64:67], v[148:151], v[4:7]\n" \
"ds_read_b64_tr_b16 v[90:91], v234 offset:4096\n" \
"v_mfma_f32_16x16x32_bf16 v[8:11], v[68:71], v[144:147], v[8:11]\n" \
"v_add_u32 v235, s82, v235\n" \
"ds_read_b64_tr_b16 v[92:93], v235 offset:0\n" \
"buffer_load_dwordx4 v[208:211], v224, %[rs], s83 offen sc1\n" \
"v_mfma_f32_16x16x32_bf16 v[12:15], v[68:71], v[148:151], v[12:15]\n" \
"ds_read_b64_tr_b16 v[94:95], v235 offset:4096\n" \
"s_waitcnt lgkmcnt(8)\n" \
"v_mfma_f32_16x16x32_bf16 v[16:19], v[72:75], v[144:147], v[16:19]\n" \
"ds_read_b64_tr_b16 v[64:65], v228 offset:8192\n" \
"s_add_u32 s83, s71, 26624\n" \
"v_mfma_f32_16x16x32_bf16 v[20:23], v[72:75], v[148:151], v[20:23]\n" \
"ds_read_b64_tr_b16 v[66:67], v228 offset:12288\n" \
"v_mfma_f32_16x16x32_bf16 v[24:27], v[76:79], v[144:147], v[24:27]\n" \
"ds_read_b64_tr_b16 v[68:69], v229 offset:8192\n" \
"buffer_load_dwordx4 v[212:215], v224, %[rs], s83 offen sc1\n" \
"v_mfma_f32_16x16x32_bf16 v[28:31], v[76:79], v[148:151], v[28:31]\n" \
"ds_read_b64_tr_b16 v[70:71], v229 offset:12288\n" \
"s_waitcnt lgkmcnt(8)\n" \
"v_mfma_f32_16x16x32_bf16 v[32:35], v[80:83], v[144:147], v[32:35]\n" \
"ds_read_b64_tr_b16 v[72:73], v230 offset:8192\n" \
"s_add_u32 s83, s71, 13312\n" \
"v_mfma_f32_16x16x32_bf16 v[36:39], v[80:83], v[148:151], v[36:39]\n" \
"ds_read_b64_tr_b16 v[74:75], v230 offset:12288\n" \
"v_mfma_f32_16x16x32_bf16 v[40:43], v[84:87], v[144:147], v[40:43]\n" \
"ds_read_b64_tr_b16 v[76:77], v231 offset:8192\n" \
"buffer_load_dwordx4 v[216:219], v224, %[rs], s83 offen sc1\n" \
"v_mfma_f32_16x16x32_bf16 v[44:47], v[84:87], v[148:151], v[44:47]\n" \
"ds_read_b64_tr_b16 v[78:79], v231 offset:12288\n" \
"s_waitcnt lgkmcnt(8)\n" \
"v_mfma_f32_16x16x32_bf16 v[48:51], v[88:91], v[144:147], v[48:51]\n" \
"ds_read_b64_tr_b16 v[80:81], v232 offset:8192\n" \
"s_add_u32 s83, s71, 27648\n" \
"v_mfma_f32_16x16x32_bf16 v[52:55], v[88:91], v[148:151], v[52:55]\n" \
"ds_read_b64_tr_b16 v[82:83], v232 offset:12288\n" \
"v_mfma_f32_16x16x32_bf16 v[56:59], v[92:95], v[144:147], v[56:59]\n" \
"ds_read_b64_tr_b16 v[84:85], v233 offset:8192\n" \
"buffer_load_dwordx4 v[220:223], v224, %[rs], s83 offen sc1\n" \
"v_mfma_f32_16x16x32_bf16 v[60:63], v[92:95], v[148:151], v[60:63]\n" \
"ds_read_b64_tr_b16 v[86:87], v233 offset:12288\n" \
"s_waitcnt lgkmcnt(8)\n" \
"s_waitcnt vmcnt(16)\n" \
"v_mfma_f32_16x16x32_bf16 v[0:3], v[64:67], v[152:155], v[0:3]\n" \
"ds_read_b64_tr_b16 v[88:89], v234 offset:8192\n" \
"s_add_u32 s83, s72, 0\n" \
"v_mfma_f32_16x16x32_bf16 v[4:7], v[64:67], v[156:159], v[4:7]\n" \
"ds_read_b64_tr_b16 v[90:91], v234 offset:12288\n" \
"v_mfma_f32_16x16x32_bf16 v[8:11], v[68:71], v[152:155], v[8:11]\n" \
"ds_read_b64_tr_b16 v[92:93], v235 offset:8192\n" \
"buffer_load_dwordx4 v[96:99], v236, %[rs], s83 offen offset:0 sc1\n" \
"v_mfma_f32_16x16x32_bf16 v[12:15], v[68:71], v[156:159], v[12:15]\n" \
"ds_read_b64_tr_b16 v[94:95], v235 offset:12288\n" \
"s_waitcnt lgkmcnt(8)\n" \
"v_mfma_f32_16x16x32_bf16 v[16:19], v[72:75], v[152:155], v[16:19]\n" \
"ds_read_b64_tr_b16 v[64:65], v228 offset:16384\n" \
"s_add_u32 s83, s72, 0\n" \
"v_mfma_f32_16x16x32_bf16 v[20:23], v[72:75], v[156:159], v[20:23]\n" \
"ds_read_b64_tr_b16 v[66:67], v228 offset:20480\n" \
"v_mfma_f32_16x16x32_bf16 v[24:27], v[76:79], v[152:155], v[24:27]\n" \
"ds_read_b64_tr_b16 v[68:69], v229 offset:16384\n" \
"buffer_load_dwordx4 v[100:103], v236, %[rs], s83 offen offset:16 sc1\n" \
"v_mfma_f32_16x16x32_bf16 v[28:31], v[76:79], v[156:159], v[28:31]\n" \
"ds_read_b64_tr_b16 v[70:71], v229 offset:20480\n" \
"s_waitcnt lgkmcnt(8)\n" \
"v_mfma_f32_16x16x32_bf16 v[32:35], v[80:83], v[152:155], v[32:35]\n" \
"ds_read_b64_tr_b16 v[72:73], v230 offset:16384\n" \
"s_add_u32 s83, s72, 2048\n" \
"v_mfma_f32_16x16x32_bf16 v[36:39], v[80:83], v[156:159], v[36:39]\n" \
"ds_read_b64_tr_b16 v[74:75], v230 offset:20480\n" \
"v_mfma_f32_16x16x32_bf16 v[40:43], v[84:87], v[152:155], v[40:43]\n" \
"ds_read_b64_tr_b16 v[76:77], v231 offset:16384\n" \
"buffer_load_dwordx4 v[104:107], v236, %[rs], s83 offen offset:0 sc1\n" \
"v_mfma_f32_16x16x32_bf16 v[44:47], v[84:87], v[156:159], v[44:47]\n" \
"ds_read_b64_tr_b16 v[78:79], v231 offset:20480\n" \
"s_waitcnt lgkmcnt(8)\n" \
"v_mfma_f32_16x16x32_bf16 v[48:51], v[88:91], v[152:155], v[48:51]\n" \
"ds_read_b64_tr_b16 v[80:81], v232 offset:16384\n" \
"s_add_u32 s83, s72, 2048\n" \
"v_mfma_f32_16x16x32_bf16 v[52:55], v[88:91], v[156:159], v[52:55]\n" \
"ds_read_b64_tr_b16 v[82:83], v232 offset:20480\n" \
"v_mfma_f32_16x16x32_bf16 v[56:59], v[92:95], v[152:155], v[56:59]\n" \
"ds_read_b64_tr_b16 v[84:85], v233 offset:16384\n" \
"buffer_load_dwordx4 v[108:111], v236, %[rs], s83 offen offset:16 sc1\n" \
"v_mfma_f32_16x16x32_bf16 v[60:63], v[92:95], v[156:159], v[60:63]\n" \
"ds_read_b64_tr_b16 v[86:87], v233 offset:20480\n" \
"s_waitcnt lgkmcnt(8)\n" \
"s_waitcnt vmcnt(18)\n" \
"v_mfma_f32_16x16x32_bf16 v[0:3], v[64:67], v[160:163], v[0:3]\n" \
"ds_read_b64_tr_b16 v[88:89], v234 offset:16384\n" \
"v_mfma_f32_16x16x32_bf16 v[4:7], v[64:67], v[164:167], v[4:7]\n" \
"ds_read_b64_tr_b16 v[90:91], v234 offset:20480\n" \
"v_mfma_f32_16x16x32_bf16 v[8:11], v[68:71], v[160:163], v[8:11]\n" \
"ds_read_b64_tr_b16 v[92:93], v235 offset:16384\n" \
"v_mfma_f32_16x16x32_bf16 v[12:15], v[68:71], v[164:167], v[12:15]\n" \
"ds_read_b64_tr_b16 v[94:95], v235 offset:20480\n" \
"s_waitcnt lgkmcnt(8)\n" \
"v_mfma_f32_16x16x32_bf16 v[16:19], v[72:75], v[160:163], v[16:19]\n" \
"ds_read_b64_tr_b16 v[64:65], v228 offset:24576\n" \
"v_mfma_f32_16x16x32_bf16 v[20:23], v[72:75], v[164:167], v[20:23]\n" \
"ds_read_b64_tr_b16 v[66:67], v228 offset:28672\n" \
"v_mfma_f32_16x16x32_bf16 v[24:27], v[76:79], v[160:163], v[24:27]\n" \
"ds_read_b64_tr_b16 v[68:69], v229 offset:24576\n" \
"v_mfma_f32_16x16x32_bf16 v[28:31], v[76:79], v[164:167], v[28:31]\n" \
"ds_read_b64_tr_b16 v[70:71], v229 offset:28672\n" \
"s_waitcnt lgkmcnt(8)\n" \
"v_mfma_f32_16x16x32_bf16 v[32:35], v[80:83], v[160:163], v[32:35]\n" \
"ds_read_b64_tr_b16 v[72:73], v230 offset:24576\n" \
"v_mfma_f32_16x16x32_bf16 v[36:39], v[80:83], v[164:167], v[36:39]\n" \
"ds_read_b64_tr_b16 v[74:75], v230 offset:28672\n" \
"v_mfma_f32_16x16x32_bf16 v[40:43], v[84:87], v[160:163], v[40:43]\n" \
"ds_read_b64_tr_b16 v[76:77], v231 offset:24576\n" \
"v_mfma_f32_16x16x32_bf16 v[44:47], v[84:87], v[164:167], v[44:47]\n" \
"ds_read_b64_tr_b16 v[78:79], v231 offset:28672\n" \
"s_waitcnt lgkmcnt(8)\n" \
"v_mfma_f32_16x16x32_bf16 v[48:51], v[88:91], v[160:163], v[48:51]\n" \
"ds_read_b64_tr_b16 v[80:81], v232 offset:24576\n" \
"v_mfma_f32_16x16x32_bf16 v[52:55], v[88:91], v[164:167], v[52:55]\n" \
"ds_read_b64_tr_b16 v[82:83], v232 offset:28672\n" \
"v_mfma_f32_16x16x32_bf16 v[56:59], v[92:95], v[160:163], v[56:59]\n" \
"ds_read_b64_tr_b16 v[84:85], v233 offset:24576\n" \
"v_mfma_f32_16x16x32_bf16 v[60:63], v[92:95], v[164:167], v[60:63]\n" \
"ds_read_b64_tr_b16 v[86:87], v233 offset:28672\n" \
"s_waitcnt lgkmcnt(8)\n" \
"s_waitcnt vmcnt(16)\n" \
"v_mfma_f32_16x16x32_bf16 v[0:3], v[64:67], v[168:171], v[0:3]\n" \
"ds_read_b64_tr_b16 v[88:89], v234 offset:24576\n" \
"v_mfma_f32_16x16x32_bf16 v[4:7], v[64:67], v[172:175], v[4:7]\n" \
"ds_read_b64_tr_b16 v[90:91], v234 offset:28672\n" \
"v_mfma_f32_16x16x32_bf16 v[8:11], v[68:71], v[168:171], v[8:11]\n" \
"ds_read_b64_tr_b16 v[92:93], v235 offset:24576\n" \
"v_mfma_f32_16x16x32_bf16 v[12:15], v[68:71], v[172:175], v[12:15]\n" \
"ds_read_b64_tr_b16 v[94:95], v235 offset:28672\n" \
"s_waitcnt lgkmcnt(8)\n" \
"v_mfma_f32_16x16x32_bf16 v[16:19], v[72:75], v[168:171], v[16:19]\n" \
"s_sub_u32 s82, s56, s55\n" \
"v_add_u32 v228, s82, v228\n" \
"ds_read_b64_tr_b16 v[64:65], v228 offset:0\n" \
"v_mfma_f32_16x16x32_bf16 v[20:23], v[72:75], v[172:175], v[20:23]\n" \
"ds_read_b64_tr_b16 v[66:67], v228 offset:4096\n" \
"v_mfma_f32_16x16x32_bf16 v[24:27], v[76:79], v[168:171], v[24:27]\n" \
"v_add_u32 v229, s82, v229\n" \
"ds_read_b64_tr_b16 v[68:69], v229 offset:0\n" \
"v_mfma_f32_16x16x32_bf16 v[28:31], v[76:79], v[172:175], v[28:31]\n" \
"ds_read_b64_tr_b16 v[70:71], v229 offset:4096\n" \
"s_waitcnt lgkmcnt(8)\n" \
"v_mfma_f32_16x16x32_bf16 v[32:35], v[80:83], v[168:171], v[32:35]\n" \
"v_add_u32 v230, s82, v230\n" \
"ds_read_b64_tr_b16 v[72:73], v230 offset:0\n" \
"v_mfma_f32_16x16x32_bf16 v[36:39], v[80:83], v[172:175], v[36:39]\n" \
"ds_read_b64_tr_b16 v[74:75], v230 offset:4096\n" \
"v_mfma_f32_16x16x32_bf16 v[40:43], v[84:87], v[168:171], v[40:43]\n" \
"v_add_u32 v231, s82, v231\n" \
"ds_read_b64_tr_b16 v[76:77], v231 offset:0\n" \
"v_mfma_f32_16x16x32_bf16 v[44:47], v[84:87], v[172:175], v[44:47]\n" \
"ds_read_b64_tr_b16 v[78:79], v231 offset:4096\n" \
"s_waitcnt lgkmcnt(8)\n" \
"v_mfma_f32_16x16x32_bf16 v[48:51], v[88:91], v[168:171], v[48:51]\n" \
"v_add_u32 v232, s82, v232\n" \
"ds_read_b64_tr_b16 v[80:81], v232 offset:0\n" \
"v_mfma_f32_16x16x32_bf16 v[52:55], v[88:91], v[172:175], v[52:55]\n" \
"ds_read_b64_tr_b16 v[82:83], v232 offset:4096\n" \
"v_mfma_f32_16x16x32_bf16 v[56:59], v[92:95], v[168:171], v[56:59]\n" \
"v_add_u32 v233, s82, v233\n" \
"ds_read_b64_tr_b16 v[84:85], v233 offset:0\n" \
"v_mfma_f32_16x16x32_bf16 v[60:63], v[92:95], v[172:175], v[60:63]\n" \
"ds_read_b64_tr_b16 v[86:87], v233 offset:4096\n" \
"s_waitcnt lgkmcnt(8)\n" \
"s_waitcnt vmcnt(14)\n" \
"v_mfma_f32_16x16x32_bf16 v[0:3], v[64:67], v[176:179], v[0:3]\n" \
"v_add_u32 v234, s82, v234\n" \
"ds_read_b64_tr_b16 v[88:89], v234 offset:0\n" \
"v_mfma_f32_16x16x32_bf16 v[4:7], v[64:67], v[180:183], v[4:7]\n" \
"ds_read_b64_tr_b16 v[90:91], v234 offset:4096\n" \
"v_mfma_f32_16x16x32_bf16 v[8:11], v[68:71], v[176:179], v[8:11]\n" \
"v_add_u32 v235, s82, v235\n" \
"ds_read_b64_tr_b16 v[92:93], v235 offset:0\n" \
"v_mfma_f32_16x16x32_bf16 v[12:15], v[68:71], v[180:183], v[12:15]\n" \
"ds_read_b64_tr_b16 v[94:95], v235 offset:4096\n" \
"s_waitcnt lgkmcnt(8)\n" \
"v_mfma_f32_16x16x32_bf16 v[16:19], v[72:75], v[176:179], v[16:19]\n" \
"ds_read_b64_tr_b16 v[64:65], v228 offset:8192\n" \
"v_mfma_f32_16x16x32_bf16 v[20:23], v[72:75], v[180:183], v[20:23]\n" \
"ds_read_b64_tr_b16 v[66:67], v228 offset:12288\n" \
"v_mfma_f32_16x16x32_bf16 v[24:27], v[76:79], v[176:179], v[24:27]\n" \
"ds_read_b64_tr_b16 v[68:69], v229 offset:8192\n" \
"v_mfma_f32_16x16x32_bf16 v[28:31], v[76:79], v[180:183], v[28:31]\n" \
"ds_read_b64_tr_b16 v[70:71], v229 offset:12288\n" \
"s_waitcnt lgkmcnt(8)\n" \
"v_mfma_f32_16x16x32_bf16 v[32:35], v[80:83], v[176:179], v[32:35]\n" \
"ds_read_b64_tr_b16 v[72:73], v230 offset:8192\n" \
"v_mfma_f32_16x16x32_bf16 v[36:39], v[80:83], v[180:183], v[36:39]\n" \
"ds_read_b64_tr_b16 v[74:75], v230 offset:12288\n" \
"v_mfma_f32_16x16x32_bf16 v[40:43], v[84:87], v[176:179], v[40:43]\n" \
"ds_read_b64_tr_b16 v[76:77], v231 offset:8192\n" \
"v_mfma_f32_16x16x32_bf16 v[44:47], v[84:87], v[180:183], v[44:47]\n" \
"ds_read_b64_tr_b16 v[78:79], v231 offset:12288\n" \
"s_waitcnt lgkmcnt(8)\n" \
"v_mfma_f32_16x16x32_bf16 v[48:51], v[88:91], v[176:179], v[48:51]\n" \
"ds_read_b64_tr_b16 v[80:81], v232 offset:8192\n" \
"v_mfma_f32_16x16x32_bf16 v[52:55], v[88:91], v[180:183], v[52:55]\n" \
"ds_read_b64_tr_b16 v[82:83], v232 offset:12288\n" \
"v_mfma_f32_16x16x32_bf16 v[56:59], v[92:95], v[176:179], v[56:59]\n" \
"ds_read_b64_tr_b16 v[84:85], v233 offset:8192\n" \
"v_mfma_f32_16x16x32_bf16 v[60:63], v[92:95], v[180:183], v[60:63]\n" \
"ds_read_b64_tr_b16 v[86:87], v233 offset:12288\n" \
"s_waitcnt lgkmcnt(8)\n" \
"s_waitcnt vmcnt(0)\n" \
"s_waitcnt vmcnt(12)\n" \
"v_mfma_f32_16x16x32_bf16 v[0:3], v[64:67], v[184:187], v[0:3]\n" \
"ds_read_b64_tr_b16 v[88:89], v234 offset:8192\n" \
"v_cmp_gt_u32 vcc, 3, v226\n" \
"v_mfma_f32_16x16x32_bf16 v[4:7], v[64:67], v[188:191], v[4:7]\n" \
"ds_read_b64_tr_b16 v[90:91], v234 offset:12288\n" \
"v_cndmask_b32 v100, 0, v100, vcc\n" \
"v_mfma_f32_16x16x32_bf16 v[8:11], v[68:71], v[184:187], v[8:11]\n" \
"ds_read_b64_tr_b16 v[92:93], v235 offset:8192\n" \
"v_cndmask_b32 v101, 0, v101, vcc\n" \
"v_mfma_f32_16x16x32_bf16 v[12:15], v[68:71], v[188:191], v[12:15]\n" \
"ds_read_b64_tr_b16 v[94:95], v235 offset:12288\n" \
"s_waitcnt lgkmcnt(8)\n" \
"v_mfma_f32_16x16x32_bf16 v[16:19], v[72:75], v[184:187], v[16:19]\n" \
"ds_read_b64_tr_b16 v[64:65], v228 offset:16384\n" \
"v_cndmask_b32 v102, 0, v102, vcc\n" \
"v_mfma_f32_16x16x32_bf16 v[20:23], v[72:75], v[188:191], v[20:23]\n" \
"ds_read_b64_tr_b16 v[66:67], v228 offset:20480\n" \
"v_cndmask_b32 v103, 0, v103, vcc\n" \
"v_mfma_f32_16x16x32_bf16 v[24:27], v[76:79], v[184:187], v[24:27]\n" \
"ds_read_b64_tr_b16 v[68:69], v229 offset:16384\n" \
"v_add_f32 v120, v96, v98\n" \
"v_mfma_f32_16x16x32_bf16 v[28:31], v[76:79], v[188:191], v[28:31]\n" \
"ds_read_b64_tr_b16 v[70:71], v229 offset:20480\n" \
"s_waitcnt lgkmcnt(8)\n" \
"v_mfma_f32_16x16x32_bf16 v[32:35], v[80:83], v[184:187], v[32:35]\n" \
"ds_read_b64_tr_b16 v[72:73], v230 offset:16384\n" \
"v_add_f32 v120, v120, v100\n" \
"v_mfma_f32_16x16x32_bf16 v[36:39], v[80:83], v[188:191], v[36:39]\n" \
"ds_read_b64_tr_b16 v[74:75], v230 offset:20480\n" \
"v_add_f32 v120, v120, v102\n" \
"v_mfma_f32_16x16x32_bf16 v[40:43], v[84:87], v[184:187], v[40:43]\n" \
"ds_read_b64_tr_b16 v[76:77], v231 offset:16384\n" \
"v_mov_b32 v121, v120\n" \
"v_mfma_f32_16x16x32_bf16 v[44:47], v[84:87], v[188:191], v[44:47]\n" \
"ds_read_b64_tr_b16 v[78:79], v231 offset:20480\n" \
"s_waitcnt lgkmcnt(8)\n" \
"v_mfma_f32_16x16x32_bf16 v[48:51], v[88:91], v[184:187], v[48:51]\n" \
"ds_read_b64_tr_b16 v[80:81], v232 offset:16384\n" \
"v_permlane16_swap_b32 v121, v120\n" \
"v_mfma_f32_16x16x32_bf16 v[52:55], v[88:91], v[188:191], v[52:55]\n" \
"ds_read_b64_tr_b16 v[82:83], v232 offset:20480\n" \
"v_add_f32 v120, v120, v121\n" \
"v_mfma_f32_16x16x32_bf16 v[56:59], v[92:95], v[184:187], v[56:59]\n" \
"ds_read_b64_tr_b16 v[84:85], v233 offset:16384\n" \
"v_mov_b32 v121, v120\n" \
"v_mfma_f32_16x16x32_bf16 v[60:63], v[92:95], v[188:191], v[60:63]\n" \
"ds_read_b64_tr_b16 v[86:87], v233 offset:20480\n" \
"s_waitcnt lgkmcnt(8)\n" \
"v_mfma_f32_16x16x32_bf16 v[0:3], v[64:67], v[192:195], v[0:3]\n" \
"ds_read_b64_tr_b16 v[88:89], v234 offset:16384\n" \
"v_permlane32_swap_b32 v121, v120\n" \
"v_mfma_f32_16x16x32_bf16 v[4:7], v[64:67], v[196:199], v[4:7]\n" \
"ds_read_b64_tr_b16 v[90:91], v234 offset:20480\n" \
"v_add_f32 v120, v120, v121\n" \
"v_mfma_f32_16x16x32_bf16 v[8:11], v[68:71], v[192:195], v[8:11]\n" \
"ds_read_b64_tr_b16 v[92:93], v235 offset:16384\n" \
"v_mul_f32 v120, 0x3d924925, v120\n" \
"v_mfma_f32_16x16x32_bf16 v[12:15], v[68:71], v[196:199], v[12:15]\n" \
"ds_read_b64_tr_b16 v[94:95], v235 offset:20480\n" \
"s_waitcnt lgkmcnt(8)\n" \
"v_mfma_f32_16x16x32_bf16 v[16:19], v[72:75], v[192:195], v[16:19]\n" \
"ds_read_b64_tr_b16 v[64:65], v228 offset:24576\n" \
"v_sub_f32 v96, v96, v120\n" \
"v_mfma_f32_16x16x32_bf16 v[20:23], v[72:75], v[196:199], v[20:23]\n" \
"ds_read_b64_tr_b16 v[66:67], v228 offset:28672\n" \
"v_sub_f32 v98, v98, v120\n" \
"v_mfma_f32_16x16x32_bf16 v[24:27], v[76:79], v[192:195], v[24:27]\n" \
"ds_read_b64_tr_b16 v[68:69], v229 offset:24576\n" \
"v_sub_f32 v100, v100, v120\n" \
"v_mfma_f32_16x16x32_bf16 v[28:31], v[76:79], v[196:199], v[28:31]\n" \
"ds_read_b64_tr_b16 v[70:71], v229 offset:28672\n" \
"s_waitcnt lgkmcnt(8)\n" \
"v_mfma_f32_16x16x32_bf16 v[32:35], v[80:83], v[192:195], v[32:35]\n" \
"ds_read_b64_tr_b16 v[72:73], v230 offset:24576\n" \
"v_sub_f32 v102, v102, v120\n" \
"v_mfma_f32_16x16x32_bf16 v[36:39], v[80:83], v[196:199], v[36:39]\n" \
"ds_read_b64_tr_b16 v[74:75], v230 offset:28672\n" \
"v_cndmask_b32 v100, 0, v100, vcc\n" \
"v_mfma_f32_16x16x32_bf16 v[40:43], v[84:87], v[192:195], v[40:43]\n" \
"ds_read_b64_tr_b16 v[76:77], v231 offset:24576\n" \
"v_cndmask_b32 v102, 0, v102, vcc\n" \
"v_mfma_f32_16x16x32_bf16 v[44:47], v[84:87], v[196:199], v[44:47]\n" \
"ds_read_b64_tr_b16 v[78:79], v231 offset:28672\n" \
"s_waitcnt lgkmcnt(8)\n" \
"v_mfma_f32_16x16x32_bf16 v[48:51], v[88:91], v[192:195], v[48:51]\n" \
"ds_read_b64_tr_b16 v[80:81], v232 offset:24576\n" \
"v_mul_f32 v122, v96, v96\n" \
"v_mfma_f32_16x16x32_bf16 v[52:55], v[88:91], v[196:199], v[52:55]\n" \
"ds_read_b64_tr_b16 v[82:83], v232 offset:28672\n" \
"v_fmac_f32 v122, v98, v98\n" \
"v_mfma_f32_16x16x32_bf16 v[56:59], v[92:95], v[192:195], v[56:59]\n" \
"ds_read_b64_tr_b16 v[84:85], v233 offset:24576\n" \
"v_fmac_f32 v122, v100, v100\n" \
"v_mfma_f32_16x16x32_bf16 v[60:63], v[92:95], v[196:199], v[60:63]\n" \
"ds_read_b64_tr_b16 v[86:87], v233 offset:28672\n" \
"s_waitcnt lgkmcnt(8)\n" \
"v_mfma_f32_16x16x32_bf16 v[0:3], v[64:67], v[200:203], v[0:3]\n" \
"ds_read_b64_tr_b16 v[88:89], v234 offset:24576\n" \
"v_fmac_f32 v122, v102, v102\n" \
"v_mfma_f32_16x16x32_bf16 v[4:7], v[64:67], v[204:207], v[4:7]\n" \
"ds_read_b64_tr_b16 v[90:91], v234 offset:28672\n" \
"v_mul_f32 v122, 0x42000000, v122\n" \
"v_mfma_f32_16x16x32_bf16 v[8:11], v[68:71], v[200:203], v[8:11]\n" \
"ds_read_b64_tr_b16 v[92:93], v235 offset:24576\n" \
"v_add_f32 v122, v122, v97\n" \
"v_mfma_f32_16x16x32_bf16 v[12:15], v[68:71], v[204:207], v[12:15]\n" \
"ds_read_b64_tr_b16 v[94:95], v235 offset:28672\n" \
"s_waitcnt lgkmcnt(8)\n" \
"v_mfma_f32_16x16x32_bf16 v[16:19], v[72:75], v[200:203], v[16:19]\n" \
"s_sub_u32 s82, s57, s56\n" \
"v_add_u32 v228, s82, v228\n" \
"ds_read_b64_tr_b16 v[64:65], v228 offset:0\n" \
"v_mfma_f32_16x16x32_bf16 v[20:23], v[72:75], v[204:207], v[20:23]\n" \
"ds_read_b64_tr_b16 v[66:67], v228 offset:4096\n" \
"v_add_f32 v122, v122, v99\n" \
"v_mfma_f32_16x16x32_bf16 v[24:27], v[76:79], v[200:203], v[24:27]\n" \
"v_add_u32 v229, s82, v229\n" \
"ds_read_b64_tr_b16 v[68:69], v229 offset:0\n" \
"v_mfma_f32_16x16x32_bf16 v[28:31], v[76:79], v[204:207], v[28:31]\n" \
"ds_read_b64_tr_b16 v[70:71], v229 offset:4096\n" \
"s_waitcnt lgkmcnt(8)\n" \
"v_mfma_f32_16x16x32_bf16 v[32:35], v[80:83], v[200:203], v[32:35]\n" \
"v_add_u32 v230, s82, v230\n" \
"ds_read_b64_tr_b16 v[72:73], v230 offset:0\n" \
"v_mfma_f32_16x16x32_bf16 v[36:39], v[80:83], v[204:207], v[36:39]\n" \
"ds_read_b64_tr_b16 v[74:75], v230 offset:4096\n" \
"v_add_f32 v122, v122, v101\n" \
"v_mfma_f32_16x16x32_bf16 v[40:43], v[84:87], v[200:203], v[40:43]\n" \
"v_add_u32 v231, s82, v231\n" \
"ds_read_b64_tr_b16 v[76:77], v231 offset:0\n" \
"v_mfma_f32_16x16x32_bf16 v[44:47], v[84:87], v[204:207], v[44:47]\n" \
"ds_read_b64_tr_b16 v[78:79], v231 offset:4096\n" \
"s_waitcnt lgkmcnt(8)\n" \
"v_mfma_f32_16x16x32_bf16 v[48:51], v[88:91], v[200:203], v[48:51]\n" \
"v_add_u32 v232, s82, v232\n" \
"ds_read_b64_tr_b16 v[80:81], v232 offset:0\n" \
"v_mfma_f32_16x16x32_bf16 v[52:55], v[88:91], v[204:207], v[52:55]\n" \
"ds_read_b64_tr_b16 v[82:83], v232 offset:4096\n" \
"v_add_f32 v122, v122, v103\n" \
"v_mfma_f32_16x16x32_bf16 v[56:59], v[92:95], v[200:203], v[56:59]\n" \
"v_add_u32 v233, s82, v233\n" \
"ds_read_b64_tr_b16 v[84:85], v233 offset:0\n" \
"v_mfma_f32_16x16x32_bf16 v[60:63], v[92:95], v[204:207], v[60:63]\n" \
"ds_read_b64_tr_b16 v[86:87], v233 offset:4096\n" \
"s_waitcnt lgkmcnt(8)\n" \
"v_mfma_f32_16x16x32_bf16 v[0:3], v[64:67], v[208:211], v[0:3]\n" \
"v_add_u32 v234, s82, v234\n" \
"ds_read_b64_tr_b16 v[88:89], v234 offset:0\n" \
"v_mfma_f32_16x16x32_bf16 v[4:7], v[64:67], v[212:215], v[4:7]\n" \
"ds_read_b64_tr_b16 v[90:91], v234 offset:4096\n" \
"v_mov_b32 v121, v122\n" \
"v_mfma_f32_16x16x32_bf16 v[8:11], v[68:71], v[208:211], v[8:11]\n" \
"v_add_u32 v235, s82, v235\n" \
"ds_read_b64_tr_b16 v[92:93], v235 offset:0\n" \
"v_mfma_f32_16x16x32_bf16 v[12:15], v[68:71], v[212:215], v[12:15]\n" \
"ds_read_b64_tr_b16 v[94:95], v235 offset:4096\n" \
"s_waitcnt lgkmcnt(8)\n" \
"v_mfma_f32_16x16x32_bf16 v[16:19], v[72:75], v[208:211], v[16:19]\n" \
"ds_read_b64_tr_b16 v[64:65], v228 offset:8192\n" \
"v_permlane16_swap_b32 v121, v122\n" \
"v_mfma_f32_16x16x32_bf16 v[20:23], v[72:75], v[212:215], v[20:23]\n" \
"ds_read_b64_tr_b16 v[66:67], v228 offset:12288\n" \
"v_add_f32 v122, v122, v121\n" \
"v_mfma_f32_16x16x32_bf16 v[24:27], v[76:79], v[208:211], v[24:27]\n" \
"ds_read_b64_tr_b16 v[68:69], v229 offset:8192\n" \
"v_mov_b32 v121, v122\n" \
"v_mfma_f32_16x16x32_bf16 v[28:31], v[76:79], v[212:215], v[28:31]\n" \
"ds_read_b64_tr_b16 v[70:71], v229 offset:12288\n" \
"s_waitcnt lgkmcnt(8)\n" \
"v_mfma_f32_16x16x32_bf16 v[32:35], v[80:83], v[208:211], v[32:35]\n" \
"ds_read_b64_tr_b16 v[72:73], v230 offset:8192\n" \
"v_permlane32_swap_b32 v121, v122\n" \
"v_mfma_f32_16x16x32_bf16 v[36:39], v[80:83], v[212:215], v[36:39]\n" \
"ds_read_b64_tr_b16 v[74:75], v230 offset:12288\n" \
"v_add_f32 v122, v122, v121\n" \
"v_mfma_f32_16x16x32_bf16 v[40:43], v[84:87], v[208:211], v[40:43]\n" \
"ds_read_b64_tr_b16 v[76:77], v231 offset:8192\n" \
"v_mov_b32 v112, 0x3727c5ac\n" \
"v_mfma_f32_16x16x32_bf16 v[44:47], v[84:87], v[212:215], v[44:47]\n" \
"ds_read_b64_tr_b16 v[78:79], v231 offset:12288\n" \
"s_waitcnt lgkmcnt(8)\n" \
"v_mfma_f32_16x16x32_bf16 v[48:51], v[88:91], v[208:211], v[48:51]\n" \
"ds_read_b64_tr_b16 v[80:81], v232 offset:8192\n" \
"v_fmac_f32 v112, 0x3b124925, v122\n" \
"v_mfma_f32_16x16x32_bf16 v[52:55], v[88:91], v[212:215], v[52:55]\n" \
"ds_read_b64_tr_b16 v[82:83], v232 offset:12288\n" \
"v_rsq_f32 v112, v112\n" \
"v_mfma_f32_16x16x32_bf16 v[56:59], v[92:95], v[208:211], v[56:59]\n" \
"ds_read_b64_tr_b16 v[84:85], v233 offset:8192\n" \
"v_mov_b32 v113, v112\n" \
"v_mfma_f32_16x16x32_bf16 v[60:63], v[92:95], v[212:215], v[60:63]\n" \
"ds_read_b64_tr_b16 v[86:87], v233 offset:12288\n" \
"s_waitcnt lgkmcnt(8)\n" \
"v_mfma_f32_16x16x32_bf16 v[0:3], v[64:67], v[216:219], v[0:3]\n" \
"ds_read_b64_tr_b16 v[88:89], v234 offset:8192\n" \
"v_mul_f32 v114, -v120, v112\n" \
"v_mfma_f32_16x16x32_bf16 v[4:7], v[64:67], v[220:223], v[4:7]\n" \
"ds_read_b64_tr_b16 v[90:91], v234 offset:12288\n" \
"v_mov_b32 v115, v114\n" \
"v_mfma_f32_16x16x32_bf16 v[8:11], v[68:71], v[216:219], v[8:11]\n" \
"ds_read_b64_tr_b16 v[92:93], v235 offset:8192\n" \
"v_cndmask_b32 v108, 0, v108, vcc\n" \
"v_mfma_f32_16x16x32_bf16 v[12:15], v[68:71], v[220:223], v[12:15]\n" \
"ds_read_b64_tr_b16 v[94:95], v235 offset:12288\n" \
"s_waitcnt lgkmcnt(8)\n" \
"v_mfma_f32_16x16x32_bf16 v[16:19], v[72:75], v[216:219], v[16:19]\n" \
"v_cndmask_b32 v109, 0, v109, vcc\n" \
"v_cndmask_b32 v110, 0, v110, vcc\n" \
"v_mfma_f32_16x16x32_bf16 v[20:23], v[72:75], v[220:223], v[20:23]\n" \
"v_cndmask_b32 v111, 0, v111, vcc\n" \
"v_add_f32 v120, v104, v106\n" \
"v_mfma_f32_16x16x32_bf16 v[24:27], v[76:79], v[216:219], v[24:27]\n" \
"v_add_f32 v120, v120, v108\n" \
"v_add_f32 v120, v120, v110\n" \
"v_mfma_f32_16x16x32_bf16 v[28:31], v[76:79], v[220:223], v[28:31]\n" \
"s_waitcnt lgkmcnt(4)\n" \
"v_mov_b32 v121, v120\n" \
"v_mfma_f32_16x16x32_bf16 v[32:35], v[80:83], v[216:219], v[32:35]\n" \
"v_mfma_f32_16x16x32_bf16 v[36:39], v[80:83], v[220:223], v[36:39]\n" \
"v_permlane16_swap_b32 v121, v120\n" \
"v_add_f32 v120, v120, v121\n" \
"v_mfma_f32_16x16x32_bf16 v[40:43], v[84:87], v[216:219], v[40:43]\n" \
"v_mov_b32 v121, v120\n" \
"v_mfma_f32_16x16x32_bf16 v[44:47], v[84:87], v[220:223], v[44:47]\n" \
"s_waitcnt lgkmcnt(0)\n" \
"v_permlane32_swap_b32 v121, v120\n" \
"v_mfma_f32_16x16x32_bf16 v[48:51], v[88:91], v[216:219], v[48:51]\n" \
"v_add_f32 v120, v120, v121\n" \
"v_mul_f32 v120, 0x3d924925, v120\n" \
"v_mfma_f32_16x16x32_bf16 v[52:55], v[88:91], v[220:223], v[52:55]\n" \
"v_sub_f32 v104, v104, v120\n" \
"v_sub_f32 v106, v106, v120\n" \
"v_mfma_f32_16x16x32_bf16 v[56:59], v[92:95], v[216:219], v[56:59]\n" \
"v_sub_f32 v108, v108, v120\n" \
"v_sub_f32 v110, v110, v120\n" \
"v_mfma_f32_16x16x32_bf16 v[60:63], v[92:95], v[220:223], v[60:63]\n" \
"v_cndmask_b32 v108, 0, v108, vcc\n" \
"v_cndmask_b32 v110, 0, v110, vcc\n" \
"v_mul_f32 v122, v104, v104\n" \
"v_fmac_f32 v122, v106, v106\n" \
"v_fmac_f32 v122, v108, v108\n" \
"v_fmac_f32 v122, v110, v110\n" \
"v_mul_f32 v122, 0x42000000, v122\n" \
"v_add_f32 v122, v122, v105\n" \
"v_add_f32 v122, v122, v107\n" \
"v_add_f32 v122, v122, v109\n" \
"v_add_f32 v122, v122, v111\n" \
"v_mov_b32 v121, v122\n" \
"s_nop 1\n" \
"v_permlane16_swap_b32 v121, v122\n" \
"v_add_f32 v122, v122, v121\n" \
"v_mov_b32 v121, v122\n" \
"s_nop 1\n" \
"v_permlane32_swap_b32 v121, v122\n" \
"v_add_f32 v122, v122, v121\n" \
"v_mov_b32 v116, 0x3727c5ac\n" \
"v_fmac_f32 v116, 0x3b124925, v122\n" \
"v_rsq_f32 v116, v116\n" \
"s_nop 0\n" \
"v_mov_b32 v117, v116\n" \
"v_mul_f32 v118, -v120, v116\n" \
"v_mov_b32 v119, v118\n" \
"v_mov_b32 v124, 0xbfb8aa3b\n" \
"v_mov_b32 v125, 0xbfb8aa3b\n" \
"v_mov_b32 v126, 1.0\n" \
"v_mov_b32 v127, 1.0\n" \
"s_waitcnt vmcnt(0) lgkmcnt(0)\n" \
"s_barrier\n" \
"v_lshlrev_b32 v236, 4, v226\n" \
"v_add_u32 v236, 0x1c000, v236\n" \
"ds_read_b128 v[64:67], v236 offset:0\n" \
"ds_read_b128 v[68:71], v236 offset:64\n" \
"ds_read_b128 v[72:75], v236 offset:128\n" \
"ds_read_b128 v[76:79], v236 offset:192\n" \
"ds_read_b128 v[80:83], v236 offset:256\n" \
"ds_read_b128 v[84:87], v236 offset:320\n" \
"ds_read_b128 v[88:91], v236 offset:384\n" \
"ds_read_b128 v[92:95], v236 offset:448\n" \
"s_waitcnt lgkmcnt(0)\n" \
"v_pk_mul_f32 v[128:129], v[64:65], v[114:115]\n" \
"v_pk_mul_f32 v[130:131], v[66:67], v[114:115]\n" \
"v_pk_mul_f32 v[132:133], v[68:69], v[114:115]\n" \
"v_pk_mul_f32 v[134:135], v[70:71], v[114:115]\n" \
"v_pk_fma_f32 v[128:129], v[0:1], v[112:113], v[128:129]\n" \
"v_pk_fma_f32 v[130:131], v[2:3], v[112:113], v[130:131]\n" \
"v_pk_fma_f32 v[132:133], v[8:9], v[112:113], v[132:133]\n" \
"v_pk_fma_f32 v[134:135], v[10:11], v[112:113], v[134:135]\n" \
"v_pk_mul_f32 v[136:137], v[128:129], v[124:125]\n" \
"v_pk_mul_f32 v[138:139], v[130:131], v[124:125]\n" \
"v_pk_mul_f32 v[140:141], v[132:133], v[124:125]\n" \
"v_pk_mul_f32 v[142:143], v[134:135], v[124:125]\n" \
"v_exp_f32 v136, v136\n" \
"v_exp_f32 v137, v137\n" \
"v_exp_f32 v138, v138\n" \
"v_exp_f32 v139, v139\n" \
"v_exp_f32 v140, v140\n" \
"v_exp_f32 v141, v141\n" \
"v_exp_f32 v142, v142\n" \
"v_exp_f32 v143, v143\n" \
"s_add_u32 s80, s54, s78\n" \
"s_add_u32 m0, s80, 0\n" \
"s_add_u32 s94, s79, 114688\n" \
"buffer_load_dwordx4 v242, s[32:35], s94 offen offset:0 lds\n" \
"v_pk_add_f32 v[136:137], v[136:137], v[126:127]\n" \
"v_pk_add_f32 v[138:139], v[138:139], v[126:127]\n" \
"v_pk_add_f32 v[140:141], v[140:141], v[126:127]\n" \
"v_pk_add_f32 v[142:143], v[142:143], v[126:127]\n" \
"v_rcp_f32 v136, v136\n" \
"v_rcp_f32 v137, v137\n" \
"v_rcp_f32 v138, v138\n" \
"v_rcp_f32 v139, v139\n" \
"v_rcp_f32 v140, v140\n" \
"v_rcp_f32 v141, v141\n" \
"v_rcp_f32 v142, v142\n" \
"v_rcp_f32 v143, v143\n" \
"s_add_u32 s94, s79, 120832\n" \
"buffer_load_dwordx4 v242, s[32:35], s94 offen offset:1024 lds\n" \
"v_pk_mul_f32 v[128:129], v[128:129], v[136:137]\n" \
"v_pk_mul_f32 v[130:131], v[130:131], v[138:139]\n" \
"v_pk_mul_f32 v[132:133], v[132:133], v[140:141]\n" \
"v_pk_mul_f32 v[134:135], v[134:135], v[142:143]\n" \
"v_cvt_pk_bf16_f32 v160, v128, v129\n" \
"v_cvt_pk_bf16_f32 v161, v130, v131\n" \
"v_cvt_pk_bf16_f32 v162, v132, v133\n" \
"v_cvt_pk_bf16_f32 v163, v134, v135\n" \
"s_add_u32 s83, s73, 0\n" \
"buffer_store_dwordx4 v[160:163], v224, %[rs], s83 offen offset:0 sc1\n" \
"v_pk_mul_f32 v[144:145], v[72:73], v[114:115]\n" \
"v_pk_mul_f32 v[146:147], v[74:75], v[114:115]\n" \
"v_pk_mul_f32 v[148:149], v[76:77], v[114:115]\n" \
"v_pk_mul_f32 v[150:151], v[78:79], v[114:115]\n" \
"v_pk_fma_f32 v[144:145], v[16:17], v[112:113], v[144:145]\n" \
"v_pk_fma_f32 v[146:147], v[18:19], v[112:113], v[146:147]\n" \
"v_pk_fma_f32 v[148:149], v[24:25], v[112:113], v[148:149]\n" \
"v_pk_fma_f32 v[150:151], v[26:27], v[112:113], v[150:151]\n" \
"v_pk_mul_f32 v[152:153], v[144:145], v[124:125]\n" \
"v_pk_mul_f32 v[154:155], v[146:147], v[124:125]\n" \
"v_pk_mul_f32 v[156:157], v[148:149], v[124:125]\n" \
"v_pk_mul_f32 v[158:159], v[150:151], v[124:125]\n" \
"v_exp_f32 v152, v152\n" \
"v_exp_f32 v153, v153\n" \
"v_exp_f32 v154, v154\n" \
"v_exp_f32 v155, v155\n" \
"v_exp_f32 v156, v156\n" \
"v_exp_f32 v157, v157\n" \
"v_exp_f32 v158, v158\n" \
"v_exp_f32 v159, v159\n" \
"s_add_u32 s94, s79, 126976\n" \
"buffer_load_dwordx4 v242, s[32:35], s94 offen offset:2048 lds\n" \
"v_pk_add_f32 v[152:153], v[152:153], v[126:127]\n" \
"v_pk_add_f32 v[154:155], v[154:155], v[126:127]\n" \
"v_pk_add_f32 v[156:157], v[156:157], v[126:127]\n" \
"v_pk_add_f32 v[158:159], v[158:159], v[126:127]\n" \
"v_rcp_f32 v152, v152\n" \
"v_rcp_f32 v153, v153\n" \
"v_rcp_f32 v154, v154\n" \
"v_rcp_f32 v155, v155\n" \
"v_rcp_f32 v156, v156\n" \
"v_rcp_f32 v157, v157\n" \
"v_rcp_f32 v158, v158\n" \
"v_rcp_f32 v159, v159\n" \
"s_add_u32 s94, s79, 133120\n" \
"buffer_load_dwordx4 v242, s[32:35], s94 offen offset:3072 lds\n" \
"v_pk_mul_f32 v[144:145], v[144:145], v[152:153]\n" \
"v_pk_mul_f32 v[146:147], v[146:147], v[154:155]\n" \
"v_pk_mul_f32 v[148:149], v[148:149], v[156:157]\n" \
"v_pk_mul_f32 v[150:151], v[150:151], v[158:159]\n" \
"v_cvt_pk_bf16_f32 v164, v144, v145\n" \
"v_cvt_pk_bf16_f32 v165, v146, v147\n" \
"v_cvt_pk_bf16_f32 v166, v148, v149\n" \
"v_cvt_pk_bf16_f32 v167, v150, v151\n" \
"buffer_store_dwordx4 v[164:167], v224, %[rs], s83 offen offset:1024 sc1\n" \
"v_pk_mul_f32 v[128:129], v[80:81], v[114:115]\n" \
"v_pk_mul_f32 v[130:131], v[82:83], v[114:115]\n" \
"v_pk_mul_f32 v[132:133], v[84:85], v[114:115]\n" \
"v_pk_mul_f32 v[134:135], v[86:87], v[114:115]\n" \
"v_pk_fma_f32 v[128:129], v[32:33], v[112:113], v[128:129]\n" \
"v_pk_fma_f32 v[130:131], v[34:35], v[112:113], v[130:131]\n" \
"v_pk_fma_f32 v[132:133], v[40:41], v[112:113], v[132:133]\n" \
"v_pk_fma_f32 v[134:135], v[42:43], v[112:113], v[134:135]\n" \
"v_pk_mul_f32 v[136:137], v[128:129], v[124:125]\n" \
"v_pk_mul_f32 v[138:139], v[130:131], v[124:125]\n" \
"v_pk_mul_f32 v[140:141], v[132:133], v[124:125]\n" \
"v_pk_mul_f32 v[142:143], v[134:135], v[124:125]\n" \
"v_exp_f32 v136, v136\n" \
"v_exp_f32 v137, v137\n" \
"v_exp_f32 v138, v138\n" \
"v_exp_f32 v139, v139\n" \
"v_exp_f32 v140, v140\n" \
"v_exp_f32 v141, v141\n" \
"v_exp_f32 v142, v142\n" \
"v_exp_f32 v143, v143\n" \
"s_add_u32 s80, s54, s78\n" \
"s_add_u32 m0, s80, 4096\n" \
"s_add_u32 s94, s79, 143360\n" \
"buffer_load_dwordx4 v242, s[32:35], s94 offen offset:0 lds\n" \
"v_pk_add_f32 v[136:137], v[136:137], v[126:127]\n" \
"v_pk_add_f32 v[138:139], v[138:139], v[126:127]\n" \
"v_pk_add_f32 v[140:141], v[140:141], v[126:127]\n" \
"v_pk_add_f32 v[142:143], v[142:143], v[126:127]\n" \
"v_rcp_f32 v136, v136\n" \
"v_rcp_f32 v137, v137\n" \
"v_rcp_f32 v138, v138\n" \
"v_rcp_f32 v139, v139\n" \
"v_rcp_f32 v140, v140\n" \
"v_rcp_f32 v141, v141\n" \
"v_rcp_f32 v142, v142\n" \
"v_rcp_f32 v143, v143\n" \
"s_add_u32 s94, s79, 149504\n" \
"buffer_load_dwordx4 v242, s[32:35], s94 offen offset:1024 lds\n" \
"v_pk_mul_f32 v[128:129], v[128:129], v[136:137]\n" \
"v_pk_mul_f32 v[130:131], v[130:131], v[138:139]\n" \
"v_pk_mul_f32 v[132:133], v[132:133], v[140:141]\n" \
"v_pk_mul_f32 v[134:135], v[134:135], v[142:143]\n" \
"v_cvt_pk_bf16_f32 v160, v128, v129\n" \
"v_cvt_pk_bf16_f32 v161, v130, v131\n" \
"v_cvt_pk_bf16_f32 v162, v132, v133\n" \
"v_cvt_pk_bf16_f32 v163, v134, v135\n" \
"buffer_store_dwordx4 v[160:163], v224, %[rs], s83 offen offset:2048 sc1\n" \
"v_pk_mul_f32 v[144:145], v[88:89], v[114:115]\n" \
"v_pk_mul_f32 v[146:147], v[90:91], v[114:115]\n" \
"v_pk_mul_f32 v[148:149], v[92:93], v[114:115]\n" \
"v_pk_mul_f32 v[150:151], v[94:95], v[114:115]\n" \
"v_pk_fma_f32 v[144:145], v[48:49], v[112:113], v[144:145]\n" \
"v_pk_fma_f32 v[146:147], v[50:51], v[112:113], v[146:147]\n" \
"v_pk_fma_f32 v[148:149], v[56:57], v[112:113], v[148:149]\n" \
"v_pk_fma_f32 v[150:151], v[58:59], v[112:113], v[150:151]\n" \
"v_pk_mul_f32 v[152:153], v[144:145], v[124:125]\n" \
"v_pk_mul_f32 v[154:155], v[146:147], v[124:125]\n" \
"v_pk_mul_f32 v[156:157], v[148:149], v[124:125]\n" \
"v_pk_mul_f32 v[158:159], v[150:151], v[124:125]\n" \
"v_exp_f32 v152, v152\n" \
"v_exp_f32 v153, v153\n" \
"v_exp_f32 v154, v154\n" \
"v_exp_f32 v155, v155\n" \
"v_exp_f32 v156, v156\n" \
"v_exp_f32 v157, v157\n" \
"v_exp_f32 v158, v158\n" \
"v_exp_f32 v159, v159\n" \
"s_add_u32 s94, s79, 155648\n" \
"buffer_load_dwordx4 v242, s[32:35], s94 offen offset:2048 lds\n" \
"v_pk_add_f32 v[152:153], v[152:153], v[126:127]\n" \
"v_pk_add_f32 v[154:155], v[154:155], v[126:127]\n" \
"v_pk_add_f32 v[156:157], v[156:157], v[126:127]\n" \
"v_pk_add_f32 v[158:159], v[158:159], v[126:127]\n" \
"v_rcp_f32 v152, v152\n" \
"v_rcp_f32 v153, v153\n" \
"v_rcp_f32 v154, v154\n" \
"v_rcp_f32 v155, v155\n" \
"v_rcp_f32 v156, v156\n" \
"v_rcp_f32 v157, v157\n" \
"v_rcp_f32 v158, v158\n" \
"v_rcp_f32 v159, v159\n" \
"s_add_u32 s94, s79, 161792\n" \
"buffer_load_dwordx4 v242, s[32:35], s94 offen offset:3072 lds\n" \
"v_pk_mul_f32 v[144:145], v[144:145], v[152:153]\n" \
"v_pk_mul_f32 v[146:147], v[146:147], v[154:155]\n" \
"v_pk_mul_f32 v[148:149], v[148:149], v[156:157]\n" \
"v_pk_mul_f32 v[150:151], v[150:151], v[158:159]\n" \
"v_cvt_pk_bf16_f32 v164, v144, v145\n" \
"v_cvt_pk_bf16_f32 v165, v146, v147\n" \
"v_cvt_pk_bf16_f32 v166, v148, v149\n" \
"v_cvt_pk_bf16_f32 v167, v150, v151\n" \
"buffer_store_dwordx4 v[164:167], v224, %[rs], s83 offen offset:3072 sc1\n" \
"v_pk_mul_f32 v[128:129], v[64:65], v[118:119]\n" \
"v_pk_mul_f32 v[130:131], v[66:67], v[118:119]\n" \
"v_pk_mul_f32 v[132:133], v[68:69], v[118:119]\n" \
"v_pk_mul_f32 v[134:135], v[70:71], v[118:119]\n" \
"v_pk_fma_f32 v[128:129], v[4:5], v[116:117], v[128:129]\n" \
"v_pk_fma_f32 v[130:131], v[6:7], v[116:117], v[130:131]\n" \
"v_pk_fma_f32 v[132:133], v[12:13], v[116:117], v[132:133]\n" \
"v_pk_fma_f32 v[134:135], v[14:15], v[116:117], v[134:135]\n" \
"v_pk_mul_f32 v[136:137], v[128:129], v[124:125]\n" \
"v_pk_mul_f32 v[138:139], v[130:131], v[124:125]\n" \
"v_pk_mul_f32 v[140:141], v[132:133], v[124:125]\n" \
"v_pk_mul_f32 v[142:143], v[134:135], v[124:125]\n" \
"v_exp_f32 v136, v136\n" \
"v_exp_f32 v137, v137\n" \
"v_exp_f32 v138, v138\n" \
"v_exp_f32 v139, v139\n" \
"v_exp_f32 v140, v140\n" \
"v_exp_f32 v141, v141\n" \
"v_exp_f32 v142, v142\n" \
"v_exp_f32 v143, v143\n" \
"s_add_u32 s80, s55, s78\n" \
"s_add_u32 m0, s80, 0\n" \
"s_add_u32 s94, s79, 229376\n" \
"buffer_load_dwordx4 v242, s[32:35], s94 offen offset:0 lds\n" \
"v_pk_add_f32 v[136:137], v[136:137], v[126:127]\n" \
"v_pk_add_f32 v[138:139], v[138:139], v[126:127]\n" \
"v_pk_add_f32 v[140:141], v[140:141], v[126:127]\n" \
"v_pk_add_f32 v[142:143], v[142:143], v[126:127]\n" \
"v_rcp_f32 v136, v136\n" \
"v_rcp_f32 v137, v137\n" \
"v_rcp_f32 v138, v138\n" \
"v_rcp_f32 v139, v139\n" \
"v_rcp_f32 v140, v140\n" \
"v_rcp_f32 v141, v141\n" \
"v_rcp_f32 v142, v142\n" \
"v_rcp_f32 v143, v143\n" \
"s_add_u32 s94, s79, 235520\n" \
"buffer_load_dwordx4 v242, s[32:35], s94 offen offset:1024 lds\n" \
"v_pk_mul_f32 v[128:129], v[128:129], v[136:137]\n" \
"v_pk_mul_f32 v[130:131], v[130:131], v[138:139]\n" \
"v_pk_mul_f32 v[132:133], v[132:133], v[140:141]\n" \
"v_pk_mul_f32 v[134:135], v[134:135], v[142:143]\n" \
"v_cvt_pk_bf16_f32 v160, v128, v129\n" \
"v_cvt_pk_bf16_f32 v161, v130, v131\n" \
"v_cvt_pk_bf16_f32 v162, v132, v133\n" \
"v_cvt_pk_bf16_f32 v163, v134, v135\n" \
"s_add_u32 s83, s73, 57344\n" \
"buffer_store_dwordx4 v[160:163], v224, %[rs], s83 offen offset:0 sc1\n" \
"v_pk_mul_f32 v[144:145], v[72:73], v[118:119]\n" \
"v_pk_mul_f32 v[146:147], v[74:75], v[118:119]\n" \
"v_pk_mul_f32 v[148:149], v[76:77], v[118:119]\n" \
"v_pk_mul_f32 v[150:151], v[78:79], v[118:119]\n" \
"v_pk_fma_f32 v[144:145], v[20:21], v[116:117], v[144:145]\n" \
"v_pk_fma_f32 v[146:147], v[22:23], v[116:117], v[146:147]\n" \
"v_pk_fma_f32 v[148:149], v[28:29], v[116:117], v[148:149]\n" \
"v_pk_fma_f32 v[150:151], v[30:31], v[116:117], v[150:151]\n" \
"v_pk_mul_f32 v[152:153], v[144:145], v[124:125]\n" \
"v_pk_mul_f32 v[154:155], v[146:147], v[124:125]\n" \
"v_pk_mul_f32 v[156:157], v[148:149], v[124:125]\n" \
"v_pk_mul_f32 v[158:159], v[150:151], v[124:125]\n" \
"v_exp_f32 v152, v152\n" \
"v_exp_f32 v153, v153\n" \
"v_exp_f32 v154, v154\n" \
"v_exp_f32 v155, v155\n" \
"v_exp_f32 v156, v156\n" \
"v_exp_f32 v157, v157\n" \
"v_exp_f32 v158, v158\n" \
"v_exp_f32 v159, v159\n" \
"s_add_u32 s94, s79, 241664\n" \
"buffer_load_dwordx4 v242, s[32:35], s94 offen offset:2048 lds\n" \
"v_pk_add_f32 v[152:153], v[152:153], v[126:127]\n" \
"v_pk_add_f32 v[154:155], v[154:155], v[126:127]\n" \
"v_pk_add_f32 v[156:157], v[156:157], v[126:127]\n" \
"v_pk_add_f32 v[158:159], v[158:159], v[126:127]\n" \
"v_rcp_f32 v152, v152\n" \
"v_rcp_f32 v153, v153\n" \
"v_rcp_f32 v154, v154\n" \
"v_rcp_f32 v155, v155\n" \
"v_rcp_f32 v156, v156\n" \
"v_rcp_f32 v157, v157\n" \
"v_rcp_f32 v158, v158\n" \
"v_rcp_f32 v159, v159\n" \
"s_add_u32 s94, s79, 247808\n" \
"buffer_load_dwordx4 v242, s[32:35], s94 offen offset:3072 lds\n" \
"v_pk_mul_f32 v[144:145], v[144:145], v[152:153]\n" \
"v_pk_mul_f32 v[146:147], v[146:147], v[154:155]\n" \
"v_pk_mul_f32 v[148:149], v[148:149], v[156:157]\n" \
"v_pk_mul_f32 v[150:151], v[150:151], v[158:159]\n" \
"v_cvt_pk_bf16_f32 v164, v144, v145\n" \
"v_cvt_pk_bf16_f32 v165, v146, v147\n" \
"v_cvt_pk_bf16_f32 v166, v148, v149\n" \
"v_cvt_pk_bf16_f32 v167, v150, v151\n" \
"buffer_store_dwordx4 v[164:167], v224, %[rs], s83 offen offset:1024 sc1\n" \
"v_pk_mul_f32 v[128:129], v[80:81], v[118:119]\n" \
"v_pk_mul_f32 v[130:131], v[82:83], v[118:119]\n" \
"v_pk_mul_f32 v[132:133], v[84:85], v[118:119]\n" \
"v_pk_mul_f32 v[134:135], v[86:87], v[118:119]\n" \
"v_pk_fma_f32 v[128:129], v[36:37], v[116:117], v[128:129]\n" \
"v_pk_fma_f32 v[130:131], v[38:39], v[116:117], v[130:131]\n" \
"v_pk_fma_f32 v[132:133], v[44:45], v[116:117], v[132:133]\n" \
"v_pk_fma_f32 v[134:135], v[46:47], v[116:117], v[134:135]\n" \
"v_pk_mul_f32 v[136:137], v[128:129], v[124:125]\n" \
"v_pk_mul_f32 v[138:139], v[130:131], v[124:125]\n" \
"v_pk_mul_f32 v[140:141], v[132:133], v[124:125]\n" \
"v_pk_mul_f32 v[142:143], v[134:135], v[124:125]\n" \
"v_exp_f32 v136, v136\n" \
"v_exp_f32 v137, v137\n" \
"v_exp_f32 v138, v138\n" \
"v_exp_f32 v139, v139\n" \
"v_exp_f32 v140, v140\n" \
"v_exp_f32 v141, v141\n" \
"v_exp_f32 v142, v142\n" \
"v_exp_f32 v143, v143\n" \
"s_add_u32 s80, s55, s78\n" \
"s_add_u32 m0, s80, 4096\n" \
"s_add_u32 s94, s79, 258048\n" \
"buffer_load_dwordx4 v242, s[32:35], s94 offen offset:0 lds\n" \
"v_pk_add_f32 v[136:137], v[136:137], v[126:127]\n" \
"v_pk_add_f32 v[138:139], v[138:139], v[126:127]\n" \
"v_pk_add_f32 v[140:141], v[140:141], v[126:127]\n" \
"v_pk_add_f32 v[142:143], v[142:143], v[126:127]\n" \
"v_rcp_f32 v136, v136\n" \
"v_rcp_f32 v137, v137\n" \
"v_rcp_f32 v138, v138\n" \
"v_rcp_f32 v139, v139\n" \
"v_rcp_f32 v140, v140\n" \
"v_rcp_f32 v141, v141\n" \
"v_rcp_f32 v142, v142\n" \
"v_rcp_f32 v143, v143\n" \
"s_add_u32 s94, s79, 264192\n" \
"buffer_load_dwordx4 v242, s[32:35], s94 offen offset:1024 lds\n" \
"v_pk_mul_f32 v[128:129], v[128:129], v[136:137]\n" \
"v_pk_mul_f32 v[130:131], v[130:131], v[138:139]\n" \
"v_pk_mul_f32 v[132:133], v[132:133], v[140:141]\n" \
"v_pk_mul_f32 v[134:135], v[134:135], v[142:143]\n" \
"v_cvt_pk_bf16_f32 v160, v128, v129\n" \
"v_cvt_pk_bf16_f32 v161, v130, v131\n" \
"v_cvt_pk_bf16_f32 v162, v132, v133\n" \
"v_cvt_pk_bf16_f32 v163, v134, v135\n" \
"buffer_store_dwordx4 v[160:163], v224, %[rs], s83 offen offset:2048 sc1\n" \
"v_pk_mul_f32 v[144:145], v[88:89], v[118:119]\n" \
"v_pk_mul_f32 v[146:147], v[90:91], v[118:119]\n" \
"v_pk_mul_f32 v[148:149], v[92:93], v[118:119]\n" \
"v_pk_mul_f32 v[150:151], v[94:95], v[118:119]\n" \
"v_pk_fma_f32 v[144:145], v[52:53], v[116:117], v[144:145]\n" \
"v_pk_fma_f32 v[146:147], v[54:55], v[116:117], v[146:147]\n" \
"v_pk_fma_f32 v[148:149], v[60:61], v[116:117], v[148:149]\n" \
"v_pk_fma_f32 v[150:151], v[62:63], v[116:117], v[150:151]\n" \
"v_pk_mul_f32 v[152:153], v[144:145], v[124:125]\n" \
"v_pk_mul_f32 v[154:155], v[146:147], v[124:125]\n" \
"v_pk_mul_f32 v[156:157], v[148:149], v[124:125]\n" \
"v_pk_mul_f32 v[158:159], v[150:151], v[124:125]\n" \
"v_exp_f32 v152, v152\n" \
"v_exp_f32 v153, v153\n" \
"v_exp_f32 v154, v154\n" \
"v_exp_f32 v155, v155\n" \
"v_exp_f32 v156, v156\n" \
"v_exp_f32 v157, v157\n" \
"v_exp_f32 v158, v158\n" \
"v_exp_f32 v159, v159\n" \
"s_add_u32 s94, s79, 270336\n" \
"buffer_load_dwordx4 v242, s[32:35], s94 offen offset:2048 lds\n" \
"v_pk_add_f32 v[152:153], v[152:153], v[126:127]\n" \
"v_pk_add_f32 v[154:155], v[154:155], v[126:127]\n" \
"v_pk_add_f32 v[156:157], v[156:157], v[126:127]\n" \
"v_pk_add_f32 v[158:159], v[158:159], v[126:127]\n" \
"v_rcp_f32 v152, v152\n" \
"v_rcp_f32 v153, v153\n" \
"v_rcp_f32 v154, v154\n" \
"v_rcp_f32 v155, v155\n" \
"v_rcp_f32 v156, v156\n" \
"v_rcp_f32 v157, v157\n" \
"v_rcp_f32 v158, v158\n" \
"v_rcp_f32 v159, v159\n" \
"s_add_u32 s94, s79, 276480\n" \
"buffer_load_dwordx4 v242, s[32:35], s94 offen offset:3072 lds\n" \
"v_pk_mul_f32 v[144:145], v[144:145], v[152:153]\n" \
"v_pk_mul_f32 v[146:147], v[146:147], v[154:155]\n" \
"v_pk_mul_f32 v[148:149], v[148:149], v[156:157]\n" \
"v_pk_mul_f32 v[150:151], v[150:151], v[158:159]\n" \
"v_cvt_pk_bf16_f32 v164, v144, v145\n" \
"v_cvt_pk_bf16_f32 v165, v146, v147\n" \
"v_cvt_pk_bf16_f32 v166, v148, v149\n" \
"v_cvt_pk_bf16_f32 v167, v150, v151\n" \
"buffer_store_dwordx4 v[164:167], v224, %[rs], s83 offen offset:3072 sc1\n" \
"s_waitcnt vmcnt(0)\n" \
"s_barrier\n" \
"s_lshl_b32 s82, s40, 2\n" \
"s_add_u32 s80, s82, s62\n" \
"s_cmp_lg_u32 s36, 0\n" \
"s_cbranch_scc1 Lts3%=\n" \
"s_mov_b64 s[96:97], exec\n" \
"s_mov_b64 exec, 1\n" \
"v_mov_b32 v236, %[tag]\n" \
"buffer_store_dword v236, off, %[rs], s80 sc1\n" \
"s_mov_b64 exec, s[96:97]\n" \
"Lts3%=:\n" \
"s_add_u32 s80, s56, s78\n" \
"s_add_u32 m0, s80, 0\n" \
"s_add_u32 s94, s79, 344064\n" \
"buffer_load_dwordx4 v242, s[32:35], s94 offen offset:0 lds\n" \
"s_add_u32 s94, s79, 350208\n" \
"buffer_load_dwordx4 v242, s[32:35], s94 offen offset:1024 lds\n" \
"s_add_u32 s94, s79, 356352\n" \
"buffer_load_dwordx4 v242, s[32:35], s94 offen offset:2048 lds\n" \
"s_add_u32 s94, s79, 362496\n" \
"buffer_load_dwordx4 v242, s[32:35], s94 offen offset:3072 lds\n" \
"s_add_u32 s80, s56, s78\n" \
"s_add_u32 m0, s80, 4096\n" \
"s_add_u32 s94, s79, 372736\n" \
"buffer_load_dwordx4 v242, s[32:35], s94 offen offset:0 lds\n" \
"s_add_u32 s94, s79, 378880\n" \
"buffer_load_dwordx4 v242, s[32:35], s94 offen offset:1024 lds\n" \
"s_add_u32 s94, s79, 385024\n" \
"buffer_load_dwordx4 v242, s[32:35], s94 offen offset:2048 lds\n" \
"s_add_u32 s94, s79, 391168\n" \
"buffer_load_dwordx4 v242, s[32:35], s94 offen offset:3072 lds\n" \
"s_add_u32 s80, s57, s78\n" \
"s_add_u32 m0, s80, 0\n" \
"s_add_u32 s94, s79, 458752\n" \
"buffer_load_dwordx4 v242, s[32:35], s94 offen offset:0 lds\n" \
"s_add_u32 s94, s79, 464896\n" \
"buffer_load_dwordx4 v242, s[32:35], s94 offen offset:1024 lds\n" \
"s_add_u32 s94, s79, 471040\n" \
"buffer_load_dwordx4 v242, s[32:35], s94 offen offset:2048 lds\n" \
"s_add_u32 s94, s79, 477184\n" \
"buffer_load_dwordx4 v242, s[32:35], s94 offen offset:3072 lds\n" \
"s_add_u32 s80, s57, s78\n" \
"s_add_u32 m0, s80, 4096\n" \
"s_add_u32 s94, s79, 487424\n" \
"buffer_load_dwordx4 v242, s[32:35], s94 offen offset:0 lds\n" \
"s_add_u32 s94, s79, 493568\n" \
"buffer_load_dwordx4 v242, s[32:35], s94 offen offset:1024 lds\n" \
"s_add_u32 s94, s79, 499712\n" \
"buffer_load_dwordx4 v242, s[32:35], s94 offen offset:2048 lds\n" \
"s_add_u32 s94, s79, 505856\n" \
"buffer_load_dwordx4 v242, s[32:35], s94 offen offset:3072 lds\n" \
"buffer_load_dwordx4 v[96:99], v224, %[rs], s76 offen sc1\n" \
"buffer_load_dwordx4 v[100:103], v224, %[rs], s76 offen offset:1024 sc1\n" \
"v_bfe_u32 v236, v227, 1, 2\n" \
"v_lshlrev_b32 v236, 5, v236\n" \
"v_bfe_u32 v237, v224, 4, 2\n" \
"v_lshlrev_b32 v237, 3, v237\n" \
"v_lshl_add_u32 v237, v227, 7, v237\n" \
"s_and_b32 s80, s78, 0x4000\n" \
"s_add_u32 s80, s80, s58\n" \
"v_xor_b32 v229, 0, v236\n" \
"v_xor_b32 v230, 32, v236\n" \
"v_xor_b32 v231, 64, v236\n" \
"v_xor_b32 v232, 96, v236\n" \
"v_add3_u32 v229, v229, v237, s80\n" \
"v_add3_u32 v230, v230, v237, s80\n" \
"v_add3_u32 v231, v231, v237, s80\n" \
"v_add3_u32 v232, v232, v237, s80\n" \
"s_mov_b32 s81, 0\n" \
"Ltw3_p%=:\n" \
"buffer_load_dword v243, v241, %[rs], s62 offen sc1\n" \
"s_waitcnt vmcnt(0)\n" \
"v_cmp_ne_u32 s[96:97], %[tag], v243\n" \
"s_nop 1\n" \
"s_cmp_eq_u64 s[96:97], 0\n" \
"s_cbranch_scc1 Ltw3_d%=\n" \
"s_sleep 4\n" \
"s_add_u32 s81, s81, 1\n" \
"s_cmp_lt_u32 s81, 0x20000\n" \
"s_cbranch_scc1 Ltw3_p%=\n" \
"Ltw3_d%=:\n" \
"s_barrier\n" \
"s_add_u32 s83, s75, 0\n" \
"buffer_load_dwordx4 v[104:107], v224, %[rs], s83 offen sc1\n" \
"s_add_u32 s83, s75, 57344\n" \
"buffer_load_dwordx4 v[108:111], v224, %[rs], s83 offen sc1\n" \
"s_add_u32 s83, s75, 1024\n" \
"buffer_load_dwordx4 v[112:115], v224, %[rs], s83 offen sc1\n" \
"s_add_u32 s83, s75, 58368\n" \
"buffer_load_dwordx4 v[116:119], v224, %[rs], s83 offen sc1\n" \
"s_add_u32 s83, s75, 2048\n" \
"buffer_load_dwordx4 v[120:123], v224, %[rs], s83 offen sc1\n" \
"s_add_u32 s83, s75, 59392\n" \
"buffer_load_dwordx4 v[124:127], v224, %[rs], s83 offen sc1\n" \
"s_add_u32 s83, s75, 3072\n" \
"buffer_load_dwordx4 v[128:131], v224, %[rs], s83 offen sc1\n" \
"s_add_u32 s83, s75, 60416\n" \
"buffer_load_dwordx4 v[132:135], v224, %[rs], s83 offen sc1\n" \
"ds_read_b64_tr_b16 v[64:65], v229 offset:0\n" \
"ds_read_b64_tr_b16 v[66:67], v229 offset:2048\n" \
"ds_read_b64_tr_b16 v[68:69], v230 offset:0\n" \
"ds_read_b64_tr_b16 v[70:71], v230 offset:2048\n" \
"ds_read_b64_tr_b16 v[72:73], v231 offset:0\n" \
"ds_read_b64_tr_b16 v[74:75], v231 offset:2048\n" \
"ds_read_b64_tr_b16 v[76:77], v232 offset:0\n" \
"ds_read_b64_tr_b16 v[78:79], v232 offset:2048\n" \
"ds_read_b64_tr_b16 v[80:81], v229 offset:4096\n" \
"ds_read_b64_tr_b16 v[82:83], v229 offset:6144\n" \
"ds_read_b64_tr_b16 v[84:85], v230 offset:4096\n" \
"ds_read_b64_tr_b16 v[86:87], v230 offset:6144\n" \
"s_waitcnt lgkmcnt(8)\n" \
"s_waitcnt vmcnt(6)\n" \
"v_mfma_f32_16x16x32_bf16 v[0:3], v[64:67], v[104:107], 0\n" \
"ds_read_b64_tr_b16 v[88:89], v231 offset:4096\n" \
"s_add_u32 s83, s75, 4096\n" \
"v_mfma_f32_16x16x32_bf16 v[4:7], v[64:67], v[108:111], 0\n" \
"ds_read_b64_tr_b16 v[90:91], v231 offset:6144\n" \
"buffer_load_dwordx4 v[136:139], v224, %[rs], s83 offen sc1\n" \
"v_mfma_f32_16x16x32_bf16 v[8:11], v[68:71], v[104:107], 0\n" \
"ds_read_b64_tr_b16 v[92:93], v232 offset:4096\n" \
"s_add_u32 s83, s75, 61440\n" \
"v_mfma_f32_16x16x32_bf16 v[12:15], v[68:71], v[108:111], 0\n" \
"ds_read_b64_tr_b16 v[94:95], v232 offset:6144\n" \
"s_waitcnt lgkmcnt(8)\n" \
"buffer_load_dwordx4 v[140:143], v224, %[rs], s83 offen sc1\n" \
"v_mfma_f32_16x16x32_bf16 v[16:19], v[72:75], v[104:107], 0\n" \
"ds_read_b64_tr_b16 v[64:65], v229 offset:8192\n" \
"s_add_u32 s83, s75, 5120\n" \
"v_mfma_f32_16x16x32_bf16 v[20:23], v[72:75], v[108:111], 0\n" \
"ds_read_b64_tr_b16 v[66:67], v229 offset:10240\n" \
"buffer_load_dwordx4 v[144:147], v224, %[rs], s83 offen sc1\n" \
"v_mfma_f32_16x16x32_bf16 v[24:27], v[76:79], v[104:107], 0\n" \
"ds_read_b64_tr_b16 v[68:69], v230 offset:8192\n" \
"s_add_u32 s83, s75, 62464\n" \
"v_mfma_f32_16x16x32_bf16 v[28:31], v[76:79], v[108:111], 0\n" \
"ds_read_b64_tr_b16 v[70:71], v230 offset:10240\n" \
"s_waitcnt lgkmcnt(8)\n" \
"buffer_load_dwordx4 v[148:151], v224, %[rs], s83 offen sc1\n" \
"s_waitcnt vmcnt(8)\n" \
"v_mfma_f32_16x16x32_bf16 v[0:3], v[80:83], v[112:115], v[0:3]\n" \
"ds_read_b64_tr_b16 v[72:73], v231 offset:8192\n" \
"s_add_u32 s83, s75, 6144\n" \
"v_mfma_f32_16x16x32_bf16 v[4:7], v[80:83], v[116:119], v[4:7]\n" \
"ds_read_b64_tr_b16 v[74:75], v231 offset:10240\n" \
"buffer_load_dwordx4 v[152:155], v224, %[rs], s83 offen sc1\n" \
"v_mfma_f32_16x16x32_bf16 v[8:11], v[84:87], v[112:115], v[8:11]\n" \
"ds_read_b64_tr_b16 v[76:77], v232 offset:8192\n" \
"s_add_u32 s83, s75, 63488\n" \
"v_mfma_f32_16x16x32_bf16 v[12:15], v[84:87], v[116:119], v[12:15]\n" \
"ds_read_b64_tr_b16 v[78:79], v232 offset:10240\n" \
"s_waitcnt lgkmcnt(8)\n" \
"buffer_load_dwordx4 v[156:159], v224, %[rs], s83 offen sc1\n" \
"v_mfma_f32_16x16x32_bf16 v[16:19], v[88:91], v[112:115], v[16:19]\n" \
"ds_read_b64_tr_b16 v[80:81], v229 offset:12288\n" \
"s_add_u32 s83, s75, 7168\n" \
"v_mfma_f32_16x16x32_bf16 v[20:23], v[88:91], v[116:119], v[20:23]\n" \
"ds_read_b64_tr_b16 v[82:83], v229 offset:14336\n" \
"buffer_load_dwordx4 v[160:163], v224, %[rs], s83 offen sc1\n" \
"v_mfma_f32_16x16x32_bf16 v[24:27], v[92:95], v[112:115], v[24:27]\n" \
"ds_read_b64_tr_b16 v[84:85], v230 offset:12288\n" \
"s_add_u32 s83, s75, 64512\n" \
"v_mfma_f32_16x16x32_bf16 v[28:31], v[92:95], v[116:119], v[28:31]\n" \
"ds_read_b64_tr_b16 v[86:87], v230 offset:14336\n" \
"s_waitcnt lgkmcnt(8)\n" \
"buffer_load_dwordx4 v[164:167], v224, %[rs], s83 offen sc1\n" \
"s_waitcnt vmcnt(10)\n" \
"v_mfma_f32_16x16x32_bf16 v[0:3], v[64:67], v[120:123], v[0:3]\n" \
"ds_read_b64_tr_b16 v[88:89], v231 offset:12288\n" \
"s_add_u32 s83, s75, 8192\n" \
"v_mfma_f32_16x16x32_bf16 v[4:7], v[64:67], v[124:127], v[4:7]\n" \
"ds_read_b64_tr_b16 v[90:91], v231 offset:14336\n" \
"buffer_load_dwordx4 v[168:171], v224, %[rs], s83 offen sc1\n" \
"v_mfma_f32_16x16x32_bf16 v[8:11], v[68:71], v[120:123], v[8:11]\n" \
"ds_read_b64_tr_b16 v[92:93], v232 offset:12288\n" \
"s_add_u32 s83, s75, 65536\n" \
"v_mfma_f32_16x16x32_bf16 v[12:15], v[68:71], v[124:127], v[12:15]\n" \
"ds_read_b64_tr_b16 v[94:95], v232 offset:14336\n" \
"s_waitcnt lgkmcnt(8)\n" \
"buffer_load_dwordx4 v[172:175], v224, %[rs], s83 offen sc1\n" \
"s_barrier\n" \
"v_mfma_f32_16x16x32_bf16 v[16:19], v[72:75], v[120:123], v[16:19]\n" \
"s_sub_u32 s82, s54, s58\n" \
"v_add_u32 v229, s82, v229\n" \
"ds_read_b64_tr_b16 v[64:65], v229 offset:0\n" \
"s_add_u32 s83, s75, 9216\n" \
"v_mfma_f32_16x16x32_bf16 v[20:23], v[72:75], v[124:127], v[20:23]\n" \
"ds_read_b64_tr_b16 v[66:67], v229 offset:2048\n" \
"buffer_load_dwordx4 v[176:179], v224, %[rs], s83 offen sc1\n" \
"v_mfma_f32_16x16x32_bf16 v[24:27], v[76:79], v[120:123], v[24:27]\n" \
"v_add_u32 v230, s82, v230\n" \
"ds_read_b64_tr_b16 v[68:69], v230 offset:0\n" \
"s_add_u32 s83, s75, 66560\n" \
"v_mfma_f32_16x16x32_bf16 v[28:31], v[76:79], v[124:127], v[28:31]\n" \
"ds_read_b64_tr_b16 v[70:71], v230 offset:2048\n" \
"s_waitcnt lgkmcnt(8)\n" \
"buffer_load_dwordx4 v[180:183], v224, %[rs], s83 offen sc1\n" \
"s_waitcnt vmcnt(12)\n" \
"v_mfma_f32_16x16x32_bf16 v[0:3], v[80:83], v[128:131], v[0:3]\n" \
"v_add_u32 v231, s82, v231\n" \
"ds_read_b64_tr_b16 v[72:73], v231 offset:0\n" \
"s_add_u32 s83, s75, 10240\n" \
"v_mfma_f32_16x16x32_bf16 v[4:7], v[80:83], v[132:135], v[4:7]\n" \
"ds_read_b64_tr_b16 v[74:75], v231 offset:2048\n" \
"buffer_load_dwordx4 v[184:187], v224, %[rs], s83 offen sc1\n" \
"v_mfma_f32_16x16x32_bf16 v[8:11], v[84:87], v[128:131], v[8:11]\n" \
"v_add_u32 v232, s82, v232\n" \
"ds_read_b64_tr_b16 v[76:77], v232 offset:0\n" \
"s_add_u32 s83, s75, 67584\n" \
"v_mfma_f32_16x16x32_bf16 v[12:15], v[84:87], v[132:135], v[12:15]\n" \
"ds_read_b64_tr_b16 v[78:79], v232 offset:2048\n" \
"s_waitcnt lgkmcnt(8)\n" \
"buffer_load_dwordx4 v[188:191], v224, %[rs], s83 offen sc1\n" \
"v_mfma_f32_16x16x32_bf16 v[16:19], v[88:91], v[128:131], v[16:19]\n" \
"ds_read_b64_tr_b16 v[80:81], v229 offset:4096\n" \
"s_add_u32 s83, s75, 11264\n" \
"v_mfma_f32_16x16x32_bf16 v[20:23], v[88:91], v[132:135], v[20:23]\n" \
"ds_read_b64_tr_b16 v[82:83], v229 offset:6144\n" \
"buffer_load_dwordx4 v[192:195], v224, %[rs], s83 offen sc1\n" \
"v_mfma_f32_16x16x32_bf16 v[24:27], v[92:95], v[128:131], v[24:27]\n" \
"ds_read_b64_tr_b16 v[84:85], v230 offset:4096\n" \
"s_add_u32 s83, s75, 68608\n" \
"v_mfma_f32_16x16x32_bf16 v[28:31], v[92:95], v[132:135], v[28:31]\n" \
"ds_read_b64_tr_b16 v[86:87], v230 offset:6144\n" \
"s_waitcnt lgkmcnt(8)\n" \
"buffer_load_dwordx4 v[196:199], v224, %[rs], s83 offen sc1\n" \
"s_waitcnt vmcnt(14)\n" \
"v_mfma_f32_16x16x32_bf16 v[0:3], v[64:67], v[136:139], v[0:3]\n" \
"ds_read_b64_tr_b16 v[88:89], v231 offset:4096\n" \
"s_add_u32 s83, s75, 12288\n" \
"v_mfma_f32_16x16x32_bf16 v[4:7], v[64:67], v[140:143], v[4:7]\n" \
"ds_read_b64_tr_b16 v[90:91], v231 offset:6144\n" \
"buffer_load_dwordx4 v[200:203], v224, %[rs], s83 offen sc1\n" \
"v_mfma_f32_16x16x32_bf16 v[8:11], v[68:71], v[136:139], v[8:11]\n" \
"ds_read_b64_tr_b16 v[92:93], v232 offset:4096\n" \
"s_add_u32 s83, s75, 69632\n" \
"v_mfma_f32_16x16x32_bf16 v[12:15], v[68:71], v[140:143], v[12:15]\n" \
"ds_read_b64_tr_b16 v[94:95], v232 offset:6144\n" \
"s_waitcnt lgkmcnt(8)\n" \
"buffer_load_dwordx4 v[204:207], v224, %[rs], s83 offen sc1\n" \
"v_mfma_f32_16x16x32_bf16 v[16:19], v[72:75], v[136:139], v[16:19]\n" \
"ds_read_b64_tr_b16 v[64:65], v229 offset:8192\n" \
"s_add_u32 s83, s75, 13312\n" \
"v_mfma_f32_16x16x32_bf16 v[20:23], v[72:75], v[140:143], v[20:23]\n" \
"ds_read_b64_tr_b16 v[66:67], v229 offset:10240\n" \
"buffer_load_dwordx4 v[208:211], v224, %[rs], s83 offen sc1\n" \
"v_mfma_f32_16x16x32_bf16 v[24:27], v[76:79], v[136:139], v[24:27]\n" \
"ds_read_b64_tr_b16 v[68:69], v230 offset:8192\n" \
"s_add_u32 s83, s75, 70656\n" \
"v_mfma_f32_16x16x32_bf16 v[28:31], v[76:79], v[140:143], v[28:31]\n" \
"ds_read_b64_tr_b16 v[70:71], v230 offset:10240\n" \
"s_waitcnt lgkmcnt(8)\n" \
"buffer_load_dwordx4 v[212:215], v224, %[rs], s83 offen sc1\n" \
"s_waitcnt vmcnt(16)\n" \
"v_mfma_f32_16x16x32_bf16 v[0:3], v[80:83], v[144:147], v[0:3]\n" \
"ds_read_b64_tr_b16 v[72:73], v231 offset:8192\n" \
"s_add_u32 s83, s75, 14336\n" \
"v_mfma_f32_16x16x32_bf16 v[4:7], v[80:83], v[148:151], v[4:7]\n" \
"ds_read_b64_tr_b16 v[74:75], v231 offset:10240\n" \
"buffer_load_dwordx4 v[216:219], v224, %[rs], s83 offen sc1\n" \
"v_mfma_f32_16x16x32_bf16 v[8:11], v[84:87], v[144:147], v[8:11]\n" \
"ds_read_b64_tr_b16 v[76:77], v232 offset:8192\n" \
"s_add_u32 s83, s75, 71680\n" \
"v_mfma_f32_16x16x32_bf16 v[12:15], v[84:87], v[148:151], v[12:15]\n" \
"ds_read_b64_tr_b16 v[78:79], v232 offset:10240\n" \
"s_waitcnt lgkmcnt(8)\n" \
"buffer_load_dwordx4 v[220:223], v224, %[rs], s83 offen sc1\n" \
"v_mfma_f32_16x16x32_bf16 v[16:19], v[88:91], v[144:147], v[16:19]\n" \
"ds_read_b64_tr_b16 v[80:81], v229 offset:12288\n" \
"s_add_u32 s83, s75, 15360\n" \
"v_mfma_f32_16x16x32_bf16 v[20:23], v[88:91], v[148:151], v[20:23]\n" \
"ds_read_b64_tr_b16 v[82:83], v229 offset:14336\n" \
"buffer_load_dwordx4 v[104:107], v224, %[rs], s83 offen sc1\n" \
"v_mfma_f32_16x16x32_bf16 v[24:27], v[92:95], v[144:147], v[24:27]\n" \
"ds_read_b64_tr_b16 v[84:85], v230 offset:12288\n" \
"s_add_u32 s83, s75, 72704\n" \
"v_mfma_f32_16x16x32_bf16 v[28:31], v[92:95], v[148:151], v[28:31]\n" \
"ds_read_b64_tr_b16 v[86:87], v230 offset:14336\n" \
"s_waitcnt lgkmcnt(8)\n" \
"buffer_load_dwordx4 v[108:111], v224, %[rs], s83 offen sc1\n" \
"s_waitcnt vmcnt(18)\n" \
"v_mfma_f32_16x16x32_bf16 v[0:3], v[64:67], v[152:155], v[0:3]\n" \
"ds_read_b64_tr_b16 v[88:89], v231 offset:12288\n" \
"v_mfma_f32_16x16x32_bf16 v[4:7], v[64:67], v[156:159], v[4:7]\n" \
"ds_read_b64_tr_b16 v[90:91], v231 offset:14336\n" \
"v_mfma_f32_16x16x32_bf16 v[8:11], v[68:71], v[152:155], v[8:11]\n" \
"ds_read_b64_tr_b16 v[92:93], v232 offset:12288\n" \
"v_mfma_f32_16x16x32_bf16 v[12:15], v[68:71], v[156:159], v[12:15]\n" \
"ds_read_b64_tr_b16 v[94:95], v232 offset:14336\n" \
"s_waitcnt lgkmcnt(8)\n" \
"s_barrier\n" \
"v_mfma_f32_16x16x32_bf16 v[16:19], v[72:75], v[152:155], v[16:19]\n" \
"s_sub_u32 s82, s55, s54\n" \
"v_add_u32 v229, s82, v229\n" \
"ds_read_b64_tr_b16 v[64:65], v229 offset:0\n" \
"v_mfma_f32_16x16x32_bf16 v[20:23], v[72:75], v[156:159], v[20:23]\n" \
"ds_read_b64_tr_b16 v[66:67], v229 offset:2048\n" \
"s_add_u32 s80, s58, s78\n" \
"s_add_u32 m0, s80, 0\n" \
"v_mfma_f32_16x16x32_bf16 v[24:27], v[76:79], v[152:155], v[24:27]\n" \
"v_add_u32 v230, s82, v230\n" \
"ds_read_b64_tr_b16 v[68:69], v230 offset:0\n" \
"s_add_u32 s94, s79, 573440\n" \
"v_mfma_f32_16x16x32_bf16 v[28:31], v[76:79], v[156:159], v[28:31]\n" \
"ds_read_b64_tr_b16 v[70:71], v230 offset:2048\n" \
"s_waitcnt lgkmcnt(8)\n" \
"buffer_load_dwordx4 v242, s[32:35], s94 offen offset:0 lds\n" \
"s_waitcnt vmcnt(17)\n" \
"v_mfma_f32_16x16x32_bf16 v[0:3], v[80:83], v[160:163], v[0:3]\n" \
"v_add_u32 v231, s82, v231\n" \
"ds_read_b64_tr_b16 v[72:73], v231 offset:0\n" \
"s_add_u32 s94, s79, 579584\n" \
"v_mfma_f32_16x16x32_bf16 v[4:7], v[80:83], v[164:167], v[4:7]\n" \
"ds_read_b64_tr_b16 v[74:75], v231 offset:2048\n" \
"buffer_load_dwordx4 v242, s[32:35], s94 offen offset:1024 lds\n" \
"v_mfma_f32_16x16x32_bf16 v[8:11], v[84:87], v[160:163], v[8:11]\n" \
"v_add_u32 v232, s82, v232\n" \
"ds_read_b64_tr_b16 v[76:77], v232 offset:0\n" \
"s_add_u32 s83, s75, 16384\n" \
"v_mfma_f32_16x16x32_bf16 v[12:15], v[84:87], v[164:167], v[12:15]\n" \
"ds_read_b64_tr_b16 v[78:79], v232 offset:2048\n" \
"s_waitcnt lgkmcnt(8)\n" \
"buffer_load_dwordx4 v[112:115], v224, %[rs], s83 offen sc1\n" \
"v_mfma_f32_16x16x32_bf16 v[16:19], v[88:91], v[160:163], v[16:19]\n" \
"ds_read_b64_tr_b16 v[80:81], v229 offset:4096\n" \
"s_add_u32 s83, s75, 73728\n" \
"v_mfma_f32_16x16x32_bf16 v[20:23], v[88:91], v[164:167], v[20:23]\n" \
"ds_read_b64_tr_b16 v[82:83], v229 offset:6144\n" \
"buffer_load_dwordx4 v[116:119], v224, %[rs], s83 offen sc1\n" \
"v_mfma_f32_16x16x32_bf16 v[24:27], v[92:95], v[160:163], v[24:27]\n" \
"ds_read_b64_tr_b16 v[84:85], v230 offset:4096\n" \
"s_add_u32 s94, s79, 585728\n" \
"v_mfma_f32_16x16x32_bf16 v[28:31], v[92:95], v[164:167], v[28:31]\n" \
"ds_read_b64_tr_b16 v[86:87], v230 offset:6144\n" \
"s_waitcnt lgkmcnt(8)\n" \
"buffer_load_dwordx4 v242, s[32:35], s94 offen offset:2048 lds\n" \
"s_waitcnt vmcnt(19)\n" \
"v_mfma_f32_16x16x32_bf16 v[0:3], v[64:67], v[168:171], v[0:3]\n" \
"ds_read_b64_tr_b16 v[88:89], v231 offset:4096\n" \
"s_add_u32 s94, s79, 591872\n" \
"v_mfma_f32_16x16x32_bf16 v[4:7], v[64:67], v[172:175], v[4:7]\n" \
"ds_read_b64_tr_b16 v[90:91], v231 offset:6144\n" \
"buffer_load_dwordx4 v242, s[32:35], s94 offen offset:3072 lds\n" \
"v_mfma_f32_16x16x32_bf16 v[8:11], v[68:71], v[168:171], v[8:11]\n" \
"ds_read_b64_tr_b16 v[92:93], v232 offset:4096\n" \
"s_add_u32 s83, s75, 17408\n" \
"v_mfma_f32_16x16x32_bf16 v[12:15], v[68:71], v[172:175], v[12:15]\n" \
"ds_read_b64_tr_b16 v[94:95], v232 offset:6144\n" \
"s_waitcnt lgkmcnt(8)\n" \
"buffer_load_dwordx4 v[120:123], v224, %[rs], s83 offen sc1\n" \
"v_mfma_f32_16x16x32_bf16 v[16:19], v[72:75], v[168:171], v[16:19]\n" \
"ds_read_b64_tr_b16 v[64:65], v229 offset:8192\n" \
"s_add_u32 s83, s75, 74752\n" \
"v_mfma_f32_16x16x32_bf16 v[20:23], v[72:75], v[172:175], v[20:23]\n" \
"ds_read_b64_tr_b16 v[66:67], v229 offset:10240\n" \
"buffer_load_dwordx4 v[124:127], v224, %[rs], s83 offen sc1\n" \
"v_mfma_f32_16x16x32_bf16 v[24:27], v[76:79], v[168:171], v[24:27]\n" \
"ds_read_b64_tr_b16 v[68:69], v230 offset:8192\n" \
"s_add_u32 s80, s58, s78\n" \
"s_add_u32 m0, s80, 4096\n" \
"v_mfma_f32_16x16x32_bf16 v[28:31], v[76:79], v[172:175], v[28:31]\n" \
"ds_read_b64_tr_b16 v[70:71], v230 offset:10240\n" \
"s_waitcnt lgkmcnt(8)\n" \
"s_add_u32 s94, s79, 602112\n" \
"s_waitcnt vmcnt(20)\n" \
"v_mfma_f32_16x16x32_bf16 v[0:3], v[80:83], v[176:179], v[0:3]\n" \
"ds_read_b64_tr_b16 v[72:73], v231 offset:8192\n" \
"buffer_load_dwordx4 v242, s[32:35], s94 offen offset:0 lds\n" \
"v_mfma_f32_16x16x32_bf16 v[4:7], v[80:83], v[180:183], v[4:7]\n" \
"ds_read_b64_tr_b16 v[74:75], v231 offset:10240\n" \
"s_add_u32 s94, s79, 608256\n" \
"v_mfma_f32_16x16x32_bf16 v[8:11], v[84:87], v[176:179], v[8:11]\n" \
"ds_read_b64_tr_b16 v[76:77], v232 offset:8192\n" \
"buffer_load_dwordx4 v242, s[32:35], s94 offen offset:1024 lds\n" \
"v_mfma_f32_16x16x32_bf16 v[12:15], v[84:87], v[180:183], v[12:15]\n" \
"ds_read_b64_tr_b16 v[78:79], v232 offset:10240\n" \
"s_waitcnt lgkmcnt(8)\n" \
"s_add_u32 s83, s75, 18432\n" \
"v_mfma_f32_16x16x32_bf16 v[16:19], v[88:91], v[176:179], v[16:19]\n" \
"ds_read_b64_tr_b16 v[80:81], v229 offset:12288\n" \
"buffer_load_dwordx4 v[128:131], v224, %[rs], s83 offen sc1\n" \
"v_mfma_f32_16x16x32_bf16 v[20:23], v[88:91], v[180:183], v[20:23]\n" \
"ds_read_b64_tr_b16 v[82:83], v229 offset:14336\n" \
"s_add_u32 s83, s75, 75776\n" \
"v_mfma_f32_16x16x32_bf16 v[24:27], v[92:95], v[176:179], v[24:27]\n" \
"ds_read_b64_tr_b16 v[84:85], v230 offset:12288\n" \
"buffer_load_dwordx4 v[132:135], v224, %[rs], s83 offen sc1\n" \
"v_mfma_f32_16x16x32_bf16 v[28:31], v[92:95], v[180:183], v[28:31]\n" \
"ds_read_b64_tr_b16 v[86:87], v230 offset:14336\n" \
"s_waitcnt lgkmcnt(8)\n" \
"s_add_u32 s94, s79, 614400\n" \
"s_waitcnt vmcnt(22)\n" \
"v_mfma_f32_16x16x32_bf16 v[0:3], v[64:67], v[184:187], v[0:3]\n" \
"ds_read_b64_tr_b16 v[88:89], v231 offset:12288\n" \
"buffer_load_dwordx4 v242, s[32:35], s94 offen offset:2048 lds\n" \
"v_mfma_f32_16x16x32_bf16 v[4:7], v[64:67], v[188:191], v[4:7]\n" \
"ds_read_b64_tr_b16 v[90:91], v231 offset:14336\n" \
"s_add_u32 s94, s79, 620544\n" \
"v_mfma_f32_16x16x32_bf16 v[8:11], v[68:71], v[184:187], v[8:11]\n" \
"ds_read_b64_tr_b16 v[92:93], v232 offset:12288\n" \
"buffer_load_dwordx4 v242, s[32:35], s94 offen offset:3072 lds\n" \
"v_mfma_f32_16x16x32_bf16 v[12:15], v[68:71], v[188:191], v[12:15]\n" \
"ds_read_b64_tr_b16 v[94:95], v232 offset:14336\n" \
"s_waitcnt lgkmcnt(8)\n" \
"s_add_u32 s83, s75, 19456\n" \
"s_barrier\n" \
"v_mfma_f32_16x16x32_bf16 v[16:19], v[72:75], v[184:187], v[16:19]\n" \
"s_sub_u32 s82, s56, s55\n" \
"v_add_u32 v229, s82, v229\n" \
"ds_read_b64_tr_b16 v[64:65], v229 offset:0\n" \
"buffer_load_dwordx4 v[136:139], v224, %[rs], s83 offen sc1\n" \
"v_mfma_f32_16x16x32_bf16 v[20:23], v[72:75], v[188:191], v[20:23]\n" \
"ds_read_b64_tr_b16 v[66:67], v229 offset:2048\n" \
"s_add_u32 s83, s75, 76800\n" \
"v_mfma_f32_16x16x32_bf16 v[24:27], v[76:79], v[184:187], v[24:27]\n" \
"v_add_u32 v230, s82, v230\n" \
"ds_read_b64_tr_b16 v[68:69], v230 offset:0\n" \
"buffer_load_dwordx4 v[140:143], v224, %[rs], s83 offen sc1\n" \
"v_mfma_f32_16x16x32_bf16 v[28:31], v[76:79], v[188:191], v[28:31]\n" \
"ds_read_b64_tr_b16 v[70:71], v230 offset:2048\n" \
"s_waitcnt lgkmcnt(8)\n" \
"s_add_u32 s83, s75, 20480\n" \
"s_waitcnt vmcnt(24)\n" \
"v_mfma_f32_16x16x32_bf16 v[0:3], v[80:83], v[192:195], v[0:3]\n" \
"v_add_u32 v231, s82, v231\n" \
"ds_read_b64_tr_b16 v[72:73], v231 offset:0\n" \
"buffer_load_dwordx4 v[144:147], v224, %[rs], s83 offen sc1\n" \
"v_mfma_f32_16x16x32_bf16 v[4:7], v[80:83], v[196:199], v[4:7]\n" \
"ds_read_b64_tr_b16 v[74:75], v231 offset:2048\n" \
"s_add_u32 s83, s75, 77824\n" \
"v_mfma_f32_16x16x32_bf16 v[8:11], v[84:87], v[192:195], v[8:11]\n" \
"v_add_u32 v232, s82, v232\n" \
"ds_read_b64_tr_b16 v[76:77], v232 offset:0\n" \
"buffer_load_dwordx4 v[148:151], v224, %[rs], s83 offen sc1\n" \
"v_mfma_f32_16x16x32_bf16 v[12:15], v[84:87], v[196:199], v[12:15]\n" \
"ds_read_b64_tr_b16 v[78:79], v232 offset:2048\n" \
"s_waitcnt lgkmcnt(8)\n" \
"s_add_u32 s80, s54, s78\n" \
"s_add_u32 m0, s80, 0\n" \
"v_mfma_f32_16x16x32_bf16 v[16:19], v[88:91], v[192:195], v[16:19]\n" \
"ds_read_b64_tr_b16 v[80:81], v229 offset:4096\n" \
"s_add_u32 s94, s79, 688128\n" \
"v_mfma_f32_16x16x32_bf16 v[20:23], v[88:91], v[196:199], v[20:23]\n" \
"ds_read_b64_tr_b16 v[82:83], v229 offset:6144\n" \
"buffer_load_dwordx4 v242, s[32:35], s94 offen offset:0 lds\n" \
"v_mfma_f32_16x16x32_bf16 v[24:27], v[92:95], v[192:195], v[24:27]\n" \
"ds_read_b64_tr_b16 v[84:85], v230 offset:4096\n" \
"s_add_u32 s94, s79, 694272\n" \
"v_mfma_f32_16x16x32_bf16 v[28:31], v[92:95], v[196:199], v[28:31]\n" \
"ds_read_b64_tr_b16 v[86:87], v230 offset:6144\n" \
"s_waitcnt lgkmcnt(8)\n" \
"buffer_load_dwordx4 v242, s[32:35], s94 offen offset:1024 lds\n" \
"s_waitcnt vmcnt(26)\n" \
"v_mfma_f32_16x16x32_bf16 v[0:3], v[64:67], v[200:203], v[0:3]\n" \
"ds_read_b64_tr_b16 v[88:89], v231 offset:4096\n" \
"s_add_u32 s83, s75, 21504\n" \
"v_mfma_f32_16x16x32_bf16 v[4:7], v[64:67], v[204:207], v[4:7]\n" \
"ds_read_b64_tr_b16 v[90:91], v231 offset:6144\n" \
"buffer_load_dwordx4 v[152:155], v224, %[rs], s83 offen sc1\n" \
"v_mfma_f32_16x16x32_bf16 v[8:11], v[68:71], v[200:203], v[8:11]\n" \
"ds_read_b64_tr_b16 v[92:93], v232 offset:4096\n" \
"s_add_u32 s83, s75, 78848\n" \
"v_mfma_f32_16x16x32_bf16 v[12:15], v[68:71], v[204:207], v[12:15]\n" \
"ds_read_b64_tr_b16 v[94:95], v232 offset:6144\n" \
"s_waitcnt lgkmcnt(8)\n" \
"buffer_load_dwordx4 v[156:159], v224, %[rs], s83 offen sc1\n" \
"v_mfma_f32_16x16x32_bf16 v[16:19], v[72:75], v[200:203], v[16:19]\n" \
"ds_read_b64_tr_b16 v[64:65], v229 offset:8192\n" \
"s_add_u32 s94, s79, 700416\n" \
"v_mfma_f32_16x16x32_bf16 v[20:23], v[72:75], v[204:207], v[20:23]\n" \
"ds_read_b64_tr_b16 v[66:67], v229 offset:10240\n" \
"buffer_load_dwordx4 v242, s[32:35], s94 offen offset:2048 lds\n" \
"v_mfma_f32_16x16x32_bf16 v[24:27], v[76:79], v[200:203], v[24:27]\n" \
"ds_read_b64_tr_b16 v[68:69], v230 offset:8192\n" \
"s_add_u32 s94, s79, 706560\n" \
"v_mfma_f32_16x16x32_bf16 v[28:31], v[76:79], v[204:207], v[28:31]\n" \
"ds_read_b64_tr_b16 v[70:71], v230 offset:10240\n" \
"s_waitcnt lgkmcnt(8)\n" \
"buffer_load_dwordx4 v242, s[32:35], s94 offen offset:3072 lds\n" \
"s_waitcnt vmcnt(28)\n" \
"v_mfma_f32_16x16x32_bf16 v[0:3], v[80:83], v[208:211], v[0:3]\n" \
"ds_read_b64_tr_b16 v[72:73], v231 offset:8192\n" \
"s_add_u32 s83, s75, 22528\n" \
"v_mfma_f32_16x16x32_bf16 v[4:7], v[80:83], v[212:215], v[4:7]\n" \
"ds_read_b64_tr_b16 v[74:75], v231 offset:10240\n" \
"buffer_load_dwordx4 v[160:163], v224, %[rs], s83 offen sc1\n" \
"v_mfma_f32_16x16x32_bf16 v[8:11], v[84:87], v[208:211], v[8:11]\n" \
"ds_read_b64_tr_b16 v[76:77], v232 offset:8192\n" \
"s_add_u32 s83, s75, 79872\n" \
"v_mfma_f32_16x16x32_bf16 v[12:15], v[84:87], v[212:215], v[12:15]\n" \
"ds_read_b64_tr_b16 v[78:79], v232 offset:10240\n" \
"s_waitcnt lgkmcnt(8)\n" \
"buffer_load_dwordx4 v[164:167], v224, %[rs], s83 offen sc1\n" \
"v_mfma_f32_16x16x32_bf16 v[16:19], v[88:91], v[208:211], v[16:19]\n" \
"ds_read_b64_tr_b16 v[80:81], v229 offset:12288\n" \
"s_add_u32 s80, s54, s78\n" \
"s_add_u32 m0, s80, 4096\n" \
"v_mfma_f32_16x16x32_bf16 v[20:23], v[88:91], v[212:215], v[20:23]\n" \
"ds_read_b64_tr_b16 v[82:83], v229 offset:14336\n" \
"s_add_u32 s94, s79, 716800\n" \
"v_mfma_f32_16x16x32_bf16 v[24:27], v[92:95], v[208:211], v[24:27]\n" \
"ds_read_b64_tr_b16 v[84:85], v230 offset:12288\n" \
"buffer_load_dwordx4 v242, s[32:35], s94 offen offset:0 lds\n" \
"v_mfma_f32_16x16x32_bf16 v[28:31], v[92:95], v[212:215], v[28:31]\n" \
"ds_read_b64_tr_b16 v[86:87], v230 offset:14336\n" \
"s_waitcnt lgkmcnt(8)\n" \
"s_add_u32 s94, s79, 722944\n" \
"s_waitcnt vmcnt(29)\n" \
"v_mfma_f32_16x16x32_bf16 v[0:3], v[64:67], v[216:219], v[0:3]\n" \
"ds_read_b64_tr_b16 v[88:89], v231 offset:12288\n" \
"buffer_load_dwordx4 v242, s[32:35], s94 offen offset:1024 lds\n" \
"v_mfma_f32_16x16x32_bf16 v[4:7], v[64:67], v[220:223], v[4:7]\n" \
"ds_read_b64_tr_b16 v[90:91], v231 offset:14336\n" \
"s_add_u32 s83, s75, 23552\n" \
"v_mfma_f32_16x16x32_bf16 v[8:11], v[68:71], v[216:219], v[8:11]\n" \
"ds_read_b64_tr_b16 v[92:93], v232 offset:12288\n" \
"buffer_load_dwordx4 v[168:171], v224, %[rs], s83 offen sc1\n" \
"v_mfma_f32_16x16x32_bf16 v[12:15], v[68:71], v[220:223], v[12:15]\n" \
"ds_read_b64_tr_b16 v[94:95], v232 offset:14336\n" \
"s_waitcnt lgkmcnt(8)\n" \
"s_add_u32 s83, s75, 80896\n" \
"s_barrier\n" \
"v_mfma_f32_16x16x32_bf16 v[16:19], v[72:75], v[216:219], v[16:19]\n" \
"s_sub_u32 s82, s57, s56\n" \
"v_add_u32 v229, s82, v229\n" \
"ds_read_b64_tr_b16 v[64:65], v229 offset:0\n" \
"buffer_load_dwordx4 v[172:175], v224, %[rs], s83 offen sc1\n" \
"v_mfma_f32_16x16x32_bf16 v[20:23], v[72:75], v[220:223], v[20:23]\n" \
"ds_read_b64_tr_b16 v[66:67], v229 offset:2048\n" \
"s_add_u32 s94, s79, 729088\n" \
"v_mfma_f32_16x16x32_bf16 v[24:27], v[76:79], v[216:219], v[24:27]\n" \
"v_add_u32 v230, s82, v230\n" \
"ds_read_b64_tr_b16 v[68:69], v230 offset:0\n" \
"buffer_load_dwordx4 v242, s[32:35], s94 offen offset:2048 lds\n" \
"v_mfma_f32_16x16x32_bf16 v[28:31], v[76:79], v[220:223], v[28:31]\n" \
"ds_read_b64_tr_b16 v[70:71], v230 offset:2048\n" \
"s_waitcnt lgkmcnt(8)\n" \
"s_add_u32 s94, s79, 735232\n" \
"s_waitcnt vmcnt(31)\n" \
"v_mfma_f32_16x16x32_bf16 v[0:3], v[80:83], v[104:107], v[0:3]\n" \
"v_add_u32 v231, s82, v231\n" \
"ds_read_b64_tr_b16 v[72:73], v231 offset:0\n" \
"buffer_load_dwordx4 v242, s[32:35], s94 offen offset:3072 lds\n" \
"v_mfma_f32_16x16x32_bf16 v[4:7], v[80:83], v[108:111], v[4:7]\n" \
"ds_read_b64_tr_b16 v[74:75], v231 offset:2048\n" \
"s_add_u32 s83, s75, 24576\n" \
"v_mfma_f32_16x16x32_bf16 v[8:11], v[84:87], v[104:107], v[8:11]\n" \
"v_add_u32 v232, s82, v232\n" \
"ds_read_b64_tr_b16 v[76:77], v232 offset:0\n" \
"buffer_load_dwordx4 v[176:179], v224, %[rs], s83 offen sc1\n" \
"v_mfma_f32_16x16x32_bf16 v[12:15], v[84:87], v[108:111], v[12:15]\n" \
"ds_read_b64_tr_b16 v[78:79], v232 offset:2048\n" \
"s_waitcnt lgkmcnt(8)\n" \
"s_add_u32 s83, s75, 81920\n" \
"v_mfma_f32_16x16x32_bf16 v[16:19], v[88:91], v[104:107], v[16:19]\n" \
"ds_read_b64_tr_b16 v[80:81], v229 offset:4096\n" \
"buffer_load_dwordx4 v[180:183], v224, %[rs], s83 offen sc1\n" \
"v_mfma_f32_16x16x32_bf16 v[20:23], v[88:91], v[108:111], v[20:23]\n" \
"ds_read_b64_tr_b16 v[82:83], v229 offset:6144\n" \
"s_add_u32 s83, s75, 25600\n" \
"v_mfma_f32_16x16x32_bf16 v[24:27], v[92:95], v[104:107], v[24:27]\n" \
"ds_read_b64_tr_b16 v[84:85], v230 offset:4096\n" \
"buffer_load_dwordx4 v[184:187], v224, %[rs], s83 offen sc1\n" \
"v_mfma_f32_16x16x32_bf16 v[28:31], v[92:95], v[108:111], v[28:31]\n" \
"ds_read_b64_tr_b16 v[86:87], v230 offset:6144\n" \
"s_waitcnt lgkmcnt(8)\n" \
"s_add_u32 s83, s75, 82944\n" \
"s_waitcnt vmcnt(31)\n" \
"v_mfma_f32_16x16x32_bf16 v[0:3], v[64:67], v[112:115], v[0:3]\n" \
"ds_read_b64_tr_b16 v[88:89], v231 offset:4096\n" \
"buffer_load_dwordx4 v[188:191], v224, %[rs], s83 offen sc1\n" \
"v_mfma_f32_16x16x32_bf16 v[4:7], v[64:67], v[116:119], v[4:7]\n" \
"ds_read_b64_tr_b16 v[90:91], v231 offset:6144\n" \
"s_add_u32 s83, s75, 26624\n" \
"v_mfma_f32_16x16x32_bf16 v[8:11], v[68:71], v[112:115], v[8:11]\n" \
"ds_read_b64_tr_b16 v[92:93], v232 offset:4096\n" \
"buffer_load_dwordx4 v[192:195], v224, %[rs], s83 offen sc1\n" \
"v_mfma_f32_16x16x32_bf16 v[12:15], v[68:71], v[116:119], v[12:15]\n" \
"ds_read_b64_tr_b16 v[94:95], v232 offset:6144\n" \
"s_waitcnt lgkmcnt(8)\n" \
"s_add_u32 s83, s75, 83968\n" \
"v_mfma_f32_16x16x32_bf16 v[16:19], v[72:75], v[112:115], v[16:19]\n" \
"ds_read_b64_tr_b16 v[64:65], v229 offset:8192\n" \
"buffer_load_dwordx4 v[196:199], v224, %[rs], s83 offen sc1\n" \
"v_mfma_f32_16x16x32_bf16 v[20:23], v[72:75], v[116:119], v[20:23]\n" \
"ds_read_b64_tr_b16 v[66:67], v229 offset:10240\n" \
"s_add_u32 s83, s75, 27648\n" \
"v_mfma_f32_16x16x32_bf16 v[24:27], v[76:79], v[112:115], v[24:27]\n" \
"ds_read_b64_tr_b16 v[68:69], v230 offset:8192\n" \
"buffer_load_dwordx4 v[200:203], v224, %[rs], s83 offen sc1\n" \
"v_mfma_f32_16x16x32_bf16 v[28:31], v[76:79], v[116:119], v[28:31]\n" \
"ds_read_b64_tr_b16 v[70:71], v230 offset:10240\n" \
"s_waitcnt lgkmcnt(8)\n" \
"s_add_u32 s83, s75, 84992\n" \
"s_waitcnt vmcnt(31)\n" \
"v_mfma_f32_16x16x32_bf16 v[0:3], v[80:83], v[120:123], v[0:3]\n" \
"ds_read_b64_tr_b16 v[72:73], v231 offset:8192\n" \
"buffer_load_dwordx4 v[204:207], v224, %[rs], s83 offen sc1\n" \
"v_mfma_f32_16x16x32_bf16 v[4:7], v[80:83], v[124:127], v[4:7]\n" \
"ds_read_b64_tr_b16 v[74:75], v231 offset:10240\n" \
"v_mfma_f32_16x16x32_bf16 v[8:11], v[84:87], v[120:123], v[8:11]\n" \
"ds_read_b64_tr_b16 v[76:77], v232 offset:8192\n" \
"v_mfma_f32_16x16x32_bf16 v[12:15], v[84:87], v[124:127], v[12:15]\n" \
"ds_read_b64_tr_b16 v[78:79], v232 offset:10240\n" \
"s_waitcnt lgkmcnt(8)\n" \
"v_mfma_f32_16x16x32_bf16 v[16:19], v[88:91], v[120:123], v[16:19]\n" \
"ds_read_b64_tr_b16 v[80:81], v229 offset:12288\n" \
"v_mfma_f32_16x16x32_bf16 v[20:23], v[88:91], v[124:127], v[20:23]\n" \
"ds_read_b64_tr_b16 v[82:83], v229 offset:14336\n" \
"v_mfma_f32_16x16x32_bf16 v[24:27], v[92:95], v[120:123], v[24:27]\n" \
"ds_read_b64_tr_b16 v[84:85], v230 offset:12288\n" \
"v_mfma_f32_16x16x32_bf16 v[28:31], v[92:95], v[124:127], v[28:31]\n" \
"ds_read_b64_tr_b16 v[86:87], v230 offset:14336\n" \
"s_waitcnt lgkmcnt(8)\n" \
"s_waitcnt vmcnt(28)\n" \
"v_mfma_f32_16x16x32_bf16 v[0:3], v[64:67], v[128:131], v[0:3]\n" \
"ds_read_b64_tr_b16 v[88:89], v231 offset:12288\n" \
"v_mfma_f32_16x16x32_bf16 v[4:7], v[64:67], v[132:135], v[4:7]\n" \
"ds_read_b64_tr_b16 v[90:91], v231 offset:14336\n" \
"v_mfma_f32_16x16x32_bf16 v[8:11], v[68:71], v[128:131], v[8:11]\n" \
"ds_read_b64_tr_b16 v[92:93], v232 offset:12288\n" \
"v_mfma_f32_16x16x32_bf16 v[12:15], v[68:71], v[132:135], v[12:15]\n" \
"ds_read_b64_tr_b16 v[94:95], v232 offset:14336\n" \
"s_waitcnt lgkmcnt(8)\n" \
"s_waitcnt vmcnt(26)\n" \
"s_barrier\n" \
"v_mfma_f32_16x16x32_bf16 v[16:19], v[72:75], v[128:131], v[16:19]\n" \
"s_sub_u32 s82, s58, s57\n" \
"v_add_u32 v229, s82, v229\n" \
"ds_read_b64_tr_b16 v[64:65], v229 offset:0\n" \
"v_mfma_f32_16x16x32_bf16 v[20:23], v[72:75], v[132:135], v[20:23]\n" \
"ds_read_b64_tr_b16 v[66:67], v229 offset:2048\n" \
"v_mfma_f32_16x16x32_bf16 v[24:27], v[76:79], v[128:131], v[24:27]\n" \
"v_add_u32 v230, s82, v230\n" \
"ds_read_b64_tr_b16 v[68:69], v230 offset:0\n" \
"v_mfma_f32_16x16x32_bf16 v[28:31], v[76:79], v[132:135], v[28:31]\n" \
"ds_read_b64_tr_b16 v[70:71], v230 offset:2048\n" \
"s_waitcnt lgkmcnt(8)\n" \
"s_waitcnt vmcnt(24)\n" \
"v_mfma_f32_16x16x32_bf16 v[0:3], v[80:83], v[136:139], v[0:3]\n" \
"v_add_u32 v231, s82, v231\n" \
"ds_read_b64_tr_b16 v[72:73], v231 offset:0\n" \
"v_mfma_f32_16x16x32_bf16 v[4:7], v[80:83], v[140:143], v[4:7]\n" \
"ds_read_b64_tr_b16 v[74:75], v231 offset:2048\n" \
"v_mfma_f32_16x16x32_bf16 v[8:11], v[84:87], v[136:139], v[8:11]\n" \
"v_add_u32 v232, s82, v232\n" \
"ds_read_b64_tr_b16 v[76:77], v232 offset:0\n" \
"v_mfma_f32_16x16x32_bf16 v[12:15], v[84:87], v[140:143], v[12:15]\n" \
"ds_read_b64_tr_b16 v[78:79], v232 offset:2048\n" \
"s_waitcnt lgkmcnt(8)\n" \
"v_mfma_f32_16x16x32_bf16 v[16:19], v[88:91], v[136:139], v[16:19]\n" \
"ds_read_b64_tr_b16 v[80:81], v229 offset:4096\n" \
"v_mfma_f32_16x16x32_bf16 v[20:23], v[88:91], v[140:143], v[20:23]\n" \
"ds_read_b64_tr_b16 v[82:83], v229 offset:6144\n" \
"v_mfma_f32_16x16x32_bf16 v[24:27], v[92:95], v[136:139], v[24:27]\n" \
"ds_read_b64_tr_b16 v[84:85], v230 offset:4096\n" \
"v_mfma_f32_16x16x32_bf16 v[28:31], v[92:95], v[140:143], v[28:31]\n" \
"ds_read_b64_tr_b16 v[86:87], v230 offset:6144\n" \
"s_waitcnt lgkmcnt(8)\n" \
"s_waitcnt vmcnt(22)\n" \
"v_mfma_f32_16x16x32_bf16 v[0:3], v[64:67], v[144:147], v[0:3]\n" \
"ds_read_b64_tr_b16 v[88:89], v231 offset:4096\n" \
"v_mfma_f32_16x16x32_bf16 v[4:7], v[64:67], v[148:151], v[4:7]\n" \
"ds_read_b64_tr_b16 v[90:91], v231 offset:6144\n" \
"v_mfma_f32_16x16x32_bf16 v[8:11], v[68:71], v[144:147], v[8:11]\n" \
"ds_read_b64_tr_b16 v[92:93], v232 offset:4096\n" \
"v_mfma_f32_16x16x32_bf16 v[12:15], v[68:71], v[148:151], v[12:15]\n" \
"ds_read_b64_tr_b16 v[94:95], v232 offset:6144\n" \
"s_waitcnt lgkmcnt(8)\n" \
"v_mfma_f32_16x16x32_bf16 v[16:19], v[72:75], v[144:147], v[16:19]\n" \
"ds_read_b64_tr_b16 v[64:65], v229 offset:8192\n" \
"v_mfma_f32_16x16x32_bf16 v[20:23], v[72:75], v[148:151], v[20:23]\n" \
"ds_read_b64_tr_b16 v[66:67], v229 offset:10240\n" \
"v_mfma_f32_16x16x32_bf16 v[24:27], v[76:79], v[144:147], v[24:27]\n" \
"ds_read_b64_tr_b16 v[68:69], v230 offset:8192\n" \
"v_mfma_f32_16x16x32_bf16 v[28:31], v[76:79], v[148:151], v[28:31]\n" \
"ds_read_b64_tr_b16 v[70:71], v230 offset:10240\n" \
"s_waitcnt lgkmcnt(8)\n" \
"s_waitcnt vmcnt(18)\n" \
"v_mfma_f32_16x16x32_bf16 v[0:3], v[80:83], v[152:155], v[0:3]\n" \
"ds_read_b64_tr_b16 v[72:73], v231 offset:8192\n" \
"v_mfma_f32_16x16x32_bf16 v[4:7], v[80:83], v[156:159], v[4:7]\n" \
"ds_read_b64_tr_b16 v[74:75], v231 offset:10240\n" \
"v_mfma_f32_16x16x32_bf16 v[8:11], v[84:87], v[152:155], v[8:11]\n" \
"ds_read_b64_tr_b16 v[76:77], v232 offset:8192\n" \
"v_mfma_f32_16x16x32_bf16 v[12:15], v[84:87], v[156:159], v[12:15]\n" \
"ds_read_b64_tr_b16 v[78:79], v232 offset:10240\n" \
"s_waitcnt lgkmcnt(8)\n" \
"v_mfma_f32_16x16x32_bf16 v[16:19], v[88:91], v[152:155], v[16:19]\n" \
"ds_read_b64_tr_b16 v[80:81], v229 offset:12288\n" \
"v_mfma_f32_16x16x32_bf16 v[20:23], v[88:91], v[156:159], v[20:23]\n" \
"ds_read_b64_tr_b16 v[82:83], v229 offset:14336\n" \
"v_mfma_f32_16x16x32_bf16 v[24:27], v[92:95], v[152:155], v[24:27]\n" \
"ds_read_b64_tr_b16 v[84:85], v230 offset:12288\n" \
"v_mfma_f32_16x16x32_bf16 v[28:31], v[92:95], v[156:159], v[28:31]\n" \
"ds_read_b64_tr_b16 v[86:87], v230 offset:14336\n" \
"s_waitcnt lgkmcnt(8)\n" \
"s_waitcnt vmcnt(14)\n" \
"v_mfma_f32_16x16x32_bf16 v[0:3], v[64:67], v[160:163], v[0:3]\n" \
"ds_read_b64_tr_b16 v[88:89], v231 offset:12288\n" \
"v_mfma_f32_16x16x32_bf16 v[4:7], v[64:67], v[164:167], v[4:7]\n" \
"ds_read_b64_tr_b16 v[90:91], v231 offset:14336\n" \
"v_mfma_f32_16x16x32_bf16 v[8:11], v[68:71], v[160:163], v[8:11]\n" \
"ds_read_b64_tr_b16 v[92:93], v232 offset:12288\n" \
"v_mfma_f32_16x16x32_bf16 v[12:15], v[68:71], v[164:167], v[12:15]\n" \
"ds_read_b64_tr_b16 v[94:95], v232 offset:14336\n" \
"s_waitcnt lgkmcnt(8)\n" \
"s_waitcnt vmcnt(8)\n" \
"s_barrier\n" \
"v_mfma_f32_16x16x32_bf16 v[16:19], v[72:75], v[160:163], v[16:19]\n" \
"s_sub_u32 s82, s54, s58\n" \
"v_add_u32 v229, s82, v229\n" \
"ds_read_b64_tr_b16 v[64:65], v229 offset:0\n" \
"v_mfma_f32_16x16x32_bf16 v[20:23], v[72:75], v[164:167], v[20:23]\n" \
"ds_read_b64_tr_b16 v[66:67], v229 offset:2048\n" \
"v_mfma_f32_16x16x32_bf16 v[24:27], v[76:79], v[160:163], v[24:27]\n" \
"v_add_u32 v230, s82, v230\n" \
"ds_read_b64_tr_b16 v[68:69], v230 offset:0\n" \
"v_mfma_f32_16x16x32_bf16 v[28:31], v[76:79], v[164:167], v[28:31]\n" \
"ds_read_b64_tr_b16 v[70:71], v230 offset:2048\n" \
"s_waitcnt lgkmcnt(8)\n" \
"v_mfma_f32_16x16x32_bf16 v[0:3], v[80:83], v[168:171], v[0:3]\n" \
"v_add_u32 v231, s82, v231\n" \
"ds_read_b64_tr_b16 v[72:73], v231 offset:0\n" \
"v_mfma_f32_16x16x32_bf16 v[4:7], v[80:83], v[172:175], v[4:7]\n" \
"ds_read_b64_tr_b16 v[74:75], v231 offset:2048\n" \
"v_mfma_f32_16x16x32_bf16 v[8:11], v[84:87], v[168:171], v[8:11]\n" \
"v_add_u32 v232, s82, v232\n" \
"ds_read_b64_tr_b16 v[76:77], v232 offset:0\n" \
"v_mfma_f32_16x16x32_bf16 v[12:15], v[84:87], v[172:175], v[12:15]\n" \
"ds_read_b64_tr_b16 v[78:79], v232 offset:2048\n" \
"s_waitcnt lgkmcnt(8)\n" \
"v_mfma_f32_16x16x32_bf16 v[16:19], v[88:91], v[168:171], v[16:19]\n" \
"ds_read_b64_tr_b16 v[80:81], v229 offset:4096\n" \
"v_mfma_f32_16x16x32_bf16 v[20:23], v[88:91], v[172:175], v[20:23]\n" \
"ds_read_b64_tr_b16 v[82:83], v229 offset:6144\n" \
"v_mfma_f32_16x16x32_bf16 v[24:27], v[92:95], v[168:171], v[24:27]\n" \
"ds_read_b64_tr_b16 v[84:85], v230 offset:4096\n" \
"v_mfma_f32_16x16x32_bf16 v[28:31], v[92:95], v[172:175], v[28:31]\n" \
"ds_read_b64_tr_b16 v[86:87], v230 offset:6144\n" \
"s_waitcnt lgkmcnt(8)\n" \
"s_waitcnt vmcnt(6)\n" \
"v_mfma_f32_16x16x32_bf16 v[0:3], v[64:67], v[176:179], v[0:3]\n" \
"ds_read_b64_tr_b16 v[88:89], v231 offset:4096\n" \
"v_mfma_f32_16x16x32_bf16 v[4:7], v[64:67], v[180:183], v[4:7]\n" \
"ds_read_b64_tr_b16 v[90:91], v231 offset:6144\n" \
"v_mfma_f32_16x16x32_bf16 v[8:11], v[68:71], v[176:179], v[8:11]\n" \
"ds_read_b64_tr_b16 v[92:93], v232 offset:4096\n" \
"v_mfma_f32_16x16x32_bf16 v[12:15], v[68:71], v[180:183], v[12:15]\n" \
"ds_read_b64_tr_b16 v[94:95], v232 offset:6144\n" \
"s_waitcnt lgkmcnt(8)\n" \
"v_mfma_f32_16x16x32_bf16 v[16:19], v[72:75], v[176:179], v[16:19]\n" \
"ds_read_b64_tr_b16 v[64:65], v229 offset:8192\n" \
"v_mfma_f32_16x16x32_bf16 v[20:23], v[72:75], v[180:183], v[20:23]\n" \
"ds_read_b64_tr_b16 v[66:67], v229 offset:10240\n" \
"v_mfma_f32_16x16x32_bf16 v[24:27], v[76:79], v[176:179], v[24:27]\n" \
"ds_read_b64_tr_b16 v[68:69], v230 offset:8192\n" \
"v_mfma_f32_16x16x32_bf16 v[28:31], v[76:79], v[180:183], v[28:31]\n" \
"ds_read_b64_tr_b16 v[70:71], v230 offset:10240\n" \
"s_waitcnt lgkmcnt(8)\n" \
"s_waitcnt vmcnt(4)\n" \
"v_mfma_f32_16x16x32_bf16 v[0:3], v[80:83], v[184:187], v[0:3]\n" \
"ds_read_b64_tr_b16 v[72:73], v231 offset:8192\n" \
"v_mfma_f32_16x16x32_bf16 v[4:7], v[80:83], v[188:191], v[4:7]\n" \
"ds_read_b64_tr_b16 v[74:75], v231 offset:10240\n" \
"v_mfma_f32_16x16x32_bf16 v[8:11], v[84:87], v[184:187], v[8:11]\n" \
"ds_read_b64_tr_b16 v[76:77], v232 offset:8192\n" \
"v_mfma_f32_16x16x32_bf16 v[12:15], v[84:87], v[188:191], v[12:15]\n" \
"ds_read_b64_tr_b16 v[78:79], v232 offset:10240\n" \
"s_waitcnt lgkmcnt(8)\n" \
"v_mfma_f32_16x16x32_bf16 v[16:19], v[88:91], v[184:187], v[16:19]\n" \
"ds_read_b64_tr_b16 v[80:81], v229 offset:12288\n" \
"v_mfma_f32_16x16x32_bf16 v[20:23], v[88:91], v[188:191], v[20:23]\n" \
"ds_read_b64_tr_b16 v[82:83], v229 offset:14336\n" \
"v_mfma_f32_16x16x32_bf16 v[24:27], v[92:95], v[184:187], v[24:27]\n" \
"ds_read_b64_tr_b16 v[84:85], v230 offset:12288\n" \
"v_mfma_f32_16x16x32_bf16 v[28:31], v[92:95], v[188:191], v[28:31]\n" \
"ds_read_b64_tr_b16 v[86:87], v230 offset:14336\n" \
"s_waitcnt lgkmcnt(8)\n" \
"s_waitcnt vmcnt(2)\n" \
"v_mfma_f32_16x16x32_bf16 v[0:3], v[64:67], v[192:195], v[0:3]\n" \
"ds_read_b64_tr_b16 v[88:89], v231 offset:12288\n" \
"v_mfma_f32_16x16x32_bf16 v[4:7], v[64:67], v[196:199], v[4:7]\n" \
"ds_read_b64_tr_b16 v[90:91], v231 offset:14336\n" \
"v_mfma_f32_16x16x32_bf16 v[8:11], v[68:71], v[192:195], v[8:11]\n" \
"ds_read_b64_tr_b16 v[92:93], v232 offset:12288\n" \
"v_mfma_f32_16x16x32_bf16 v[12:15], v[68:71], v[196:199], v[12:15]\n" \
"ds_read_b64_tr_b16 v[94:95], v232 offset:14336\n" \
"s_waitcnt lgkmcnt(8)\n" \
"v_mfma_f32_16x16x32_bf16 v[16:19], v[72:75], v[192:195], v[16:19]\n" \
"v_mfma_f32_16x16x32_bf16 v[20:23], v[72:75], v[196:199], v[20:23]\n" \
"v_mfma_f32_16x16x32_bf16 v[24:27], v[76:79], v[192:195], v[24:27]\n" \
"v_mfma_f32_16x16x32_bf16 v[28:31], v[76:79], v[196:199], v[28:31]\n" \
"s_waitcnt lgkmcnt(4)\n" \
"s_waitcnt vmcnt(0)\n" \
"v_mfma_f32_16x16x32_bf16 v[0:3], v[80:83], v[200:203], v[0:3]\n" \
"v_mfma_f32_16x16x32_bf16 v[4:7], v[80:83], v[204:207], v[4:7]\n" \
"v_mfma_f32_16x16x32_bf16 v[8:11], v[84:87], v[200:203], v[8:11]\n" \
"v_mfma_f32_16x16x32_bf16 v[12:15], v[84:87], v[204:207], v[12:15]\n" \
"s_waitcnt lgkmcnt(0)\n" \
"v_mfma_f32_16x16x32_bf16 v[16:19], v[88:91], v[200:203], v[16:19]\n" \
"v_mfma_f32_16x16x32_bf16 v[20:23], v[88:91], v[204:207], v[20:23]\n" \
"v_mfma_f32_16x16x32_bf16 v[24:27], v[92:95], v[200:203], v[24:27]\n" \
"v_mfma_f32_16x16x32_bf16 v[28:31], v[92:95], v[204:207], v[28:31]\n" \
"s_waitcnt vmcnt(0) lgkmcnt(0)\n" \
"s_barrier\n" \
"s_lshr_b32 s80, s36, 1\n" \
"s_and_b32 s81, s36, 1\n" \
"s_lshl_b32 s81, s81, 13\n" \
"s_lshl_b32 s83, s80, 12\n" \
"s_add_u32 s83, s83, s81\n" \
"s_xor_b32 s94, s83, 0x1000\n" \
"v_add_u32 v236, s94, v224\n" \
"v_add_u32 v237, s83, v224\n" \
"s_cmp_lg_u32 s80, 0\n" \
"s_cbranch_scc1 Lx1%=\n" \
"ds_write_b128 v236, v[4:7] offset:0\n" \
"ds_write_b128 v236, v[12:15] offset:1024\n" \
"ds_write_b128 v236, v[20:23] offset:2048\n" \
"ds_write_b128 v236, v[28:31] offset:3072\n" \
"s_branch Lx2%=\n" \
"Lx1%=:\n" \
"ds_write_b128 v236, v[0:3] offset:0\n" \
"ds_write_b128 v236, v[8:11] offset:1024\n" \
"ds_write_b128 v236, v[16:19] offset:2048\n" \
"ds_write_b128 v236, v[24:27] offset:3072\n" \
"s_nop 1\n" \
"v_mov_b32 v0, v4\n" \
"v_mov_b32 v1, v5\n" \
"v_mov_b32 v2, v6\n" \
"v_mov_b32 v3, v7\n" \
"v_mov_b32 v8, v12\n" \
"v_mov_b32 v9, v13\n" \
"v_mov_b32 v10, v14\n" \
"v_mov_b32 v11, v15\n" \
"v_mov_b32 v16, v20\n" \
"v_mov_b32 v17, v21\n" \
"v_mov_b32 v18, v22\n" \
"v_mov_b32 v19, v23\n" \
"v_mov_b32 v24, v28\n" \
"v_mov_b32 v25, v29\n" \
"v_mov_b32 v26, v30\n" \
"v_mov_b32 v27, v31\n" \
"Lx2%=:\n" \
"s_waitcnt lgkmcnt(0)\n" \
"s_barrier\n" \
"ds_read_b128 v[32:35], v237 offset:0\n" \
"ds_read_b128 v[36:39], v237 offset:1024\n" \
"ds_read_b128 v[40:43], v237 offset:2048\n" \
"ds_read_b128 v[44:47], v237 offset:3072\n" \
"v_lshlrev_b32 v48, 16, v96\n" \
"v_and_b32 v49, 0xffff0000, v96\n" \
"v_lshlrev_b32 v50, 16, v97\n" \
"v_and_b32 v51, 0xffff0000, v97\n" \
"v_lshlrev_b32 v52, 16, v98\n" \
"v_and_b32 v53, 0xffff0000, v98\n" \
"v_lshlrev_b32 v54, 16, v99\n" \
"v_and_b32 v55, 0xffff0000, v99\n" \
"v_lshlrev_b32 v56, 16, v100\n" \
"v_and_b32 v57, 0xffff0000, v100\n" \
"v_lshlrev_b32 v58, 16, v101\n" \
"v_and_b32 v59, 0xffff0000, v101\n" \
"v_lshlrev_b32 v60, 16, v102\n" \
"v_and_b32 v61, 0xffff0000, v102\n" \
"v_lshlrev_b32 v62, 16, v103\n" \
"v_and_b32 v63, 0xffff0000, v103\n" \
"s_waitcnt lgkmcnt(0)\n" \
"v_pk_add_f32 v[0:1], v[0:1], v[32:33]\n" \
"v_pk_add_f32 v[2:3], v[2:3], v[34:35]\n" \
"v_pk_add_f32 v[8:9], v[8:9], v[36:37]\n" \
"v_pk_add_f32 v[10:11], v[10:11], v[38:39]\n" \
"v_pk_add_f32 v[16:17], v[16:17], v[40:41]\n" \
"v_pk_add_f32 v[18:19], v[18:19], v[42:43]\n" \
"v_pk_add_f32 v[24:25], v[24:25], v[44:45]\n" \
"v_pk_add_f32 v[26:27], v[26:27], v[46:47]\n" \
"v_cvt_pk_bf16_f32 v4, v0, v1\n" \
"v_cvt_pk_bf16_f32 v5, v2, v3\n" \
"v_cvt_pk_bf16_f32 v12, v8, v9\n" \
"v_cvt_pk_bf16_f32 v13, v10, v11\n" \
"v_cvt_pk_bf16_f32 v20, v16, v17\n" \
"v_cvt_pk_bf16_f32 v21, v18, v19\n" \
"v_cvt_pk_bf16_f32 v28, v24, v25\n" \
"v_cvt_pk_bf16_f32 v29, v26, v27\n" \
"v_lshlrev_b32 v0, 16, v4\n" \
"v_and_b32 v1, 0xffff0000, v4\n" \
"v_lshlrev_b32 v2, 16, v5\n" \
"v_and_b32 v3, 0xffff0000, v5\n" \
"v_lshlrev_b32 v8, 16, v12\n" \
"v_and_b32 v9, 0xffff0000, v12\n" \
"v_lshlrev_b32 v10, 16, v13\n" \
"v_and_b32 v11, 0xffff0000, v13\n" \
"v_lshlrev_b32 v16, 16, v20\n" \
"v_and_b32 v17, 0xffff0000, v20\n" \
"v_lshlrev_b32 v18, 16, v21\n" \
"v_and_b32 v19, 0xffff0000, v21\n" \
"v_lshlrev_b32 v24, 16, v28\n" \
"v_and_b32 v25, 0xffff0000, v28\n" \
"v_lshlrev_b32 v26, 16, v29\n" \
"v_and_b32 v27, 0xffff0000, v29\n" \
"v_pk_add_f32 v[0:1], v[0:1], v[48:49]\n" \
"v_pk_add_f32 v[2:3], v[2:3], v[50:51]\n" \
"v_pk_add_f32 v[8:9], v[8:9], v[52:53]\n" \
"v_pk_add_f32 v[10:11], v[10:11], v[54:55]\n" \
"v_pk_add_f32 v[16:17], v[16:17], v[56:57]\n" \
"v_pk_add_f32 v[18:19], v[18:19], v[58:59]\n" \
"v_pk_add_f32 v[24:25], v[24:25], v[60:61]\n" \
"v_pk_add_f32 v[26:27], v[26:27], v[62:63]\n" \
"v_cvt_pk_bf16_f32 v4, v0, v1\n" \
"v_cvt_pk_bf16_f32 v5, v2, v3\n" \
"buffer_store_dwordx2 v[4:5], v225, s[20:23], s77 offen offset:0\n" \
"v_cvt_pk_bf16_f32 v12, v8, v9\n" \
"v_cvt_pk_bf16_f32 v13, v10, v11\n" \
"buffer_store_dwordx2 v[12:13], v225, s[20:23], s77 offen offset:32\n" \
"v_cvt_pk_bf16_f32 v20, v16, v17\n" \
"v_cvt_pk_bf16_f32 v21, v18, v19\n" \
"buffer_store_dwordx2 v[20:21], v225, s[20:23], s77 offen offset:64\n" \
"v_cvt_pk_bf16_f32 v28, v24, v25\n" \
"v_cvt_pk_bf16_f32 v29, v26, v27\n" \
"buffer_store_dwordx2 v[28:29], v225, s[20:23], s77 offen offset:96\n" \
"Lend%=:\n" \
".Lblk_code_end:\n" \
""
#define M1_VCLOB "v0","v1","v2","v3","v4","v5","v6","v7","v8","v9","v10","v11","v12","v13","v14","v15","v16","v17","v18","v19","v20","v21","v22","v23","v24","v25","v26","v27","v28","v29","v30","v31","v32","v33","v34","v35","v36","v37","v38","v39","v40","v41","v42","v43","v44","v45","v46","v47","v48","v49","v50","v51","v52","v53","v54","v55","v56","v57","v58","v59","v60","v61","v62","v63","v64","v65","v66","v67","v68","v69","v70","v71","v72","v73","v74","v75","v76","v77","v78","v79","v80","v81","v82","v83","v84","v85","v86","v87","v88","v89","v90","v91","v92","v93","v94","v95","v96","v97","v98","v99","v100","v101","v102","v103","v104","v105","v106","v107","v108","v109","v110","v111","v112","v113","v114","v115","v116","v117","v118","v119","v120","v121","v122","v123","v124","v125","v126","v127","v128","v129","v130","v131","v132","v133","v134","v135","v136","v137","v138","v139","v140","v141","v142","v143","v144","v145","v146","v147","v148","v149","v150","v151","v152","v153","v154","v155","v156","v157","v158","v159","v160","v161","v162","v163","v164","v165","v166","v167","v168","v169","v170","v171","v172","v173","v174","v175","v176","v177","v178","v179","v180","v181","v182","v183","v184","v185","v186","v187","v188","v189","v190","v191","v192","v193","v194","v195","v196","v197","v198","v199","v200","v201","v202","v203","v204","v205","v206","v207","v208","v209","v210","v211","v212","v213","v214","v215","v216","v217","v218","v219","v220","v221","v222","v223","v224","v225","v226","v227","v228","v229","v230","v231","v232","v233","v234","v235","v236","v237","v238","v239","v240","v241","v242","v243","v244"
#define M1_ACLOB "a0","a1","a2","a3","a4","a5","a6","a7","a8","a9","a10","a11","a12","a13","a14","a15","a16","a17","a18","a19","a20","a21","a22","a23","a24","a25","a26","a27","a28","a29","a30","a31","a32","a33","a34","a35","a36","a37","a38","a39","a40","a41","a42","a43","a44","a45","a46","a47","a48","a49","a50","a51","a52","a53","a54","a55","a56","a57","a58","a59","a60","a61","a62","a63","a64","a65","a66","a67","a68","a69","a70","a71","a72","a73","a74","a75","a76","a77","a78","a79","a80","a81","a82","a83","a84","a85","a86","a87","a88","a89","a90","a91","a92","a93","a94","a95","a96","a97","a98","a99","a100","a101","a102","a103","a104","a105","a106","a107","a108","a109","a110","a111","a112","a113","a114","a115","a116","a117","a118","a119","a120","a121","a122","a123","a124","a125","a126","a127","a128","a129","a130","a131","a132","a133","a134","a135","a136","a137","a138","a139","a140","a141","a142","a143","a144","a145","a146","a147","a148","a149","a150","a151","a152","a153","a154","a155","a156","a157","a158","a159","a160","a161","a162","a163","a164","a165","a166","a167","a168","a169","a170","a171","a172","a173","a174","a175","a176","a177","a178","a179","a180","a181","a182","a183","a184","a185","a186","a187","a188","a189","a190","a191","a192","a193","a194","a195","a196","a197","a198","a199","a200","a201","a202","a203","a204","a205","a206","a207","a208","a209","a210","a211","a212","a213","a214","a215","a216","a217","a218","a219","a220","a221","a222","a223","a224","a225","a226","a227","a228","a229","a230","a231","a232","a233","a234","a235","a236","a237","a238","a239","a240","a241","a242","a243","a244","a245","a246","a247","a248","a249","a250","a251","a252","a253","a254","a255"
#define M1_SCLOB "s16","s17","s18","s19","s20","s21","s22","s23","s24","s25","s26","s27","s28","s29","s30","s31","s32","s33","s34","s35","s36","s37","s38","s39","s40","s41","s42","s43","s44","s45","s46","s47","s48","s49","s50","s51","s52","s53","s54","s55","s56","s57","s58","s59","s60","s61","s62","s63","s64","s65","s66","s67","s68","s69","s70","s71","s72","s73","s74","s75","s76","s77","s78","s79","s80","s81","s82","s83","s84","s85","s86","s87","s88","s89","s90","s91","s92","s93","s94","s95","s96","s97","s98","s99"

constexpr unsigned WS_FLAGS = 0, WS_KF = 1u << 20, WS_VF = WS_KF + (16u << 20), WS_XN = WS_VF + (16u << 20), WS_QP = WS_XN + (2u << 20), WS_PT = WS_QP + (8u << 20),
                   WS_WOF = WS_PT + (9u << 20), WS_SLAB = WS_WOF + (4u << 20), WS_H = WS_SLAB + (28u << 20), WS_ST = WS_H + (2u << 20), WS_G = WS_ST + (1u << 20), WS_END = WS_G + (8u << 20);
constexpr unsigned FL_BYTES = 208 * 256;
constexpr int M1_LDS = 160 * 1024;

extern "C" __device__ unsigned long long llvm_dispatch_id() __asm("llvm.amdgcn.dispatch.id");
__global__ __launch_bounds__(256, 1) void block_kernel(const u16* __restrict__ x, const u16* __restrict__ c, const u16* __restrict__ wq, const u16* __restrict__ wuk, const u16* __restrict__ wuv,
                                                       const u16* __restrict__ wo, const u16* __restrict__ w1, const u16* __restrict__ w2, char* __restrict__ ws, u16* __restrict__ out) {
  const unsigned tid = threadIdx.x, lane = tid & 63u, wid = rfl(tid >> 6);
  const unsigned b = blockIdx.x, head = 2u * (b & 7u) + ((b >> 3) & 1u), idx = b >> 4;
  typedef unsigned u32x4_ __attribute__((ext_vector_type(4)));
  u32x4_ pfd;
  asm volatile("s_getpc_b64 s[20:21]\n.Lblk_code_beg:\ns_and_b32 s21, s21, 0xffff\ns_mov_b32 s22, .Lblk_code_end-.Lblk_code_beg\ns_mov_b32 s23, 0x20000\n"
               "buffer_load_dwordx4 %0, %1, s[20:23], 0 offen" : "=v"(pfd) : "v"((b >> 3) * 4096u + tid * 16u) : "s20", "s21", "s22", "s23", "scc", "memory");
  __builtin_amdgcn_sched_barrier(0);
  const RSRC rc = mk_rsrc(c + (size_t)(idx >> 1) * 256 * 1024, 256u * 2048u);
  const RSRC rw = mk_rsrc(((idx & 1u) ? wuv : wuk) + head * 256, 1024u * 8192u - head * 512u);
  const RSRC rs = mk_rsrc(ws, WS_END);
  const unsigned vc0 = (lane >> 3) * 2048u + (((lane & 7u) ^ (lane >> 4)) << 4);
  const unsigned vw0 = (lane >> 5) * 8192u + (((lane & 31u) ^ (((lane >> 5) | ((wid & 1u) << 2)) << 1)) << 4);
  const unsigned did = (unsigned)llvm_dispatch_id(), qmix = (unsigned)((size_t)__builtin_amdgcn_dispatch_ptr() >> 21) * 0x9E3779B1u;
  const unsigned tag = (((did >> 7) << 16) ^ (qmix & 0xFFF00000u)) | ((did & 0x7Fu) << 8) | 0x805Au;
  asm volatile("s_nop 4\n" M1_ASM
    :
    : [rc] "s"(rc), [rw] "s"(rw), [rs] "s"(rs), [ka] "s"(__builtin_amdgcn_kernarg_segment_ptr()), [bw] "s"(b | wid << 8), [tag] "s"(tag), [vl16] "v"(lane * 16u), [vc0] "v"(vc0), [vw0] "v"(vw0)
      , "v"(pfd)
    : "memory", "vcc", "scc", M1_VCLOB, M1_ACLOB, M1_SCLOB);
}

extern "C" void kernel_launch(void* const* d_in, const int* in_sizes, int n_in, void* d_out, int out_size, void* d_ws, size_t ws_size, hipStream_t stream) {
  char* ws = (char*)d_ws;
  (void)hipFuncSetAttribute((const void*)block_kernel, hipFuncAttributeMaxDynamicSharedMemorySize, M1_LDS);
  block_kernel<<<256, 256, M1_LDS, stream>>>((const u16*)d_in[0], (const u16*)d_in[1], (const u16*)d_in[2], (const u16*)d_in[3], (const u16*)d_in[4], (const u16*)d_in[5],
                                             (const u16*)d_in[7], (const u16*)d_in[8], ws, (u16*)d_out);
}
```
